```python
import torch

def _softmax(x, axis=-1):
    return torch.softmax(x, dim=axis)
def _gelu(x):
    return torch.nn.functional.gelu(x, approximate="tanh")
def _silu(x):
    return torch.nn.functional.silu(x)
def _sigmoid(x):
    return torch.sigmoid(x)
def _layernorm(x, axis=-1, eps=1e-5):
    m = x.mean(dim=axis, keepdim=True)
    v = x.var(dim=axis, keepdim=True, unbiased=False)
    return (x - m) / torch.sqrt(v + eps)
def _scatter_add(tgt, idx, src, axis=0):
    return tgt.clone().index_add_(axis, idx, src)
def _segment_sum(src, seg, n):
    out = torch.zeros((n,) + src.shape[1:], dtype=src.dtype)
    return out.index_add_(0, seg, src)
def _pad(x, pw):
    flat = tuple(v for pair in reversed(pw) for v in pair)
    return torch.nn.functional.pad(x, flat)

kernel_name = 'recurrent_scan__sliding_block_cached__mixer_token_mlp__prompt'

def setup_inputs():

    _w = (lambda a: (lambda t: t.to(torch.bfloat16) if t.is_floating_point() else t)(torch.as_tensor(a)))
    def _A_setup():
        B, Sq, D = 1, 2048, 576
        Hq, Hkv, Sk, Dh = 32, 16, 2048, 128
        assert Hq % Hkv == 0, (Hq, Hkv)
        return {
            "x": _w((lambda *s: torch.randn(*s, dtype=torch.bfloat16) * 0.05)(B, Sq, D)),
            "k_cache": _w((lambda *s: torch.randn(*s, dtype=torch.bfloat16) * 3.2)(B, Hkv, Sk, Dh)),
            "v_cache": _w((lambda *s: torch.randn(*s, dtype=torch.bfloat16) * 3.2)(B, Hkv, Sk, Dh)),
            "wq": _w((lambda *s: torch.randn(*s, dtype=torch.bfloat16) * 3.2)(D, Hq * Dh) * (D ** -0.5)),
            "wo": _w((lambda *s: torch.randn(*s, dtype=torch.bfloat16) * 3.2)(Hq * Dh, D) * ((Hq * Dh) ** -0.5)),
        }
    _A_in = _A_setup()

    def _B_setup():
        B, S, D, F = 1, 2048, 576, 4608
        return {
            "x": _w((lambda *s: torch.randn(*s, dtype=torch.bfloat16) * 0.05)(B, S, D)),
            "w1": _w((lambda *s: torch.randn(*s, dtype=torch.bfloat16) * 3.2)(S, F) * (S ** -0.5)),
            "b1": _w((lambda *s: torch.randn(*s, dtype=torch.bfloat16) * 3.2)(F) * 0.02),
            "w2": _w((lambda *s: torch.randn(*s, dtype=torch.bfloat16) * 0.4)(F, S) * (F ** -0.5)),
            "b2": _w((lambda *s: torch.randn(*s, dtype=torch.bfloat16) * 0.4)(S) * 0.02),
        }
    _B_in = _B_setup()

    return {
        "A_x": _A_in["x"],
        "A_k_cache": _A_in["k_cache"],
        "A_v_cache": _A_in["v_cache"],
        "A_wq": _A_in["wq"],
        "A_wo": _A_in["wo"],
        "B_x": _B_in["x"],
        "B_w1": _B_in["w1"],
        "B_b1": _B_in["b1"],
        "B_w2": _B_in["w2"],
        "B_b2": _B_in["b2"],
    }

def reference(A_x, A_k_cache, A_v_cache, A_wq, A_wo, B_x, B_w1, B_b1, B_w2, B_b2):
    def _A_ref(x, k_cache, v_cache, wq, wo):
        B, Sq, D = x.shape
        Hq, Dh = 32, 128
        Hkv, Sk = k_cache.shape[1], k_cache.shape[2]
        W = min(4096, max(256, Sk // 4))
        rep = Hq // Hkv
        q = (lambda x, *d: x.permute(*d))((x @ wq).reshape(B, Sq, Hq, Dh), 0, 2, 1, 3)
        k = (lambda x, n, axis: torch.repeat_interleave(x, n, dim=axis))(k_cache[:, :, Sk - W:, :], rep, axis=1)
        v = (lambda x, n, axis: torch.repeat_interleave(x, n, dim=axis))(v_cache[:, :, Sk - W:, :], rep, axis=1)
        scores = torch.einsum("bhqd,bhkd->bhqk", q, k) * (Dh ** -0.5)
        p = _softmax(scores, axis=-1)
        o = torch.einsum("bhqk,bhkd->bhqd", p, v)
        return (lambda x, *d: x.permute(*d))(o, 0, 2, 1, 3).reshape(B, Sq, Hq * Dh) @ wo

    def _B_ref(x, w1, b1, w2, b2):
        B, S, D = x.shape
        h = _layernorm(x, axis=-1)
        u = torch.einsum("bsd,sf->bdf", h, w1) + b1
        u = _gelu(u)
        y = torch.einsum("bdf,fs->bds", u, w2) + b2
        return (lambda x, *d: x.permute(*d))(y, 0, 2, 1)

    x = A_x
    xn = _layernorm(x, axis=-1)
    s = _A_ref(xn, A_k_cache, A_v_cache, A_wq, A_wo)
    g = _silu(_B_ref(xn, B_w1, B_b1, B_w2, B_b2))
    return x + s * g
```

```cpp
#include <hip/hip_runtime.h>
#include <hip/hip_bf16.h>
#include <stdint.h>
typedef unsigned short u16;
__device__ __forceinline__ float bf2f(u16 v){ return __uint_as_float(((unsigned)v)<<16); }
__device__ __forceinline__ u16 f2bf(float f){ __hip_bfloat16 b = __float2bfloat16(f); return *(u16*)&b; }
constexpr int S=2048,D=576,HQ=32,HKV=16,DH=128,W=512,F=4608,NQ=HQ*DH;

__global__ void ln_k(const u16* x, u16* xn, u16* h){
  int row=blockIdx.x; int t=threadIdx.x;
  float v[9]; float s=0;
  for(int i=0;i<9;i++){ v[i]=bf2f(x[row*D+t+64*i]); s+=v[i]; }
  for(int o=32;o;o>>=1) s+=__shfl_xor(s,o);
  float m=s/D; float q=0;
  for(int i=0;i<9;i++){ float d=v[i]-m; q+=d*d; }
  for(int o=32;o;o>>=1) q+=__shfl_xor(q,o);
  float r=rsqrtf(q/D+1e-5f);
  s=0;
  for(int i=0;i<9;i++){ u16 b=f2bf((v[i]-m)*r); xn[row*D+t+64*i]=b; v[i]=bf2f(b); s+=v[i]; }
  for(int o=32;o;o>>=1) s+=__shfl_xor(s,o);
  m=s/D; q=0;
  for(int i=0;i<9;i++){ float d=v[i]-m; q+=d*d; }
  for(int o=32;o;o>>=1) q+=__shfl_xor(q,o);
  r=rsqrtf(q/D+1e-5f);
  for(int i=0;i<9;i++){ h[row*D+t+64*i]=f2bf((v[i]-m)*r); }
}
template<int MODE>
__global__ void gemm_k(const u16* A,int sam,int sak,const u16* B,int ldb,u16* C,int ldc,int M,int N,int K,const u16* bias){
  __shared__ float As[16][17], Bs[16][17];
  int tx=threadIdx.x&15, ty=threadIdx.x>>4; int m=blockIdx.y*16+ty, n=blockIdx.x*16+tx;
  float acc=0;
  for(int k0=0;k0<K;k0+=16){
    As[ty][tx]=bf2f(A[(size_t)m*sam+(size_t)(k0+tx)*sak]); Bs[ty][tx]=bf2f(B[(size_t)(k0+ty)*ldb+n]);
    __syncthreads();
    for(int k=0;k<16;k++) acc+=As[ty][k]*Bs[k][tx];
    __syncthreads();
  }
  if(MODE==1){ acc+=bf2f(bias[n]); float z=0.7978845608f*(acc+0.044715f*acc*acc*acc); acc=0.5f*acc*(1.f+tanhf(z)); }
  if(MODE==2){ acc+=bf2f(bias[n]); }
  C[(size_t)m*ldc+n]=f2bf(acc);
}
__global__ void attn_k(const u16* q,const u16* kc,const u16* vc,u16* o){
  int row=blockIdx.x, h=blockIdx.y, t=threadIdx.x;
  __shared__ float qs[DH], p[W], red[128];
  qs[t]=bf2f(q[(size_t)row*NQ+h*DH+t]); __syncthreads();
  const u16* K=kc+((size_t)(h/2)*S+(S-W))*DH; const u16* V=vc+((size_t)(h/2)*S+(S-W))*DH;
  float mx=-1e30f;
  for(int j=t;j<W;j+=128){ float s=0; for(int d=0;d<DH;d++) s+=qs[d]*bf2f(K[j*DH+d]); s*=0.08838834764f; p[j]=s; mx=fmaxf(mx,s);}
  red[t]=mx; __syncthreads(); for(int o2=64;o2;o2>>=1){ if(t<o2) red[t]=fmaxf(red[t],red[t+o2]); __syncthreads(); }
  mx=red[0]; __syncthreads(); float sm=0;
  for(int j=t;j<W;j+=128){ p[j]=__expf(p[j]-mx); sm+=p[j]; }
  red[t]=sm; __syncthreads(); for(int o2=64;o2;o2>>=1){ if(t<o2) red[t]+=red[t+o2]; __syncthreads(); }
  sm=red[0]; float a=0; for(int j=0;j<W;j++) a+=p[j]*bf2f(V[j*DH+t]);
  o[(size_t)row*NQ+h*DH+t]=f2bf(a/sm);
}
__global__ void fin_k(const u16* x,const u16* s,const u16* y,u16* out){
  int i=blockIdx.x*256+threadIdx.x; int r=i/D,c=i%D; float yy=bf2f(y[(size_t)c*S+r]); float g=yy/(1.f+__expf(-yy));
  out[i]=f2bf(bf2f(x[i])+bf2f(s[i])*g);
}
extern "C" void kernel_launch(void* const* d_in, const int* in_sizes, int n_in, void* d_out, int out_size, void* d_ws, size_t ws_size, hipStream_t stream){
  const u16 *x=(const u16*)d_in[0],*kc=(const u16*)d_in[1],*vc=(const u16*)d_in[2],*wq=(const u16*)d_in[3],*wo=(const u16*)d_in[4],*w1=(const u16*)d_in[6],*b1=(const u16*)d_in[7],*w2=(const u16*)d_in[8],*b2=(const u16*)d_in[9];
  u16* ws=(u16*)d_ws; u16 *xn=ws,*h=xn+S*D,*q=h+S*D,*o=q+S*NQ,*u=o+S*NQ,*s=u+D*F,*y=s+S*D;
  ln_k<<<S,64,0,stream>>>(x,xn,h);
  gemm_k<0><<<dim3(NQ/16,S/16),256,0,stream>>>(xn,D,1,wq,NQ,q,NQ,S,NQ,D,nullptr);
  attn_k<<<dim3(S,HQ),128,0,stream>>>(q,kc,vc,o);
  gemm_k<0><<<dim3(D/16,S/16),256,0,stream>>>(o,NQ,1,wo,D,s,D,S,D,NQ,nullptr);
  gemm_k<1><<<dim3(F/16,D/16),256,0,stream>>>(h,1,D,w1,F,u,F,D,F,S,b1);
  gemm_k<2><<<dim3(S/16,D/16),256,0,stream>>>(u,F,1,w2,S,y,S,D,S,F,b2);
  fin_k<<<S*D/256,256,0,stream>>>(x,s,y,(u16*)d_out);
}
```

```cpp
#include <hip/hip_runtime.h>
#include <hip/hip_bf16.h>
typedef unsigned short u16;

__global__ __launch_bounds__(256, 1) void fused_block(const u16* x, const u16* kc, const u16* vc, const u16* wq, const u16* wo, const u16* w1, const u16* b1,
                                               const u16* w2, const u16* b2, u16* out, char* ws) {
  __shared__ __attribute__((aligned(16))) char smem[163840];
  const unsigned wid = __builtin_amdgcn_readfirstlane(threadIdx.x >> 6);
  asm volatile(
"L_code0:\n"
"s_load_dwordx16 s[12:27], %0, 0x0\n"
"s_load_dwordx4 s[28:31], %0, 0x40\n"
"s_load_dwordx2 s[32:33], %0, 0x50\n"
"v_mbcnt_lo_u32_b32 v0, -1, 0\n"
"v_mbcnt_hi_u32_b32 v0, -1, v0\n"
"v_lshlrev_b32 v254, 7, v0\n"
"s_mov_b32 s10, %1\n"
"s_mov_b32 s11, %2\n"
"s_getpc_b64 s[92:93]\n"
"L_pc:\n"
"s_and_b32 s93, s93, 0xffff\n"
"s_mov_b32 s94, L_code1-L_pc\n"
"s_mov_b32 s95, 0x20000\n"
"s_bfe_u32 s59, s10, 0x20003\n"
"s_lshl2_add_u32 s59, s59, s11\n"
"s_lshl_b32 s59, s59, 13\n"
"buffer_load_dword v253, v254, s[92:95], s59 offen\n"
"s_and_b32 s57, s10, 3\n"
"s_lshl_b32 s58, s10, 1\n"
"s_sub_u32 s58, s58, s57\n"
"s_and_b32 s59, s11, 2\n"
"s_lshl1_add_u32 s58, s59, s58\n"
"s_and_b32 s59, s11, 1\n"
"s_lshl1_add_u32 s58, s58, s59\n"
"s_mul_i32 s76, s58, 2304\n"
"v_lshrrev_b32 v6, 5, v0\n"
"v_mul_u32_u24 v6, 1152, v6\n"
"v_and_b32 v7, 31, v0\n"
"v_lshl_add_u32 v236, v7, 4, v6\n"
"v_lshl_add_u32 v237, v7, 2, v6\n"
"v_mov_b32 v250, 0\n"
"s_mov_b32 s59, 0x2ad8000\n"
"s_waitcnt lgkmcnt(0)\n"
"s_mov_b32 s92, s12\n"
"s_and_b32 s93, s13, 0xffff\n"
"s_mov_b32 s94, 0x240000\n"
"s_mov_b32 s95, 0x20000\n"
"buffer_load_dwordx4 v[224:227], v236, s[92:95], s76 offen\n"
"buffer_load_dwordx4 v[228:231], v236, s[92:95], s76 offen offset:512\n"
"buffer_load_dword v232, v237, s[92:95], s76 offen offset:1024\n"
"s_mov_b32 s36, s32\n"
"s_and_b32 s37, s33, 0xffff\n"
"s_mov_b32 s38, 0x2b28000\n"
"s_mov_b32 s39, 0x20000\n"
"buffer_load_dword v252, v250, s[36:39], s59 offen\n"
"s_mov_b32 s92, s22\n"
"s_and_b32 s93, s23, 0xffff\n"
"s_mov_b32 s94, 0x240000\n"
"s_mov_b32 s95, 0x20000\n"
"s_lshl_b32 s58, s11, 8\n"
"s_add_u32 s58, s58, s10\n"
"s_lshl_b32 s58, s58, 13\n"
"buffer_load_dword v253, v254, s[92:95], s58 offen\n"
"v_lshlrev_b32 v1, 4, v0\n"
"v_lshrrev_b32 v2, 4, v0\n"
"v_and_b32 v3, 15, v0\n"
"v_bfe_u32 v4, v0, 2, 2\n"
"v_and_b32 v5, 3, v0\n"
"v_lshlrev_b32 v251, 3, v0\n"
"s_and_b32 s52, s10, 7\n"
"s_lshr_b32 s53, s10, 3\n"
"s_lshr_b32 s54, s53, 4\n"
"s_lshr_b32 s80, s10, 2\n"
"s_and_b32 s81, s10, 3\n"
"v_lshrrev_b32 v6, 5, v0\n"
"v_mul_u32_u24 v6, 1216, v6\n"
"s_mul_i32 s0, s11, 2432\n"
"s_add_u32 s0, s0, 65536\n"
"v_add_u32 v6, s0, v6\n"
"v_and_b32 v7, 31, v0\n"
"v_lshl_add_u32 v28, v7, 4, v6\n"
"v_lshl_add_u32 v29, v7, 2, v6\n"
"s_lshl_b32 s0, s11, 7\n"
"s_add_u32 s0, s0, 65536\n"
"v_mul_u32_u24 v30, 1216, v4\n"
"v_lshl_add_u32 v30, v2, 5, v30\n"
"v_lshl_add_u32 v30, v5, 3, v30\n"
"v_add_u32 v30, s0, v30\n"
"v_and_b32 v6, 1, v2\n"
"v_lshrrev_b32 v7, 1, v2\n"
"v_lshl_add_u32 v31, v6, 2, v5\n"
"v_mul_u32_u24 v31, 1216, v31\n"
"v_lshl_add_u32 v31, v7, 6, v31\n"
"v_lshl_add_u32 v31, v4, 4, v31\n"
"v_add_u32 v31, s0, v31\n"
"v_lshlrev_b32 v234, 4, v3\n"
"v_lshl_add_u32 v235, v6, 10, v234\n"
"v_lshl_add_u32 v235, v7, 12, v235\n"
"v_lshl_add_u32 v234, v2, 10, v234\n"
"s_mul_i32 s0, s80, 36864\n"
"s_lshl_b32 s1, s81, 8\n"
"s_add_u32 s0, s0, s1\n"
"s_lshl_b32 s2, s11, 12\n"
"s_add_u32 s0, s0, s2\n"
"s_add_u32 s0, s0, 0x240000\n"
"s_lshr_b32 s2, s80, 1\n"
"s_mul_i32 s2, s2, 73728\n"
"s_add_u32 s1, s1, s2\n"
"s_and_b32 s2, s80, 1\n"
"s_lshl_b32 s2, s2, 11\n"
"s_add_u32 s1, s1, s2\n"
"s_lshl_b32 s2, s11, 13\n"
"s_add_u32 s1, s1, s2\n"
"s_add_u32 s2, s0, 16384\n"
"s_add_u32 s3, s1, 32768\n"
"s_add_u32 s5, s0, 32768\n"
"s_add_u32 s55, s1, 65536\n"
"s_cmp_eq_u32 s11, 0\n"
"s_cselect_b32 s5, s5, 0x80000000\n"
"s_cselect_b32 s55, s55, 0x80000000\n"
"s_lshl_b32 s87, s10, 2\n"
"s_add_u32 s87, s87, 0x2ad9000\n"
"s_waitcnt vmcnt(1)\n"
"v_add_u32 v252, 0x9e3779b9, v252\n"
"v_lshlrev_b32 v160, 16, v224\n"
"v_and_b32 v161, 0xffff0000, v224\n"
"v_lshlrev_b32 v162, 16, v225\n"
"v_and_b32 v163, 0xffff0000, v225\n"
"v_lshlrev_b32 v164, 16, v226\n"
"v_and_b32 v165, 0xffff0000, v226\n"
"v_lshlrev_b32 v166, 16, v227\n"
"v_and_b32 v167, 0xffff0000, v227\n"
"v_lshlrev_b32 v168, 16, v228\n"
"v_and_b32 v169, 0xffff0000, v228\n"
"v_lshlrev_b32 v170, 16, v229\n"
"v_and_b32 v171, 0xffff0000, v229\n"
"v_lshlrev_b32 v172, 16, v230\n"
"v_and_b32 v173, 0xffff0000, v230\n"
"v_lshlrev_b32 v174, 16, v231\n"
"v_and_b32 v175, 0xffff0000, v231\n"
"v_lshlrev_b32 v176, 16, v232\n"
"v_and_b32 v177, 0xffff0000, v232\n"
"v_readfirstlane_b32 s34, v252\n"
"v_pk_add_f32 v[180:181], v[160:161], v[162:163]\n"
"v_pk_add_f32 v[182:183], v[164:165], v[166:167]\n"
"v_pk_add_f32 v[180:181], v[180:181], v[168:169]\n"
"v_pk_add_f32 v[182:183], v[182:183], v[170:171]\n"
"v_pk_add_f32 v[180:181], v[180:181], v[172:173]\n"
"v_pk_add_f32 v[182:183], v[182:183], v[174:175]\n"
"v_pk_add_f32 v[180:181], v[180:181], v[176:177]\n"
"v_pk_add_f32 v[180:181], v[180:181], v[182:183]\n"
"v_add_f32 v180, v180, v181\n"
"s_nop 1\n"
"v_add_f32_dpp v180, v180, v180 row_ror:8 row_mask:0xf bank_mask:0xf\n"
"s_nop 1\n"
"v_add_f32_dpp v180, v180, v180 row_ror:4 row_mask:0xf bank_mask:0xf\n"
"s_nop 1\n"
"v_add_f32_dpp v180, v180, v180 row_ror:2 row_mask:0xf bank_mask:0xf\n"
"s_nop 1\n"
"v_add_f32_dpp v180, v180, v180 row_ror:1 row_mask:0xf bank_mask:0xf\n"
"v_mov_b32 v181, v180\n"
"s_nop 1\n"
"v_permlane16_swap_b32 v181, v180\n"
"v_add_f32 v180, v180, v181\n"
"v_mul_f32 v184, 0xbae38e39, v180\n"
"v_pk_add_f32 v[160:161], v[160:161], v[184:185] op_sel_hi:[1,0]\n"
"v_pk_add_f32 v[162:163], v[162:163], v[184:185] op_sel_hi:[1,0]\n"
"v_pk_add_f32 v[164:165], v[164:165], v[184:185] op_sel_hi:[1,0]\n"
"v_pk_add_f32 v[166:167], v[166:167], v[184:185] op_sel_hi:[1,0]\n"
"v_pk_add_f32 v[168:169], v[168:169], v[184:185] op_sel_hi:[1,0]\n"
"v_pk_add_f32 v[170:171], v[170:171], v[184:185] op_sel_hi:[1,0]\n"
"v_pk_add_f32 v[172:173], v[172:173], v[184:185] op_sel_hi:[1,0]\n"
"v_pk_add_f32 v[174:175], v[174:175], v[184:185] op_sel_hi:[1,0]\n"
"v_pk_add_f32 v[176:177], v[176:177], v[184:185] op_sel_hi:[1,0]\n"
"v_pk_mul_f32 v[180:181], v[160:161], v[160:161]\n"
"v_pk_mul_f32 v[182:183], v[162:163], v[162:163]\n"
"v_pk_fma_f32 v[180:181], v[164:165], v[164:165], v[180:181]\n"
"v_pk_fma_f32 v[182:183], v[166:167], v[166:167], v[182:183]\n"
"v_pk_fma_f32 v[180:181], v[168:169], v[168:169], v[180:181]\n"
"v_pk_fma_f32 v[182:183], v[170:171], v[170:171], v[182:183]\n"
"v_pk_fma_f32 v[180:181], v[172:173], v[172:173], v[180:181]\n"
"v_pk_fma_f32 v[182:183], v[174:175], v[174:175], v[182:183]\n"
"v_pk_fma_f32 v[180:181], v[176:177], v[176:177], v[180:181]\n"
"v_pk_add_f32 v[180:181], v[180:181], v[182:183]\n"
"v_add_f32 v180, v180, v181\n"
"s_nop 1\n"
"v_add_f32_dpp v180, v180, v180 row_ror:8 row_mask:0xf bank_mask:0xf\n"
"s_nop 1\n"
"v_add_f32_dpp v180, v180, v180 row_ror:4 row_mask:0xf bank_mask:0xf\n"
"s_nop 1\n"
"v_add_f32_dpp v180, v180, v180 row_ror:2 row_mask:0xf bank_mask:0xf\n"
"s_nop 1\n"
"v_add_f32_dpp v180, v180, v180 row_ror:1 row_mask:0xf bank_mask:0xf\n"
"v_mov_b32 v181, v180\n"
"s_nop 1\n"
"v_permlane16_swap_b32 v181, v180\n"
"v_add_f32 v180, v180, v181\n"
"v_mov_b32 v184, 0x3727c5ac\n"
"v_fmac_f32 v184, 0x3ae38e39, v180\n"
"v_rsq_f32 v184, v184\n"
"s_nop 0\n"
"v_mul_f32 v186, 0x3e0293ee, v184\n"
"v_pk_mul_f32 v[180:181], v[160:161], v[186:187] op_sel_hi:[1,0]\n"
"v_cvt_pk_bf16_f32 v200, v180, v181\n"
"v_pk_mul_f32 v[182:183], v[162:163], v[186:187] op_sel_hi:[1,0]\n"
"v_cvt_pk_bf16_f32 v201, v182, v183\n"
"v_pk_mul_f32 v[180:181], v[164:165], v[186:187] op_sel_hi:[1,0]\n"
"v_cvt_pk_bf16_f32 v202, v180, v181\n"
"v_pk_mul_f32 v[182:183], v[166:167], v[186:187] op_sel_hi:[1,0]\n"
"v_cvt_pk_bf16_f32 v203, v182, v183\n"
"v_pk_mul_f32 v[180:181], v[168:169], v[186:187] op_sel_hi:[1,0]\n"
"v_cvt_pk_bf16_f32 v204, v180, v181\n"
"v_pk_mul_f32 v[182:183], v[170:171], v[186:187] op_sel_hi:[1,0]\n"
"v_cvt_pk_bf16_f32 v205, v182, v183\n"
"v_pk_mul_f32 v[180:181], v[172:173], v[186:187] op_sel_hi:[1,0]\n"
"v_cvt_pk_bf16_f32 v206, v180, v181\n"
"v_pk_mul_f32 v[182:183], v[174:175], v[186:187] op_sel_hi:[1,0]\n"
"v_cvt_pk_bf16_f32 v207, v182, v183\n"
"v_pk_mul_f32 v[180:181], v[176:177], v[186:187] op_sel_hi:[1,0]\n"
"v_cvt_pk_bf16_f32 v208, v180, v181\n"
"ds_write_b128 v28, v[200:203] offset:0\n"
"ds_write_b128 v28, v[204:207] offset:512\n"
"ds_write_b32 v29, v208 offset:1024\n"
"v_pk_mul_f32 v[180:181], v[160:161], v[184:185] op_sel_hi:[1,0]\n"
"v_cvt_pk_bf16_f32 v188, v180, v181\n"
"v_pk_mul_f32 v[182:183], v[162:163], v[184:185] op_sel_hi:[1,0]\n"
"v_cvt_pk_bf16_f32 v189, v182, v183\n"
"v_pk_mul_f32 v[180:181], v[164:165], v[184:185] op_sel_hi:[1,0]\n"
"v_cvt_pk_bf16_f32 v190, v180, v181\n"
"v_pk_mul_f32 v[182:183], v[166:167], v[184:185] op_sel_hi:[1,0]\n"
"v_cvt_pk_bf16_f32 v191, v182, v183\n"
"v_pk_mul_f32 v[180:181], v[168:169], v[184:185] op_sel_hi:[1,0]\n"
"v_cvt_pk_bf16_f32 v192, v180, v181\n"
"v_pk_mul_f32 v[182:183], v[170:171], v[184:185] op_sel_hi:[1,0]\n"
"v_cvt_pk_bf16_f32 v193, v182, v183\n"
"v_pk_mul_f32 v[180:181], v[172:173], v[184:185] op_sel_hi:[1,0]\n"
"v_cvt_pk_bf16_f32 v194, v180, v181\n"
"v_pk_mul_f32 v[182:183], v[174:175], v[184:185] op_sel_hi:[1,0]\n"
"v_cvt_pk_bf16_f32 v195, v182, v183\n"
"v_pk_mul_f32 v[180:181], v[176:177], v[184:185] op_sel_hi:[1,0]\n"
"v_cvt_pk_bf16_f32 v196, v180, v181\n"
"v_lshlrev_b32 v160, 16, v188\n"
"v_and_b32 v161, 0xffff0000, v188\n"
"v_lshlrev_b32 v162, 16, v189\n"
"v_and_b32 v163, 0xffff0000, v189\n"
"v_lshlrev_b32 v164, 16, v190\n"
"v_and_b32 v165, 0xffff0000, v190\n"
"v_lshlrev_b32 v166, 16, v191\n"
"v_and_b32 v167, 0xffff0000, v191\n"
"v_lshlrev_b32 v168, 16, v192\n"
"v_and_b32 v169, 0xffff0000, v192\n"
"v_lshlrev_b32 v170, 16, v193\n"
"v_and_b32 v171, 0xffff0000, v193\n"
"v_lshlrev_b32 v172, 16, v194\n"
"v_and_b32 v173, 0xffff0000, v194\n"
"v_lshlrev_b32 v174, 16, v195\n"
"v_and_b32 v175, 0xffff0000, v195\n"
"v_lshlrev_b32 v176, 16, v196\n"
"v_and_b32 v177, 0xffff0000, v196\n"
"v_pk_add_f32 v[180:181], v[160:161], v[162:163]\n"
"v_pk_mul_f32 v[212:213], v[160:161], v[160:161]\n"
"v_pk_fma_f32 v[212:213], v[162:163], v[162:163], v[212:213]\n"
"v_pk_add_f32 v[180:181], v[180:181], v[164:165]\n"
"v_pk_fma_f32 v[212:213], v[164:165], v[164:165], v[212:213]\n"
"v_pk_add_f32 v[180:181], v[180:181], v[166:167]\n"
"v_pk_fma_f32 v[212:213], v[166:167], v[166:167], v[212:213]\n"
"v_pk_add_f32 v[180:181], v[180:181], v[168:169]\n"
"v_pk_fma_f32 v[212:213], v[168:169], v[168:169], v[212:213]\n"
"v_pk_add_f32 v[180:181], v[180:181], v[170:171]\n"
"v_pk_fma_f32 v[212:213], v[170:171], v[170:171], v[212:213]\n"
"v_pk_add_f32 v[180:181], v[180:181], v[172:173]\n"
"v_pk_fma_f32 v[212:213], v[172:173], v[172:173], v[212:213]\n"
"v_pk_add_f32 v[180:181], v[180:181], v[174:175]\n"
"v_pk_fma_f32 v[212:213], v[174:175], v[174:175], v[212:213]\n"
"v_pk_add_f32 v[180:181], v[180:181], v[176:177]\n"
"v_pk_fma_f32 v[212:213], v[176:177], v[176:177], v[212:213]\n"
"v_add_f32 v180, v180, v181\n"
"v_add_f32 v212, v212, v213\n"
"s_nop 0\n"
"v_add_f32_dpp v180, v180, v180 row_ror:8 row_mask:0xf bank_mask:0xf\n"
"v_add_f32_dpp v212, v212, v212 row_ror:8 row_mask:0xf bank_mask:0xf\n"
"s_nop 0\n"
"v_add_f32_dpp v180, v180, v180 row_ror:4 row_mask:0xf bank_mask:0xf\n"
"v_add_f32_dpp v212, v212, v212 row_ror:4 row_mask:0xf bank_mask:0xf\n"
"s_nop 0\n"
"v_add_f32_dpp v180, v180, v180 row_ror:2 row_mask:0xf bank_mask:0xf\n"
"v_add_f32_dpp v212, v212, v212 row_ror:2 row_mask:0xf bank_mask:0xf\n"
"s_nop 0\n"
"v_add_f32_dpp v180, v180, v180 row_ror:1 row_mask:0xf bank_mask:0xf\n"
"v_add_f32_dpp v212, v212, v212 row_ror:1 row_mask:0xf bank_mask:0xf\n"
"v_mov_b32 v181, v180\n"
"v_mov_b32 v213, v212\n"
"s_nop 0\n"
"v_permlane16_swap_b32 v181, v180\n"
"v_permlane16_swap_b32 v213, v212\n"
"v_add_f32 v180, v180, v181\n"
"v_add_f32 v212, v212, v213\n"
"v_mul_f32 v184, 0xbae38e39, v180\n"
"v_mov_b32 v186, 0x3727c5ac\n"
"v_fmac_f32 v186, 0x3ae38e39, v212\n"
"v_fma_f32 v186, -v184, v184, v186\n"
"v_rsq_f32 v186, v186\n"
"v_pk_add_f32 v[160:161], v[160:161], v[184:185] op_sel_hi:[1,0]\n"
"v_pk_add_f32 v[162:163], v[162:163], v[184:185] op_sel_hi:[1,0]\n"
"v_pk_add_f32 v[164:165], v[164:165], v[184:185] op_sel_hi:[1,0]\n"
"v_pk_add_f32 v[166:167], v[166:167], v[184:185] op_sel_hi:[1,0]\n"
"v_pk_add_f32 v[168:169], v[168:169], v[184:185] op_sel_hi:[1,0]\n"
"v_pk_add_f32 v[170:171], v[170:171], v[184:185] op_sel_hi:[1,0]\n"
"v_pk_add_f32 v[172:173], v[172:173], v[184:185] op_sel_hi:[1,0]\n"
"v_pk_add_f32 v[174:175], v[174:175], v[184:185] op_sel_hi:[1,0]\n"
"v_pk_add_f32 v[176:177], v[176:177], v[184:185] op_sel_hi:[1,0]\n"
"v_pk_mul_f32 v[180:181], v[160:161], v[186:187] op_sel_hi:[1,0]\n"
"v_cvt_pk_bf16_f32 v188, v180, v181\n"
"v_pk_mul_f32 v[182:183], v[162:163], v[186:187] op_sel_hi:[1,0]\n"
"v_cvt_pk_bf16_f32 v189, v182, v183\n"
"v_pk_mul_f32 v[180:181], v[164:165], v[186:187] op_sel_hi:[1,0]\n"
"v_cvt_pk_bf16_f32 v190, v180, v181\n"
"v_pk_mul_f32 v[182:183], v[166:167], v[186:187] op_sel_hi:[1,0]\n"
"v_cvt_pk_bf16_f32 v191, v182, v183\n"
"v_pk_mul_f32 v[180:181], v[168:169], v[186:187] op_sel_hi:[1,0]\n"
"v_cvt_pk_bf16_f32 v192, v180, v181\n"
"v_pk_mul_f32 v[182:183], v[170:171], v[186:187] op_sel_hi:[1,0]\n"
"v_cvt_pk_bf16_f32 v193, v182, v183\n"
"v_pk_mul_f32 v[180:181], v[172:173], v[186:187] op_sel_hi:[1,0]\n"
"v_cvt_pk_bf16_f32 v194, v180, v181\n"
"v_pk_mul_f32 v[182:183], v[174:175], v[186:187] op_sel_hi:[1,0]\n"
"v_cvt_pk_bf16_f32 v195, v182, v183\n"
"v_pk_mul_f32 v[180:181], v[176:177], v[186:187] op_sel_hi:[1,0]\n"
"v_cvt_pk_bf16_f32 v196, v180, v181\n"
"ds_write_b128 v28, v[188:191] offset:9728\n"
"ds_write_b128 v28, v[192:195] offset:10240\n"
"ds_write_b32 v29, v196 offset:10752\n"
"s_waitcnt lgkmcnt(0)\n"
"s_barrier\n"
"ds_read_b128 v[160:163], v31 offset:0\n"
"ds_read_b64_tr_b16 v[188:189], v30 offset:9728\n"
"ds_read_b64_tr_b16 v[190:191], v30 offset:14592\n"
"ds_read_b128 v[164:167], v31 offset:512\n"
"ds_read_b64_tr_b16 v[192:193], v30 offset:10240\n"
"ds_read_b64_tr_b16 v[194:195], v30 offset:15104\n"
"ds_read_b128 v[168:171], v31 offset:1024\n"
"ds_read_b64_tr_b16 v[196:197], v30 offset:10752\n"
"ds_read_b64_tr_b16 v[198:199], v30 offset:15616\n"
"s_waitcnt lgkmcnt(8)\n"
"buffer_store_dwordx4 v[160:163], v235, s[36:39], s1 offen sc1\n"
"s_waitcnt lgkmcnt(6)\n"
"buffer_store_dwordx4 v[188:191], v234, s[36:39], s0 offen sc1\n"
"s_waitcnt lgkmcnt(5)\n"
"buffer_store_dwordx4 v[164:167], v235, s[36:39], s3 offen sc1\n"
"s_waitcnt lgkmcnt(3)\n"
"buffer_store_dwordx4 v[192:195], v234, s[36:39], s2 offen sc1\n"
"s_waitcnt lgkmcnt(2)\n"
"buffer_store_dwordx4 v[168:171], v235, s[36:39], s55 offen sc1\n"
"s_waitcnt lgkmcnt(0)\n"
"buffer_store_dwordx4 v[196:199], v234, s[36:39], s5 offen sc1\n"
"v_lshlrev_b32 v6, 10, v0\n"
"s_waitcnt vmcnt(0)\n"
"s_barrier\n"
"s_cmp_lg_u32 s11, 0\n"
"s_cbranch_scc1 L_ln2\n"
"s_mov_b64 exec, 0xff\n"
"buffer_store_dword v252, v6, s[36:39], s87 offen sc1\n"
"s_mov_b64 exec, -1\n"
"L_ln2:\n"
"s_lshl_b32 s91, s52, 10\n"
"s_add_u32 s91, s91, 0x2ad9000\n"
"s_lshr_b32 s80, s52, 1\n"
"s_lshl_b32 s80, s80, 3\n"
"s_bfe_u32 s81, s53, 0x10003\n"
"s_lshl2_add_u32 s80, s81, s80\n"
"s_bfe_u32 s81, s53, 0x10002\n"
"s_add_u32 s80, s80, s81\n"
"s_xor_b32 s81, s54, 1\n"
"s_lshl1_add_u32 s80, s81, s80\n"
"s_and_b32 s81, s52, 1\n"
"s_and_b32 s68, s53, 3\n"
"s_lshl2_add_u32 s81, s81, s68\n"
"s_lshl_b32 s81, s81, 3\n"
"s_bfm_b64 s[98:99], 8, s81\n"
"s_cmp_eq_u32 s54, 1\n"
"s_cselect_b64 s[98:99], s[98:99], -1\n"
"s_cmp_eq_u32 s54, 1\n"
"s_cbranch_scc1 L_preB\n"
"s_cmp_eq_u32 s53, 15\n"
"s_cselect_b32 s56, s52, s53\n"
"s_mul_i32 s57, s56, 43\n"
"s_lshr_b32 s57, s57, 7\n"
"s_mul_i32 s58, s57, 3\n"
"s_sub_u32 s58, s56, s58\n"
"s_mul_i32 s59, s52, 5\n"
"s_cmp_eq_u32 s53, 15\n"
"s_cselect_b32 s59, 40, s59\n"
"s_add_u32 s57, s57, s59\n"
"s_and_b32 s60, s57, 1\n"
"s_mul_i32 s61, s58, 12\n"
"s_mul_i32 s62, s11, 3\n"
"s_add_u32 s61, s61, s62\n"
"s_lshl_b32 s61, s61, 10\n"
"s_mov_b32 s72, s61\n"
"s_mul_i32 s73, s11, 147456\n"
"s_mul_i32 s62, s57, 224\n"
"s_andn2_b32 s62, s62, 0\n"
"s_add_u32 s73, s73, s62\n"
"s_mul_i32 s75, s11, 3584\n"
"s_sub_u32 s68, 9216, s62\n"
"s_mul_i32 s90, s57, 3\n"
"s_add_u32 s90, s90, s58\n"
"s_lshl_b32 s90, s90, 2\n"
"s_add_u32 s90, s90, 0x2adc000\n"
"s_mov_b32 s40, s22\n"
"s_and_b32 s41, s23, 0xffff\n"
"s_mov_b32 s42, 0x1200000\n"
"s_mov_b32 s43, 0x20000\n"
"s_add_u32 s44, s32, 0x240000\n"
"s_addc_u32 s45, s33, 0\n"
"s_and_b32 s45, s45, 0xffff\n"
"s_mov_b32 s46, 0x240000\n"
"s_mov_b32 s47, 0x20000\n"
"s_mov_b32 s48, s24\n"
"s_and_b32 s49, s25, 0xffff\n"
"s_mov_b32 s50, 0x2400\n"
"s_mov_b32 s51, 0x20000\n"
"s_lshr_b32 s68, s68, 4\n"
"s_cmp_eq_u32 s57, 42\n"
"s_cselect_b32 s68, 0, s68\n"
"v_mov_b32 v18, 0x80000000\n"
"s_mov_b32 s96, -1\n"
"s_mov_b32 s97, 0\n"
"v_add_u32 v16, 0, v0\n"
"v_mul_u32_u24 v8, 4682, v16\n"
"v_lshrrev_b32 v8, 16, v8\n"
"v_mul_u32_u24 v17, 14, v8\n"
"v_sub_u32 v16, v16, v17\n"
"v_mul_u32_u24 v8, 9216, v8\n"
"v_lshl_add_u32 v8, v16, 4, v8\n"
"v_cmp_gt_i32 vcc, s68, v16\n"
"s_nop 1\n"
"v_cndmask_b32 v8, v18, v8, vcc\n"
"v_add_u32 v16, 64, v0\n"
"v_mul_u32_u24 v9, 4682, v16\n"
"v_lshrrev_b32 v9, 16, v9\n"
"v_mul_u32_u24 v17, 14, v9\n"
"v_sub_u32 v16, v16, v17\n"
"v_mul_u32_u24 v9, 9216, v9\n"
"v_lshl_add_u32 v9, v16, 4, v9\n"
"v_subrev_u32 v9, 1024, v9\n"
"v_cmp_gt_i32 vcc, s68, v16\n"
"s_nop 1\n"
"v_cndmask_b32 v9, v18, v9, vcc\n"
"v_add_u32 v16, 128, v0\n"
"v_mul_u32_u24 v10, 4682, v16\n"
"v_lshrrev_b32 v10, 16, v10\n"
"v_mul_u32_u24 v17, 14, v10\n"
"v_sub_u32 v16, v16, v17\n"
"v_mul_u32_u24 v10, 9216, v10\n"
"v_lshl_add_u32 v10, v16, 4, v10\n"
"v_subrev_u32 v10, 2048, v10\n"
"v_cmp_gt_i32 vcc, s68, v16\n"
"s_nop 1\n"
"v_cndmask_b32 v10, v18, v10, vcc\n"
"v_add_u32 v16, 192, v0\n"
"v_mul_u32_u24 v11, 4682, v16\n"
"v_lshrrev_b32 v11, 16, v11\n"
"v_mul_u32_u24 v17, 14, v11\n"
"v_sub_u32 v16, v16, v17\n"
"v_mul_u32_u24 v11, 9216, v11\n"
"v_lshl_add_u32 v11, v16, 4, v11\n"
"v_subrev_u32 v11, 3072, v11\n"
"v_cmp_gt_i32 vcc, s68, v16\n"
"s_nop 1\n"
"v_cndmask_b32 v11, v18, v11, vcc\n"
"v_lshl_add_u32 v12, v2, 2, v4\n"
"v_mul_u32_u24 v12, 224, v12\n"
"v_lshl_add_u32 v12, v5, 3, v12\n"
"s_lshl_b32 s62, s60, 5\n"
"s_mul_i32 s63, s60, 192\n"
"s_sub_u32 s63, 192, s63\n"
"v_add_u32 v14, s63, v12\n"
"v_add_u32 v12, s62, v12\n"
"s_mul_i32 s64, s57, 224\n"
"s_add_u32 s65, s64, s62\n"
"s_add_u32 s66, s64, s63\n"
"v_lshlrev_b32 v16, 3, v2\n"
"s_mov_b32 s77, s65\n"
"s_mov_b32 s78, s66\n"
"s_mul_i32 s64, s57, 7\n"
"s_add_u32 s65, s64, s60\n"
"s_lshr_b32 s65, s65, 1\n"
"s_mul_i32 s65, s65, 36864\n"
"s_add_u32 s65, s65, s61\n"
"s_add_u32 s85, s65, 0x480000\n"
"s_mul_i32 s66, s60, 6\n"
"s_sub_u32 s66, 6, s66\n"
"s_add_u32 s66, s66, s64\n"
"s_lshr_b32 s66, s66, 1\n"
"s_mul_i32 s66, s66, 36864\n"
"s_add_u32 s66, s66, s61\n"
"s_lshl_b32 s67, s60, 9\n"
"s_add_u32 s66, s66, s67\n"
"s_add_u32 s86, s66, 0x480000\n"
"buffer_load_dwordx2 v[120:121], v16, s[48:51], s77 offen offset:0\n"
"buffer_load_dwordx2 v[122:123], v16, s[48:51], s77 offen offset:32\n"
"buffer_load_dwordx2 v[124:125], v16, s[48:51], s77 offen offset:64\n"
"buffer_load_dwordx2 v[126:127], v16, s[48:51], s77 offen offset:96\n"
"buffer_load_dwordx2 v[128:129], v16, s[48:51], s77 offen offset:128\n"
"buffer_load_dwordx2 v[130:131], v16, s[48:51], s77 offen offset:160\n"
"buffer_load_dwordx2 v[132:133], v16, s[48:51], s78 offen offset:0\n"
"s_add_u32 m0, s75, 0\n"
"s_nop 0\n"
"buffer_load_dwordx4 v8, s[40:43], s73 offen offset:0 lds\n"
"buffer_load_dwordx4 v9, s[40:43], s73 offen offset:1024 lds\n"
"buffer_load_dwordx4 v10, s[40:43], s73 offen offset:2048 lds\n"
"s_mov_b64 exec, s[96:97]\n"
"buffer_load_dwordx4 v11, s[40:43], s73 offen offset:3072 lds\n"
"s_mov_b64 exec, -1\n"
"s_add_u32 s73, s73, 589824\n"
"s_add_u32 m0, s75, 14336\n"
"s_nop 0\n"
"buffer_load_dwordx4 v8, s[40:43], s73 offen offset:0 lds\n"
"buffer_load_dwordx4 v9, s[40:43], s73 offen offset:1024 lds\n"
"buffer_load_dwordx4 v10, s[40:43], s73 offen offset:2048 lds\n"
"s_mov_b64 exec, s[96:97]\n"
"buffer_load_dwordx4 v11, s[40:43], s73 offen offset:3072 lds\n"
"s_mov_b64 exec, -1\n"
"s_add_u32 s73, s73, 589824\n"
"s_add_u32 m0, s75, 28672\n"
"s_nop 0\n"
"buffer_load_dwordx4 v8, s[40:43], s73 offen offset:0 lds\n"
"buffer_load_dwordx4 v9, s[40:43], s73 offen offset:1024 lds\n"
"buffer_load_dwordx4 v10, s[40:43], s73 offen offset:2048 lds\n"
"s_mov_b64 exec, s[96:97]\n"
"buffer_load_dwordx4 v11, s[40:43], s73 offen offset:3072 lds\n"
"s_mov_b64 exec, -1\n"
"s_add_u32 s73, s73, 589824\n"
"s_add_u32 m0, s75, 43008\n"
"s_nop 0\n"
"buffer_load_dwordx4 v8, s[40:43], s73 offen offset:0 lds\n"
"buffer_load_dwordx4 v9, s[40:43], s73 offen offset:1024 lds\n"
"buffer_load_dwordx4 v10, s[40:43], s73 offen offset:2048 lds\n"
"s_mov_b64 exec, s[96:97]\n"
"buffer_load_dwordx4 v11, s[40:43], s73 offen offset:3072 lds\n"
"s_mov_b64 exec, -1\n"
"s_add_u32 s73, s73, 589824\n"
"L_preA2:\n"
"v_add_u32 v6, 0, v0\n"
"v_mul_u32_u24 v28, 3641, v6\n"
"v_lshrrev_b32 v28, 16, v28\n"
"v_mul_u32_u24 v7, 18, v28\n"
"v_sub_u32 v6, v6, v7\n"
"v_mul_u32_u24 v28, 1152, v28\n"
"v_lshl_add_u32 v28, v6, 4, v28\n"
"v_add_u32 v6, 64, v0\n"
"v_mul_u32_u24 v29, 3641, v6\n"
"v_lshrrev_b32 v29, 16, v29\n"
"v_mul_u32_u24 v7, 18, v29\n"
"v_sub_u32 v6, v6, v7\n"
"v_mul_u32_u24 v29, 1152, v29\n"
"v_lshl_add_u32 v29, v6, 4, v29\n"
"v_subrev_u32 v29, 1024, v29\n"
"v_add_u32 v6, 128, v0\n"
"v_mul_u32_u24 v30, 3641, v6\n"
"v_lshrrev_b32 v30, 16, v30\n"
"v_mul_u32_u24 v7, 18, v30\n"
"v_sub_u32 v6, v6, v7\n"
"v_mul_u32_u24 v30, 1152, v30\n"
"v_lshl_add_u32 v30, v6, 4, v30\n"
"v_subrev_u32 v30, 2048, v30\n"
"v_add_u32 v6, 192, v0\n"
"v_mul_u32_u24 v31, 3641, v6\n"
"v_lshrrev_b32 v31, 16, v31\n"
"v_mul_u32_u24 v7, 18, v31\n"
"v_sub_u32 v6, v6, v7\n"
"v_mul_u32_u24 v31, 1152, v31\n"
"v_lshl_add_u32 v31, v6, 4, v31\n"
"v_subrev_u32 v31, 3072, v31\n"
"v_add_u32 v6, 256, v0\n"
"v_mul_u32_u24 v240, 3641, v6\n"
"v_lshrrev_b32 v240, 16, v240\n"
"v_mul_u32_u24 v7, 18, v240\n"
"v_sub_u32 v6, v6, v7\n"
"v_mul_u32_u24 v240, 1152, v240\n"
"v_lshl_add_u32 v240, v6, 4, v240\n"
"v_lshl_add_u32 v6, v2, 2, v4\n"
"v_mul_u32_u24 v6, 288, v6\n"
"v_lshl_add_u32 v6, v5, 3, v6\n"
"s_lshl_b32 s0, s54, 12\n"
"s_add_u32 s0, s0, 98304\n"
"v_add_u32 v241, s0, v6\n"
"v_add_u32 v242, 32, v241\n"
"s_mul_i32 s1, s11, 4608\n"
"s_add_u32 s0, s0, s1\n"
"s_mov_b32 s96, -1\n"
"s_mov_b32 s97, 0\n"
"s_waitcnt vmcnt(16)\n"
"v_lshlrev_b32 v32, 16, v120\n"
"v_mov_b32 v36, v32\n"
"v_mov_b32 v40, v32\n"
"v_and_b32 v33, 0xffff0000, v120\n"
"v_mov_b32 v37, v33\n"
"v_mov_b32 v41, v33\n"
"v_lshlrev_b32 v34, 16, v121\n"
"v_mov_b32 v38, v34\n"
"v_mov_b32 v42, v34\n"
"v_and_b32 v35, 0xffff0000, v121\n"
"v_mov_b32 v39, v35\n"
"v_mov_b32 v43, v35\n"
"v_lshlrev_b32 v44, 16, v122\n"
"v_mov_b32 v48, v44\n"
"v_mov_b32 v52, v44\n"
"v_and_b32 v45, 0xffff0000, v122\n"
"v_mov_b32 v49, v45\n"
"v_mov_b32 v53, v45\n"
"v_lshlrev_b32 v46, 16, v123\n"
"v_mov_b32 v50, v46\n"
"v_mov_b32 v54, v46\n"
"v_and_b32 v47, 0xffff0000, v123\n"
"v_mov_b32 v51, v47\n"
"v_mov_b32 v55, v47\n"
"v_lshlrev_b32 v56, 16, v124\n"
"v_mov_b32 v60, v56\n"
"v_mov_b32 v64, v56\n"
"v_and_b32 v57, 0xffff0000, v124\n"
"v_mov_b32 v61, v57\n"
"v_mov_b32 v65, v57\n"
"v_lshlrev_b32 v58, 16, v125\n"
"v_mov_b32 v62, v58\n"
"v_mov_b32 v66, v58\n"
"v_and_b32 v59, 0xffff0000, v125\n"
"v_mov_b32 v63, v59\n"
"v_mov_b32 v67, v59\n"
"v_lshlrev_b32 v68, 16, v126\n"
"v_mov_b32 v72, v68\n"
"v_mov_b32 v76, v68\n"
"v_and_b32 v69, 0xffff0000, v126\n"
"v_mov_b32 v73, v69\n"
"v_mov_b32 v77, v69\n"
"v_lshlrev_b32 v70, 16, v127\n"
"v_mov_b32 v74, v70\n"
"v_mov_b32 v78, v70\n"
"v_and_b32 v71, 0xffff0000, v127\n"
"v_mov_b32 v75, v71\n"
"v_mov_b32 v79, v71\n"
"v_lshlrev_b32 v80, 16, v128\n"
"v_mov_b32 v84, v80\n"
"v_mov_b32 v88, v80\n"
"v_and_b32 v81, 0xffff0000, v128\n"
"v_mov_b32 v85, v81\n"
"v_mov_b32 v89, v81\n"
"v_lshlrev_b32 v82, 16, v129\n"
"v_mov_b32 v86, v82\n"
"v_mov_b32 v90, v82\n"
"v_and_b32 v83, 0xffff0000, v129\n"
"v_mov_b32 v87, v83\n"
"v_mov_b32 v91, v83\n"
"v_lshlrev_b32 v92, 16, v130\n"
"v_mov_b32 v96, v92\n"
"v_mov_b32 v100, v92\n"
"v_and_b32 v93, 0xffff0000, v130\n"
"v_mov_b32 v97, v93\n"
"v_mov_b32 v101, v93\n"
"v_lshlrev_b32 v94, 16, v131\n"
"v_mov_b32 v98, v94\n"
"v_mov_b32 v102, v94\n"
"v_and_b32 v95, 0xffff0000, v131\n"
"v_mov_b32 v99, v95\n"
"v_mov_b32 v103, v95\n"
"v_lshlrev_b32 v104, 16, v132\n"
"v_mov_b32 v108, v104\n"
"v_mov_b32 v112, v104\n"
"v_and_b32 v105, 0xffff0000, v132\n"
"v_mov_b32 v109, v105\n"
"v_mov_b32 v113, v105\n"
"v_lshlrev_b32 v106, 16, v133\n"
"v_mov_b32 v110, v106\n"
"v_mov_b32 v114, v106\n"
"v_and_b32 v107, 0xffff0000, v133\n"
"v_mov_b32 v111, v107\n"
"v_mov_b32 v115, v107\n"
"s_mov_b32 s94, 0\n"
"s_mov_b32 s4, 0x80000000\n"
"s_branch L_ln3\n"
"L_preB:\n"
"s_sub_u32 s60, s53, 16\n"
"s_lshl2_add_u32 s60, s60, s11\n"
"v_mul_u32_u24 v6, 5462, v0\n"
"v_lshrrev_b32 v6, 16, v6\n"
"v_mul_u32_u24 v7, 12, v6\n"
"v_sub_u32 v7, v0, v7\n"
"s_mul_i32 s61, s52, 1120\n"
"s_andn2_b32 s61, s61, 63\n"
"s_lshr_b32 s61, s61, 7\n"
"v_add_u32 v243, s61, v7\n"
"s_movk_i32 s61, 70\n"
"s_movk_i32 s62, 71\n"
"s_cmp_lt_u32 s52, 3\n"
"s_cselect_b32 s61, s61, 0x800000\n"
"s_cmp_lt_u32 s52, 6\n"
"s_cselect_b32 s62, s62, 0x800000\n"
"v_mov_b32 v242, s61\n"
"v_cmp_eq_u32 vcc, 10, v7\n"
"s_nop 1\n"
"v_cndmask_b32 v243, v243, v242, vcc\n"
"v_mov_b32 v242, s62\n"
"v_cmp_eq_u32 vcc, 11, v7\n"
"s_nop 1\n"
"v_cndmask_b32 v243, v243, v242, vcc\n"
"v_mov_b32 v242, 0x800000\n"
"v_cmp_eq_u32 vcc, 5, v6\n"
"s_nop 1\n"
"v_cndmask_b32 v243, v243, v242, vcc\n"
"v_mul_u32_u24 v6, 9216, v6\n"
"v_lshl_add_u32 v243, v243, 7, v6\n"
"s_mov_b32 s92, s22\n"
"s_and_b32 s93, s23, 0xffff\n"
"s_mov_b32 s94, 0x1200000\n"
"s_mov_b32 s95, 0x20000\n"
"s_add_u32 s61, s60, 51\n"
"s_mul_i32 s87, s61, 46080\n"
"s_mov_b32 s7, 2949120\n"
"buffer_load_dword v253, v243, s[92:95], s87 offen\n"
"s_add_u32 s87, s87, s7\n"
"s_lshl_b32 s4, s60, 13\n"
"s_add_u32 s4, s4, 0x280000\n"
"L_tse0:\n"
"s_cmp_lt_u32 s60, 20\n"
"s_cbranch_scc0 L_tse1\n"
"s_add_u32 s63, s60, 16\n"
"s_mov_b32 s92, s18\n"
"s_and_b32 s93, s19, 0xffff\n"
"s_mov_b32 s94, 0x480000\n"
"s_mov_b32 s95, 0x20000\n"
"s_lshl_b32 s80, s63, 17\n"
"s_lshl_b32 s81, s52, 10\n"
"s_add_u32 s80, s80, s81\n"
"s_add_u32 s80, s80, 0\n"
"v_lshrrev_b32 v6, 2, v0\n"
"v_lshlrev_b32 v6, 13, v6\n"
"v_lshl_add_u32 v6, v5, 7, v6\n"
"s_branch L_tsee\n"
"L_tse1:\n"
"s_cmp_lt_u32 s60, 52\n"
"s_cbranch_scc0 L_tse2\n"
"s_sub_u32 s63, s60, 20\n"
"s_bitcmp1_b32 s63, 4\n"
"s_cselect_b32 s92, s16, s14\n"
"s_cselect_b32 s93, s17, s15\n"
"s_and_b32 s93, s93, 0xffff\n"
"s_mov_b32 s94, 0x800000\n"
"s_mov_b32 s95, 0x20000\n"
"s_and_b32 s80, s63, 15\n"
"s_lshl_b32 s80, s80, 13\n"
"s_lshl_b32 s81, s52, 20\n"
"s_add_u32 s80, s80, s81\n"
"s_add_u32 s80, s80, 0x60000\n"
"v_mov_b32 v6, v254\n"
"s_branch L_tsee\n"
"L_tse2:\n"
"s_sub_u32 s63, s60, 20\n"
"s_mov_b32 s92, s18\n"
"s_and_b32 s93, s19, 0xffff\n"
"s_mov_b32 s94, 0x480000\n"
"s_mov_b32 s95, 0x20000\n"
"s_lshl_b32 s80, s63, 17\n"
"s_lshl_b32 s81, s52, 10\n"
"s_add_u32 s80, s80, s81\n"
"s_add_u32 s80, s80, 512\n"
"v_lshrrev_b32 v6, 2, v0\n"
"v_lshlrev_b32 v6, 13, v6\n"
"v_lshl_add_u32 v6, v5, 7, v6\n"
"L_tsee:\n"
"buffer_load_dword v253, v6, s[92:95], s80 offen\n"
"s_mov_b32 s92, s22\n"
"s_and_b32 s93, s23, 0xffff\n"
"s_mov_b32 s94, 0x1200000\n"
"s_mov_b32 s95, 0x20000\n"
"s_lshr_b32 s56, s52, 1\n"
"s_lshl_b32 s56, s56, 3\n"
"s_bfe_u32 s57, s53, 0x10003\n"
"s_lshl2_add_u32 s56, s57, s56\n"
"s_bfe_u32 s57, s53, 0x10002\n"
"s_add_u32 s56, s56, s57\n"
"s_xor_b32 s57, s54, 1\n"
"s_lshl1_add_u32 s56, s57, s56\n"
"s_and_b32 s57, s52, 1\n"
"s_and_b32 s58, s53, 3\n"
"s_lshl2_add_u32 s57, s57, s58\n"
"s_lshl2_add_u32 s58, s57, s11\n"
"s_lshl_b32 s73, s11, 17\n"
"s_lshl_b32 s59, s56, 8\n"
"s_add_u32 s73, s73, s59\n"
"s_lshl_b32 s75, s11, 12\n"
"s_sub_u32 s40, s18, 0x1000\n"
"s_subb_u32 s41, s19, 0\n"
"s_and_b32 s41, s41, 0xffff\n"
"s_mov_b32 s42, 0x481000\n"
"s_mov_b32 s43, 0x20000\n"
"v_bfe_u32 v212, v0, 2, 3\n"
"v_mul_u32_u24 v212, 8192, v212\n"
"v_lshrrev_b32 v213, 5, v0\n"
"v_lshl_add_u32 v212, v213, 6, v212\n"
"v_mov_b32 v213, v5\n"
"v_lshl_add_u32 v8, v213, 4, v212\n"
"v_add_u32 v8, 4096, v8\n"
"v_lshl_add_u32 v9, v213, 4, v212\n"
"v_add_u32 v9, 3200, v9\n"
"v_lshl_add_u32 v10, v213, 4, v212\n"
"v_add_u32 v10, 67584, v10\n"
"v_lshl_add_u32 v11, v213, 4, v212\n"
"v_add_u32 v11, 66688, v11\n"
"s_add_u32 m0, s75, 98304\n"
"s_nop 0\n"
"buffer_load_dwordx4 v8, s[40:43], s73 offen offset:0 lds\n"
"buffer_load_dwordx4 v9, s[40:43], s73 offen offset:1024 lds\n"
"buffer_load_dwordx4 v10, s[40:43], s73 offen offset:2048 lds\n"
"buffer_load_dwordx4 v11, s[40:43], s73 offen offset:3072 lds\n"
"s_add_u32 s73, s73, 524288\n"
"s_add_u32 m0, s75, 114688\n"
"s_nop 0\n"
"buffer_load_dwordx4 v8, s[40:43], s73 offen offset:0 lds\n"
"buffer_load_dwordx4 v9, s[40:43], s73 offen offset:1024 lds\n"
"buffer_load_dwordx4 v10, s[40:43], s73 offen offset:2048 lds\n"
"buffer_load_dwordx4 v11, s[40:43], s73 offen offset:3072 lds\n"
"s_add_u32 s73, s73, 524288\n"
"s_add_u32 m0, s75, 131072\n"
"s_nop 0\n"
"buffer_load_dwordx4 v8, s[40:43], s73 offen offset:0 lds\n"
"buffer_load_dwordx4 v9, s[40:43], s73 offen offset:1024 lds\n"
"buffer_load_dwordx4 v10, s[40:43], s73 offen offset:2048 lds\n"
"buffer_load_dwordx4 v11, s[40:43], s73 offen offset:3072 lds\n"
"s_add_u32 s73, s73, 524288\n"
"s_add_u32 m0, s75, 147456\n"
"s_nop 0\n"
"buffer_load_dwordx4 v8, s[40:43], s73 offen offset:0 lds\n"
"buffer_load_dwordx4 v9, s[40:43], s73 offen offset:1024 lds\n"
"buffer_load_dwordx4 v10, s[40:43], s73 offen offset:2048 lds\n"
"buffer_load_dwordx4 v11, s[40:43], s73 offen offset:3072 lds\n"
"s_add_u32 s73, s73, 524288\n"
"s_mul_i32 s72, s58, 73728\n"
"v_lshrrev_b32 v212, 2, v3\n"
"v_lshlrev_b32 v212, 8, v212\n"
"v_lshl_add_u32 v212, v2, 6, v212\n"
"v_lshl_add_u32 v27, v5, 4, v212\n"
"s_lshl_b32 s59, s58, 8\n"
"s_lshl_b32 s60, s56, 2\n"
"s_add_u32 s59, s59, s60\n"
"s_lshl_b32 s59, s59, 11\n"
"s_add_u32 s82, s59, 0x9d8000\n"
"s_lshl_b32 s59, s58, 7\n"
"s_add_u32 s59, s59, s60\n"
"s_add_u32 s79, s59, 0x2ae0000\n"
"s_lshr_b32 s59, s56, 1\n"
"s_lshl_b32 s59, s59, 19\n"
"s_add_u32 s6, s59, 393216\n"
"s_sub_u32 s59, s6, 4096\n"
"s_lshl_b32 s74, s11, 12\n"
"s_mov_b32 s83, s74\n"
"s_add_u32 s44, s14, s59\n"
"s_addc_u32 s45, s15, 0\n"
"s_and_b32 s45, s45, 0xffff\n"
"s_mov_b32 s46, 0x21000\n"
"s_mov_b32 s47, 0x20000\n"
"s_add_u32 s48, s16, s59\n"
"s_addc_u32 s49, s17, 0\n"
"s_and_b32 s49, s49, 0xffff\n"
"s_mov_b32 s50, 0x21000\n"
"s_mov_b32 s51, 0x20000\n"
"v_add_u32 v6, 0, v0\n"
"v_mul_u32_u24 v28, 3641, v6\n"
"v_lshrrev_b32 v28, 16, v28\n"
"v_mul_u32_u24 v7, 18, v28\n"
"v_sub_u32 v6, v6, v7\n"
"v_mul_u32_u24 v28, 1152, v28\n"
"v_lshl_add_u32 v28, v6, 4, v28\n"
"v_add_u32 v6, 64, v0\n"
"v_mul_u32_u24 v29, 3641, v6\n"
"v_lshrrev_b32 v29, 16, v29\n"
"v_mul_u32_u24 v7, 18, v29\n"
"v_sub_u32 v6, v6, v7\n"
"v_mul_u32_u24 v29, 1152, v29\n"
"v_lshl_add_u32 v29, v6, 4, v29\n"
"v_subrev_u32 v29, 1024, v29\n"
"v_add_u32 v6, 128, v0\n"
"v_mul_u32_u24 v30, 3641, v6\n"
"v_lshrrev_b32 v30, 16, v30\n"
"v_mul_u32_u24 v7, 18, v30\n"
"v_sub_u32 v6, v6, v7\n"
"v_mul_u32_u24 v30, 1152, v30\n"
"v_lshl_add_u32 v30, v6, 4, v30\n"
"v_subrev_u32 v30, 2048, v30\n"
"v_add_u32 v6, 192, v0\n"
"v_mul_u32_u24 v31, 3641, v6\n"
"v_lshrrev_b32 v31, 16, v31\n"
"v_mul_u32_u24 v7, 18, v31\n"
"v_sub_u32 v6, v6, v7\n"
"v_mul_u32_u24 v31, 1152, v31\n"
"v_lshl_add_u32 v31, v6, 4, v31\n"
"v_subrev_u32 v31, 3072, v31\n"
"v_add_u32 v6, 256, v0\n"
"v_mul_u32_u24 v240, 3641, v6\n"
"v_lshrrev_b32 v240, 16, v240\n"
"v_mul_u32_u24 v7, 18, v240\n"
"v_sub_u32 v6, v6, v7\n"
"v_mul_u32_u24 v240, 1152, v240\n"
"v_lshl_add_u32 v240, v6, 4, v240\n"
"v_lshl_add_u32 v6, v2, 2, v4\n"
"v_mul_u32_u24 v6, 288, v6\n"
"v_lshl_add_u32 v6, v5, 3, v6\n"
"s_lshl_b32 s0, s54, 12\n"
"s_add_u32 s0, s0, 98304\n"
"v_add_u32 v241, s0, v6\n"
"v_add_u32 v242, 32, v241\n"
"s_mul_i32 s1, s11, 4608\n"
"s_add_u32 s0, s0, s1\n"
"s_mov_b32 s96, -1\n"
"s_mov_b32 s97, 0\n"
"v_bfe_u32 v212, v0, 2, 3\n"
"v_mul_u32_u24 v212, 256, v212\n"
"v_lshrrev_b32 v213, 5, v0\n"
"v_lshl_add_u32 v212, v213, 6, v212\n"
"v_mov_b32 v213, v5\n"
"v_lshl_add_u32 v12, v213, 4, v212\n"
"v_add_u32 v12, 4096, v12\n"
"v_lshl_add_u32 v13, v213, 4, v212\n"
"v_add_u32 v13, 3200, v13\n"
"v_xor_b32 v213, 2, v213\n"
"v_lshl_add_u32 v14, v213, 4, v212\n"
"v_add_u32 v14, 4096, v14\n"
"v_lshl_add_u32 v15, v213, 4, v212\n"
"v_add_u32 v15, 3200, v15\n"
"v_bfe_u32 v212, v0, 2, 3\n"
"v_mul_u32_u24 v212, 256, v212\n"
"v_lshrrev_b32 v213, 5, v0\n"
"v_lshl_add_u32 v212, v213, 6, v212\n"
"v_bfe_u32 v213, v0, 4, 1\n"
"v_lshlrev_b32 v213, 1, v213\n"
"v_xor_b32 v213, v213, v5\n"
"v_lshl_add_u32 v16, v213, 4, v212\n"
"v_add_u32 v16, 4096, v16\n"
"v_lshl_add_u32 v17, v213, 4, v212\n"
"v_add_u32 v17, 3200, v17\n"
"v_lshl_add_u32 v18, v213, 4, v212\n"
"v_add_u32 v18, 4096, v18\n"
"v_lshl_add_u32 v19, v213, 4, v212\n"
"v_add_u32 v19, 3200, v19\n"
"v_lshlrev_b32 v20, 11, v2\n"
"v_lshl_add_u32 v20, v4, 6, v20\n"
"v_lshl_add_u32 v20, v5, 4, v20\n"
"v_add_u32 v20, 98304, v20\n"
"v_lshrrev_b32 v212, 3, v3\n"
"v_lshlrev_b32 v213, 1, v212\n"
"v_xor_b32 v213, v213, v2\n"
"v_lshlrev_b32 v212, 11, v212\n"
"v_lshl_add_u32 v212, v213, 4, v212\n"
"v_and_b32 v213, 7, v3\n"
"v_lshl_add_u32 v24, v213, 6, v212\n"
"v_lshrrev_b32 v212, 1, v2\n"
"v_lshlrev_b32 v212, 11, v212\n"
"v_and_b32 v213, 1, v2\n"
"v_lshl_add_u32 v214, v213, 2, v4\n"
"v_lshl_add_u32 v212, v214, 6, v212\n"
"v_lshl_add_u32 v212, v5, 3, v212\n"
"v_lshl_add_u32 v212, v213, 5, v212\n"
"v_add_u32 v25, 16384, v212\n"
"v_xor_b32 v26, 32, v25\n"
"v_mov_b32 v21, v24\n"
"v_add_u32 v22, 32768, v25\n"
"v_add_u32 v23, 32768, v26\n"
"L_ln3:\n"
"s_cmp_lg_u32 s11, 0\n"
"s_cbranch_scc1 L_ln4\n"
"buffer_load_dwordx4 v[244:247], v1, s[36:39], s91 offen sc1\n"
"s_waitcnt vmcnt(0)\n"
"v_cmp_eq_u32 s[88:89], s34, v244\n"
"v_cmp_eq_u32 vcc, s34, v245\n"
"s_nop 0\n"
"s_and_b64 s[88:89], s[88:89], vcc\n"
"v_cmp_eq_u32 vcc, s34, v246\n"
"s_nop 0\n"
"s_and_b64 s[88:89], s[88:89], vcc\n"
"v_cmp_eq_u32 vcc, s34, v247\n"
"s_nop 0\n"
"s_and_b64 s[88:89], s[88:89], vcc\n"
"s_andn2_b64 s[88:89], s[98:99], s[88:89]\n"
"s_cbranch_scc0 L_pln_ok\n"
"s_mov_b32 s55, 0x30000\n"
"L_pln_re:\n"
"s_sleep 1\n"
"buffer_load_dwordx4 v[244:247], v1, s[36:39], s91 offen sc1\n"
"s_waitcnt vmcnt(0)\n"
"v_cmp_eq_u32 s[88:89], s34, v244\n"
"v_cmp_eq_u32 vcc, s34, v245\n"
"s_nop 0\n"
"s_and_b64 s[88:89], s[88:89], vcc\n"
"v_cmp_eq_u32 vcc, s34, v246\n"
"s_nop 0\n"
"s_and_b64 s[88:89], s[88:89], vcc\n"
"v_cmp_eq_u32 vcc, s34, v247\n"
"s_nop 0\n"
"s_and_b64 s[88:89], s[88:89], vcc\n"
"s_andn2_b64 s[88:89], s[98:99], s[88:89]\n"
"s_cbranch_scc0 L_pln_ok\n"
"s_sub_u32 s55, s55, 1\n"
"s_cmp_lg_u32 s55, 0\n"
"s_cbranch_scc1 L_pln_re\n"
"L_pln_ok:\n"
"s_mov_b32 s56, 0x2ad8000\n"
"s_cmp_eq_u32 s10, 0\n"
"s_cselect_b32 s56, s56, 0x80000000\n"
"s_mov_b64 exec, 1\n"
"buffer_store_dword v252, v250, s[36:39], s56 offen sc1\n"
"s_mov_b64 exec, -1\n"
"L_ln4:\n"
"s_waitcnt vmcnt(12)\n"
"s_barrier\n"
"s_cmp_eq_u32 s54, 1\n"
"s_cbranch_scc1 L_bpro\n"
"buffer_load_dwordx4 a[160:163], v1, s[44:47], s72 offen offset:0 sc1\n"
"buffer_load_dwordx4 a[164:167], v1, s[44:47], s72 offen offset:1024 sc1\n"
"buffer_load_dwordx4 a[168:171], v1, s[44:47], s72 offen offset:2048 sc1\n"
"s_add_u32 s72, s72, 36864\n"
"buffer_load_dwordx4 a[172:175], v1, s[44:47], s72 offen offset:0 sc1\n"
"buffer_load_dwordx4 a[176:179], v1, s[44:47], s72 offen offset:1024 sc1\n"
"buffer_load_dwordx4 a[180:183], v1, s[44:47], s72 offen offset:2048 sc1\n"
"s_add_u32 s72, s72, 36864\n"
"buffer_load_dwordx4 a[184:187], v1, s[44:47], s72 offen offset:0 sc1\n"
"buffer_load_dwordx4 a[188:191], v1, s[44:47], s72 offen offset:1024 sc1\n"
"buffer_load_dwordx4 a[192:195], v1, s[44:47], s72 offen offset:2048 sc1\n"
"s_add_u32 s72, s72, 36864\n"
"buffer_load_dwordx4 a[196:199], v1, s[44:47], s72 offen offset:0 sc1\n"
"buffer_load_dwordx4 a[200:203], v1, s[44:47], s72 offen offset:1024 sc1\n"
"buffer_load_dwordx4 a[204:207], v1, s[44:47], s72 offen offset:2048 sc1\n"
"s_add_u32 s72, s72, 36864\n"
"buffer_load_dwordx4 a[208:211], v1, s[44:47], s72 offen offset:0 sc1\n"
"ds_read_b64_tr_b16 a[104:105], v12 offset:0\n"
"ds_read_b64_tr_b16 a[106:107], v12 offset:3584\n"
"ds_read_b64_tr_b16 a[108:109], v12 offset:32\n"
"ds_read_b64_tr_b16 a[110:111], v12 offset:3616\n"
"ds_read_b64_tr_b16 a[112:113], v12 offset:64\n"
"ds_read_b64_tr_b16 a[114:115], v12 offset:3648\n"
"ds_read_b64_tr_b16 a[116:117], v12 offset:96\n"
"ds_read_b64_tr_b16 a[118:119], v12 offset:3680\n"
"ds_read_b64_tr_b16 a[120:121], v12 offset:128\n"
"ds_read_b64_tr_b16 a[122:123], v12 offset:3712\n"
"ds_read_b64_tr_b16 a[124:125], v12 offset:160\n"
"ds_read_b64_tr_b16 a[126:127], v12 offset:3744\n"
"ds_read_b64_tr_b16 a[128:129], v14 offset:0\n"
"ds_read_b64_tr_b16 a[130:131], v14 offset:3584\n"
"s_mov_b32 s76, 7\n"
"L_g4:\n"
"s_waitcnt vmcnt(10) lgkmcnt(0)\n"
"v_mfma_f32_16x16x32_bf16 v[32:35], a[104:107], a[160:163], v[32:35]\n"
"ds_read_b64_tr_b16 a[132:133], v12 offset:7168\n"
"v_mfma_f32_16x16x32_bf16 v[36:39], a[104:107], a[164:167], v[36:39]\n"
"buffer_load_dwordx4 a[212:215], v1, s[44:47], s72 offen offset:1024 sc1\n"
"v_mfma_f32_16x16x32_bf16 v[40:43], a[104:107], a[168:171], v[40:43]\n"
"ds_read_b64_tr_b16 a[134:135], v12 offset:10752\n"
"ds_read_b64_tr_b16 a[136:137], v12 offset:7200\n"
"v_mfma_f32_16x16x32_bf16 v[52:55], a[108:111], a[168:171], v[52:55]\n"
"ds_read_b64_tr_b16 a[138:139], v12 offset:10784\n"
"v_mfma_f32_16x16x32_bf16 v[48:51], a[108:111], a[164:167], v[48:51]\n"
"ds_read_b64_tr_b16 a[140:141], v12 offset:7232\n"
"v_mfma_f32_16x16x32_bf16 v[44:47], a[108:111], a[160:163], v[44:47]\n"
"buffer_load_dwordx4 a[216:219], v1, s[44:47], s72 offen offset:2048 sc1\n"
"v_mfma_f32_16x16x32_bf16 v[56:59], a[112:115], a[160:163], v[56:59]\n"
"s_add_u32 s72, s72, 36864\n"
"ds_read_b64_tr_b16 a[142:143], v12 offset:10816\n"
"v_mfma_f32_16x16x32_bf16 v[60:63], a[112:115], a[164:167], v[60:63]\n"
"ds_read_b64_tr_b16 a[144:145], v12 offset:7264\n"
"v_mfma_f32_16x16x32_bf16 v[64:67], a[112:115], a[168:171], v[64:67]\n"
"ds_read_b64_tr_b16 a[146:147], v12 offset:10848\n"
"v_mfma_f32_16x16x32_bf16 v[76:79], a[116:119], a[168:171], v[76:79]\n"
"buffer_load_dwordx4 a[220:223], v1, s[44:47], s72 offen offset:0 sc1\n"
"v_mfma_f32_16x16x32_bf16 v[72:75], a[116:119], a[164:167], v[72:75]\n"
"ds_read_b64_tr_b16 a[148:149], v12 offset:7296\n"
"ds_read_b64_tr_b16 a[150:151], v12 offset:10880\n"
"v_mfma_f32_16x16x32_bf16 v[68:71], a[116:119], a[160:163], v[68:71]\n"
"v_mfma_f32_16x16x32_bf16 v[80:83], a[120:123], a[160:163], v[80:83]\n"
"ds_read_b64_tr_b16 a[152:153], v12 offset:7328\n"
"v_mfma_f32_16x16x32_bf16 v[84:87], a[120:123], a[164:167], v[84:87]\n"
"buffer_load_dwordx4 a[224:227], v1, s[44:47], s72 offen offset:1024 sc1\n"
"v_mfma_f32_16x16x32_bf16 v[88:91], a[120:123], a[168:171], v[88:91]\n"
"ds_read_b64_tr_b16 a[154:155], v12 offset:10912\n"
"ds_read_b64_tr_b16 a[156:157], v14 offset:7168\n"
"v_mfma_f32_16x16x32_bf16 v[100:103], a[124:127], a[168:171], v[100:103]\n"
"ds_read_b64_tr_b16 a[158:159], v14 offset:10752\n"
"v_mfma_f32_16x16x32_bf16 v[96:99], a[124:127], a[164:167], v[96:99]\n"
"v_mfma_f32_16x16x32_bf16 v[92:95], a[124:127], a[160:163], v[92:95]\n"
"buffer_load_dwordx4 a[228:231], v1, s[44:47], s72 offen offset:2048 sc1\n"
"v_mfma_f32_16x16x32_bf16 v[104:107], a[128:131], a[160:163], v[104:107]\n"
"s_add_u32 s72, s72, 36864\n"
"v_mfma_f32_16x16x32_bf16 v[108:111], a[128:131], a[164:167], v[108:111]\n"
"v_mfma_f32_16x16x32_bf16 v[112:115], a[128:131], a[168:171], v[112:115]\n"
"s_waitcnt vmcnt(12) lgkmcnt(0)\n"
"v_mfma_f32_16x16x32_bf16 v[32:35], a[132:135], a[172:175], v[32:35]\n"
"s_nop 0\n"
"s_barrier\n"
"v_mfma_f32_16x16x32_bf16 v[36:39], a[132:135], a[176:179], v[36:39]\n"
"s_add_u32 m0, s75, 0\n"
"ds_read_b64_tr_b16 a[104:105], v12 offset:14336\n"
"v_mfma_f32_16x16x32_bf16 v[40:43], a[132:135], a[180:183], v[40:43]\n"
"buffer_load_dwordx4 v8, s[40:43], s73 offen offset:0 lds\n"
"v_mfma_f32_16x16x32_bf16 v[52:55], a[136:139], a[180:183], v[52:55]\n"
"ds_read_b64_tr_b16 a[106:107], v12 offset:17920\n"
"ds_read_b64_tr_b16 a[108:109], v12 offset:14368\n"
"v_mfma_f32_16x16x32_bf16 v[48:51], a[136:139], a[176:179], v[48:51]\n"
"ds_read_b64_tr_b16 a[110:111], v12 offset:17952\n"
"v_mfma_f32_16x16x32_bf16 v[44:47], a[136:139], a[172:175], v[44:47]\n"
"ds_read_b64_tr_b16 a[112:113], v12 offset:14400\n"
"v_mfma_f32_16x16x32_bf16 v[56:59], a[140:143], a[172:175], v[56:59]\n"
"buffer_load_dwordx4 v9, s[40:43], s73 offen offset:1024 lds\n"
"v_mfma_f32_16x16x32_bf16 v[60:63], a[140:143], a[176:179], v[60:63]\n"
"ds_read_b64_tr_b16 a[114:115], v12 offset:17984\n"
"ds_read_b64_tr_b16 a[116:117], v12 offset:14432\n"
"v_mfma_f32_16x16x32_bf16 v[64:67], a[140:143], a[180:183], v[64:67]\n"
"v_mfma_f32_16x16x32_bf16 v[76:79], a[144:147], a[180:183], v[76:79]\n"
"ds_read_b64_tr_b16 a[118:119], v12 offset:18016\n"
"v_mfma_f32_16x16x32_bf16 v[72:75], a[144:147], a[176:179], v[72:75]\n"
"buffer_load_dwordx4 v10, s[40:43], s73 offen offset:2048 lds\n"
"v_mfma_f32_16x16x32_bf16 v[68:71], a[144:147], a[172:175], v[68:71]\n"
"ds_read_b64_tr_b16 a[120:121], v12 offset:14464\n"
"ds_read_b64_tr_b16 a[122:123], v12 offset:18048\n"
"v_mfma_f32_16x16x32_bf16 v[80:83], a[148:151], a[172:175], v[80:83]\n"
"ds_read_b64_tr_b16 a[124:125], v12 offset:14496\n"
"v_mfma_f32_16x16x32_bf16 v[84:87], a[148:151], a[176:179], v[84:87]\n"
"ds_read_b64_tr_b16 a[126:127], v12 offset:18080\n"
"v_mfma_f32_16x16x32_bf16 v[88:91], a[148:151], a[180:183], v[88:91]\n"
"s_mov_b64 exec, s[96:97]\n"
"buffer_load_dwordx4 v11, s[40:43], s73 offen offset:3072 lds\n"
"s_mov_b64 exec, -1\n"
"v_mfma_f32_16x16x32_bf16 v[100:103], a[152:155], a[180:183], v[100:103]\n"
"s_add_u32 s73, s73, 589824\n"
"ds_read_b64_tr_b16 a[128:129], v14 offset:14336\n"
"v_mfma_f32_16x16x32_bf16 v[96:99], a[152:155], a[176:179], v[96:99]\n"
"ds_read_b64_tr_b16 a[130:131], v14 offset:17920\n"
"v_mfma_f32_16x16x32_bf16 v[92:95], a[152:155], a[172:175], v[92:95]\n"
"v_mfma_f32_16x16x32_bf16 v[104:107], a[156:159], a[172:175], v[104:107]\n"
"buffer_load_dwordx4 a[232:235], v1, s[44:47], s72 offen offset:0 sc1\n"
"v_mfma_f32_16x16x32_bf16 v[108:111], a[156:159], a[176:179], v[108:111]\n"
"v_mfma_f32_16x16x32_bf16 v[112:115], a[156:159], a[180:183], v[112:115]\n"
"s_waitcnt vmcnt(14) lgkmcnt(0)\n"
"v_mfma_f32_16x16x32_bf16 v[32:35], a[104:107], a[184:187], v[32:35]\n"
"ds_read_b64_tr_b16 a[132:133], v12 offset:21504\n"
"v_mfma_f32_16x16x32_bf16 v[36:39], a[104:107], a[188:191], v[36:39]\n"
"buffer_load_dwordx4 a[236:239], v1, s[44:47], s72 offen offset:1024 sc1\n"
"v_mfma_f32_16x16x32_bf16 v[40:43], a[104:107], a[192:195], v[40:43]\n"
"ds_read_b64_tr_b16 a[134:135], v12 offset:25088\n"
"ds_read_b64_tr_b16 a[136:137], v12 offset:21536\n"
"v_mfma_f32_16x16x32_bf16 v[52:55], a[108:111], a[192:195], v[52:55]\n"
"ds_read_b64_tr_b16 a[138:139], v12 offset:25120\n"
"v_mfma_f32_16x16x32_bf16 v[48:51], a[108:111], a[188:191], v[48:51]\n"
"ds_read_b64_tr_b16 a[140:141], v12 offset:21568\n"
"v_mfma_f32_16x16x32_bf16 v[44:47], a[108:111], a[184:187], v[44:47]\n"
"buffer_load_dwordx4 a[240:243], v1, s[44:47], s72 offen offset:2048 sc1\n"
"v_mfma_f32_16x16x32_bf16 v[56:59], a[112:115], a[184:187], v[56:59]\n"
"s_add_u32 s72, s72, 36864\n"
"ds_read_b64_tr_b16 a[142:143], v12 offset:25152\n"
"v_mfma_f32_16x16x32_bf16 v[60:63], a[112:115], a[188:191], v[60:63]\n"
"ds_read_b64_tr_b16 a[144:145], v12 offset:21600\n"
"v_mfma_f32_16x16x32_bf16 v[64:67], a[112:115], a[192:195], v[64:67]\n"
"ds_read_b64_tr_b16 a[146:147], v12 offset:25184\n"
"v_mfma_f32_16x16x32_bf16 v[76:79], a[116:119], a[192:195], v[76:79]\n"
"buffer_load_dwordx4 a[244:247], v1, s[44:47], s72 offen offset:0 sc1\n"
"v_mfma_f32_16x16x32_bf16 v[72:75], a[116:119], a[188:191], v[72:75]\n"
"ds_read_b64_tr_b16 a[148:149], v12 offset:21632\n"
"ds_read_b64_tr_b16 a[150:151], v12 offset:25216\n"
"v_mfma_f32_16x16x32_bf16 v[68:71], a[116:119], a[184:187], v[68:71]\n"
"v_mfma_f32_16x16x32_bf16 v[80:83], a[120:123], a[184:187], v[80:83]\n"
"ds_read_b64_tr_b16 a[152:153], v12 offset:21664\n"
"v_mfma_f32_16x16x32_bf16 v[84:87], a[120:123], a[188:191], v[84:87]\n"
"buffer_load_dwordx4 a[248:251], v1, s[44:47], s72 offen offset:1024 sc1\n"
"v_mfma_f32_16x16x32_bf16 v[88:91], a[120:123], a[192:195], v[88:91]\n"
"ds_read_b64_tr_b16 a[154:155], v12 offset:25248\n"
"ds_read_b64_tr_b16 a[156:157], v14 offset:21504\n"
"v_mfma_f32_16x16x32_bf16 v[100:103], a[124:127], a[192:195], v[100:103]\n"
"ds_read_b64_tr_b16 a[158:159], v14 offset:25088\n"
"v_mfma_f32_16x16x32_bf16 v[96:99], a[124:127], a[188:191], v[96:99]\n"
"v_mfma_f32_16x16x32_bf16 v[92:95], a[124:127], a[184:187], v[92:95]\n"
"buffer_load_dwordx4 a[252:255], v1, s[44:47], s72 offen offset:2048 sc1\n"
"v_mfma_f32_16x16x32_bf16 v[104:107], a[128:131], a[184:187], v[104:107]\n"
"s_add_u32 s72, s72, 36864\n"
"v_mfma_f32_16x16x32_bf16 v[108:111], a[128:131], a[188:191], v[108:111]\n"
"v_mfma_f32_16x16x32_bf16 v[112:115], a[128:131], a[192:195], v[112:115]\n"
"s_waitcnt vmcnt(16) lgkmcnt(0)\n"
"v_mfma_f32_16x16x32_bf16 v[32:35], a[132:135], a[196:199], v[32:35]\n"
"s_nop 0\n"
"s_barrier\n"
"v_mfma_f32_16x16x32_bf16 v[36:39], a[132:135], a[200:203], v[36:39]\n"
"s_add_u32 m0, s75, 14336\n"
"ds_read_b64_tr_b16 a[104:105], v12 offset:28672\n"
"v_mfma_f32_16x16x32_bf16 v[40:43], a[132:135], a[204:207], v[40:43]\n"
"buffer_load_dwordx4 v8, s[40:43], s73 offen offset:0 lds\n"
"v_mfma_f32_16x16x32_bf16 v[52:55], a[136:139], a[204:207], v[52:55]\n"
"ds_read_b64_tr_b16 a[106:107], v12 offset:32256\n"
"ds_read_b64_tr_b16 a[108:109], v12 offset:28704\n"
"v_mfma_f32_16x16x32_bf16 v[48:51], a[136:139], a[200:203], v[48:51]\n"
"ds_read_b64_tr_b16 a[110:111], v12 offset:32288\n"
"v_mfma_f32_16x16x32_bf16 v[44:47], a[136:139], a[196:199], v[44:47]\n"
"ds_read_b64_tr_b16 a[112:113], v12 offset:28736\n"
"v_mfma_f32_16x16x32_bf16 v[56:59], a[140:143], a[196:199], v[56:59]\n"
"buffer_load_dwordx4 v9, s[40:43], s73 offen offset:1024 lds\n"
"v_mfma_f32_16x16x32_bf16 v[60:63], a[140:143], a[200:203], v[60:63]\n"
"ds_read_b64_tr_b16 a[114:115], v12 offset:32320\n"
"ds_read_b64_tr_b16 a[116:117], v12 offset:28768\n"
"v_mfma_f32_16x16x32_bf16 v[64:67], a[140:143], a[204:207], v[64:67]\n"
"v_mfma_f32_16x16x32_bf16 v[76:79], a[144:147], a[204:207], v[76:79]\n"
"ds_read_b64_tr_b16 a[118:119], v12 offset:32352\n"
"v_mfma_f32_16x16x32_bf16 v[72:75], a[144:147], a[200:203], v[72:75]\n"
"buffer_load_dwordx4 v10, s[40:43], s73 offen offset:2048 lds\n"
"v_mfma_f32_16x16x32_bf16 v[68:71], a[144:147], a[196:199], v[68:71]\n"
"ds_read_b64_tr_b16 a[120:121], v12 offset:28800\n"
"ds_read_b64_tr_b16 a[122:123], v12 offset:32384\n"
"v_mfma_f32_16x16x32_bf16 v[80:83], a[148:151], a[196:199], v[80:83]\n"
"ds_read_b64_tr_b16 a[124:125], v12 offset:28832\n"
"v_mfma_f32_16x16x32_bf16 v[84:87], a[148:151], a[200:203], v[84:87]\n"
"ds_read_b64_tr_b16 a[126:127], v12 offset:32416\n"
"v_mfma_f32_16x16x32_bf16 v[88:91], a[148:151], a[204:207], v[88:91]\n"
"s_mov_b64 exec, s[96:97]\n"
"buffer_load_dwordx4 v11, s[40:43], s73 offen offset:3072 lds\n"
"s_mov_b64 exec, -1\n"
"v_mfma_f32_16x16x32_bf16 v[100:103], a[152:155], a[204:207], v[100:103]\n"
"s_add_u32 s73, s73, 589824\n"
"ds_read_b64_tr_b16 a[128:129], v14 offset:28672\n"
"v_mfma_f32_16x16x32_bf16 v[96:99], a[152:155], a[200:203], v[96:99]\n"
"ds_read_b64_tr_b16 a[130:131], v14 offset:32256\n"
"v_mfma_f32_16x16x32_bf16 v[92:95], a[152:155], a[196:199], v[92:95]\n"
"v_mfma_f32_16x16x32_bf16 v[104:107], a[156:159], a[196:199], v[104:107]\n"
"buffer_load_dwordx4 a[160:163], v1, s[44:47], s72 offen offset:0 sc1\n"
"v_mfma_f32_16x16x32_bf16 v[108:111], a[156:159], a[200:203], v[108:111]\n"
"v_mfma_f32_16x16x32_bf16 v[112:115], a[156:159], a[204:207], v[112:115]\n"
"s_waitcnt vmcnt(18) lgkmcnt(0)\n"
"v_mfma_f32_16x16x32_bf16 v[32:35], a[104:107], a[208:211], v[32:35]\n"
"ds_read_b64_tr_b16 a[132:133], v12 offset:35840\n"
"v_mfma_f32_16x16x32_bf16 v[36:39], a[104:107], a[212:215], v[36:39]\n"
"buffer_load_dwordx4 a[164:167], v1, s[44:47], s72 offen offset:1024 sc1\n"
"v_mfma_f32_16x16x32_bf16 v[40:43], a[104:107], a[216:219], v[40:43]\n"
"ds_read_b64_tr_b16 a[134:135], v12 offset:39424\n"
"ds_read_b64_tr_b16 a[136:137], v12 offset:35872\n"
"v_mfma_f32_16x16x32_bf16 v[52:55], a[108:111], a[216:219], v[52:55]\n"
"ds_read_b64_tr_b16 a[138:139], v12 offset:39456\n"
"v_mfma_f32_16x16x32_bf16 v[48:51], a[108:111], a[212:215], v[48:51]\n"
"ds_read_b64_tr_b16 a[140:141], v12 offset:35904\n"
"v_mfma_f32_16x16x32_bf16 v[44:47], a[108:111], a[208:211], v[44:47]\n"
"buffer_load_dwordx4 a[168:171], v1, s[44:47], s72 offen offset:2048 sc1\n"
"v_mfma_f32_16x16x32_bf16 v[56:59], a[112:115], a[208:211], v[56:59]\n"
"s_add_u32 s72, s72, 36864\n"
"ds_read_b64_tr_b16 a[142:143], v12 offset:39488\n"
"v_mfma_f32_16x16x32_bf16 v[60:63], a[112:115], a[212:215], v[60:63]\n"
"ds_read_b64_tr_b16 a[144:145], v12 offset:35936\n"
"v_mfma_f32_16x16x32_bf16 v[64:67], a[112:115], a[216:219], v[64:67]\n"
"ds_read_b64_tr_b16 a[146:147], v12 offset:39520\n"
"v_mfma_f32_16x16x32_bf16 v[76:79], a[116:119], a[216:219], v[76:79]\n"
"buffer_load_dwordx4 a[172:175], v1, s[44:47], s72 offen offset:0 sc1\n"
"v_mfma_f32_16x16x32_bf16 v[72:75], a[116:119], a[212:215], v[72:75]\n"
"ds_read_b64_tr_b16 a[148:149], v12 offset:35968\n"
"ds_read_b64_tr_b16 a[150:151], v12 offset:39552\n"
"v_mfma_f32_16x16x32_bf16 v[68:71], a[116:119], a[208:211], v[68:71]\n"
"v_mfma_f32_16x16x32_bf16 v[80:83], a[120:123], a[208:211], v[80:83]\n"
"ds_read_b64_tr_b16 a[152:153], v12 offset:36000\n"
"v_mfma_f32_16x16x32_bf16 v[84:87], a[120:123], a[212:215], v[84:87]\n"
"buffer_load_dwordx4 a[176:179], v1, s[44:47], s72 offen offset:1024 sc1\n"
"v_mfma_f32_16x16x32_bf16 v[88:91], a[120:123], a[216:219], v[88:91]\n"
"ds_read_b64_tr_b16 a[154:155], v12 offset:39584\n"
"ds_read_b64_tr_b16 a[156:157], v14 offset:35840\n"
"v_mfma_f32_16x16x32_bf16 v[100:103], a[124:127], a[216:219], v[100:103]\n"
"ds_read_b64_tr_b16 a[158:159], v14 offset:39424\n"
"v_mfma_f32_16x16x32_bf16 v[96:99], a[124:127], a[212:215], v[96:99]\n"
"v_mfma_f32_16x16x32_bf16 v[92:95], a[124:127], a[208:211], v[92:95]\n"
"buffer_load_dwordx4 a[180:183], v1, s[44:47], s72 offen offset:2048 sc1\n"
"v_mfma_f32_16x16x32_bf16 v[104:107], a[128:131], a[208:211], v[104:107]\n"
"s_add_u32 s72, s72, 36864\n"
"v_mfma_f32_16x16x32_bf16 v[108:111], a[128:131], a[212:215], v[108:111]\n"
"v_mfma_f32_16x16x32_bf16 v[112:115], a[128:131], a[216:219], v[112:115]\n"
"s_waitcnt vmcnt(20) lgkmcnt(0)\n"
"v_mfma_f32_16x16x32_bf16 v[32:35], a[132:135], a[220:223], v[32:35]\n"
"s_nop 0\n"
"s_barrier\n"
"v_mfma_f32_16x16x32_bf16 v[36:39], a[132:135], a[224:227], v[36:39]\n"
"s_add_u32 m0, s75, 28672\n"
"ds_read_b64_tr_b16 a[104:105], v12 offset:43008\n"
"v_mfma_f32_16x16x32_bf16 v[40:43], a[132:135], a[228:231], v[40:43]\n"
"buffer_load_dwordx4 v8, s[40:43], s73 offen offset:0 lds\n"
"v_mfma_f32_16x16x32_bf16 v[52:55], a[136:139], a[228:231], v[52:55]\n"
"ds_read_b64_tr_b16 a[106:107], v12 offset:46592\n"
"ds_read_b64_tr_b16 a[108:109], v12 offset:43040\n"
"v_mfma_f32_16x16x32_bf16 v[48:51], a[136:139], a[224:227], v[48:51]\n"
"ds_read_b64_tr_b16 a[110:111], v12 offset:46624\n"
"v_mfma_f32_16x16x32_bf16 v[44:47], a[136:139], a[220:223], v[44:47]\n"
"ds_read_b64_tr_b16 a[112:113], v12 offset:43072\n"
"v_mfma_f32_16x16x32_bf16 v[56:59], a[140:143], a[220:223], v[56:59]\n"
"buffer_load_dwordx4 v9, s[40:43], s73 offen offset:1024 lds\n"
"v_mfma_f32_16x16x32_bf16 v[60:63], a[140:143], a[224:227], v[60:63]\n"
"ds_read_b64_tr_b16 a[114:115], v12 offset:46656\n"
"ds_read_b64_tr_b16 a[116:117], v12 offset:43104\n"
"v_mfma_f32_16x16x32_bf16 v[64:67], a[140:143], a[228:231], v[64:67]\n"
"v_mfma_f32_16x16x32_bf16 v[76:79], a[144:147], a[228:231], v[76:79]\n"
"ds_read_b64_tr_b16 a[118:119], v12 offset:46688\n"
"v_mfma_f32_16x16x32_bf16 v[72:75], a[144:147], a[224:227], v[72:75]\n"
"buffer_load_dwordx4 v10, s[40:43], s73 offen offset:2048 lds\n"
"v_mfma_f32_16x16x32_bf16 v[68:71], a[144:147], a[220:223], v[68:71]\n"
"ds_read_b64_tr_b16 a[120:121], v12 offset:43136\n"
"ds_read_b64_tr_b16 a[122:123], v12 offset:46720\n"
"v_mfma_f32_16x16x32_bf16 v[80:83], a[148:151], a[220:223], v[80:83]\n"
"ds_read_b64_tr_b16 a[124:125], v12 offset:43168\n"
"v_mfma_f32_16x16x32_bf16 v[84:87], a[148:151], a[224:227], v[84:87]\n"
"ds_read_b64_tr_b16 a[126:127], v12 offset:46752\n"
"v_mfma_f32_16x16x32_bf16 v[88:91], a[148:151], a[228:231], v[88:91]\n"
"s_mov_b64 exec, s[96:97]\n"
"buffer_load_dwordx4 v11, s[40:43], s73 offen offset:3072 lds\n"
"s_mov_b64 exec, -1\n"
"v_mfma_f32_16x16x32_bf16 v[100:103], a[152:155], a[228:231], v[100:103]\n"
"s_add_u32 s73, s73, 589824\n"
"ds_read_b64_tr_b16 a[128:129], v14 offset:43008\n"
"v_mfma_f32_16x16x32_bf16 v[96:99], a[152:155], a[224:227], v[96:99]\n"
"ds_read_b64_tr_b16 a[130:131], v14 offset:46592\n"
"v_mfma_f32_16x16x32_bf16 v[92:95], a[152:155], a[220:223], v[92:95]\n"
"v_mfma_f32_16x16x32_bf16 v[104:107], a[156:159], a[220:223], v[104:107]\n"
"buffer_load_dwordx4 a[184:187], v1, s[44:47], s72 offen offset:0 sc1\n"
"v_mfma_f32_16x16x32_bf16 v[108:111], a[156:159], a[224:227], v[108:111]\n"
"v_mfma_f32_16x16x32_bf16 v[112:115], a[156:159], a[228:231], v[112:115]\n"
"s_waitcnt vmcnt(18) lgkmcnt(0)\n"
"v_mfma_f32_16x16x32_bf16 v[32:35], a[104:107], a[232:235], v[32:35]\n"
"ds_read_b64_tr_b16 a[132:133], v12 offset:50176\n"
"v_mfma_f32_16x16x32_bf16 v[36:39], a[104:107], a[236:239], v[36:39]\n"
"buffer_load_dwordx4 a[188:191], v1, s[44:47], s72 offen offset:1024 sc1\n"
"v_mfma_f32_16x16x32_bf16 v[40:43], a[104:107], a[240:243], v[40:43]\n"
"ds_read_b64_tr_b16 a[134:135], v12 offset:53760\n"
"ds_read_b64_tr_b16 a[136:137], v12 offset:50208\n"
"v_mfma_f32_16x16x32_bf16 v[52:55], a[108:111], a[240:243], v[52:55]\n"
"ds_read_b64_tr_b16 a[138:139], v12 offset:53792\n"
"v_mfma_f32_16x16x32_bf16 v[48:51], a[108:111], a[236:239], v[48:51]\n"
"ds_read_b64_tr_b16 a[140:141], v12 offset:50240\n"
"v_mfma_f32_16x16x32_bf16 v[44:47], a[108:111], a[232:235], v[44:47]\n"
"buffer_load_dwordx4 a[192:195], v1, s[44:47], s72 offen offset:2048 sc1\n"
"v_mfma_f32_16x16x32_bf16 v[56:59], a[112:115], a[232:235], v[56:59]\n"
"s_add_u32 s72, s72, 36864\n"
"ds_read_b64_tr_b16 a[142:143], v12 offset:53824\n"
"v_mfma_f32_16x16x32_bf16 v[60:63], a[112:115], a[236:239], v[60:63]\n"
"ds_read_b64_tr_b16 a[144:145], v12 offset:50272\n"
"v_mfma_f32_16x16x32_bf16 v[64:67], a[112:115], a[240:243], v[64:67]\n"
"ds_read_b64_tr_b16 a[146:147], v12 offset:53856\n"
"v_mfma_f32_16x16x32_bf16 v[76:79], a[116:119], a[240:243], v[76:79]\n"
"buffer_load_dwordx4 a[196:199], v1, s[44:47], s72 offen offset:0 sc1\n"
"v_mfma_f32_16x16x32_bf16 v[72:75], a[116:119], a[236:239], v[72:75]\n"
"ds_read_b64_tr_b16 a[148:149], v12 offset:50304\n"
"ds_read_b64_tr_b16 a[150:151], v12 offset:53888\n"
"v_mfma_f32_16x16x32_bf16 v[68:71], a[116:119], a[232:235], v[68:71]\n"
"v_mfma_f32_16x16x32_bf16 v[80:83], a[120:123], a[232:235], v[80:83]\n"
"ds_read_b64_tr_b16 a[152:153], v12 offset:50336\n"
"v_mfma_f32_16x16x32_bf16 v[84:87], a[120:123], a[236:239], v[84:87]\n"
"buffer_load_dwordx4 a[200:203], v1, s[44:47], s72 offen offset:1024 sc1\n"
"v_mfma_f32_16x16x32_bf16 v[88:91], a[120:123], a[240:243], v[88:91]\n"
"ds_read_b64_tr_b16 a[154:155], v12 offset:53920\n"
"ds_read_b64_tr_b16 a[156:157], v14 offset:50176\n"
"v_mfma_f32_16x16x32_bf16 v[100:103], a[124:127], a[240:243], v[100:103]\n"
"ds_read_b64_tr_b16 a[158:159], v14 offset:53760\n"
"v_mfma_f32_16x16x32_bf16 v[96:99], a[124:127], a[236:239], v[96:99]\n"
"v_mfma_f32_16x16x32_bf16 v[92:95], a[124:127], a[232:235], v[92:95]\n"
"buffer_load_dwordx4 a[204:207], v1, s[44:47], s72 offen offset:2048 sc1\n"
"v_mfma_f32_16x16x32_bf16 v[104:107], a[128:131], a[232:235], v[104:107]\n"
"s_add_u32 s72, s72, 36864\n"
"v_mfma_f32_16x16x32_bf16 v[108:111], a[128:131], a[236:239], v[108:111]\n"
"v_mfma_f32_16x16x32_bf16 v[112:115], a[128:131], a[240:243], v[112:115]\n"
"s_waitcnt vmcnt(20) lgkmcnt(0)\n"
"v_mfma_f32_16x16x32_bf16 v[32:35], a[132:135], a[244:247], v[32:35]\n"
"s_nop 0\n"
"s_barrier\n"
"v_mfma_f32_16x16x32_bf16 v[36:39], a[132:135], a[248:251], v[36:39]\n"
"s_add_u32 m0, s75, 43008\n"
"ds_read_b64_tr_b16 a[104:105], v12 offset:0\n"
"v_mfma_f32_16x16x32_bf16 v[40:43], a[132:135], a[252:255], v[40:43]\n"
"buffer_load_dwordx4 v8, s[40:43], s73 offen offset:0 lds\n"
"v_mfma_f32_16x16x32_bf16 v[52:55], a[136:139], a[252:255], v[52:55]\n"
"ds_read_b64_tr_b16 a[106:107], v12 offset:3584\n"
"ds_read_b64_tr_b16 a[108:109], v12 offset:32\n"
"v_mfma_f32_16x16x32_bf16 v[48:51], a[136:139], a[248:251], v[48:51]\n"
"ds_read_b64_tr_b16 a[110:111], v12 offset:3616\n"
"v_mfma_f32_16x16x32_bf16 v[44:47], a[136:139], a[244:247], v[44:47]\n"
"ds_read_b64_tr_b16 a[112:113], v12 offset:64\n"
"v_mfma_f32_16x16x32_bf16 v[56:59], a[140:143], a[244:247], v[56:59]\n"
"buffer_load_dwordx4 v9, s[40:43], s73 offen offset:1024 lds\n"
"v_mfma_f32_16x16x32_bf16 v[60:63], a[140:143], a[248:251], v[60:63]\n"
"ds_read_b64_tr_b16 a[114:115], v12 offset:3648\n"
"ds_read_b64_tr_b16 a[116:117], v12 offset:96\n"
"v_mfma_f32_16x16x32_bf16 v[64:67], a[140:143], a[252:255], v[64:67]\n"
"v_mfma_f32_16x16x32_bf16 v[76:79], a[144:147], a[252:255], v[76:79]\n"
"ds_read_b64_tr_b16 a[118:119], v12 offset:3680\n"
"v_mfma_f32_16x16x32_bf16 v[72:75], a[144:147], a[248:251], v[72:75]\n"
"buffer_load_dwordx4 v10, s[40:43], s73 offen offset:2048 lds\n"
"v_mfma_f32_16x16x32_bf16 v[68:71], a[144:147], a[244:247], v[68:71]\n"
"ds_read_b64_tr_b16 a[120:121], v12 offset:128\n"
"ds_read_b64_tr_b16 a[122:123], v12 offset:3712\n"
"v_mfma_f32_16x16x32_bf16 v[80:83], a[148:151], a[244:247], v[80:83]\n"
"ds_read_b64_tr_b16 a[124:125], v12 offset:160\n"
"v_mfma_f32_16x16x32_bf16 v[84:87], a[148:151], a[248:251], v[84:87]\n"
"ds_read_b64_tr_b16 a[126:127], v12 offset:3744\n"
"v_mfma_f32_16x16x32_bf16 v[88:91], a[148:151], a[252:255], v[88:91]\n"
"s_mov_b64 exec, s[96:97]\n"
"buffer_load_dwordx4 v11, s[40:43], s73 offen offset:3072 lds\n"
"s_mov_b64 exec, -1\n"
"v_mfma_f32_16x16x32_bf16 v[100:103], a[152:155], a[252:255], v[100:103]\n"
"s_add_u32 s73, s73, 589824\n"
"ds_read_b64_tr_b16 a[128:129], v14 offset:0\n"
"v_mfma_f32_16x16x32_bf16 v[96:99], a[152:155], a[248:251], v[96:99]\n"
"ds_read_b64_tr_b16 a[130:131], v14 offset:3584\n"
"v_mfma_f32_16x16x32_bf16 v[92:95], a[152:155], a[244:247], v[92:95]\n"
"v_mfma_f32_16x16x32_bf16 v[104:107], a[156:159], a[244:247], v[104:107]\n"
"buffer_load_dwordx4 a[208:211], v1, s[44:47], s72 offen offset:0 sc1\n"
"v_mfma_f32_16x16x32_bf16 v[108:111], a[156:159], a[248:251], v[108:111]\n"
"v_mfma_f32_16x16x32_bf16 v[112:115], a[156:159], a[252:255], v[112:115]\n"
"s_sub_u32 s76, s76, 1\n"
"s_cmp_lg_u32 s76, 0\n"
"s_cbranch_scc1 L_g4\n"
"s_waitcnt vmcnt(18) lgkmcnt(0)\n"
"v_mfma_f32_16x16x32_bf16 v[32:35], a[104:107], a[160:163], v[32:35]\n"
"ds_read_b64_tr_b16 a[132:133], v12 offset:7168\n"
"s_lshr_b32 s56, s52, 1\n"
"v_mfma_f32_16x16x32_bf16 v[36:39], a[104:107], a[164:167], v[36:39]\n"
"buffer_load_dwordx4 a[212:215], v1, s[44:47], s72 offen offset:1024 sc1\n"
"v_mfma_f32_16x16x32_bf16 v[40:43], a[104:107], a[168:171], v[40:43]\n"
"s_lshl_b32 s56, s56, 3\n"
"ds_read_b64_tr_b16 a[134:135], v12 offset:10752\n"
"v_mfma_f32_16x16x32_bf16 v[52:55], a[108:111], a[168:171], v[52:55]\n"
"s_bfe_u32 s57, s53, 0x10003\n"
"s_lshl2_add_u32 s56, s57, s56\n"
"v_mfma_f32_16x16x32_bf16 v[48:51], a[108:111], a[164:167], v[48:51]\n"
"ds_read_b64_tr_b16 a[136:137], v12 offset:7200\n"
"s_bfe_u32 s57, s53, 0x10002\n"
"v_mfma_f32_16x16x32_bf16 v[44:47], a[108:111], a[160:163], v[44:47]\n"
"buffer_load_dwordx4 a[216:219], v1, s[44:47], s72 offen offset:2048 sc1\n"
"v_mfma_f32_16x16x32_bf16 v[56:59], a[112:115], a[160:163], v[56:59]\n"
"s_add_u32 s72, s72, 36864\n"
"s_add_u32 s56, s56, s57\n"
"v_mfma_f32_16x16x32_bf16 v[60:63], a[112:115], a[164:167], v[60:63]\n"
"ds_read_b64_tr_b16 a[138:139], v12 offset:10784\n"
"s_xor_b32 s57, s54, 1\n"
"v_mfma_f32_16x16x32_bf16 v[64:67], a[112:115], a[168:171], v[64:67]\n"
"s_lshl1_add_u32 s56, s57, s56\n"
"ds_read_b64_tr_b16 a[140:141], v12 offset:7232\n"
"v_mfma_f32_16x16x32_bf16 v[76:79], a[116:119], a[168:171], v[76:79]\n"
"buffer_load_dwordx4 a[220:223], v1, s[44:47], s72 offen offset:0 sc1\n"
"v_mfma_f32_16x16x32_bf16 v[72:75], a[116:119], a[164:167], v[72:75]\n"
"s_and_b32 s57, s52, 1\n"
"s_and_b32 s58, s53, 3\n"
"v_mfma_f32_16x16x32_bf16 v[68:71], a[116:119], a[160:163], v[68:71]\n"
"ds_read_b64_tr_b16 a[142:143], v12 offset:10816\n"
"s_lshl2_add_u32 s57, s57, s58\n"
"v_mfma_f32_16x16x32_bf16 v[80:83], a[120:123], a[160:163], v[80:83]\n"
"s_lshl2_add_u32 s58, s57, s11\n"
"ds_read_b64_tr_b16 a[144:145], v12 offset:7264\n"
"v_mfma_f32_16x16x32_bf16 v[84:87], a[120:123], a[164:167], v[84:87]\n"
"buffer_load_dwordx4 a[224:227], v1, s[44:47], s72 offen offset:1024 sc1\n"
"v_mfma_f32_16x16x32_bf16 v[88:91], a[120:123], a[168:171], v[88:91]\n"
"s_lshl_b32 s73, s11, 17\n"
"s_lshl_b32 s59, s56, 8\n"
"v_mfma_f32_16x16x32_bf16 v[100:103], a[124:127], a[168:171], v[100:103]\n"
"ds_read_b64_tr_b16 a[146:147], v12 offset:10848\n"
"s_add_u32 s73, s73, s59\n"
"v_mfma_f32_16x16x32_bf16 v[96:99], a[124:127], a[164:167], v[96:99]\n"
"s_lshl_b32 s75, s11, 12\n"
"ds_read_b64_tr_b16 a[148:149], v12 offset:7296\n"
"ds_read_b64_tr_b16 a[150:151], v12 offset:10880\n"
"ds_read_b64_tr_b16 a[152:153], v12 offset:7328\n"
"ds_read_b64_tr_b16 a[154:155], v12 offset:10912\n"
"ds_read_b64_tr_b16 a[156:157], v14 offset:7168\n"
"ds_read_b64_tr_b16 a[158:159], v14 offset:10752\n"
"v_mfma_f32_16x16x32_bf16 v[92:95], a[124:127], a[160:163], v[92:95]\n"
"buffer_load_dwordx4 a[228:231], v1, s[44:47], s72 offen offset:2048 sc1\n"
"v_mfma_f32_16x16x32_bf16 v[104:107], a[128:131], a[160:163], v[104:107]\n"
"s_add_u32 s72, s72, 36864\n"
"v_mfma_f32_16x16x32_bf16 v[108:111], a[128:131], a[164:167], v[108:111]\n"
"s_sub_u32 s40, s18, 0x1000\n"
"s_subb_u32 s41, s19, 0\n"
"v_mfma_f32_16x16x32_bf16 v[112:115], a[128:131], a[168:171], v[112:115]\n"
"s_and_b32 s41, s41, 0xffff\n"
"s_mov_b32 s42, 0x481000\n"
"s_mov_b32 s43, 0x20000\n"
"v_bfe_u32 v212, v0, 2, 3\n"
"v_mul_u32_u24 v212, 8192, v212\n"
"v_lshrrev_b32 v213, 5, v0\n"
"v_lshl_add_u32 v212, v213, 6, v212\n"
"v_mov_b32 v213, v5\n"
"v_lshl_add_u32 v8, v213, 4, v212\n"
"v_add_u32 v8, 4096, v8\n"
"v_lshl_add_u32 v9, v213, 4, v212\n"
"v_add_u32 v9, 3200, v9\n"
"v_lshl_add_u32 v10, v213, 4, v212\n"
"v_add_u32 v10, 67584, v10\n"
"v_lshl_add_u32 v11, v213, 4, v212\n"
"v_add_u32 v11, 66688, v11\n"
"s_waitcnt vmcnt(20) lgkmcnt(0)\n"
"v_mfma_f32_16x16x32_bf16 v[32:35], a[132:135], a[172:175], v[32:35]\n"
"s_nop 0\n"
"s_barrier\n"
"v_mfma_f32_16x16x32_bf16 v[36:39], a[132:135], a[176:179], v[36:39]\n"
"ds_read_b64_tr_b16 a[104:105], v12 offset:14336\n"
"s_add_u32 m0, s75, 98304\n"
"v_mfma_f32_16x16x32_bf16 v[40:43], a[132:135], a[180:183], v[40:43]\n"
"ds_read_b64_tr_b16 a[106:107], v12 offset:17920\n"
"v_mfma_f32_16x16x32_bf16 v[52:55], a[136:139], a[180:183], v[52:55]\n"
"buffer_load_dwordx4 v8, s[40:43], s73 offen offset:0 lds\n"
"v_mfma_f32_16x16x32_bf16 v[48:51], a[136:139], a[176:179], v[48:51]\n"
"ds_read_b64_tr_b16 a[108:109], v12 offset:14368\n"
"ds_read_b64_tr_b16 a[110:111], v12 offset:17952\n"
"v_mfma_f32_16x16x32_bf16 v[44:47], a[136:139], a[172:175], v[44:47]\n"
"buffer_load_dwordx4 v9, s[40:43], s73 offen offset:1024 lds\n"
"v_mfma_f32_16x16x32_bf16 v[56:59], a[140:143], a[172:175], v[56:59]\n"
"ds_read_b64_tr_b16 a[112:113], v12 offset:14400\n"
"ds_read_b64_tr_b16 a[114:115], v12 offset:17984\n"
"v_mfma_f32_16x16x32_bf16 v[60:63], a[140:143], a[176:179], v[60:63]\n"
"buffer_load_dwordx4 v10, s[40:43], s73 offen offset:2048 lds\n"
"v_mfma_f32_16x16x32_bf16 v[64:67], a[140:143], a[180:183], v[64:67]\n"
"ds_read_b64_tr_b16 a[116:117], v12 offset:14432\n"
"v_mfma_f32_16x16x32_bf16 v[76:79], a[144:147], a[180:183], v[76:79]\n"
"ds_read_b64_tr_b16 a[118:119], v12 offset:18016\n"
"v_mfma_f32_16x16x32_bf16 v[72:75], a[144:147], a[176:179], v[72:75]\n"
"buffer_load_dwordx4 v11, s[40:43], s73 offen offset:3072 lds\n"
"v_mfma_f32_16x16x32_bf16 v[68:71], a[144:147], a[172:175], v[68:71]\n"
"ds_read_b64_tr_b16 a[120:121], v12 offset:14464\n"
"ds_read_b64_tr_b16 a[122:123], v12 offset:18048\n"
"v_mfma_f32_16x16x32_bf16 v[80:83], a[148:151], a[172:175], v[80:83]\n"
"s_add_u32 s73, s73, 524288\n"
"ds_read_b64_tr_b16 a[124:125], v12 offset:14496\n"
"v_mfma_f32_16x16x32_bf16 v[84:87], a[148:151], a[176:179], v[84:87]\n"
"ds_read_b64_tr_b16 a[126:127], v12 offset:18080\n"
"v_mfma_f32_16x16x32_bf16 v[88:91], a[148:151], a[180:183], v[88:91]\n"
"s_add_u32 m0, s75, 114688\n"
"ds_read_b64_tr_b16 a[128:129], v14 offset:14336\n"
"v_mfma_f32_16x16x32_bf16 v[100:103], a[152:155], a[180:183], v[100:103]\n"
"ds_read_b64_tr_b16 a[130:131], v14 offset:17920\n"
"v_mfma_f32_16x16x32_bf16 v[96:99], a[152:155], a[176:179], v[96:99]\n"
"buffer_load_dwordx4 v8, s[40:43], s73 offen offset:0 lds\n"
"v_mfma_f32_16x16x32_bf16 v[92:95], a[152:155], a[172:175], v[92:95]\n"
"v_mfma_f32_16x16x32_bf16 v[104:107], a[156:159], a[172:175], v[104:107]\n"
"buffer_load_dwordx4 a[232:235], v1, s[44:47], s72 offen offset:0 sc1\n"
"v_mfma_f32_16x16x32_bf16 v[108:111], a[156:159], a[176:179], v[108:111]\n"
"buffer_load_dwordx4 v9, s[40:43], s73 offen offset:1024 lds\n"
"v_mfma_f32_16x16x32_bf16 v[112:115], a[156:159], a[180:183], v[112:115]\n"
"s_waitcnt vmcnt(20) lgkmcnt(0)\n"
"v_mfma_f32_16x16x32_bf16 v[32:35], a[104:107], a[184:187], v[32:35]\n"
"ds_read_b64_tr_b16 a[132:133], v12 offset:21504\n"
"v_mfma_f32_16x16x32_bf16 v[36:39], a[104:107], a[188:191], v[36:39]\n"
"buffer_load_dwordx4 a[236:239], v1, s[44:47], s72 offen offset:1024 sc1\n"
"v_mfma_f32_16x16x32_bf16 v[40:43], a[104:107], a[192:195], v[40:43]\n"
"buffer_load_dwordx4 v10, s[40:43], s73 offen offset:2048 lds\n"
"v_mfma_f32_16x16x32_bf16 v[52:55], a[108:111], a[192:195], v[52:55]\n"
"ds_read_b64_tr_b16 a[134:135], v12 offset:25088\n"
"ds_read_b64_tr_b16 a[136:137], v12 offset:21536\n"
"v_mfma_f32_16x16x32_bf16 v[48:51], a[108:111], a[188:191], v[48:51]\n"
"buffer_load_dwordx4 v11, s[40:43], s73 offen offset:3072 lds\n"
"v_mfma_f32_16x16x32_bf16 v[44:47], a[108:111], a[184:187], v[44:47]\n"
"buffer_load_dwordx4 a[240:243], v1, s[44:47], s72 offen offset:2048 sc1\n"
"v_mfma_f32_16x16x32_bf16 v[56:59], a[112:115], a[184:187], v[56:59]\n"
"s_add_u32 s72, s72, 36864\n"
"ds_read_b64_tr_b16 a[138:139], v12 offset:25120\n"
"v_mfma_f32_16x16x32_bf16 v[60:63], a[112:115], a[188:191], v[60:63]\n"
"s_add_u32 s73, s73, 524288\n"
"ds_read_b64_tr_b16 a[140:141], v12 offset:21568\n"
"v_mfma_f32_16x16x32_bf16 v[64:67], a[112:115], a[192:195], v[64:67]\n"
"ds_read_b64_tr_b16 a[142:143], v12 offset:25152\n"
"s_add_u32 m0, s75, 131072\n"
"v_mfma_f32_16x16x32_bf16 v[76:79], a[116:119], a[192:195], v[76:79]\n"
"buffer_load_dwordx4 a[244:247], v1, s[44:47], s72 offen offset:0 sc1\n"
"v_mfma_f32_16x16x32_bf16 v[72:75], a[116:119], a[188:191], v[72:75]\n"
"ds_read_b64_tr_b16 a[144:145], v12 offset:21600\n"
"ds_read_b64_tr_b16 a[146:147], v12 offset:25184\n"
"v_mfma_f32_16x16x32_bf16 v[68:71], a[116:119], a[184:187], v[68:71]\n"
"buffer_load_dwordx4 v8, s[40:43], s73 offen offset:0 lds\n"
"v_mfma_f32_16x16x32_bf16 v[80:83], a[120:123], a[184:187], v[80:83]\n"
"ds_read_b64_tr_b16 a[148:149], v12 offset:21632\n"
"ds_read_b64_tr_b16 a[150:151], v12 offset:25216\n"
"v_mfma_f32_16x16x32_bf16 v[84:87], a[120:123], a[188:191], v[84:87]\n"
"buffer_load_dwordx4 a[248:251], v1, s[44:47], s72 offen offset:1024 sc1\n"
"v_mfma_f32_16x16x32_bf16 v[88:91], a[120:123], a[192:195], v[88:91]\n"
"buffer_load_dwordx4 v9, s[40:43], s73 offen offset:1024 lds\n"
"v_mfma_f32_16x16x32_bf16 v[100:103], a[124:127], a[192:195], v[100:103]\n"
"ds_read_b64_tr_b16 a[152:153], v12 offset:21664\n"
"ds_read_b64_tr_b16 a[154:155], v12 offset:25248\n"
"ds_read_b64_tr_b16 a[156:157], v14 offset:21504\n"
"v_mfma_f32_16x16x32_bf16 v[96:99], a[124:127], a[188:191], v[96:99]\n"
"buffer_load_dwordx4 v10, s[40:43], s73 offen offset:2048 lds\n"
"ds_read_b64_tr_b16 a[158:159], v14 offset:25088\n"
"v_mfma_f32_16x16x32_bf16 v[92:95], a[124:127], a[184:187], v[92:95]\n"
"buffer_load_dwordx4 a[252:255], v1, s[44:47], s72 offen offset:2048 sc1\n"
"v_mfma_f32_16x16x32_bf16 v[104:107], a[128:131], a[184:187], v[104:107]\n"
"s_add_u32 s72, s72, 36864\n"
"v_mfma_f32_16x16x32_bf16 v[108:111], a[128:131], a[188:191], v[108:111]\n"
"buffer_load_dwordx4 v11, s[40:43], s73 offen offset:3072 lds\n"
"v_mfma_f32_16x16x32_bf16 v[112:115], a[128:131], a[192:195], v[112:115]\n"
"s_add_u32 s73, s73, 524288\n"
"s_waitcnt vmcnt(28) lgkmcnt(0)\n"
"v_mfma_f32_16x16x32_bf16 v[32:35], a[132:135], a[196:199], v[32:35]\n"
"s_nop 0\n"
"s_barrier\n"
"v_mfma_f32_16x16x32_bf16 v[36:39], a[132:135], a[200:203], v[36:39]\n"
"ds_read_b64_tr_b16 a[104:105], v12 offset:28672\n"
"s_add_u32 m0, s75, 147456\n"
"v_mfma_f32_16x16x32_bf16 v[40:43], a[132:135], a[204:207], v[40:43]\n"
"ds_read_b64_tr_b16 a[106:107], v12 offset:32256\n"
"v_mfma_f32_16x16x32_bf16 v[52:55], a[136:139], a[204:207], v[52:55]\n"
"buffer_load_dwordx4 v8, s[40:43], s73 offen offset:0 lds\n"
"v_mfma_f32_16x16x32_bf16 v[48:51], a[136:139], a[200:203], v[48:51]\n"
"ds_read_b64_tr_b16 a[108:109], v12 offset:28704\n"
"ds_read_b64_tr_b16 a[110:111], v12 offset:32288\n"
"v_mfma_f32_16x16x32_bf16 v[44:47], a[136:139], a[196:199], v[44:47]\n"
"buffer_load_dwordx4 v9, s[40:43], s73 offen offset:1024 lds\n"
"v_mfma_f32_16x16x32_bf16 v[56:59], a[140:143], a[196:199], v[56:59]\n"
"ds_read_b64_tr_b16 a[112:113], v12 offset:28736\n"
"ds_read_b64_tr_b16 a[114:115], v12 offset:32320\n"
"v_mfma_f32_16x16x32_bf16 v[60:63], a[140:143], a[200:203], v[60:63]\n"
"buffer_load_dwordx4 v10, s[40:43], s73 offen offset:2048 lds\n"
"v_mfma_f32_16x16x32_bf16 v[64:67], a[140:143], a[204:207], v[64:67]\n"
"ds_read_b64_tr_b16 a[116:117], v12 offset:28768\n"
"v_mfma_f32_16x16x32_bf16 v[76:79], a[144:147], a[204:207], v[76:79]\n"
"ds_read_b64_tr_b16 a[118:119], v12 offset:32352\n"
"v_mfma_f32_16x16x32_bf16 v[72:75], a[144:147], a[200:203], v[72:75]\n"
"buffer_load_dwordx4 v11, s[40:43], s73 offen offset:3072 lds\n"
"v_mfma_f32_16x16x32_bf16 v[68:71], a[144:147], a[196:199], v[68:71]\n"
"ds_read_b64_tr_b16 a[120:121], v12 offset:28800\n"
"s_add_u32 s73, s73, 524288\n"
"v_mfma_f32_16x16x32_bf16 v[80:83], a[148:151], a[196:199], v[80:83]\n"
"ds_read_b64_tr_b16 a[122:123], v12 offset:32384\n"
"ds_read_b64_tr_b16 a[124:125], v12 offset:28832\n"
"v_mfma_f32_16x16x32_bf16 v[84:87], a[148:151], a[200:203], v[84:87]\n"
"s_mul_i32 s72, s58, 73728\n"
"ds_read_b64_tr_b16 a[126:127], v12 offset:32416\n"
"v_mfma_f32_16x16x32_bf16 v[88:91], a[148:151], a[204:207], v[88:91]\n"
"v_lshrrev_b32 v212, 2, v3\n"
"v_mfma_f32_16x16x32_bf16 v[100:103], a[152:155], a[204:207], v[100:103]\n"
"ds_read_b64_tr_b16 a[128:129], v14 offset:28672\n"
"ds_read_b64_tr_b16 a[130:131], v14 offset:32256\n"
"v_mfma_f32_16x16x32_bf16 v[96:99], a[152:155], a[200:203], v[96:99]\n"
"v_lshlrev_b32 v212, 8, v212\n"
"v_mfma_f32_16x16x32_bf16 v[92:95], a[152:155], a[196:199], v[92:95]\n"
"v_lshl_add_u32 v212, v2, 6, v212\n"
"v_mfma_f32_16x16x32_bf16 v[104:107], a[156:159], a[196:199], v[104:107]\n"
"v_mfma_f32_16x16x32_bf16 v[108:111], a[156:159], a[200:203], v[108:111]\n"
"v_lshl_add_u32 v27, v5, 4, v212\n"
"v_mfma_f32_16x16x32_bf16 v[112:115], a[156:159], a[204:207], v[112:115]\n"
"s_waitcnt vmcnt(25) lgkmcnt(0)\n"
"v_mfma_f32_16x16x32_bf16 v[32:35], a[104:107], a[208:211], v[32:35]\n"
"ds_read_b64_tr_b16 a[132:133], v12 offset:35840\n"
"v_mfma_f32_16x16x32_bf16 v[36:39], a[104:107], a[212:215], v[36:39]\n"
"buffer_load_dwordx4 a[0:3], v27, s[36:39], s72 offen offset:0 sc1\n"
"v_mfma_f32_16x16x32_bf16 v[40:43], a[104:107], a[216:219], v[40:43]\n"
"ds_read_b64_tr_b16 a[134:135], v12 offset:39424\n"
"ds_read_b64_tr_b16 a[136:137], v12 offset:35872\n"
"v_mfma_f32_16x16x32_bf16 v[52:55], a[108:111], a[216:219], v[52:55]\n"
"buffer_load_dwordx4 a[4:7], v27, s[36:39], s72 offen offset:1024 sc1\n"
"v_mfma_f32_16x16x32_bf16 v[48:51], a[108:111], a[212:215], v[48:51]\n"
"ds_read_b64_tr_b16 a[138:139], v12 offset:39456\n"
"ds_read_b64_tr_b16 a[140:141], v12 offset:35904\n"
"v_mfma_f32_16x16x32_bf16 v[44:47], a[108:111], a[208:211], v[44:47]\n"
"v_mfma_f32_16x16x32_bf16 v[56:59], a[112:115], a[208:211], v[56:59]\n"
"buffer_load_dwordx4 a[8:11], v27, s[36:39], s72 offen offset:2048 sc1\n"
"v_mfma_f32_16x16x32_bf16 v[60:63], a[112:115], a[212:215], v[60:63]\n"
"ds_read_b64_tr_b16 a[142:143], v12 offset:39488\n"
"ds_read_b64_tr_b16 a[144:145], v12 offset:35936\n"
"v_mfma_f32_16x16x32_bf16 v[64:67], a[112:115], a[216:219], v[64:67]\n"
"ds_read_b64_tr_b16 a[146:147], v12 offset:39520\n"
"v_mfma_f32_16x16x32_bf16 v[76:79], a[116:119], a[216:219], v[76:79]\n"
"buffer_load_dwordx4 a[12:15], v27, s[36:39], s72 offen offset:3072 sc1\n"
"v_mfma_f32_16x16x32_bf16 v[72:75], a[116:119], a[212:215], v[72:75]\n"
"ds_read_b64_tr_b16 a[148:149], v12 offset:35968\n"
"ds_read_b64_tr_b16 a[150:151], v12 offset:39552\n"
"v_mfma_f32_16x16x32_bf16 v[68:71], a[116:119], a[208:211], v[68:71]\n"
"v_mfma_f32_16x16x32_bf16 v[80:83], a[120:123], a[208:211], v[80:83]\n"
"s_add_u32 s72, s72, 4096\n"
"ds_read_b64_tr_b16 a[152:153], v12 offset:36000\n"
"v_mfma_f32_16x16x32_bf16 v[84:87], a[120:123], a[212:215], v[84:87]\n"
"ds_read_b64_tr_b16 a[154:155], v12 offset:39584\n"
"v_mfma_f32_16x16x32_bf16 v[88:91], a[120:123], a[216:219], v[88:91]\n"
"ds_read_b64_tr_b16 a[156:157], v14 offset:35840\n"
"v_mfma_f32_16x16x32_bf16 v[100:103], a[124:127], a[216:219], v[100:103]\n"
"buffer_load_dwordx4 a[16:19], v27, s[36:39], s72 offen offset:0 sc1\n"
"v_mfma_f32_16x16x32_bf16 v[96:99], a[124:127], a[212:215], v[96:99]\n"
"ds_read_b64_tr_b16 a[158:159], v14 offset:39424\n"
"v_mfma_f32_16x16x32_bf16 v[92:95], a[124:127], a[208:211], v[92:95]\n"
"v_mfma_f32_16x16x32_bf16 v[104:107], a[128:131], a[208:211], v[104:107]\n"
"buffer_load_dwordx4 a[20:23], v27, s[36:39], s72 offen offset:1024 sc1\n"
"v_mfma_f32_16x16x32_bf16 v[108:111], a[128:131], a[212:215], v[108:111]\n"
"v_mfma_f32_16x16x32_bf16 v[112:115], a[128:131], a[216:219], v[112:115]\n"
"s_waitcnt vmcnt(28) lgkmcnt(0)\n"
"v_mfma_f32_16x16x32_bf16 v[32:35], a[132:135], a[220:223], v[32:35]\n"
"s_nop 0\n"
"s_barrier\n"
"v_mfma_f32_16x16x32_bf16 v[36:39], a[132:135], a[224:227], v[36:39]\n"
"ds_read_b64_tr_b16 a[104:105], v12 offset:43008\n"
"v_mfma_f32_16x16x32_bf16 v[40:43], a[132:135], a[228:231], v[40:43]\n"
"buffer_load_dwordx4 a[24:27], v27, s[36:39], s72 offen offset:2048 sc1\n"
"v_mfma_f32_16x16x32_bf16 v[52:55], a[136:139], a[228:231], v[52:55]\n"
"ds_read_b64_tr_b16 a[106:107], v12 offset:46592\n"
"ds_read_b64_tr_b16 a[108:109], v12 offset:43040\n"
"v_mfma_f32_16x16x32_bf16 v[48:51], a[136:139], a[224:227], v[48:51]\n"
"buffer_load_dwordx4 a[28:31], v27, s[36:39], s72 offen offset:3072 sc1\n"
"v_mfma_f32_16x16x32_bf16 v[44:47], a[136:139], a[220:223], v[44:47]\n"
"ds_read_b64_tr_b16 a[110:111], v12 offset:46624\n"
"ds_read_b64_tr_b16 a[112:113], v12 offset:43072\n"
"v_mfma_f32_16x16x32_bf16 v[56:59], a[140:143], a[220:223], v[56:59]\n"
"ds_read_b64_tr_b16 a[114:115], v12 offset:46656\n"
"s_add_u32 s72, s72, 4096\n"
"v_mfma_f32_16x16x32_bf16 v[60:63], a[140:143], a[224:227], v[60:63]\n"
"ds_read_b64_tr_b16 a[116:117], v12 offset:43104\n"
"v_mfma_f32_16x16x32_bf16 v[64:67], a[140:143], a[228:231], v[64:67]\n"
"v_mfma_f32_16x16x32_bf16 v[76:79], a[144:147], a[228:231], v[76:79]\n"
"ds_read_b64_tr_b16 a[118:119], v12 offset:46688\n"
"v_mfma_f32_16x16x32_bf16 v[72:75], a[144:147], a[224:227], v[72:75]\n"
"buffer_load_dwordx4 a[32:35], v27, s[36:39], s72 offen offset:0 sc1\n"
"v_mfma_f32_16x16x32_bf16 v[68:71], a[144:147], a[220:223], v[68:71]\n"
"ds_read_b64_tr_b16 a[120:121], v12 offset:43136\n"
"ds_read_b64_tr_b16 a[122:123], v12 offset:46720\n"
"v_mfma_f32_16x16x32_bf16 v[80:83], a[148:151], a[220:223], v[80:83]\n"
"ds_read_b64_tr_b16 a[124:125], v12 offset:43168\n"
"v_mfma_f32_16x16x32_bf16 v[84:87], a[148:151], a[224:227], v[84:87]\n"
"buffer_load_dwordx4 a[36:39], v27, s[36:39], s72 offen offset:1024 sc1\n"
"v_mfma_f32_16x16x32_bf16 v[88:91], a[148:151], a[228:231], v[88:91]\n"
"ds_read_b64_tr_b16 a[126:127], v12 offset:46752\n"
"ds_read_b64_tr_b16 a[128:129], v14 offset:43008\n"
"v_mfma_f32_16x16x32_bf16 v[100:103], a[152:155], a[228:231], v[100:103]\n"
"buffer_load_dwordx4 a[40:43], v27, s[36:39], s72 offen offset:2048 sc1\n"
"v_mfma_f32_16x16x32_bf16 v[96:99], a[152:155], a[224:227], v[96:99]\n"
"ds_read_b64_tr_b16 a[130:131], v14 offset:46592\n"
"v_mfma_f32_16x16x32_bf16 v[92:95], a[152:155], a[220:223], v[92:95]\n"
"v_mfma_f32_16x16x32_bf16 v[104:107], a[156:159], a[220:223], v[104:107]\n"
"buffer_load_dwordx4 a[44:47], v27, s[36:39], s72 offen offset:3072 sc1\n"
"v_mfma_f32_16x16x32_bf16 v[108:111], a[156:159], a[224:227], v[108:111]\n"
"v_mfma_f32_16x16x32_bf16 v[112:115], a[156:159], a[228:231], v[112:115]\n"
"s_waitcnt vmcnt(23) lgkmcnt(0)\n"
"v_mfma_f32_16x16x32_bf16 v[32:35], a[104:107], a[232:235], v[32:35]\n"
"ds_read_b64_tr_b16 a[132:133], v12 offset:50176\n"
"s_add_u32 s72, s72, 4096\n"
"v_mfma_f32_16x16x32_bf16 v[36:39], a[104:107], a[236:239], v[36:39]\n"
"ds_read_b64_tr_b16 a[134:135], v12 offset:53760\n"
"v_mfma_f32_16x16x32_bf16 v[40:43], a[104:107], a[240:243], v[40:43]\n"
"ds_read_b64_tr_b16 a[136:137], v12 offset:50208\n"
"v_mfma_f32_16x16x32_bf16 v[52:55], a[108:111], a[240:243], v[52:55]\n"
"buffer_load_dwordx4 a[48:51], v27, s[36:39], s72 offen offset:0 sc1\n"
"v_mfma_f32_16x16x32_bf16 v[48:51], a[108:111], a[236:239], v[48:51]\n"
"ds_read_b64_tr_b16 a[138:139], v12 offset:53792\n"
"ds_read_b64_tr_b16 a[140:141], v12 offset:50240\n"
"v_mfma_f32_16x16x32_bf16 v[44:47], a[108:111], a[232:235], v[44:47]\n"
"buffer_load_dwordx4 a[52:55], v27, s[36:39], s72 offen offset:1024 sc1\n"
"v_mfma_f32_16x16x32_bf16 v[56:59], a[112:115], a[232:235], v[56:59]\n"
"ds_read_b64_tr_b16 a[142:143], v12 offset:53824\n"
"v_mfma_f32_16x16x32_bf16 v[60:63], a[112:115], a[236:239], v[60:63]\n"
"ds_read_b64_tr_b16 a[144:145], v12 offset:50272\n"
"v_mfma_f32_16x16x32_bf16 v[64:67], a[112:115], a[240:243], v[64:67]\n"
"buffer_load_dwordx4 a[56:59], v27, s[36:39], s72 offen offset:2048 sc1\n"
"v_mfma_f32_16x16x32_bf16 v[76:79], a[116:119], a[240:243], v[76:79]\n"
"ds_read_b64_tr_b16 a[146:147], v12 offset:53856\n"
"ds_read_b64_tr_b16 a[148:149], v12 offset:50304\n"
"v_mfma_f32_16x16x32_bf16 v[72:75], a[116:119], a[236:239], v[72:75]\n"
"buffer_load_dwordx4 a[60:63], v27, s[36:39], s72 offen offset:3072 sc1\n"
"v_mfma_f32_16x16x32_bf16 v[68:71], a[116:119], a[232:235], v[68:71]\n"
"ds_read_b64_tr_b16 a[150:151], v12 offset:53888\n"
"v_mfma_f32_16x16x32_bf16 v[80:83], a[120:123], a[232:235], v[80:83]\n"
"ds_read_b64_tr_b16 a[152:153], v12 offset:50336\n"
"v_mfma_f32_16x16x32_bf16 v[84:87], a[120:123], a[236:239], v[84:87]\n"
"s_add_u32 s72, s72, 4096\n"
"ds_read_b64_tr_b16 a[154:155], v12 offset:53920\n"
"v_mfma_f32_16x16x32_bf16 v[88:91], a[120:123], a[240:243], v[88:91]\n"
"ds_read_b64_tr_b16 a[156:157], v14 offset:50176\n"
"v_mfma_f32_16x16x32_bf16 v[100:103], a[124:127], a[240:243], v[100:103]\n"
"buffer_load_dwordx4 a[64:67], v27, s[36:39], s72 offen offset:0 sc1\n"
"v_mfma_f32_16x16x32_bf16 v[96:99], a[124:127], a[236:239], v[96:99]\n"
"ds_read_b64_tr_b16 a[158:159], v14 offset:53760\n"
"v_mfma_f32_16x16x32_bf16 v[92:95], a[124:127], a[232:235], v[92:95]\n"
"v_mfma_f32_16x16x32_bf16 v[104:107], a[128:131], a[232:235], v[104:107]\n"
"buffer_load_dwordx4 a[68:71], v27, s[36:39], s72 offen offset:1024 sc1\n"
"v_mfma_f32_16x16x32_bf16 v[108:111], a[128:131], a[236:239], v[108:111]\n"
"v_mfma_f32_16x16x32_bf16 v[112:115], a[128:131], a[240:243], v[112:115]\n"
"s_waitcnt vmcnt(23) lgkmcnt(0)\n"
"v_mfma_f32_16x16x32_bf16 v[32:35], a[132:135], a[244:247], v[32:35]\n"
"v_mfma_f32_16x16x32_bf16 v[36:39], a[132:135], a[248:251], v[36:39]\n"
"buffer_load_dwordx4 a[72:75], v27, s[36:39], s72 offen offset:2048 sc1\n"
"v_mfma_f32_16x16x32_bf16 v[40:43], a[132:135], a[252:255], v[40:43]\n"
"v_mfma_f32_16x16x32_bf16 v[52:55], a[136:139], a[252:255], v[52:55]\n"
"buffer_load_dwordx4 a[76:79], v27, s[36:39], s72 offen offset:3072 sc1\n"
"v_mfma_f32_16x16x32_bf16 v[48:51], a[136:139], a[248:251], v[48:51]\n"
"v_mfma_f32_16x16x32_bf16 v[44:47], a[136:139], a[244:247], v[44:47]\n"
"v_mfma_f32_16x16x32_bf16 v[56:59], a[140:143], a[244:247], v[56:59]\n"
"s_add_u32 s72, s72, 4096\n"
"v_mfma_f32_16x16x32_bf16 v[60:63], a[140:143], a[248:251], v[60:63]\n"
"v_mfma_f32_16x16x32_bf16 v[64:67], a[140:143], a[252:255], v[64:67]\n"
"v_mfma_f32_16x16x32_bf16 v[76:79], a[144:147], a[252:255], v[76:79]\n"
"buffer_load_dwordx4 a[80:83], v27, s[36:39], s72 offen offset:0 sc1\n"
"v_mfma_f32_16x16x32_bf16 v[72:75], a[144:147], a[248:251], v[72:75]\n"
"v_mfma_f32_16x16x32_bf16 v[68:71], a[144:147], a[244:247], v[68:71]\n"
"v_mfma_f32_16x16x32_bf16 v[80:83], a[148:151], a[244:247], v[80:83]\n"
"buffer_load_dwordx4 a[84:87], v27, s[36:39], s72 offen offset:1024 sc1\n"
"v_mfma_f32_16x16x32_bf16 v[84:87], a[148:151], a[248:251], v[84:87]\n"
"v_mfma_f32_16x16x32_bf16 v[88:91], a[148:151], a[252:255], v[88:91]\n"
"v_mfma_f32_16x16x32_bf16 v[100:103], a[152:155], a[252:255], v[100:103]\n"
"buffer_load_dwordx4 a[88:91], v27, s[36:39], s72 offen offset:2048 sc1\n"
"v_mfma_f32_16x16x32_bf16 v[96:99], a[152:155], a[248:251], v[96:99]\n"
"v_mfma_f32_16x16x32_bf16 v[92:95], a[152:155], a[244:247], v[92:95]\n"
"v_mfma_f32_16x16x32_bf16 v[104:107], a[156:159], a[244:247], v[104:107]\n"
"buffer_load_dwordx4 a[92:95], v27, s[36:39], s72 offen offset:3072 sc1\n"
"v_mfma_f32_16x16x32_bf16 v[108:111], a[156:159], a[248:251], v[108:111]\n"
"v_mfma_f32_16x16x32_bf16 v[112:115], a[156:159], a[252:255], v[112:115]\n"
"s_lshl_b32 s59, s58, 8\n"
"s_lshl_b32 s60, s56, 2\n"
"s_add_u32 s59, s59, s60\n"
"s_lshl_b32 s59, s59, 11\n"
"s_add_u32 s82, s59, 0x9d8000\n"
"s_lshl_b32 s59, s58, 7\n"
"s_add_u32 s59, s59, s60\n"
"s_add_u32 s79, s59, 0x2ae0000\n"
"s_lshr_b32 s59, s56, 1\n"
"s_lshl_b32 s59, s59, 19\n"
"s_add_u32 s6, s59, 393216\n"
"s_sub_u32 s59, s6, 4096\n"
"s_lshl_b32 s74, s11, 12\n"
"s_mov_b32 s83, s74\n"
"s_add_u32 s44, s14, s59\n"
"s_addc_u32 s45, s15, 0\n"
"s_and_b32 s45, s45, 0xffff\n"
"s_mov_b32 s46, 0x21000\n"
"s_mov_b32 s47, 0x20000\n"
"s_add_u32 s48, s16, s59\n"
"s_addc_u32 s49, s17, 0\n"
"s_and_b32 s49, s49, 0xffff\n"
"s_mov_b32 s50, 0x21000\n"
"s_mov_b32 s51, 0x20000\n"
"v_bfe_u32 v212, v0, 2, 3\n"
"v_mul_u32_u24 v212, 256, v212\n"
"v_lshrrev_b32 v213, 5, v0\n"
"v_lshl_add_u32 v212, v213, 6, v212\n"
"v_mov_b32 v213, v5\n"
"v_lshl_add_u32 v12, v213, 4, v212\n"
"v_add_u32 v12, 4096, v12\n"
"v_lshl_add_u32 v13, v213, 4, v212\n"
"v_add_u32 v13, 3200, v13\n"
"v_xor_b32 v213, 2, v213\n"
"v_lshl_add_u32 v14, v213, 4, v212\n"
"v_add_u32 v14, 4096, v14\n"
"v_lshl_add_u32 v15, v213, 4, v212\n"
"v_add_u32 v15, 3200, v15\n"
"v_bfe_u32 v212, v0, 2, 3\n"
"v_mul_u32_u24 v212, 256, v212\n"
"v_lshrrev_b32 v213, 5, v0\n"
"v_lshl_add_u32 v212, v213, 6, v212\n"
"v_bfe_u32 v213, v0, 4, 1\n"
"v_lshlrev_b32 v213, 1, v213\n"
"v_xor_b32 v213, v213, v5\n"
"v_lshl_add_u32 v16, v213, 4, v212\n"
"v_add_u32 v16, 4096, v16\n"
"v_lshl_add_u32 v17, v213, 4, v212\n"
"v_add_u32 v17, 3200, v17\n"
"v_lshl_add_u32 v18, v213, 4, v212\n"
"v_add_u32 v18, 4096, v18\n"
"v_lshl_add_u32 v19, v213, 4, v212\n"
"v_add_u32 v19, 3200, v19\n"
"v_lshlrev_b32 v20, 11, v2\n"
"v_lshl_add_u32 v20, v4, 6, v20\n"
"v_lshl_add_u32 v20, v5, 4, v20\n"
"v_add_u32 v20, 98304, v20\n"
"v_lshrrev_b32 v212, 3, v3\n"
"v_lshlrev_b32 v213, 1, v212\n"
"v_xor_b32 v213, v213, v2\n"
"v_lshlrev_b32 v212, 11, v212\n"
"v_lshl_add_u32 v212, v213, 4, v212\n"
"v_and_b32 v213, 7, v3\n"
"v_lshl_add_u32 v24, v213, 6, v212\n"
"v_lshrrev_b32 v212, 1, v2\n"
"v_lshlrev_b32 v212, 11, v212\n"
"v_and_b32 v213, 1, v2\n"
"v_lshl_add_u32 v214, v213, 2, v4\n"
"v_lshl_add_u32 v212, v214, 6, v212\n"
"v_lshl_add_u32 v212, v5, 3, v212\n"
"v_lshl_add_u32 v212, v213, 5, v212\n"
"v_add_u32 v25, 16384, v212\n"
"v_xor_b32 v26, 32, v25\n"
"v_mov_b32 v21, v24\n"
"v_add_u32 v22, 32768, v25\n"
"v_add_u32 v23, 32768, v26\n"
"s_mov_b32 s80, 0xbdd2d3e8\n"
"s_mov_b32 s81, 0xbdd2d3e8\n"
"v_mov_b32 v118, 0xc0135761\n"
"v_mov_b32 v119, 0xc0135761\n"
"v_pk_mul_f32 v[120:121], v[32:33], v[32:33]\n"
"v_pk_mul_f32 v[122:123], v[34:35], v[34:35]\n"
"v_pk_mul_f32 v[124:125], v[44:45], v[44:45]\n"
"v_pk_mul_f32 v[126:127], v[46:47], v[46:47]\n"
"v_pk_fma_f32 v[120:121], v[120:121], s[80:81], v[118:119]\n"
"v_pk_fma_f32 v[122:123], v[122:123], s[80:81], v[118:119]\n"
"v_pk_fma_f32 v[124:125], v[124:125], s[80:81], v[118:119]\n"
"v_pk_fma_f32 v[126:127], v[126:127], s[80:81], v[118:119]\n"
"v_pk_mul_f32 v[120:121], v[120:121], v[32:33]\n"
"v_pk_mul_f32 v[122:123], v[122:123], v[34:35]\n"
"v_pk_mul_f32 v[124:125], v[124:125], v[44:45]\n"
"v_pk_mul_f32 v[126:127], v[126:127], v[46:47]\n"
"v_exp_f32 v120, v120\n"
"v_exp_f32 v121, v121\n"
"v_exp_f32 v122, v122\n"
"v_exp_f32 v123, v123\n"
"v_exp_f32 v124, v124\n"
"v_exp_f32 v125, v125\n"
"v_exp_f32 v126, v126\n"
"v_exp_f32 v127, v127\n"
"v_pk_add_f32 v[120:121], v[120:121], 1.0 op_sel_hi:[1,0]\n"
"v_pk_add_f32 v[122:123], v[122:123], 1.0 op_sel_hi:[1,0]\n"
"v_pk_add_f32 v[124:125], v[124:125], 1.0 op_sel_hi:[1,0]\n"
"v_pk_add_f32 v[126:127], v[126:127], 1.0 op_sel_hi:[1,0]\n"
"v_rcp_f32 v120, v120\n"
"v_rcp_f32 v121, v121\n"
"v_rcp_f32 v122, v122\n"
"v_rcp_f32 v123, v123\n"
"v_rcp_f32 v124, v124\n"
"v_rcp_f32 v125, v125\n"
"v_rcp_f32 v126, v126\n"
"v_rcp_f32 v127, v127\n"
"v_pk_mul_f32 v[120:121], v[120:121], v[32:33]\n"
"v_pk_mul_f32 v[122:123], v[122:123], v[34:35]\n"
"v_pk_mul_f32 v[124:125], v[124:125], v[44:45]\n"
"v_pk_mul_f32 v[126:127], v[126:127], v[46:47]\n"
"v_cvt_pk_bf16_f32 v140, v120, v121\n"
"v_cvt_pk_bf16_f32 v141, v122, v123\n"
"v_cvt_pk_bf16_f32 v142, v124, v125\n"
"v_cvt_pk_bf16_f32 v143, v126, v127\n"
"buffer_store_dwordx2 v[140:141], v251, s[36:39], s85 offen offset:0 sc1\n"
"buffer_store_dwordx2 v[142:143], v251, s[36:39], s85 offen offset:512 sc1\n"
"v_pk_mul_f32 v[120:121], v[36:37], v[36:37]\n"
"v_pk_mul_f32 v[122:123], v[38:39], v[38:39]\n"
"v_pk_mul_f32 v[124:125], v[48:49], v[48:49]\n"
"v_pk_mul_f32 v[126:127], v[50:51], v[50:51]\n"
"v_pk_fma_f32 v[120:121], v[120:121], s[80:81], v[118:119]\n"
"v_pk_fma_f32 v[122:123], v[122:123], s[80:81], v[118:119]\n"
"v_pk_fma_f32 v[124:125], v[124:125], s[80:81], v[118:119]\n"
"v_pk_fma_f32 v[126:127], v[126:127], s[80:81], v[118:119]\n"
"v_pk_mul_f32 v[120:121], v[120:121], v[36:37]\n"
"v_pk_mul_f32 v[122:123], v[122:123], v[38:39]\n"
"v_pk_mul_f32 v[124:125], v[124:125], v[48:49]\n"
"v_pk_mul_f32 v[126:127], v[126:127], v[50:51]\n"
"v_exp_f32 v120, v120\n"
"v_exp_f32 v121, v121\n"
"v_exp_f32 v122, v122\n"
"v_exp_f32 v123, v123\n"
"v_exp_f32 v124, v124\n"
"v_exp_f32 v125, v125\n"
"v_exp_f32 v126, v126\n"
"v_exp_f32 v127, v127\n"
"v_pk_add_f32 v[120:121], v[120:121], 1.0 op_sel_hi:[1,0]\n"
"v_pk_add_f32 v[122:123], v[122:123], 1.0 op_sel_hi:[1,0]\n"
"v_pk_add_f32 v[124:125], v[124:125], 1.0 op_sel_hi:[1,0]\n"
"v_pk_add_f32 v[126:127], v[126:127], 1.0 op_sel_hi:[1,0]\n"
"v_rcp_f32 v120, v120\n"
"v_rcp_f32 v121, v121\n"
"v_rcp_f32 v122, v122\n"
"v_rcp_f32 v123, v123\n"
"v_rcp_f32 v124, v124\n"
"v_rcp_f32 v125, v125\n"
"v_rcp_f32 v126, v126\n"
"v_rcp_f32 v127, v127\n"
"v_pk_mul_f32 v[120:121], v[120:121], v[36:37]\n"
"v_pk_mul_f32 v[122:123], v[122:123], v[38:39]\n"
"v_pk_mul_f32 v[124:125], v[124:125], v[48:49]\n"
"v_pk_mul_f32 v[126:127], v[126:127], v[50:51]\n"
"v_cvt_pk_bf16_f32 v144, v120, v121\n"
"v_cvt_pk_bf16_f32 v145, v122, v123\n"
"v_cvt_pk_bf16_f32 v146, v124, v125\n"
"v_cvt_pk_bf16_f32 v147, v126, v127\n"
"buffer_store_dwordx2 v[144:145], v251, s[36:39], s85 offen offset:1024 sc1\n"
"buffer_store_dwordx2 v[146:147], v251, s[36:39], s85 offen offset:1536 sc1\n"
"v_pk_mul_f32 v[120:121], v[40:41], v[40:41]\n"
"v_pk_mul_f32 v[122:123], v[42:43], v[42:43]\n"
"v_pk_mul_f32 v[124:125], v[52:53], v[52:53]\n"
"v_pk_mul_f32 v[126:127], v[54:55], v[54:55]\n"
"v_pk_fma_f32 v[120:121], v[120:121], s[80:81], v[118:119]\n"
"v_pk_fma_f32 v[122:123], v[122:123], s[80:81], v[118:119]\n"
"v_pk_fma_f32 v[124:125], v[124:125], s[80:81], v[118:119]\n"
"v_pk_fma_f32 v[126:127], v[126:127], s[80:81], v[118:119]\n"
"v_pk_mul_f32 v[120:121], v[120:121], v[40:41]\n"
"v_pk_mul_f32 v[122:123], v[122:123], v[42:43]\n"
"v_pk_mul_f32 v[124:125], v[124:125], v[52:53]\n"
"v_pk_mul_f32 v[126:127], v[126:127], v[54:55]\n"
"v_exp_f32 v120, v120\n"
"v_exp_f32 v121, v121\n"
"v_exp_f32 v122, v122\n"
"v_exp_f32 v123, v123\n"
"v_exp_f32 v124, v124\n"
"v_exp_f32 v125, v125\n"
"v_exp_f32 v126, v126\n"
"v_exp_f32 v127, v127\n"
"v_pk_add_f32 v[120:121], v[120:121], 1.0 op_sel_hi:[1,0]\n"
"v_pk_add_f32 v[122:123], v[122:123], 1.0 op_sel_hi:[1,0]\n"
"v_pk_add_f32 v[124:125], v[124:125], 1.0 op_sel_hi:[1,0]\n"
"v_pk_add_f32 v[126:127], v[126:127], 1.0 op_sel_hi:[1,0]\n"
"v_rcp_f32 v120, v120\n"
"v_rcp_f32 v121, v121\n"
"v_rcp_f32 v122, v122\n"
"v_rcp_f32 v123, v123\n"
"v_rcp_f32 v124, v124\n"
"v_rcp_f32 v125, v125\n"
"v_rcp_f32 v126, v126\n"
"v_rcp_f32 v127, v127\n"
"v_pk_mul_f32 v[120:121], v[120:121], v[40:41]\n"
"v_pk_mul_f32 v[122:123], v[122:123], v[42:43]\n"
"v_pk_mul_f32 v[124:125], v[124:125], v[52:53]\n"
"v_pk_mul_f32 v[126:127], v[126:127], v[54:55]\n"
"v_cvt_pk_bf16_f32 v148, v120, v121\n"
"v_cvt_pk_bf16_f32 v149, v122, v123\n"
"v_cvt_pk_bf16_f32 v150, v124, v125\n"
"v_cvt_pk_bf16_f32 v151, v126, v127\n"
"buffer_store_dwordx2 v[148:149], v251, s[36:39], s85 offen offset:2048 sc1\n"
"buffer_store_dwordx2 v[150:151], v251, s[36:39], s85 offen offset:2560 sc1\n"
"s_add_u32 s85, s85, 36864\n"
"v_pk_mul_f32 v[120:121], v[56:57], v[56:57]\n"
"v_pk_mul_f32 v[122:123], v[58:59], v[58:59]\n"
"v_pk_mul_f32 v[124:125], v[68:69], v[68:69]\n"
"v_pk_mul_f32 v[126:127], v[70:71], v[70:71]\n"
"v_pk_fma_f32 v[120:121], v[120:121], s[80:81], v[118:119]\n"
"v_pk_fma_f32 v[122:123], v[122:123], s[80:81], v[118:119]\n"
"v_pk_fma_f32 v[124:125], v[124:125], s[80:81], v[118:119]\n"
"v_pk_fma_f32 v[126:127], v[126:127], s[80:81], v[118:119]\n"
"v_pk_mul_f32 v[120:121], v[120:121], v[56:57]\n"
"v_pk_mul_f32 v[122:123], v[122:123], v[58:59]\n"
"v_pk_mul_f32 v[124:125], v[124:125], v[68:69]\n"
"v_pk_mul_f32 v[126:127], v[126:127], v[70:71]\n"
"v_exp_f32 v120, v120\n"
"v_exp_f32 v121, v121\n"
"v_exp_f32 v122, v122\n"
"v_exp_f32 v123, v123\n"
"v_exp_f32 v124, v124\n"
"v_exp_f32 v125, v125\n"
"v_exp_f32 v126, v126\n"
"v_exp_f32 v127, v127\n"
"v_pk_add_f32 v[120:121], v[120:121], 1.0 op_sel_hi:[1,0]\n"
"v_pk_add_f32 v[122:123], v[122:123], 1.0 op_sel_hi:[1,0]\n"
"v_pk_add_f32 v[124:125], v[124:125], 1.0 op_sel_hi:[1,0]\n"
"v_pk_add_f32 v[126:127], v[126:127], 1.0 op_sel_hi:[1,0]\n"
"v_rcp_f32 v120, v120\n"
"v_rcp_f32 v121, v121\n"
"v_rcp_f32 v122, v122\n"
"v_rcp_f32 v123, v123\n"
"v_rcp_f32 v124, v124\n"
"v_rcp_f32 v125, v125\n"
"v_rcp_f32 v126, v126\n"
"v_rcp_f32 v127, v127\n"
"v_pk_mul_f32 v[120:121], v[120:121], v[56:57]\n"
"v_pk_mul_f32 v[122:123], v[122:123], v[58:59]\n"
"v_pk_mul_f32 v[124:125], v[124:125], v[68:69]\n"
"v_pk_mul_f32 v[126:127], v[126:127], v[70:71]\n"
"v_cvt_pk_bf16_f32 v152, v120, v121\n"
"v_cvt_pk_bf16_f32 v153, v122, v123\n"
"v_cvt_pk_bf16_f32 v154, v124, v125\n"
"v_cvt_pk_bf16_f32 v155, v126, v127\n"
"buffer_store_dwordx2 v[152:153], v251, s[36:39], s85 offen offset:0 sc1\n"
"buffer_store_dwordx2 v[154:155], v251, s[36:39], s85 offen offset:512 sc1\n"
"v_pk_mul_f32 v[120:121], v[60:61], v[60:61]\n"
"v_pk_mul_f32 v[122:123], v[62:63], v[62:63]\n"
"v_pk_mul_f32 v[124:125], v[72:73], v[72:73]\n"
"v_pk_mul_f32 v[126:127], v[74:75], v[74:75]\n"
"v_pk_fma_f32 v[120:121], v[120:121], s[80:81], v[118:119]\n"
"v_pk_fma_f32 v[122:123], v[122:123], s[80:81], v[118:119]\n"
"v_pk_fma_f32 v[124:125], v[124:125], s[80:81], v[118:119]\n"
"v_pk_fma_f32 v[126:127], v[126:127], s[80:81], v[118:119]\n"
"v_pk_mul_f32 v[120:121], v[120:121], v[60:61]\n"
"v_pk_mul_f32 v[122:123], v[122:123], v[62:63]\n"
"v_pk_mul_f32 v[124:125], v[124:125], v[72:73]\n"
"v_pk_mul_f32 v[126:127], v[126:127], v[74:75]\n"
"v_exp_f32 v120, v120\n"
"v_exp_f32 v121, v121\n"
"v_exp_f32 v122, v122\n"
"v_exp_f32 v123, v123\n"
"v_exp_f32 v124, v124\n"
"v_exp_f32 v125, v125\n"
"v_exp_f32 v126, v126\n"
"v_exp_f32 v127, v127\n"
"v_pk_add_f32 v[120:121], v[120:121], 1.0 op_sel_hi:[1,0]\n"
"v_pk_add_f32 v[122:123], v[122:123], 1.0 op_sel_hi:[1,0]\n"
"v_pk_add_f32 v[124:125], v[124:125], 1.0 op_sel_hi:[1,0]\n"
"v_pk_add_f32 v[126:127], v[126:127], 1.0 op_sel_hi:[1,0]\n"
"v_rcp_f32 v120, v120\n"
"v_rcp_f32 v121, v121\n"
"v_rcp_f32 v122, v122\n"
"v_rcp_f32 v123, v123\n"
"v_rcp_f32 v124, v124\n"
"v_rcp_f32 v125, v125\n"
"v_rcp_f32 v126, v126\n"
"v_rcp_f32 v127, v127\n"
"v_pk_mul_f32 v[120:121], v[120:121], v[60:61]\n"
"v_pk_mul_f32 v[122:123], v[122:123], v[62:63]\n"
"v_pk_mul_f32 v[124:125], v[124:125], v[72:73]\n"
"v_pk_mul_f32 v[126:127], v[126:127], v[74:75]\n"
"v_cvt_pk_bf16_f32 v156, v120, v121\n"
"v_cvt_pk_bf16_f32 v157, v122, v123\n"
"v_cvt_pk_bf16_f32 v158, v124, v125\n"
"v_cvt_pk_bf16_f32 v159, v126, v127\n"
"buffer_store_dwordx2 v[156:157], v251, s[36:39], s85 offen offset:1024 sc1\n"
"buffer_store_dwordx2 v[158:159], v251, s[36:39], s85 offen offset:1536 sc1\n"
"v_pk_mul_f32 v[120:121], v[64:65], v[64:65]\n"
"v_pk_mul_f32 v[122:123], v[66:67], v[66:67]\n"
"v_pk_mul_f32 v[124:125], v[76:77], v[76:77]\n"
"v_pk_mul_f32 v[126:127], v[78:79], v[78:79]\n"
"v_pk_fma_f32 v[120:121], v[120:121], s[80:81], v[118:119]\n"
"v_pk_fma_f32 v[122:123], v[122:123], s[80:81], v[118:119]\n"
"v_pk_fma_f32 v[124:125], v[124:125], s[80:81], v[118:119]\n"
"v_pk_fma_f32 v[126:127], v[126:127], s[80:81], v[118:119]\n"
"v_pk_mul_f32 v[120:121], v[120:121], v[64:65]\n"
"v_pk_mul_f32 v[122:123], v[122:123], v[66:67]\n"
"v_pk_mul_f32 v[124:125], v[124:125], v[76:77]\n"
"v_pk_mul_f32 v[126:127], v[126:127], v[78:79]\n"
"v_exp_f32 v120, v120\n"
"v_exp_f32 v121, v121\n"
"v_exp_f32 v122, v122\n"
"v_exp_f32 v123, v123\n"
"v_exp_f32 v124, v124\n"
"v_exp_f32 v125, v125\n"
"v_exp_f32 v126, v126\n"
"v_exp_f32 v127, v127\n"
"v_pk_add_f32 v[120:121], v[120:121], 1.0 op_sel_hi:[1,0]\n"
"v_pk_add_f32 v[122:123], v[122:123], 1.0 op_sel_hi:[1,0]\n"
"v_pk_add_f32 v[124:125], v[124:125], 1.0 op_sel_hi:[1,0]\n"
"v_pk_add_f32 v[126:127], v[126:127], 1.0 op_sel_hi:[1,0]\n"
"v_rcp_f32 v120, v120\n"
"v_rcp_f32 v121, v121\n"
"v_rcp_f32 v122, v122\n"
"v_rcp_f32 v123, v123\n"
"v_rcp_f32 v124, v124\n"
"v_rcp_f32 v125, v125\n"
"v_rcp_f32 v126, v126\n"
"v_rcp_f32 v127, v127\n"
"v_pk_mul_f32 v[120:121], v[120:121], v[64:65]\n"
"v_pk_mul_f32 v[122:123], v[122:123], v[66:67]\n"
"v_pk_mul_f32 v[124:125], v[124:125], v[76:77]\n"
"v_pk_mul_f32 v[126:127], v[126:127], v[78:79]\n"
"v_cvt_pk_bf16_f32 v160, v120, v121\n"
"v_cvt_pk_bf16_f32 v161, v122, v123\n"
"v_cvt_pk_bf16_f32 v162, v124, v125\n"
"v_cvt_pk_bf16_f32 v163, v126, v127\n"
"buffer_store_dwordx2 v[160:161], v251, s[36:39], s85 offen offset:2048 sc1\n"
"buffer_store_dwordx2 v[162:163], v251, s[36:39], s85 offen offset:2560 sc1\n"
"s_add_u32 s85, s85, 36864\n"
"v_pk_mul_f32 v[120:121], v[80:81], v[80:81]\n"
"v_pk_mul_f32 v[122:123], v[82:83], v[82:83]\n"
"v_pk_mul_f32 v[124:125], v[92:93], v[92:93]\n"
"v_pk_mul_f32 v[126:127], v[94:95], v[94:95]\n"
"v_pk_fma_f32 v[120:121], v[120:121], s[80:81], v[118:119]\n"
"v_pk_fma_f32 v[122:123], v[122:123], s[80:81], v[118:119]\n"
"v_pk_fma_f32 v[124:125], v[124:125], s[80:81], v[118:119]\n"
"v_pk_fma_f32 v[126:127], v[126:127], s[80:81], v[118:119]\n"
"v_pk_mul_f32 v[120:121], v[120:121], v[80:81]\n"
"v_pk_mul_f32 v[122:123], v[122:123], v[82:83]\n"
"v_pk_mul_f32 v[124:125], v[124:125], v[92:93]\n"
"v_pk_mul_f32 v[126:127], v[126:127], v[94:95]\n"
"v_exp_f32 v120, v120\n"
"v_exp_f32 v121, v121\n"
"v_exp_f32 v122, v122\n"
"v_exp_f32 v123, v123\n"
"v_exp_f32 v124, v124\n"
"v_exp_f32 v125, v125\n"
"v_exp_f32 v126, v126\n"
"v_exp_f32 v127, v127\n"
"v_pk_add_f32 v[120:121], v[120:121], 1.0 op_sel_hi:[1,0]\n"
"v_pk_add_f32 v[122:123], v[122:123], 1.0 op_sel_hi:[1,0]\n"
"v_pk_add_f32 v[124:125], v[124:125], 1.0 op_sel_hi:[1,0]\n"
"v_pk_add_f32 v[126:127], v[126:127], 1.0 op_sel_hi:[1,0]\n"
"v_rcp_f32 v120, v120\n"
"v_rcp_f32 v121, v121\n"
"v_rcp_f32 v122, v122\n"
"v_rcp_f32 v123, v123\n"
"v_rcp_f32 v124, v124\n"
"v_rcp_f32 v125, v125\n"
"v_rcp_f32 v126, v126\n"
"v_rcp_f32 v127, v127\n"
"v_pk_mul_f32 v[120:121], v[120:121], v[80:81]\n"
"v_pk_mul_f32 v[122:123], v[122:123], v[82:83]\n"
"v_pk_mul_f32 v[124:125], v[124:125], v[92:93]\n"
"v_pk_mul_f32 v[126:127], v[126:127], v[94:95]\n"
"v_cvt_pk_bf16_f32 v164, v120, v121\n"
"v_cvt_pk_bf16_f32 v165, v122, v123\n"
"v_cvt_pk_bf16_f32 v166, v124, v125\n"
"v_cvt_pk_bf16_f32 v167, v126, v127\n"
"buffer_store_dwordx2 v[164:165], v251, s[36:39], s85 offen offset:0 sc1\n"
"buffer_store_dwordx2 v[166:167], v251, s[36:39], s85 offen offset:512 sc1\n"
"v_pk_mul_f32 v[120:121], v[84:85], v[84:85]\n"
"v_pk_mul_f32 v[122:123], v[86:87], v[86:87]\n"
"v_pk_mul_f32 v[124:125], v[96:97], v[96:97]\n"
"v_pk_mul_f32 v[126:127], v[98:99], v[98:99]\n"
"v_pk_fma_f32 v[120:121], v[120:121], s[80:81], v[118:119]\n"
"v_pk_fma_f32 v[122:123], v[122:123], s[80:81], v[118:119]\n"
"v_pk_fma_f32 v[124:125], v[124:125], s[80:81], v[118:119]\n"
"v_pk_fma_f32 v[126:127], v[126:127], s[80:81], v[118:119]\n"
"v_pk_mul_f32 v[120:121], v[120:121], v[84:85]\n"
"v_pk_mul_f32 v[122:123], v[122:123], v[86:87]\n"
"v_pk_mul_f32 v[124:125], v[124:125], v[96:97]\n"
"v_pk_mul_f32 v[126:127], v[126:127], v[98:99]\n"
"v_exp_f32 v120, v120\n"
"v_exp_f32 v121, v121\n"
"v_exp_f32 v122, v122\n"
"v_exp_f32 v123, v123\n"
"v_exp_f32 v124, v124\n"
"v_exp_f32 v125, v125\n"
"v_exp_f32 v126, v126\n"
"v_exp_f32 v127, v127\n"
"v_pk_add_f32 v[120:121], v[120:121], 1.0 op_sel_hi:[1,0]\n"
"v_pk_add_f32 v[122:123], v[122:123], 1.0 op_sel_hi:[1,0]\n"
"v_pk_add_f32 v[124:125], v[124:125], 1.0 op_sel_hi:[1,0]\n"
"v_pk_add_f32 v[126:127], v[126:127], 1.0 op_sel_hi:[1,0]\n"
"v_rcp_f32 v120, v120\n"
"v_rcp_f32 v121, v121\n"
"v_rcp_f32 v122, v122\n"
"v_rcp_f32 v123, v123\n"
"v_rcp_f32 v124, v124\n"
"v_rcp_f32 v125, v125\n"
"v_rcp_f32 v126, v126\n"
"v_rcp_f32 v127, v127\n"
"v_pk_mul_f32 v[120:121], v[120:121], v[84:85]\n"
"v_pk_mul_f32 v[122:123], v[122:123], v[86:87]\n"
"v_pk_mul_f32 v[124:125], v[124:125], v[96:97]\n"
"v_pk_mul_f32 v[126:127], v[126:127], v[98:99]\n"
"v_cvt_pk_bf16_f32 v168, v120, v121\n"
"v_cvt_pk_bf16_f32 v169, v122, v123\n"
"v_cvt_pk_bf16_f32 v170, v124, v125\n"
"v_cvt_pk_bf16_f32 v171, v126, v127\n"
"buffer_store_dwordx2 v[168:169], v251, s[36:39], s85 offen offset:1024 sc1\n"
"buffer_store_dwordx2 v[170:171], v251, s[36:39], s85 offen offset:1536 sc1\n"
"v_pk_mul_f32 v[120:121], v[88:89], v[88:89]\n"
"v_pk_mul_f32 v[122:123], v[90:91], v[90:91]\n"
"v_pk_mul_f32 v[124:125], v[100:101], v[100:101]\n"
"v_pk_mul_f32 v[126:127], v[102:103], v[102:103]\n"
"v_pk_fma_f32 v[120:121], v[120:121], s[80:81], v[118:119]\n"
"v_pk_fma_f32 v[122:123], v[122:123], s[80:81], v[118:119]\n"
"v_pk_fma_f32 v[124:125], v[124:125], s[80:81], v[118:119]\n"
"v_pk_fma_f32 v[126:127], v[126:127], s[80:81], v[118:119]\n"
"v_pk_mul_f32 v[120:121], v[120:121], v[88:89]\n"
"v_pk_mul_f32 v[122:123], v[122:123], v[90:91]\n"
"v_pk_mul_f32 v[124:125], v[124:125], v[100:101]\n"
"v_pk_mul_f32 v[126:127], v[126:127], v[102:103]\n"
"v_exp_f32 v120, v120\n"
"v_exp_f32 v121, v121\n"
"v_exp_f32 v122, v122\n"
"v_exp_f32 v123, v123\n"
"v_exp_f32 v124, v124\n"
"v_exp_f32 v125, v125\n"
"v_exp_f32 v126, v126\n"
"v_exp_f32 v127, v127\n"
"v_pk_add_f32 v[120:121], v[120:121], 1.0 op_sel_hi:[1,0]\n"
"v_pk_add_f32 v[122:123], v[122:123], 1.0 op_sel_hi:[1,0]\n"
"v_pk_add_f32 v[124:125], v[124:125], 1.0 op_sel_hi:[1,0]\n"
"v_pk_add_f32 v[126:127], v[126:127], 1.0 op_sel_hi:[1,0]\n"
"v_rcp_f32 v120, v120\n"
"v_rcp_f32 v121, v121\n"
"v_rcp_f32 v122, v122\n"
"v_rcp_f32 v123, v123\n"
"v_rcp_f32 v124, v124\n"
"v_rcp_f32 v125, v125\n"
"v_rcp_f32 v126, v126\n"
"v_rcp_f32 v127, v127\n"
"v_pk_mul_f32 v[120:121], v[120:121], v[88:89]\n"
"v_pk_mul_f32 v[122:123], v[122:123], v[90:91]\n"
"v_pk_mul_f32 v[124:125], v[124:125], v[100:101]\n"
"v_pk_mul_f32 v[126:127], v[126:127], v[102:103]\n"
"v_cvt_pk_bf16_f32 v172, v120, v121\n"
"v_cvt_pk_bf16_f32 v173, v122, v123\n"
"v_cvt_pk_bf16_f32 v174, v124, v125\n"
"v_cvt_pk_bf16_f32 v175, v126, v127\n"
"buffer_store_dwordx2 v[172:173], v251, s[36:39], s85 offen offset:2048 sc1\n"
"buffer_store_dwordx2 v[174:175], v251, s[36:39], s85 offen offset:2560 sc1\n"
"v_pk_mul_f32 v[120:121], v[104:105], v[104:105]\n"
"v_pk_mul_f32 v[122:123], v[106:107], v[106:107]\n"
"v_pk_mul_f32 v[124:125], v[108:109], v[108:109]\n"
"v_pk_mul_f32 v[126:127], v[110:111], v[110:111]\n"
"v_pk_mul_f32 v[128:129], v[112:113], v[112:113]\n"
"v_pk_mul_f32 v[130:131], v[114:115], v[114:115]\n"
"v_pk_fma_f32 v[120:121], v[120:121], s[80:81], v[118:119]\n"
"v_pk_fma_f32 v[122:123], v[122:123], s[80:81], v[118:119]\n"
"v_pk_fma_f32 v[124:125], v[124:125], s[80:81], v[118:119]\n"
"v_pk_fma_f32 v[126:127], v[126:127], s[80:81], v[118:119]\n"
"v_pk_fma_f32 v[128:129], v[128:129], s[80:81], v[118:119]\n"
"v_pk_fma_f32 v[130:131], v[130:131], s[80:81], v[118:119]\n"
"v_pk_mul_f32 v[120:121], v[120:121], v[104:105]\n"
"v_pk_mul_f32 v[122:123], v[122:123], v[106:107]\n"
"v_pk_mul_f32 v[124:125], v[124:125], v[108:109]\n"
"v_pk_mul_f32 v[126:127], v[126:127], v[110:111]\n"
"v_pk_mul_f32 v[128:129], v[128:129], v[112:113]\n"
"v_pk_mul_f32 v[130:131], v[130:131], v[114:115]\n"
"v_exp_f32 v120, v120\n"
"v_exp_f32 v121, v121\n"
"v_exp_f32 v122, v122\n"
"v_exp_f32 v123, v123\n"
"v_exp_f32 v124, v124\n"
"v_exp_f32 v125, v125\n"
"v_exp_f32 v126, v126\n"
"v_exp_f32 v127, v127\n"
"v_exp_f32 v128, v128\n"
"v_exp_f32 v129, v129\n"
"v_exp_f32 v130, v130\n"
"v_exp_f32 v131, v131\n"
"v_pk_add_f32 v[120:121], v[120:121], 1.0 op_sel_hi:[1,0]\n"
"v_pk_add_f32 v[122:123], v[122:123], 1.0 op_sel_hi:[1,0]\n"
"v_pk_add_f32 v[124:125], v[124:125], 1.0 op_sel_hi:[1,0]\n"
"v_pk_add_f32 v[126:127], v[126:127], 1.0 op_sel_hi:[1,0]\n"
"v_pk_add_f32 v[128:129], v[128:129], 1.0 op_sel_hi:[1,0]\n"
"v_pk_add_f32 v[130:131], v[130:131], 1.0 op_sel_hi:[1,0]\n"
"v_rcp_f32 v120, v120\n"
"v_rcp_f32 v121, v121\n"
"v_rcp_f32 v122, v122\n"
"v_rcp_f32 v123, v123\n"
"v_rcp_f32 v124, v124\n"
"v_rcp_f32 v125, v125\n"
"v_rcp_f32 v126, v126\n"
"v_rcp_f32 v127, v127\n"
"v_rcp_f32 v128, v128\n"
"v_rcp_f32 v129, v129\n"
"v_rcp_f32 v130, v130\n"
"v_rcp_f32 v131, v131\n"
"v_pk_mul_f32 v[120:121], v[120:121], v[104:105]\n"
"v_pk_mul_f32 v[122:123], v[122:123], v[106:107]\n"
"v_pk_mul_f32 v[124:125], v[124:125], v[108:109]\n"
"v_pk_mul_f32 v[126:127], v[126:127], v[110:111]\n"
"v_pk_mul_f32 v[128:129], v[128:129], v[112:113]\n"
"v_pk_mul_f32 v[130:131], v[130:131], v[114:115]\n"
"v_cvt_pk_bf16_f32 v176, v120, v121\n"
"v_cvt_pk_bf16_f32 v177, v122, v123\n"
"v_cvt_pk_bf16_f32 v178, v124, v125\n"
"v_cvt_pk_bf16_f32 v179, v126, v127\n"
"v_cvt_pk_bf16_f32 v180, v128, v129\n"
"v_cvt_pk_bf16_f32 v181, v130, v131\n"
"buffer_store_dwordx2 v[176:177], v251, s[36:39], s86 offen offset:0 sc1\n"
"buffer_store_dwordx2 v[178:179], v251, s[36:39], s86 offen offset:1024 sc1\n"
"buffer_store_dwordx2 v[180:181], v251, s[36:39], s86 offen offset:2048 sc1\n"
"s_waitcnt vmcnt(21)\n"
"s_add_u32 s72, s72, 4096\n"
"buffer_load_dwordx4 a[96:99], v27, s[36:39], s72 offen offset:0 sc1\n"
"buffer_load_dwordx4 a[100:103], v27, s[36:39], s72 offen offset:1024 sc1\n"
"buffer_load_dwordx4 a[104:107], v27, s[36:39], s72 offen offset:2048 sc1\n"
"buffer_load_dwordx4 a[108:111], v27, s[36:39], s72 offen offset:3072 sc1\n"
"s_add_u32 s72, s72, 4096\n"
"buffer_load_dwordx4 a[112:115], v27, s[36:39], s72 offen offset:0 sc1\n"
"buffer_load_dwordx4 a[116:119], v27, s[36:39], s72 offen offset:1024 sc1\n"
"s_branch L_qp\n"
"L_bpro:\n"
"buffer_load_dwordx4 a[0:3], v27, s[36:39], s72 offen offset:0 sc1\n"
"buffer_load_dwordx4 a[4:7], v27, s[36:39], s72 offen offset:1024 sc1\n"
"buffer_load_dwordx4 a[8:11], v27, s[36:39], s72 offen offset:2048 sc1\n"
"buffer_load_dwordx4 a[12:15], v27, s[36:39], s72 offen offset:3072 sc1\n"
"s_add_u32 s72, s72, 4096\n"
"buffer_load_dwordx4 a[16:19], v27, s[36:39], s72 offen offset:0 sc1\n"
"buffer_load_dwordx4 a[20:23], v27, s[36:39], s72 offen offset:1024 sc1\n"
"buffer_load_dwordx4 a[24:27], v27, s[36:39], s72 offen offset:2048 sc1\n"
"buffer_load_dwordx4 a[28:31], v27, s[36:39], s72 offen offset:3072 sc1\n"
"s_add_u32 s72, s72, 4096\n"
"buffer_load_dwordx4 a[32:35], v27, s[36:39], s72 offen offset:0 sc1\n"
"buffer_load_dwordx4 a[36:39], v27, s[36:39], s72 offen offset:1024 sc1\n"
"buffer_load_dwordx4 a[40:43], v27, s[36:39], s72 offen offset:2048 sc1\n"
"buffer_load_dwordx4 a[44:47], v27, s[36:39], s72 offen offset:3072 sc1\n"
"s_add_u32 s72, s72, 4096\n"
"buffer_load_dwordx4 a[48:51], v27, s[36:39], s72 offen offset:0 sc1\n"
"buffer_load_dwordx4 a[52:55], v27, s[36:39], s72 offen offset:1024 sc1\n"
"buffer_load_dwordx4 a[56:59], v27, s[36:39], s72 offen offset:2048 sc1\n"
"buffer_load_dwordx4 a[60:63], v27, s[36:39], s72 offen offset:3072 sc1\n"
"s_add_u32 s72, s72, 4096\n"
"buffer_load_dwordx4 a[64:67], v27, s[36:39], s72 offen offset:0 sc1\n"
"buffer_load_dwordx4 a[68:71], v27, s[36:39], s72 offen offset:1024 sc1\n"
"buffer_load_dwordx4 a[72:75], v27, s[36:39], s72 offen offset:2048 sc1\n"
"buffer_load_dwordx4 a[76:79], v27, s[36:39], s72 offen offset:3072 sc1\n"
"s_add_u32 s72, s72, 4096\n"
"buffer_load_dwordx4 a[80:83], v27, s[36:39], s72 offen offset:0 sc1\n"
"buffer_load_dwordx4 a[84:87], v27, s[36:39], s72 offen offset:1024 sc1\n"
"buffer_load_dwordx4 a[88:91], v27, s[36:39], s72 offen offset:2048 sc1\n"
"buffer_load_dwordx4 a[92:95], v27, s[36:39], s72 offen offset:3072 sc1\n"
"s_add_u32 s72, s72, 4096\n"
"buffer_load_dwordx4 a[96:99], v27, s[36:39], s72 offen offset:0 sc1\n"
"buffer_load_dwordx4 a[100:103], v27, s[36:39], s72 offen offset:1024 sc1\n"
"buffer_load_dwordx4 a[104:107], v27, s[36:39], s72 offen offset:2048 sc1\n"
"buffer_load_dwordx4 a[108:111], v27, s[36:39], s72 offen offset:3072 sc1\n"
"s_add_u32 s72, s72, 4096\n"
"buffer_load_dwordx4 a[112:115], v27, s[36:39], s72 offen offset:0 sc1\n"
"buffer_load_dwordx4 a[116:119], v27, s[36:39], s72 offen offset:1024 sc1\n"
"s_waitcnt vmcnt(0)\n"
"L_qp:\n"
"s_nop 0\n"
"s_barrier\n"
"ds_read_b64_tr_b16 a[144:145], v20 offset:0\n"
"ds_read_b64_tr_b16 a[146:147], v20 offset:256\n"
"ds_read_b64_tr_b16 a[148:149], v20 offset:8\n"
"ds_read_b64_tr_b16 a[150:151], v20 offset:264\n"
"ds_read_b64_tr_b16 a[152:153], v20 offset:512\n"
"ds_read_b64_tr_b16 a[154:155], v20 offset:768\n"
"ds_read_b64_tr_b16 a[156:157], v20 offset:520\n"
"ds_read_b64_tr_b16 a[158:159], v20 offset:776\n"
"ds_read_b64_tr_b16 a[160:161], v20 offset:1024\n"
"ds_read_b64_tr_b16 a[162:163], v20 offset:1280\n"
"ds_read_b64_tr_b16 a[164:165], v20 offset:1032\n"
"ds_read_b64_tr_b16 a[166:167], v20 offset:1288\n"
"ds_read_b64_tr_b16 a[168:169], v20 offset:1536\n"
"ds_read_b64_tr_b16 a[170:171], v20 offset:1792\n"
"ds_read_b64_tr_b16 a[172:173], v20 offset:1544\n"
"ds_read_b64_tr_b16 a[174:175], v20 offset:1800\n"
"s_waitcnt lgkmcnt(0)\n"
"v_mfma_f32_16x16x32_bf16 v[96:99], a[144:147], a[0:3], 0\n"
"ds_read_b64_tr_b16 a[176:177], v20 offset:8192\n"
"v_mfma_f32_16x16x32_bf16 v[100:103], a[144:147], a[4:7], 0\n"
"buffer_load_dwordx4 a[120:123], v27, s[36:39], s72 offen offset:2048 sc1\n"
"v_mfma_f32_16x16x32_bf16 v[104:107], a[144:147], a[8:11], 0\n"
"ds_read_b64_tr_b16 a[178:179], v20 offset:8448\n"
"v_mfma_f32_16x16x32_bf16 v[108:111], a[144:147], a[12:15], 0\n"
"ds_read_b64_tr_b16 a[180:181], v20 offset:8200\n"
"v_mfma_f32_16x16x32_bf16 v[124:127], a[148:151], a[12:15], 0\n"
"v_mfma_f32_16x16x32_bf16 v[120:123], a[148:151], a[8:11], 0\n"
"ds_read_b64_tr_b16 a[182:183], v20 offset:8456\n"
"v_mfma_f32_16x16x32_bf16 v[116:119], a[148:151], a[4:7], 0\n"
"buffer_load_dwordx4 a[124:127], v27, s[36:39], s72 offen offset:3072 sc1\n"
"v_mfma_f32_16x16x32_bf16 v[112:115], a[148:151], a[0:3], 0\n"
"s_add_u32 s72, s72, 4096\n"
"ds_read_b64_tr_b16 a[184:185], v20 offset:8704\n"
"v_mfma_f32_16x16x32_bf16 v[128:131], a[152:155], a[0:3], 0\n"
"ds_read_b64_tr_b16 a[186:187], v20 offset:8960\n"
"v_mfma_f32_16x16x32_bf16 v[132:135], a[152:155], a[4:7], 0\n"
"v_mfma_f32_16x16x32_bf16 v[136:139], a[152:155], a[8:11], 0\n"
"ds_read_b64_tr_b16 a[188:189], v20 offset:8712\n"
"v_mfma_f32_16x16x32_bf16 v[140:143], a[152:155], a[12:15], 0\n"
"v_mfma_f32_16x16x32_bf16 v[156:159], a[156:159], a[12:15], 0\n"
"buffer_load_dwordx4 a[128:131], v27, s[36:39], s72 offen offset:0 sc1\n"
"v_mfma_f32_16x16x32_bf16 v[152:155], a[156:159], a[8:11], 0\n"
"ds_read_b64_tr_b16 a[190:191], v20 offset:8968\n"
"v_mfma_f32_16x16x32_bf16 v[148:151], a[156:159], a[4:7], 0\n"
"ds_read_b64_tr_b16 a[192:193], v20 offset:9216\n"
"v_mfma_f32_16x16x32_bf16 v[144:147], a[156:159], a[0:3], 0\n"
"ds_read_b64_tr_b16 a[194:195], v20 offset:9472\n"
"v_mfma_f32_16x16x32_bf16 v[160:163], a[160:163], a[0:3], 0\n"
"v_mfma_f32_16x16x32_bf16 v[164:167], a[160:163], a[4:7], 0\n"
"buffer_load_dwordx4 a[132:135], v27, s[36:39], s72 offen offset:1024 sc1\n"
"v_mfma_f32_16x16x32_bf16 v[168:171], a[160:163], a[8:11], 0\n"
"ds_read_b64_tr_b16 a[196:197], v20 offset:9224\n"
"v_mfma_f32_16x16x32_bf16 v[172:175], a[160:163], a[12:15], 0\n"
"ds_read_b64_tr_b16 a[198:199], v20 offset:9480\n"
"v_mfma_f32_16x16x32_bf16 v[188:191], a[164:167], a[12:15], 0\n"
"v_mfma_f32_16x16x32_bf16 v[184:187], a[164:167], a[8:11], 0\n"
"ds_read_b64_tr_b16 a[200:201], v20 offset:9728\n"
"v_mfma_f32_16x16x32_bf16 v[180:183], a[164:167], a[4:7], 0\n"
"buffer_load_dwordx4 a[136:139], v27, s[36:39], s72 offen offset:2048 sc1\n"
"v_mfma_f32_16x16x32_bf16 v[176:179], a[164:167], a[0:3], 0\n"
"ds_read_b64_tr_b16 a[202:203], v20 offset:9984\n"
"v_mfma_f32_16x16x32_bf16 v[192:195], a[168:171], a[0:3], 0\n"
"ds_read_b64_tr_b16 a[204:205], v20 offset:9736\n"
"v_mfma_f32_16x16x32_bf16 v[196:199], a[168:171], a[4:7], 0\n"
"v_mfma_f32_16x16x32_bf16 v[200:203], a[168:171], a[8:11], 0\n"
"ds_read_b64_tr_b16 a[206:207], v20 offset:9992\n"
"v_mfma_f32_16x16x32_bf16 v[204:207], a[168:171], a[12:15], 0\n"
"buffer_load_dwordx4 a[140:143], v27, s[36:39], s72 offen offset:3072 sc1\n"
"v_mfma_f32_16x16x32_bf16 v[220:223], a[172:175], a[12:15], 0\n"
"s_add_u32 s72, s72, 4096\n"
"v_mfma_f32_16x16x32_bf16 v[216:219], a[172:175], a[8:11], 0\n"
"v_mfma_f32_16x16x32_bf16 v[212:215], a[172:175], a[4:7], 0\n"
"v_mfma_f32_16x16x32_bf16 v[208:211], a[172:175], a[0:3], 0\n"
"s_waitcnt lgkmcnt(0)\n"
"v_mfma_f32_16x16x32_bf16 v[96:99], a[176:179], a[16:19], v[96:99]\n"
"s_nop 0\n"
"s_barrier\n"
"v_mfma_f32_16x16x32_bf16 v[100:103], a[176:179], a[20:23], v[100:103]\n"
"s_add_u32 m0, s75, 98304\n"
"ds_read_b64_tr_b16 a[144:145], v20 offset:16384\n"
"v_mfma_f32_16x16x32_bf16 v[104:107], a[176:179], a[24:27], v[104:107]\n"
"buffer_load_dwordx4 v8, s[40:43], s73 offen offset:0 lds\n"
"v_mfma_f32_16x16x32_bf16 v[108:111], a[176:179], a[28:31], v[108:111]\n"
"ds_read_b64_tr_b16 a[146:147], v20 offset:16640\n"
"v_mfma_f32_16x16x32_bf16 v[124:127], a[180:183], a[28:31], v[124:127]\n"
"ds_read_b64_tr_b16 a[148:149], v20 offset:16392\n"
"v_mfma_f32_16x16x32_bf16 v[120:123], a[180:183], a[24:27], v[120:123]\n"
"v_mfma_f32_16x16x32_bf16 v[116:119], a[180:183], a[20:23], v[116:119]\n"
"ds_read_b64_tr_b16 a[150:151], v20 offset:16648\n"
"v_mfma_f32_16x16x32_bf16 v[112:115], a[180:183], a[16:19], v[112:115]\n"
"buffer_load_dwordx4 v9, s[40:43], s73 offen offset:1024 lds\n"
"v_mfma_f32_16x16x32_bf16 v[128:131], a[184:187], a[16:19], v[128:131]\n"
"ds_read_b64_tr_b16 a[152:153], v20 offset:16896\n"
"v_mfma_f32_16x16x32_bf16 v[132:135], a[184:187], a[20:23], v[132:135]\n"
"ds_read_b64_tr_b16 a[154:155], v20 offset:17152\n"
"v_mfma_f32_16x16x32_bf16 v[136:139], a[184:187], a[24:27], v[136:139]\n"
"v_mfma_f32_16x16x32_bf16 v[140:143], a[184:187], a[28:31], v[140:143]\n"
"ds_read_b64_tr_b16 a[156:157], v20 offset:16904\n"
"v_mfma_f32_16x16x32_bf16 v[156:159], a[188:191], a[28:31], v[156:159]\n"
"buffer_load_dwordx4 v10, s[40:43], s73 offen offset:2048 lds\n"
"v_mfma_f32_16x16x32_bf16 v[152:155], a[188:191], a[24:27], v[152:155]\n"
"ds_read_b64_tr_b16 a[158:159], v20 offset:17160\n"
"v_mfma_f32_16x16x32_bf16 v[148:151], a[188:191], a[20:23], v[148:151]\n"
"ds_read_b64_tr_b16 a[160:161], v20 offset:17408\n"
"v_mfma_f32_16x16x32_bf16 v[144:147], a[188:191], a[16:19], v[144:147]\n"
"v_mfma_f32_16x16x32_bf16 v[160:163], a[192:195], a[16:19], v[160:163]\n"
"ds_read_b64_tr_b16 a[162:163], v20 offset:17664\n"
"v_mfma_f32_16x16x32_bf16 v[164:167], a[192:195], a[20:23], v[164:167]\n"
"buffer_load_dwordx4 v11, s[40:43], s73 offen offset:3072 lds\n"
"v_mfma_f32_16x16x32_bf16 v[168:171], a[192:195], a[24:27], v[168:171]\n"
"s_add_u32 s73, s73, 524288\n"
"ds_read_b64_tr_b16 a[164:165], v20 offset:17416\n"
"v_mfma_f32_16x16x32_bf16 v[172:175], a[192:195], a[28:31], v[172:175]\n"
"ds_read_b64_tr_b16 a[166:167], v20 offset:17672\n"
"v_mfma_f32_16x16x32_bf16 v[188:191], a[196:199], a[28:31], v[188:191]\n"
"v_mfma_f32_16x16x32_bf16 v[184:187], a[196:199], a[24:27], v[184:187]\n"
"ds_read_b64_tr_b16 a[168:169], v20 offset:17920\n"
"v_mfma_f32_16x16x32_bf16 v[180:183], a[196:199], a[20:23], v[180:183]\n"
"ds_read_b64_tr_b16 a[170:171], v20 offset:18176\n"
"v_mfma_f32_16x16x32_bf16 v[176:179], a[196:199], a[16:19], v[176:179]\n"
"buffer_load_dwordx4 a[0:3], v27, s[36:39], s72 offen offset:0 sc1\n"
"v_mfma_f32_16x16x32_bf16 v[192:195], a[200:203], a[16:19], v[192:195]\n"
"ds_read_b64_tr_b16 a[172:173], v20 offset:17928\n"
"v_mfma_f32_16x16x32_bf16 v[196:199], a[200:203], a[20:23], v[196:199]\n"
"v_mfma_f32_16x16x32_bf16 v[200:203], a[200:203], a[24:27], v[200:203]\n"
"ds_read_b64_tr_b16 a[174:175], v20 offset:18184\n"
"v_mfma_f32_16x16x32_bf16 v[204:207], a[200:203], a[28:31], v[204:207]\n"
"v_mfma_f32_16x16x32_bf16 v[220:223], a[204:207], a[28:31], v[220:223]\n"
"buffer_load_dwordx4 a[4:7], v27, s[36:39], s72 offen offset:1024 sc1\n"
"v_mfma_f32_16x16x32_bf16 v[216:219], a[204:207], a[24:27], v[216:219]\n"
"v_mfma_f32_16x16x32_bf16 v[212:215], a[204:207], a[20:23], v[212:215]\n"
"buffer_load_dword v253, v243, s[92:95], s87 offen\n"
"v_mfma_f32_16x16x32_bf16 v[208:211], a[204:207], a[16:19], v[208:211]\n"
"s_add_u32 s87, s87, s7\n"
"s_waitcnt lgkmcnt(0)\n"
"v_mfma_f32_16x16x32_bf16 v[96:99], a[144:147], a[32:35], v[96:99]\n"
"ds_read_b64_tr_b16 a[176:177], v20 offset:24576\n"
"v_mfma_f32_16x16x32_bf16 v[100:103], a[144:147], a[36:39], v[100:103]\n"
"buffer_load_dwordx4 a[8:11], v27, s[36:39], s72 offen offset:2048 sc1\n"
"v_mfma_f32_16x16x32_bf16 v[104:107], a[144:147], a[40:43], v[104:107]\n"
"ds_read_b64_tr_b16 a[178:179], v20 offset:24832\n"
"v_mfma_f32_16x16x32_bf16 v[108:111], a[144:147], a[44:47], v[108:111]\n"
"ds_read_b64_tr_b16 a[180:181], v20 offset:24584\n"
"v_mfma_f32_16x16x32_bf16 v[124:127], a[148:151], a[44:47], v[124:127]\n"
"v_mfma_f32_16x16x32_bf16 v[120:123], a[148:151], a[40:43], v[120:123]\n"
"ds_read_b64_tr_b16 a[182:183], v20 offset:24840\n"
"v_mfma_f32_16x16x32_bf16 v[116:119], a[148:151], a[36:39], v[116:119]\n"
"buffer_load_dwordx4 a[12:15], v27, s[36:39], s72 offen offset:3072 sc1\n"
"v_mfma_f32_16x16x32_bf16 v[112:115], a[148:151], a[32:35], v[112:115]\n"
"s_add_u32 s72, s72, 4096\n"
"ds_read_b64_tr_b16 a[184:185], v20 offset:25088\n"
"v_mfma_f32_16x16x32_bf16 v[128:131], a[152:155], a[32:35], v[128:131]\n"
"ds_read_b64_tr_b16 a[186:187], v20 offset:25344\n"
"v_mfma_f32_16x16x32_bf16 v[132:135], a[152:155], a[36:39], v[132:135]\n"
"v_mfma_f32_16x16x32_bf16 v[136:139], a[152:155], a[40:43], v[136:139]\n"
"ds_read_b64_tr_b16 a[188:189], v20 offset:25096\n"
"v_mfma_f32_16x16x32_bf16 v[140:143], a[152:155], a[44:47], v[140:143]\n"
"v_mfma_f32_16x16x32_bf16 v[156:159], a[156:159], a[44:47], v[156:159]\n"
"buffer_load_dwordx4 a[16:19], v27, s[36:39], s72 offen offset:0 sc1\n"
"v_mfma_f32_16x16x32_bf16 v[152:155], a[156:159], a[40:43], v[152:155]\n"
"ds_read_b64_tr_b16 a[190:191], v20 offset:25352\n"
"v_mfma_f32_16x16x32_bf16 v[148:151], a[156:159], a[36:39], v[148:151]\n"
"ds_read_b64_tr_b16 a[192:193], v20 offset:25600\n"
"v_mfma_f32_16x16x32_bf16 v[144:147], a[156:159], a[32:35], v[144:147]\n"
"ds_read_b64_tr_b16 a[194:195], v20 offset:25856\n"
"v_mfma_f32_16x16x32_bf16 v[160:163], a[160:163], a[32:35], v[160:163]\n"
"v_mfma_f32_16x16x32_bf16 v[164:167], a[160:163], a[36:39], v[164:167]\n"
"buffer_load_dwordx4 a[20:23], v27, s[36:39], s72 offen offset:1024 sc1\n"
"v_mfma_f32_16x16x32_bf16 v[168:171], a[160:163], a[40:43], v[168:171]\n"
"ds_read_b64_tr_b16 a[196:197], v20 offset:25608\n"
"v_mfma_f32_16x16x32_bf16 v[172:175], a[160:163], a[44:47], v[172:175]\n"
"ds_read_b64_tr_b16 a[198:199], v20 offset:25864\n"
"v_mfma_f32_16x16x32_bf16 v[188:191], a[164:167], a[44:47], v[188:191]\n"
"v_mfma_f32_16x16x32_bf16 v[184:187], a[164:167], a[40:43], v[184:187]\n"
"ds_read_b64_tr_b16 a[200:201], v20 offset:26112\n"
"v_mfma_f32_16x16x32_bf16 v[180:183], a[164:167], a[36:39], v[180:183]\n"
"buffer_load_dwordx4 a[24:27], v27, s[36:39], s72 offen offset:2048 sc1\n"
"v_mfma_f32_16x16x32_bf16 v[176:179], a[164:167], a[32:35], v[176:179]\n"
"ds_read_b64_tr_b16 a[202:203], v20 offset:26368\n"
"v_mfma_f32_16x16x32_bf16 v[192:195], a[168:171], a[32:35], v[192:195]\n"
"ds_read_b64_tr_b16 a[204:205], v20 offset:26120\n"
"v_mfma_f32_16x16x32_bf16 v[196:199], a[168:171], a[36:39], v[196:199]\n"
"v_mfma_f32_16x16x32_bf16 v[200:203], a[168:171], a[40:43], v[200:203]\n"
"ds_read_b64_tr_b16 a[206:207], v20 offset:26376\n"
"v_mfma_f32_16x16x32_bf16 v[204:207], a[168:171], a[44:47], v[204:207]\n"
"buffer_load_dwordx4 a[28:31], v27, s[36:39], s72 offen offset:3072 sc1\n"
"v_mfma_f32_16x16x32_bf16 v[220:223], a[172:175], a[44:47], v[220:223]\n"
"s_add_u32 s72, s72, 4096\n"
"v_mfma_f32_16x16x32_bf16 v[216:219], a[172:175], a[40:43], v[216:219]\n"
"v_mfma_f32_16x16x32_bf16 v[212:215], a[172:175], a[36:39], v[212:215]\n"
"v_mfma_f32_16x16x32_bf16 v[208:211], a[172:175], a[32:35], v[208:211]\n"
"s_waitcnt lgkmcnt(0)\n"
"v_mfma_f32_16x16x32_bf16 v[96:99], a[176:179], a[48:51], v[96:99]\n"
"s_nop 0\n"
"s_barrier\n"
"v_mfma_f32_16x16x32_bf16 v[100:103], a[176:179], a[52:55], v[100:103]\n"
"s_add_u32 m0, s75, 114688\n"
"ds_read_b64_tr_b16 a[144:145], v20 offset:32768\n"
"v_mfma_f32_16x16x32_bf16 v[104:107], a[176:179], a[56:59], v[104:107]\n"
"buffer_load_dwordx4 v8, s[40:43], s73 offen offset:0 lds\n"
"v_mfma_f32_16x16x32_bf16 v[108:111], a[176:179], a[60:63], v[108:111]\n"
"ds_read_b64_tr_b16 a[146:147], v20 offset:33024\n"
"v_mfma_f32_16x16x32_bf16 v[124:127], a[180:183], a[60:63], v[124:127]\n"
"ds_read_b64_tr_b16 a[148:149], v20 offset:32776\n"
"v_mfma_f32_16x16x32_bf16 v[120:123], a[180:183], a[56:59], v[120:123]\n"
"v_mfma_f32_16x16x32_bf16 v[116:119], a[180:183], a[52:55], v[116:119]\n"
"ds_read_b64_tr_b16 a[150:151], v20 offset:33032\n"
"v_mfma_f32_16x16x32_bf16 v[112:115], a[180:183], a[48:51], v[112:115]\n"
"buffer_load_dwordx4 v9, s[40:43], s73 offen offset:1024 lds\n"
"v_mfma_f32_16x16x32_bf16 v[128:131], a[184:187], a[48:51], v[128:131]\n"
"ds_read_b64_tr_b16 a[152:153], v20 offset:33280\n"
"v_mfma_f32_16x16x32_bf16 v[132:135], a[184:187], a[52:55], v[132:135]\n"
"ds_read_b64_tr_b16 a[154:155], v20 offset:33536\n"
"v_mfma_f32_16x16x32_bf16 v[136:139], a[184:187], a[56:59], v[136:139]\n"
"v_mfma_f32_16x16x32_bf16 v[140:143], a[184:187], a[60:63], v[140:143]\n"
"ds_read_b64_tr_b16 a[156:157], v20 offset:33288\n"
"v_mfma_f32_16x16x32_bf16 v[156:159], a[188:191], a[60:63], v[156:159]\n"
"buffer_load_dwordx4 v10, s[40:43], s73 offen offset:2048 lds\n"
"v_mfma_f32_16x16x32_bf16 v[152:155], a[188:191], a[56:59], v[152:155]\n"
"ds_read_b64_tr_b16 a[158:159], v20 offset:33544\n"
"v_mfma_f32_16x16x32_bf16 v[148:151], a[188:191], a[52:55], v[148:151]\n"
"ds_read_b64_tr_b16 a[160:161], v20 offset:33792\n"
"v_mfma_f32_16x16x32_bf16 v[144:147], a[188:191], a[48:51], v[144:147]\n"
"v_mfma_f32_16x16x32_bf16 v[160:163], a[192:195], a[48:51], v[160:163]\n"
"ds_read_b64_tr_b16 a[162:163], v20 offset:34048\n"
"v_mfma_f32_16x16x32_bf16 v[164:167], a[192:195], a[52:55], v[164:167]\n"
"buffer_load_dwordx4 v11, s[40:43], s73 offen offset:3072 lds\n"
"v_mfma_f32_16x16x32_bf16 v[168:171], a[192:195], a[56:59], v[168:171]\n"
"s_add_u32 s73, s73, 524288\n"
"ds_read_b64_tr_b16 a[164:165], v20 offset:33800\n"
"v_mfma_f32_16x16x32_bf16 v[172:175], a[192:195], a[60:63], v[172:175]\n"
"ds_read_b64_tr_b16 a[166:167], v20 offset:34056\n"
"v_mfma_f32_16x16x32_bf16 v[188:191], a[196:199], a[60:63], v[188:191]\n"
"v_mfma_f32_16x16x32_bf16 v[184:187], a[196:199], a[56:59], v[184:187]\n"
"ds_read_b64_tr_b16 a[168:169], v20 offset:34304\n"
"v_mfma_f32_16x16x32_bf16 v[180:183], a[196:199], a[52:55], v[180:183]\n"
"ds_read_b64_tr_b16 a[170:171], v20 offset:34560\n"
"v_mfma_f32_16x16x32_bf16 v[176:179], a[196:199], a[48:51], v[176:179]\n"
"buffer_load_dwordx4 a[32:35], v27, s[36:39], s72 offen offset:0 sc1\n"
"v_mfma_f32_16x16x32_bf16 v[192:195], a[200:203], a[48:51], v[192:195]\n"
"ds_read_b64_tr_b16 a[172:173], v20 offset:34312\n"
"v_mfma_f32_16x16x32_bf16 v[196:199], a[200:203], a[52:55], v[196:199]\n"
"v_mfma_f32_16x16x32_bf16 v[200:203], a[200:203], a[56:59], v[200:203]\n"
"ds_read_b64_tr_b16 a[174:175], v20 offset:34568\n"
"v_mfma_f32_16x16x32_bf16 v[204:207], a[200:203], a[60:63], v[204:207]\n"
"v_mfma_f32_16x16x32_bf16 v[220:223], a[204:207], a[60:63], v[220:223]\n"
"buffer_load_dwordx4 a[36:39], v27, s[36:39], s72 offen offset:1024 sc1\n"
"v_mfma_f32_16x16x32_bf16 v[216:219], a[204:207], a[56:59], v[216:219]\n"
"v_mfma_f32_16x16x32_bf16 v[212:215], a[204:207], a[52:55], v[212:215]\n"
"v_mfma_f32_16x16x32_bf16 v[208:211], a[204:207], a[48:51], v[208:211]\n"
"s_waitcnt lgkmcnt(0)\n"
"v_mfma_f32_16x16x32_bf16 v[96:99], a[144:147], a[64:67], v[96:99]\n"
"ds_read_b64_tr_b16 a[176:177], v20 offset:40960\n"
"v_mfma_f32_16x16x32_bf16 v[100:103], a[144:147], a[68:71], v[100:103]\n"
"buffer_load_dwordx4 a[40:43], v27, s[36:39], s72 offen offset:2048 sc1\n"
"v_mfma_f32_16x16x32_bf16 v[104:107], a[144:147], a[72:75], v[104:107]\n"
"ds_read_b64_tr_b16 a[178:179], v20 offset:41216\n"
"v_mfma_f32_16x16x32_bf16 v[108:111], a[144:147], a[76:79], v[108:111]\n"
"ds_read_b64_tr_b16 a[180:181], v20 offset:40968\n"
"v_mfma_f32_16x16x32_bf16 v[124:127], a[148:151], a[76:79], v[124:127]\n"
"v_mfma_f32_16x16x32_bf16 v[120:123], a[148:151], a[72:75], v[120:123]\n"
"ds_read_b64_tr_b16 a[182:183], v20 offset:41224\n"
"v_mfma_f32_16x16x32_bf16 v[116:119], a[148:151], a[68:71], v[116:119]\n"
"buffer_load_dwordx4 a[44:47], v27, s[36:39], s72 offen offset:3072 sc1\n"
"v_mfma_f32_16x16x32_bf16 v[112:115], a[148:151], a[64:67], v[112:115]\n"
"s_add_u32 s72, s72, 4096\n"
"ds_read_b64_tr_b16 a[184:185], v20 offset:41472\n"
"v_mfma_f32_16x16x32_bf16 v[128:131], a[152:155], a[64:67], v[128:131]\n"
"ds_read_b64_tr_b16 a[186:187], v20 offset:41728\n"
"v_mfma_f32_16x16x32_bf16 v[132:135], a[152:155], a[68:71], v[132:135]\n"
"v_mfma_f32_16x16x32_bf16 v[136:139], a[152:155], a[72:75], v[136:139]\n"
"ds_read_b64_tr_b16 a[188:189], v20 offset:41480\n"
"v_mfma_f32_16x16x32_bf16 v[140:143], a[152:155], a[76:79], v[140:143]\n"
"v_mfma_f32_16x16x32_bf16 v[156:159], a[156:159], a[76:79], v[156:159]\n"
"buffer_load_dwordx4 a[48:51], v27, s[36:39], s72 offen offset:0 sc1\n"
"v_mfma_f32_16x16x32_bf16 v[152:155], a[156:159], a[72:75], v[152:155]\n"
"ds_read_b64_tr_b16 a[190:191], v20 offset:41736\n"
"v_mfma_f32_16x16x32_bf16 v[148:151], a[156:159], a[68:71], v[148:151]\n"
"ds_read_b64_tr_b16 a[192:193], v20 offset:41984\n"
"v_mfma_f32_16x16x32_bf16 v[144:147], a[156:159], a[64:67], v[144:147]\n"
"ds_read_b64_tr_b16 a[194:195], v20 offset:42240\n"
"v_mfma_f32_16x16x32_bf16 v[160:163], a[160:163], a[64:67], v[160:163]\n"
"v_mfma_f32_16x16x32_bf16 v[164:167], a[160:163], a[68:71], v[164:167]\n"
"buffer_load_dwordx4 a[52:55], v27, s[36:39], s72 offen offset:1024 sc1\n"
"v_mfma_f32_16x16x32_bf16 v[168:171], a[160:163], a[72:75], v[168:171]\n"
"ds_read_b64_tr_b16 a[196:197], v20 offset:41992\n"
"v_mfma_f32_16x16x32_bf16 v[172:175], a[160:163], a[76:79], v[172:175]\n"
"ds_read_b64_tr_b16 a[198:199], v20 offset:42248\n"
"v_mfma_f32_16x16x32_bf16 v[188:191], a[164:167], a[76:79], v[188:191]\n"
"v_mfma_f32_16x16x32_bf16 v[184:187], a[164:167], a[72:75], v[184:187]\n"
"ds_read_b64_tr_b16 a[200:201], v20 offset:42496\n"
"v_mfma_f32_16x16x32_bf16 v[180:183], a[164:167], a[68:71], v[180:183]\n"
"buffer_load_dwordx4 a[56:59], v27, s[36:39], s72 offen offset:2048 sc1\n"
"v_mfma_f32_16x16x32_bf16 v[176:179], a[164:167], a[64:67], v[176:179]\n"
"ds_read_b64_tr_b16 a[202:203], v20 offset:42752\n"
"v_mfma_f32_16x16x32_bf16 v[192:195], a[168:171], a[64:67], v[192:195]\n"
"ds_read_b64_tr_b16 a[204:205], v20 offset:42504\n"
"v_mfma_f32_16x16x32_bf16 v[196:199], a[168:171], a[68:71], v[196:199]\n"
"v_mfma_f32_16x16x32_bf16 v[200:203], a[168:171], a[72:75], v[200:203]\n"
"ds_read_b64_tr_b16 a[206:207], v20 offset:42760\n"
"v_mfma_f32_16x16x32_bf16 v[204:207], a[168:171], a[76:79], v[204:207]\n"
"buffer_load_dwordx4 a[60:63], v27, s[36:39], s72 offen offset:3072 sc1\n"
"v_mfma_f32_16x16x32_bf16 v[220:223], a[172:175], a[76:79], v[220:223]\n"
"s_add_u32 s72, s72, 4096\n"
"v_mfma_f32_16x16x32_bf16 v[216:219], a[172:175], a[72:75], v[216:219]\n"
"v_mfma_f32_16x16x32_bf16 v[212:215], a[172:175], a[68:71], v[212:215]\n"
"v_mfma_f32_16x16x32_bf16 v[208:211], a[172:175], a[64:67], v[208:211]\n"
"s_waitcnt lgkmcnt(0)\n"
"v_mfma_f32_16x16x32_bf16 v[96:99], a[176:179], a[80:83], v[96:99]\n"
"s_waitcnt vmcnt(37)\n"
"s_barrier\n"
"s_or_b32 s56, s54, s11\n"
"s_cbranch_scc1 L_qp1\n"
"v_lshlrev_b32 v6, 10, v0\n"
"s_mov_b64 exec, 0xff\n"
"buffer_store_dword v252, v6, s[36:39], s90 offen sc1\n"
"s_mov_b64 exec, -1\n"
"L_qp1:\n"
"v_mfma_f32_16x16x32_bf16 v[100:103], a[176:179], a[84:87], v[100:103]\n"
"s_add_u32 m0, s75, 131072\n"
"ds_read_b64_tr_b16 a[144:145], v20 offset:49152\n"
"v_mfma_f32_16x16x32_bf16 v[104:107], a[176:179], a[88:91], v[104:107]\n"
"buffer_load_dwordx4 v8, s[40:43], s73 offen offset:0 lds\n"
"v_mfma_f32_16x16x32_bf16 v[108:111], a[176:179], a[92:95], v[108:111]\n"
"ds_read_b64_tr_b16 a[146:147], v20 offset:49408\n"
"v_mfma_f32_16x16x32_bf16 v[124:127], a[180:183], a[92:95], v[124:127]\n"
"ds_read_b64_tr_b16 a[148:149], v20 offset:49160\n"
"v_mfma_f32_16x16x32_bf16 v[120:123], a[180:183], a[88:91], v[120:123]\n"
"v_mfma_f32_16x16x32_bf16 v[116:119], a[180:183], a[84:87], v[116:119]\n"
"ds_read_b64_tr_b16 a[150:151], v20 offset:49416\n"
"v_mfma_f32_16x16x32_bf16 v[112:115], a[180:183], a[80:83], v[112:115]\n"
"buffer_load_dwordx4 v9, s[40:43], s73 offen offset:1024 lds\n"
"v_mfma_f32_16x16x32_bf16 v[128:131], a[184:187], a[80:83], v[128:131]\n"
"ds_read_b64_tr_b16 a[152:153], v20 offset:49664\n"
"v_mfma_f32_16x16x32_bf16 v[132:135], a[184:187], a[84:87], v[132:135]\n"
"ds_read_b64_tr_b16 a[154:155], v20 offset:49920\n"
"v_mfma_f32_16x16x32_bf16 v[136:139], a[184:187], a[88:91], v[136:139]\n"
"v_mfma_f32_16x16x32_bf16 v[140:143], a[184:187], a[92:95], v[140:143]\n"
"ds_read_b64_tr_b16 a[156:157], v20 offset:49672\n"
"v_mfma_f32_16x16x32_bf16 v[156:159], a[188:191], a[92:95], v[156:159]\n"
"buffer_load_dwordx4 v10, s[40:43], s73 offen offset:2048 lds\n"
"v_mfma_f32_16x16x32_bf16 v[152:155], a[188:191], a[88:91], v[152:155]\n"
"ds_read_b64_tr_b16 a[158:159], v20 offset:49928\n"
"v_mfma_f32_16x16x32_bf16 v[148:151], a[188:191], a[84:87], v[148:151]\n"
"ds_read_b64_tr_b16 a[160:161], v20 offset:50176\n"
"v_mfma_f32_16x16x32_bf16 v[144:147], a[188:191], a[80:83], v[144:147]\n"
"v_mfma_f32_16x16x32_bf16 v[160:163], a[192:195], a[80:83], v[160:163]\n"
"ds_read_b64_tr_b16 a[162:163], v20 offset:50432\n"
"v_mfma_f32_16x16x32_bf16 v[164:167], a[192:195], a[84:87], v[164:167]\n"
"buffer_load_dwordx4 v11, s[40:43], s73 offen offset:3072 lds\n"
"v_mfma_f32_16x16x32_bf16 v[168:171], a[192:195], a[88:91], v[168:171]\n"
"s_add_u32 s73, s73, 524288\n"
"ds_read_b64_tr_b16 a[164:165], v20 offset:50184\n"
"v_mfma_f32_16x16x32_bf16 v[172:175], a[192:195], a[92:95], v[172:175]\n"
"ds_read_b64_tr_b16 a[166:167], v20 offset:50440\n"
"v_mfma_f32_16x16x32_bf16 v[188:191], a[196:199], a[92:95], v[188:191]\n"
"v_mfma_f32_16x16x32_bf16 v[184:187], a[196:199], a[88:91], v[184:187]\n"
"ds_read_b64_tr_b16 a[168:169], v20 offset:50688\n"
"v_mfma_f32_16x16x32_bf16 v[180:183], a[196:199], a[84:87], v[180:183]\n"
"ds_read_b64_tr_b16 a[170:171], v20 offset:50944\n"
"v_mfma_f32_16x16x32_bf16 v[176:179], a[196:199], a[80:83], v[176:179]\n"
"buffer_load_dwordx4 a[64:67], v27, s[36:39], s72 offen offset:0 sc1\n"
"v_mfma_f32_16x16x32_bf16 v[192:195], a[200:203], a[80:83], v[192:195]\n"
"ds_read_b64_tr_b16 a[172:173], v20 offset:50696\n"
"v_mfma_f32_16x16x32_bf16 v[196:199], a[200:203], a[84:87], v[196:199]\n"
"v_mfma_f32_16x16x32_bf16 v[200:203], a[200:203], a[88:91], v[200:203]\n"
"ds_read_b64_tr_b16 a[174:175], v20 offset:50952\n"
"v_mfma_f32_16x16x32_bf16 v[204:207], a[200:203], a[92:95], v[204:207]\n"
"v_mfma_f32_16x16x32_bf16 v[220:223], a[204:207], a[92:95], v[220:223]\n"
"buffer_load_dwordx4 a[68:71], v27, s[36:39], s72 offen offset:1024 sc1\n"
"v_mfma_f32_16x16x32_bf16 v[216:219], a[204:207], a[88:91], v[216:219]\n"
"v_mfma_f32_16x16x32_bf16 v[212:215], a[204:207], a[84:87], v[212:215]\n"
"buffer_load_dword v253, v243, s[92:95], s87 offen\n"
"v_mfma_f32_16x16x32_bf16 v[208:211], a[204:207], a[80:83], v[208:211]\n"
"s_add_u32 s87, s87, s7\n"
"s_waitcnt vmcnt(40) lgkmcnt(0)\n"
"v_mfma_f32_16x16x32_bf16 v[96:99], a[144:147], a[96:99], v[96:99]\n"
"ds_read_b64_tr_b16 a[176:177], v20 offset:57344\n"
"v_mfma_f32_16x16x32_bf16 v[100:103], a[144:147], a[100:103], v[100:103]\n"
"buffer_load_dwordx4 a[72:75], v27, s[36:39], s72 offen offset:2048 sc1\n"
"v_mfma_f32_16x16x32_bf16 v[104:107], a[144:147], a[104:107], v[104:107]\n"
"ds_read_b64_tr_b16 a[178:179], v20 offset:57600\n"
"v_mfma_f32_16x16x32_bf16 v[108:111], a[144:147], a[108:111], v[108:111]\n"
"ds_read_b64_tr_b16 a[180:181], v20 offset:57352\n"
"v_mfma_f32_16x16x32_bf16 v[124:127], a[148:151], a[108:111], v[124:127]\n"
"v_mfma_f32_16x16x32_bf16 v[120:123], a[148:151], a[104:107], v[120:123]\n"
"ds_read_b64_tr_b16 a[182:183], v20 offset:57608\n"
"v_mfma_f32_16x16x32_bf16 v[116:119], a[148:151], a[100:103], v[116:119]\n"
"buffer_load_dwordx4 a[76:79], v27, s[36:39], s72 offen offset:3072 sc1\n"
"v_mfma_f32_16x16x32_bf16 v[112:115], a[148:151], a[96:99], v[112:115]\n"
"s_add_u32 s72, s72, 4096\n"
"ds_read_b64_tr_b16 a[184:185], v20 offset:57856\n"
"v_mfma_f32_16x16x32_bf16 v[128:131], a[152:155], a[96:99], v[128:131]\n"
"ds_read_b64_tr_b16 a[186:187], v20 offset:58112\n"
"v_mfma_f32_16x16x32_bf16 v[132:135], a[152:155], a[100:103], v[132:135]\n"
"v_mfma_f32_16x16x32_bf16 v[136:139], a[152:155], a[104:107], v[136:139]\n"
"ds_read_b64_tr_b16 a[188:189], v20 offset:57864\n"
"v_mfma_f32_16x16x32_bf16 v[140:143], a[152:155], a[108:111], v[140:143]\n"
"v_mfma_f32_16x16x32_bf16 v[156:159], a[156:159], a[108:111], v[156:159]\n"
"buffer_load_dwordx4 a[80:83], v27, s[36:39], s72 offen offset:0 sc1\n"
"v_mfma_f32_16x16x32_bf16 v[152:155], a[156:159], a[104:107], v[152:155]\n"
"ds_read_b64_tr_b16 a[190:191], v20 offset:58120\n"
"v_mfma_f32_16x16x32_bf16 v[148:151], a[156:159], a[100:103], v[148:151]\n"
"ds_read_b64_tr_b16 a[192:193], v20 offset:58368\n"
"v_mfma_f32_16x16x32_bf16 v[144:147], a[156:159], a[96:99], v[144:147]\n"
"ds_read_b64_tr_b16 a[194:195], v20 offset:58624\n"
"v_mfma_f32_16x16x32_bf16 v[160:163], a[160:163], a[96:99], v[160:163]\n"
"v_mfma_f32_16x16x32_bf16 v[164:167], a[160:163], a[100:103], v[164:167]\n"
"buffer_load_dwordx4 a[84:87], v27, s[36:39], s72 offen offset:1024 sc1\n"
"v_mfma_f32_16x16x32_bf16 v[168:171], a[160:163], a[104:107], v[168:171]\n"
"ds_read_b64_tr_b16 a[196:197], v20 offset:58376\n"
"v_mfma_f32_16x16x32_bf16 v[172:175], a[160:163], a[108:111], v[172:175]\n"
"ds_read_b64_tr_b16 a[198:199], v20 offset:58632\n"
"v_mfma_f32_16x16x32_bf16 v[188:191], a[164:167], a[108:111], v[188:191]\n"
"v_mfma_f32_16x16x32_bf16 v[184:187], a[164:167], a[104:107], v[184:187]\n"
"ds_read_b64_tr_b16 a[200:201], v20 offset:58880\n"
"v_mfma_f32_16x16x32_bf16 v[180:183], a[164:167], a[100:103], v[180:183]\n"
"buffer_load_dwordx4 a[88:91], v27, s[36:39], s72 offen offset:2048 sc1\n"
"v_mfma_f32_16x16x32_bf16 v[176:179], a[164:167], a[96:99], v[176:179]\n"
"ds_read_b64_tr_b16 a[202:203], v20 offset:59136\n"
"v_mfma_f32_16x16x32_bf16 v[192:195], a[168:171], a[96:99], v[192:195]\n"
"ds_read_b64_tr_b16 a[204:205], v20 offset:58888\n"
"v_mfma_f32_16x16x32_bf16 v[196:199], a[168:171], a[100:103], v[196:199]\n"
"v_mfma_f32_16x16x32_bf16 v[200:203], a[168:171], a[104:107], v[200:203]\n"
"ds_read_b64_tr_b16 a[206:207], v20 offset:59144\n"
"v_mfma_f32_16x16x32_bf16 v[204:207], a[168:171], a[108:111], v[204:207]\n"
"buffer_load_dwordx4 a[92:95], v27, s[36:39], s72 offen offset:3072 sc1\n"
"v_mfma_f32_16x16x32_bf16 v[220:223], a[172:175], a[108:111], v[220:223]\n"
"s_add_u32 s72, s72, 4096\n"
"v_mfma_f32_16x16x32_bf16 v[216:219], a[172:175], a[104:107], v[216:219]\n"
"v_mfma_f32_16x16x32_bf16 v[212:215], a[172:175], a[100:103], v[212:215]\n"
"v_mfma_f32_16x16x32_bf16 v[208:211], a[172:175], a[96:99], v[208:211]\n"
"s_waitcnt vmcnt(42) lgkmcnt(0)\n"
"v_mfma_f32_16x16x32_bf16 v[96:99], a[176:179], a[112:115], v[96:99]\n"
"s_waitcnt vmcnt(34)\n"
"s_barrier\n"
"v_mfma_f32_16x16x32_bf16 v[100:103], a[176:179], a[116:119], v[100:103]\n"
"s_add_u32 m0, s75, 147456\n"
"ds_read_b64_tr_b16 a[144:145], v20 offset:0\n"
"v_mfma_f32_16x16x32_bf16 v[104:107], a[176:179], a[120:123], v[104:107]\n"
"buffer_load_dwordx4 v8, s[40:43], s73 offen offset:0 lds\n"
"v_mfma_f32_16x16x32_bf16 v[108:111], a[176:179], a[124:127], v[108:111]\n"
"ds_read_b64_tr_b16 a[146:147], v20 offset:256\n"
"v_mfma_f32_16x16x32_bf16 v[124:127], a[180:183], a[124:127], v[124:127]\n"
"ds_read_b64_tr_b16 a[148:149], v20 offset:8\n"
"v_mfma_f32_16x16x32_bf16 v[120:123], a[180:183], a[120:123], v[120:123]\n"
"v_mfma_f32_16x16x32_bf16 v[116:119], a[180:183], a[116:119], v[116:119]\n"
"ds_read_b64_tr_b16 a[150:151], v20 offset:264\n"
"v_mfma_f32_16x16x32_bf16 v[112:115], a[180:183], a[112:115], v[112:115]\n"
"buffer_load_dwordx4 v9, s[40:43], s73 offen offset:1024 lds\n"
"v_mfma_f32_16x16x32_bf16 v[128:131], a[184:187], a[112:115], v[128:131]\n"
"ds_read_b64_tr_b16 a[152:153], v20 offset:512\n"
"v_mfma_f32_16x16x32_bf16 v[132:135], a[184:187], a[116:119], v[132:135]\n"
"ds_read_b64_tr_b16 a[154:155], v20 offset:768\n"
"v_mfma_f32_16x16x32_bf16 v[136:139], a[184:187], a[120:123], v[136:139]\n"
"v_mfma_f32_16x16x32_bf16 v[140:143], a[184:187], a[124:127], v[140:143]\n"
"ds_read_b64_tr_b16 a[156:157], v20 offset:520\n"
"v_mfma_f32_16x16x32_bf16 v[156:159], a[188:191], a[124:127], v[156:159]\n"
"buffer_load_dwordx4 v10, s[40:43], s73 offen offset:2048 lds\n"
"v_mfma_f32_16x16x32_bf16 v[152:155], a[188:191], a[120:123], v[152:155]\n"
"ds_read_b64_tr_b16 a[158:159], v20 offset:776\n"
"v_mfma_f32_16x16x32_bf16 v[148:151], a[188:191], a[116:119], v[148:151]\n"
"ds_read_b64_tr_b16 a[160:161], v20 offset:1024\n"
"v_mfma_f32_16x16x32_bf16 v[144:147], a[188:191], a[112:115], v[144:147]\n"
"v_mfma_f32_16x16x32_bf16 v[160:163], a[192:195], a[112:115], v[160:163]\n"
"ds_read_b64_tr_b16 a[162:163], v20 offset:1280\n"
"v_mfma_f32_16x16x32_bf16 v[164:167], a[192:195], a[116:119], v[164:167]\n"
"buffer_load_dwordx4 v11, s[40:43], s73 offen offset:3072 lds\n"
"v_mfma_f32_16x16x32_bf16 v[168:171], a[192:195], a[120:123], v[168:171]\n"
"s_add_u32 s73, s73, 524288\n"
"ds_read_b64_tr_b16 a[164:165], v20 offset:1032\n"
"v_mfma_f32_16x16x32_bf16 v[172:175], a[192:195], a[124:127], v[172:175]\n"
"ds_read_b64_tr_b16 a[166:167], v20 offset:1288\n"
"v_mfma_f32_16x16x32_bf16 v[188:191], a[196:199], a[124:127], v[188:191]\n"
"v_mfma_f32_16x16x32_bf16 v[184:187], a[196:199], a[120:123], v[184:187]\n"
"ds_read_b64_tr_b16 a[168:169], v20 offset:1536\n"
"v_mfma_f32_16x16x32_bf16 v[180:183], a[196:199], a[116:119], v[180:183]\n"
"ds_read_b64_tr_b16 a[170:171], v20 offset:1792\n"
"v_mfma_f32_16x16x32_bf16 v[176:179], a[196:199], a[112:115], v[176:179]\n"
"buffer_load_dwordx4 a[96:99], v27, s[36:39], s72 offen offset:0 sc1\n"
"v_mfma_f32_16x16x32_bf16 v[192:195], a[200:203], a[112:115], v[192:195]\n"
"ds_read_b64_tr_b16 a[172:173], v20 offset:1544\n"
"v_mfma_f32_16x16x32_bf16 v[196:199], a[200:203], a[116:119], v[196:199]\n"
"v_mfma_f32_16x16x32_bf16 v[200:203], a[200:203], a[120:123], v[200:203]\n"
"ds_read_b64_tr_b16 a[174:175], v20 offset:1800\n"
"v_mfma_f32_16x16x32_bf16 v[204:207], a[200:203], a[124:127], v[204:207]\n"
"v_mfma_f32_16x16x32_bf16 v[220:223], a[204:207], a[124:127], v[220:223]\n"
"buffer_load_dwordx4 a[100:103], v27, s[36:39], s72 offen offset:1024 sc1\n"
"v_mfma_f32_16x16x32_bf16 v[216:219], a[204:207], a[120:123], v[216:219]\n"
"v_mfma_f32_16x16x32_bf16 v[212:215], a[204:207], a[116:119], v[212:215]\n"
"v_mfma_f32_16x16x32_bf16 v[208:211], a[204:207], a[112:115], v[208:211]\n"
"s_waitcnt lgkmcnt(0)\n"
"v_mfma_f32_16x16x32_bf16 v[96:99], a[144:147], a[128:131], v[96:99]\n"
"ds_read_b64_tr_b16 a[176:177], v20 offset:8192\n"
"v_mfma_f32_16x16x32_bf16 v[100:103], a[144:147], a[132:135], v[100:103]\n"
"buffer_load_dwordx4 a[104:107], v27, s[36:39], s72 offen offset:2048 sc1\n"
"v_mfma_f32_16x16x32_bf16 v[104:107], a[144:147], a[136:139], v[104:107]\n"
"ds_read_b64_tr_b16 a[178:179], v20 offset:8448\n"
"v_mfma_f32_16x16x32_bf16 v[108:111], a[144:147], a[140:143], v[108:111]\n"
"ds_read_b64_tr_b16 a[180:181], v20 offset:8200\n"
"v_mfma_f32_16x16x32_bf16 v[124:127], a[148:151], a[140:143], v[124:127]\n"
"v_mfma_f32_16x16x32_bf16 v[120:123], a[148:151], a[136:139], v[120:123]\n"
"ds_read_b64_tr_b16 a[182:183], v20 offset:8456\n"
"v_mfma_f32_16x16x32_bf16 v[116:119], a[148:151], a[132:135], v[116:119]\n"
"buffer_load_dwordx4 a[108:111], v27, s[36:39], s72 offen offset:3072 sc1\n"
"v_mfma_f32_16x16x32_bf16 v[112:115], a[148:151], a[128:131], v[112:115]\n"
"s_add_u32 s72, s72, 4096\n"
"ds_read_b64_tr_b16 a[184:185], v20 offset:8704\n"
"v_mfma_f32_16x16x32_bf16 v[128:131], a[152:155], a[128:131], v[128:131]\n"
"ds_read_b64_tr_b16 a[186:187], v20 offset:8960\n"
"v_mfma_f32_16x16x32_bf16 v[132:135], a[152:155], a[132:135], v[132:135]\n"
"v_mfma_f32_16x16x32_bf16 v[136:139], a[152:155], a[136:139], v[136:139]\n"
"ds_read_b64_tr_b16 a[188:189], v20 offset:8712\n"
"v_mfma_f32_16x16x32_bf16 v[140:143], a[152:155], a[140:143], v[140:143]\n"
"v_mfma_f32_16x16x32_bf16 v[156:159], a[156:159], a[140:143], v[156:159]\n"
"buffer_load_dwordx4 a[112:115], v27, s[36:39], s72 offen offset:0 sc1\n"
"v_mfma_f32_16x16x32_bf16 v[152:155], a[156:159], a[136:139], v[152:155]\n"
"ds_read_b64_tr_b16 a[190:191], v20 offset:8968\n"
"v_mfma_f32_16x16x32_bf16 v[148:151], a[156:159], a[132:135], v[148:151]\n"
"ds_read_b64_tr_b16 a[192:193], v20 offset:9216\n"
"v_mfma_f32_16x16x32_bf16 v[144:147], a[156:159], a[128:131], v[144:147]\n"
"ds_read_b64_tr_b16 a[194:195], v20 offset:9472\n"
"v_mfma_f32_16x16x32_bf16 v[160:163], a[160:163], a[128:131], v[160:163]\n"
"v_mfma_f32_16x16x32_bf16 v[164:167], a[160:163], a[132:135], v[164:167]\n"
"buffer_load_dwordx4 a[116:119], v27, s[36:39], s72 offen offset:1024 sc1\n"
"v_mfma_f32_16x16x32_bf16 v[168:171], a[160:163], a[136:139], v[168:171]\n"
"ds_read_b64_tr_b16 a[196:197], v20 offset:9224\n"
"v_mfma_f32_16x16x32_bf16 v[172:175], a[160:163], a[140:143], v[172:175]\n"
"ds_read_b64_tr_b16 a[198:199], v20 offset:9480\n"
"v_mfma_f32_16x16x32_bf16 v[188:191], a[164:167], a[140:143], v[188:191]\n"
"v_mfma_f32_16x16x32_bf16 v[184:187], a[164:167], a[136:139], v[184:187]\n"
"ds_read_b64_tr_b16 a[200:201], v20 offset:9728\n"
"v_mfma_f32_16x16x32_bf16 v[180:183], a[164:167], a[132:135], v[180:183]\n"
"buffer_load_dwordx4 a[120:123], v27, s[36:39], s72 offen offset:2048 sc1\n"
"v_mfma_f32_16x16x32_bf16 v[176:179], a[164:167], a[128:131], v[176:179]\n"
"ds_read_b64_tr_b16 a[202:203], v20 offset:9984\n"
"v_mfma_f32_16x16x32_bf16 v[192:195], a[168:171], a[128:131], v[192:195]\n"
"ds_read_b64_tr_b16 a[204:205], v20 offset:9736\n"
"v_mfma_f32_16x16x32_bf16 v[196:199], a[168:171], a[132:135], v[196:199]\n"
"v_mfma_f32_16x16x32_bf16 v[200:203], a[168:171], a[136:139], v[200:203]\n"
"ds_read_b64_tr_b16 a[206:207], v20 offset:9992\n"
"v_mfma_f32_16x16x32_bf16 v[204:207], a[168:171], a[140:143], v[204:207]\n"
"buffer_load_dwordx4 a[124:127], v27, s[36:39], s72 offen offset:3072 sc1\n"
"v_mfma_f32_16x16x32_bf16 v[220:223], a[172:175], a[140:143], v[220:223]\n"
"s_add_u32 s72, s72, 4096\n"
"v_mfma_f32_16x16x32_bf16 v[216:219], a[172:175], a[136:139], v[216:219]\n"
"v_mfma_f32_16x16x32_bf16 v[212:215], a[172:175], a[132:135], v[212:215]\n"
"v_mfma_f32_16x16x32_bf16 v[208:211], a[172:175], a[128:131], v[208:211]\n"
"s_waitcnt vmcnt(41) lgkmcnt(0)\n"
"v_mfma_f32_16x16x32_bf16 v[96:99], a[176:179], a[0:3], v[96:99]\n"
"s_waitcnt vmcnt(33)\n"
"s_barrier\n"
"v_mfma_f32_16x16x32_bf16 v[100:103], a[176:179], a[4:7], v[100:103]\n"
"s_add_u32 m0, s75, 98304\n"
"ds_read_b64_tr_b16 a[144:145], v20 offset:16384\n"
"v_mfma_f32_16x16x32_bf16 v[104:107], a[176:179], a[8:11], v[104:107]\n"
"buffer_load_dwordx4 v8, s[40:43], s73 offen offset:0 lds\n"
"v_mfma_f32_16x16x32_bf16 v[108:111], a[176:179], a[12:15], v[108:111]\n"
"ds_read_b64_tr_b16 a[146:147], v20 offset:16640\n"
"v_mfma_f32_16x16x32_bf16 v[124:127], a[180:183], a[12:15], v[124:127]\n"
"ds_read_b64_tr_b16 a[148:149], v20 offset:16392\n"
"v_mfma_f32_16x16x32_bf16 v[120:123], a[180:183], a[8:11], v[120:123]\n"
"v_mfma_f32_16x16x32_bf16 v[116:119], a[180:183], a[4:7], v[116:119]\n"
"ds_read_b64_tr_b16 a[150:151], v20 offset:16648\n"
"v_mfma_f32_16x16x32_bf16 v[112:115], a[180:183], a[0:3], v[112:115]\n"
"ds_read_b64_tr_b16 a[152:153], v20 offset:16896\n"
"v_mfma_f32_16x16x32_bf16 v[128:131], a[184:187], a[0:3], v[128:131]\n"
"v_mfma_f32_16x16x32_bf16 v[132:135], a[184:187], a[4:7], v[132:135]\n"
"buffer_load_dwordx4 v9, s[40:43], s73 offen offset:1024 lds\n"
"v_mfma_f32_16x16x32_bf16 v[136:139], a[184:187], a[8:11], v[136:139]\n"
"ds_read_b64_tr_b16 a[154:155], v20 offset:17152\n"
"v_mfma_f32_16x16x32_bf16 v[140:143], a[184:187], a[12:15], v[140:143]\n"
"ds_read_b64_tr_b16 a[156:157], v20 offset:16904\n"
"v_mfma_f32_16x16x32_bf16 v[156:159], a[188:191], a[12:15], v[156:159]\n"
"ds_read_b64_tr_b16 a[158:159], v20 offset:17160\n"
"v_mfma_f32_16x16x32_bf16 v[152:155], a[188:191], a[8:11], v[152:155]\n"
"v_mfma_f32_16x16x32_bf16 v[148:151], a[188:191], a[4:7], v[148:151]\n"
"ds_read_b64_tr_b16 a[160:161], v20 offset:17408\n"
"v_mfma_f32_16x16x32_bf16 v[144:147], a[188:191], a[0:3], v[144:147]\n"
"v_mfma_f32_16x16x32_bf16 v[160:163], a[192:195], a[0:3], v[160:163]\n"
"ds_read_b64_tr_b16 a[162:163], v20 offset:17664\n"
"v_mfma_f32_16x16x32_bf16 v[164:167], a[192:195], a[4:7], v[164:167]\n"
"buffer_load_dwordx4 v10, s[40:43], s73 offen offset:2048 lds\n"
"v_mfma_f32_16x16x32_bf16 v[168:171], a[192:195], a[8:11], v[168:171]\n"
"ds_read_b64_tr_b16 a[164:165], v20 offset:17416\n"
"v_mfma_f32_16x16x32_bf16 v[172:175], a[192:195], a[12:15], v[172:175]\n"
"ds_read_b64_tr_b16 a[166:167], v20 offset:17672\n"
"v_mfma_f32_16x16x32_bf16 v[188:191], a[196:199], a[12:15], v[188:191]\n"
"v_mfma_f32_16x16x32_bf16 v[184:187], a[196:199], a[8:11], v[184:187]\n"
"ds_read_b64_tr_b16 a[168:169], v20 offset:17920\n"
"v_mfma_f32_16x16x32_bf16 v[180:183], a[196:199], a[4:7], v[180:183]\n"
"ds_read_b64_tr_b16 a[170:171], v20 offset:18176\n"
"v_mfma_f32_16x16x32_bf16 v[176:179], a[196:199], a[0:3], v[176:179]\n"
"v_mfma_f32_16x16x32_bf16 v[192:195], a[200:203], a[0:3], v[192:195]\n"
"ds_read_b64_tr_b16 a[172:173], v20 offset:17928\n"
"v_mfma_f32_16x16x32_bf16 v[196:199], a[200:203], a[4:7], v[196:199]\n"
"buffer_load_dwordx4 v11, s[40:43], s73 offen offset:3072 lds\n"
"v_mfma_f32_16x16x32_bf16 v[200:203], a[200:203], a[8:11], v[200:203]\n"
"s_add_u32 s73, s73, 524288\n"
"ds_read_b64_tr_b16 a[174:175], v20 offset:18184\n"
"v_mfma_f32_16x16x32_bf16 v[204:207], a[200:203], a[12:15], v[204:207]\n"
"v_mfma_f32_16x16x32_bf16 v[220:223], a[204:207], a[12:15], v[220:223]\n"
"v_mfma_f32_16x16x32_bf16 v[216:219], a[204:207], a[8:11], v[216:219]\n"
"v_mfma_f32_16x16x32_bf16 v[212:215], a[204:207], a[4:7], v[212:215]\n"
"buffer_load_dword v253, v243, s[92:95], s87 offen\n"
"v_mfma_f32_16x16x32_bf16 v[208:211], a[204:207], a[0:3], v[208:211]\n"
"s_add_u32 s87, s87, s7\n"
"s_waitcnt lgkmcnt(0)\n"
"v_mfma_f32_16x16x32_bf16 v[96:99], a[144:147], a[16:19], v[96:99]\n"
"ds_read_b64_tr_b16 a[176:177], v20 offset:24576\n"
"s_lshr_b32 s70, s52, 1\n"
"v_mfma_f32_16x16x32_bf16 v[100:103], a[144:147], a[20:23], v[100:103]\n"
"buffer_load_dwordx4 a[128:131], v27, s[36:39], s72 offen offset:0 sc1\n"
"v_mfma_f32_16x16x32_bf16 v[104:107], a[144:147], a[24:27], v[104:107]\n"
"v_accvgpr_write_b32 a0, 0\n"
"v_accvgpr_write_b32 a1, 0\n"
"v_mfma_f32_16x16x32_bf16 v[108:111], a[144:147], a[28:31], v[108:111]\n"
"ds_read_b64_tr_b16 a[178:179], v20 offset:24832\n"
"s_lshl_b32 s70, s70, 2\n"
"v_mfma_f32_16x16x32_bf16 v[124:127], a[148:151], a[28:31], v[124:127]\n"
"ds_read_b64_tr_b16 a[180:181], v20 offset:24584\n"
"v_mfma_f32_16x16x32_bf16 v[120:123], a[148:151], a[24:27], v[120:123]\n"
"v_accvgpr_write_b32 a2, 0\n"
"v_mfma_f32_16x16x32_bf16 v[116:119], a[148:151], a[20:23], v[116:119]\n"
"ds_read_b64_tr_b16 a[182:183], v20 offset:24840\n"
"s_bfe_u32 s71, s53, 0x20002\n"
"v_mfma_f32_16x16x32_bf16 v[112:115], a[148:151], a[16:19], v[112:115]\n"
"v_accvgpr_write_b32 a3, 0\n"
"v_mfma_f32_16x16x32_bf16 v[128:131], a[152:155], a[16:19], v[128:131]\n"
"ds_read_b64_tr_b16 a[184:185], v20 offset:25088\n"
"ds_read_b64_tr_b16 a[186:187], v20 offset:25344\n"
"v_mfma_f32_16x16x32_bf16 v[132:135], a[152:155], a[20:23], v[132:135]\n"
"buffer_load_dwordx4 a[132:135], v27, s[36:39], s72 offen offset:1024 sc1\n"
"v_mfma_f32_16x16x32_bf16 v[136:139], a[152:155], a[24:27], v[136:139]\n"
"v_accvgpr_write_b32 a4, 0\n"
"s_add_u32 s70, s70, s71\n"
"v_mfma_f32_16x16x32_bf16 v[140:143], a[152:155], a[28:31], v[140:143]\n"
"v_accvgpr_write_b32 a5, 0\n"
"v_mfma_f32_16x16x32_bf16 v[156:159], a[156:159], a[28:31], v[156:159]\n"
"ds_read_b64_tr_b16 a[188:189], v20 offset:25096\n"
"ds_read_b64_tr_b16 a[190:191], v20 offset:25352\n"
"v_mfma_f32_16x16x32_bf16 v[152:155], a[156:159], a[24:27], v[152:155]\n"
"v_accvgpr_write_b32 a6, 0\n"
"s_and_b32 s71, s53, 3\n"
"v_mfma_f32_16x16x32_bf16 v[148:151], a[156:159], a[20:23], v[148:151]\n"
"ds_read_b64_tr_b16 a[192:193], v20 offset:25600\n"
"v_mfma_f32_16x16x32_bf16 v[144:147], a[156:159], a[16:19], v[144:147]\n"
"v_accvgpr_write_b32 a7, 0\n"
"v_mfma_f32_16x16x32_bf16 v[160:163], a[160:163], a[16:19], v[160:163]\n"
"ds_read_b64_tr_b16 a[194:195], v20 offset:25856\n"
"s_and_b32 s69, s52, 1\n"
"v_mfma_f32_16x16x32_bf16 v[164:167], a[160:163], a[20:23], v[164:167]\n"
"buffer_load_dwordx4 a[136:139], v27, s[36:39], s72 offen offset:2048 sc1\n"
"v_mfma_f32_16x16x32_bf16 v[168:171], a[160:163], a[24:27], v[168:171]\n"
"v_accvgpr_write_b32 a8, 0\n"
"v_accvgpr_write_b32 a9, 0\n"
"v_mfma_f32_16x16x32_bf16 v[172:175], a[160:163], a[28:31], v[172:175]\n"
"ds_read_b64_tr_b16 a[196:197], v20 offset:25608\n"
"s_lshl1_add_u32 s69, s54, s69\n"
"v_mfma_f32_16x16x32_bf16 v[188:191], a[164:167], a[28:31], v[188:191]\n"
"ds_read_b64_tr_b16 a[198:199], v20 offset:25864\n"
"v_mfma_f32_16x16x32_bf16 v[184:187], a[164:167], a[24:27], v[184:187]\n"
"v_accvgpr_write_b32 a10, 0\n"
"v_mfma_f32_16x16x32_bf16 v[180:183], a[164:167], a[20:23], v[180:183]\n"
"ds_read_b64_tr_b16 a[200:201], v20 offset:26112\n"
"s_mul_i32 s56, s11, 18432\n"
"v_mfma_f32_16x16x32_bf16 v[176:179], a[164:167], a[16:19], v[176:179]\n"
"v_accvgpr_write_b32 a11, 0\n"
"v_mfma_f32_16x16x32_bf16 v[192:195], a[168:171], a[16:19], v[192:195]\n"
"ds_read_b64_tr_b16 a[202:203], v20 offset:26368\n"
"ds_read_b64_tr_b16 a[204:205], v20 offset:26120\n"
"v_mfma_f32_16x16x32_bf16 v[196:199], a[168:171], a[20:23], v[196:199]\n"
"buffer_load_dwordx4 a[140:143], v27, s[36:39], s72 offen offset:3072 sc1\n"
"v_mfma_f32_16x16x32_bf16 v[200:203], a[168:171], a[24:27], v[200:203]\n"
"s_add_u32 s72, s72, 4096\n"
"v_accvgpr_write_b32 a12, 0\n"
"ds_read_b64_tr_b16 a[206:207], v20 offset:26376\n"
"v_mfma_f32_16x16x32_bf16 v[204:207], a[168:171], a[28:31], v[204:207]\n"
"s_mul_i32 s80, s71, 288\n"
"v_accvgpr_write_b32 a13, 0\n"
"v_mfma_f32_16x16x32_bf16 v[220:223], a[172:175], a[28:31], v[220:223]\n"
"v_accvgpr_write_b32 a14, 0\n"
"s_andn2_b32 s80, s80, 0\n"
"v_mfma_f32_16x16x32_bf16 v[216:219], a[172:175], a[24:27], v[216:219]\n"
"v_mfma_f32_16x16x32_bf16 v[212:215], a[172:175], a[20:23], v[212:215]\n"
"v_accvgpr_write_b32 a15, 0\n"
"v_mfma_f32_16x16x32_bf16 v[208:211], a[172:175], a[16:19], v[208:211]\n"
"s_waitcnt vmcnt(38) lgkmcnt(0)\n"
"v_mfma_f32_16x16x32_bf16 v[96:99], a[176:179], a[32:35], v[96:99]\n"
"s_waitcnt vmcnt(30)\n"
"s_barrier\n"
"v_mfma_f32_16x16x32_bf16 v[100:103], a[176:179], a[36:39], v[100:103]\n"
"v_accvgpr_write_b32 a16, 0\n"
"s_add_u32 s56, s56, s80\n"
"v_mfma_f32_16x16x32_bf16 v[104:107], a[176:179], a[40:43], v[104:107]\n"
"ds_read_b64_tr_b16 a[144:145], v20 offset:32768\n"
"ds_read_b64_tr_b16 a[146:147], v20 offset:33024\n"
"v_mfma_f32_16x16x32_bf16 v[108:111], a[176:179], a[44:47], v[108:111]\n"
"v_accvgpr_write_b32 a17, 0\n"
"s_lshl2_add_u32 s57, s70, s11\n"
"v_mfma_f32_16x16x32_bf16 v[124:127], a[180:183], a[44:47], v[124:127]\n"
"v_accvgpr_write_b32 a18, 0\n"
"v_mfma_f32_16x16x32_bf16 v[120:123], a[180:183], a[40:43], v[120:123]\n"
"ds_read_b64_tr_b16 a[148:149], v20 offset:32776\n"
"s_lshl_b32 s57, s57, 18\n"
"v_mfma_f32_16x16x32_bf16 v[116:119], a[180:183], a[36:39], v[116:119]\n"
"v_accvgpr_write_b32 a19, 0\n"
"v_mfma_f32_16x16x32_bf16 v[112:115], a[180:183], a[32:35], v[112:115]\n"
"ds_read_b64_tr_b16 a[150:151], v20 offset:33032\n"
"ds_read_b64_tr_b16 a[152:153], v20 offset:33280\n"
"v_mfma_f32_16x16x32_bf16 v[128:131], a[184:187], a[32:35], v[128:131]\n"
"v_accvgpr_write_b32 a20, 0\n"
"s_mov_b32 s58, 0x86229020\n"
"v_mfma_f32_16x16x32_bf16 v[132:135], a[184:187], a[36:39], v[132:135]\n"
"ds_read_b64_tr_b16 a[154:155], v20 offset:33536\n"
"v_mfma_f32_16x16x32_bf16 v[136:139], a[184:187], a[40:43], v[136:139]\n"
"v_accvgpr_write_b32 a21, 0\n"
"v_mfma_f32_16x16x32_bf16 v[140:143], a[184:187], a[44:47], v[140:143]\n"
"ds_read_b64_tr_b16 a[156:157], v20 offset:33288\n"
"v_mfma_f32_16x16x32_bf16 v[156:159], a[188:191], a[44:47], v[156:159]\n"
"s_cmp_eq_u32 s69, 1\n"
"s_cselect_b32 s58, 0x96a6b128, s58\n"
"v_mfma_f32_16x16x32_bf16 v[152:155], a[188:191], a[40:43], v[152:155]\n"
"v_accvgpr_write_b32 a22, 0\n"
"v_mfma_f32_16x16x32_bf16 v[148:151], a[188:191], a[36:39], v[148:151]\n"
"ds_read_b64_tr_b16 a[158:159], v20 offset:33544\n"
"ds_read_b64_tr_b16 a[160:161], v20 offset:33792\n"
"v_mfma_f32_16x16x32_bf16 v[144:147], a[188:191], a[32:35], v[144:147]\n"
"v_accvgpr_write_b32 a23, 0\n"
"v_mfma_f32_16x16x32_bf16 v[160:163], a[192:195], a[32:35], v[160:163]\n"
"s_cmp_eq_u32 s69, 2\n"
"s_cselect_b32 s58, 0xa72ad230, s58\n"
"v_mfma_f32_16x16x32_bf16 v[164:167], a[192:195], a[36:39], v[164:167]\n"
"v_accvgpr_write_b32 a24, 0\n"
"v_mfma_f32_16x16x32_bf16 v[168:171], a[192:195], a[40:43], v[168:171]\n"
"ds_read_b64_tr_b16 a[162:163], v20 offset:34048\n"
"ds_read_b64_tr_b16 a[164:165], v20 offset:33800\n"
"v_mfma_f32_16x16x32_bf16 v[172:175], a[192:195], a[44:47], v[172:175]\n"
"s_cmp_eq_u32 s69, 3\n"
"s_cselect_b32 s58, 0xb7aef338, s58\n"
"v_mfma_f32_16x16x32_bf16 v[188:191], a[196:199], a[44:47], v[188:191]\n"
"v_accvgpr_write_b32 a25, 0\n"
"v_accvgpr_write_b32 a26, 0\n"
"v_mfma_f32_16x16x32_bf16 v[184:187], a[196:199], a[40:43], v[184:187]\n"
"ds_read_b64_tr_b16 a[166:167], v20 offset:34056\n"
"s_mov_b32 s59, 0x39\n"
"v_mfma_f32_16x16x32_bf16 v[180:183], a[196:199], a[36:39], v[180:183]\n"
"ds_read_b64_tr_b16 a[168:169], v20 offset:34304\n"
"ds_read_b64_tr_b16 a[170:171], v20 offset:34560\n"
"v_mfma_f32_16x16x32_bf16 v[176:179], a[196:199], a[32:35], v[176:179]\n"
"v_accvgpr_write_b32 a27, 0\n"
"v_mfma_f32_16x16x32_bf16 v[192:195], a[200:203], a[32:35], v[192:195]\n"
"s_cmp_eq_u32 s69, 1\n"
"s_cselect_b32 s59, 0x7b, s59\n"
"v_mfma_f32_16x16x32_bf16 v[196:199], a[200:203], a[36:39], v[196:199]\n"
"v_accvgpr_write_b32 a28, 0\n"
"v_mfma_f32_16x16x32_bf16 v[200:203], a[200:203], a[40:43], v[200:203]\n"
"ds_read_b64_tr_b16 a[172:173], v20 offset:34312\n"
"ds_read_b64_tr_b16 a[174:175], v20 offset:34568\n"
"v_mfma_f32_16x16x32_bf16 v[204:207], a[200:203], a[44:47], v[204:207]\n"
"v_accvgpr_write_b32 a29, 0\n"
"v_mfma_f32_16x16x32_bf16 v[220:223], a[204:207], a[44:47], v[220:223]\n"
"s_cmp_eq_u32 s69, 2\n"
"s_cselect_b32 s59, 0xbd, s59\n"
"v_mfma_f32_16x16x32_bf16 v[216:219], a[204:207], a[40:43], v[216:219]\n"
"v_accvgpr_write_b32 a30, 0\n"
"v_mfma_f32_16x16x32_bf16 v[212:215], a[204:207], a[36:39], v[212:215]\n"
"s_cmp_eq_u32 s69, 3\n"
"s_cselect_b32 s59, 0xff, s59\n"
"v_mfma_f32_16x16x32_bf16 v[208:211], a[204:207], a[32:35], v[208:211]\n"
"v_accvgpr_write_b32 a31, 0\n"
"s_waitcnt lgkmcnt(0)\n"
"v_mfma_f32_16x16x32_bf16 v[96:99], a[144:147], a[48:51], v[96:99]\n"
"ds_read_b64_tr_b16 a[176:177], v20 offset:40960\n"
"v_mfma_f32_16x16x32_bf16 v[100:103], a[144:147], a[52:55], v[100:103]\n"
"v_accvgpr_write_b32 a32, 0\n"
"v_mfma_f32_16x16x32_bf16 v[104:107], a[144:147], a[56:59], v[104:107]\n"
"ds_read_b64_tr_b16 a[178:179], v20 offset:41216\n"
"s_add_u32 m0, s75, 0\n"
"v_mfma_f32_16x16x32_bf16 v[108:111], a[144:147], a[60:63], v[108:111]\n"
"v_accvgpr_write_b32 a33, 0\n"
"v_mfma_f32_16x16x32_bf16 v[124:127], a[148:151], a[60:63], v[124:127]\n"
"ds_read_b64_tr_b16 a[180:181], v20 offset:40968\n"
"v_mfma_f32_16x16x32_bf16 v[120:123], a[148:151], a[56:59], v[120:123]\n"
"v_accvgpr_write_b32 a34, 0\n"
"v_mfma_f32_16x16x32_bf16 v[116:119], a[148:151], a[52:55], v[116:119]\n"
"ds_read_b64_tr_b16 a[182:183], v20 offset:41224\n"
"v_mfma_f32_16x16x32_bf16 v[112:115], a[148:151], a[48:51], v[112:115]\n"
"v_accvgpr_write_b32 a35, 0\n"
"v_mfma_f32_16x16x32_bf16 v[128:131], a[152:155], a[48:51], v[128:131]\n"
"ds_read_b64_tr_b16 a[184:185], v20 offset:41472\n"
"ds_read_b64_tr_b16 a[186:187], v20 offset:41728\n"
"v_mfma_f32_16x16x32_bf16 v[132:135], a[152:155], a[52:55], v[132:135]\n"
"buffer_load_dwordx4 v12, s[44:47], s74 offen offset:0 lds\n"
"v_mfma_f32_16x16x32_bf16 v[136:139], a[152:155], a[56:59], v[136:139]\n"
"v_accvgpr_write_b32 a36, 0\n"
"v_accvgpr_write_b32 a37, 0\n"
"v_mfma_f32_16x16x32_bf16 v[140:143], a[152:155], a[60:63], v[140:143]\n"
"ds_read_b64_tr_b16 a[188:189], v20 offset:41480\n"
"v_mfma_f32_16x16x32_bf16 v[156:159], a[156:159], a[60:63], v[156:159]\n"
"buffer_load_dwordx4 v13, s[44:47], s74 offen offset:1024 lds\n"
"v_mfma_f32_16x16x32_bf16 v[152:155], a[156:159], a[56:59], v[152:155]\n"
"v_accvgpr_write_b32 a38, 0\n"
"v_mfma_f32_16x16x32_bf16 v[148:151], a[156:159], a[52:55], v[148:151]\n"
"ds_read_b64_tr_b16 a[190:191], v20 offset:41736\n"
"ds_read_b64_tr_b16 a[192:193], v20 offset:41984\n"
"v_mfma_f32_16x16x32_bf16 v[144:147], a[156:159], a[48:51], v[144:147]\n"
"v_accvgpr_write_b32 a39, 0\n"
"v_mfma_f32_16x16x32_bf16 v[160:163], a[160:163], a[48:51], v[160:163]\n"
"ds_read_b64_tr_b16 a[194:195], v20 offset:42240\n"
"v_mfma_f32_16x16x32_bf16 v[164:167], a[160:163], a[52:55], v[164:167]\n"
"v_accvgpr_write_b32 a40, 0\n"
"v_mfma_f32_16x16x32_bf16 v[168:171], a[160:163], a[56:59], v[168:171]\n"
"buffer_load_dwordx4 v14, s[44:47], s74 offen offset:2048 lds\n"
"v_mfma_f32_16x16x32_bf16 v[172:175], a[160:163], a[60:63], v[172:175]\n"
"ds_read_b64_tr_b16 a[196:197], v20 offset:41992\n"
"ds_read_b64_tr_b16 a[198:199], v20 offset:42248\n"
"v_mfma_f32_16x16x32_bf16 v[188:191], a[164:167], a[60:63], v[188:191]\n"
"v_accvgpr_write_b32 a41, 0\n"
"v_accvgpr_write_b32 a42, 0\n"
"v_mfma_f32_16x16x32_bf16 v[184:187], a[164:167], a[56:59], v[184:187]\n"
"ds_read_b64_tr_b16 a[200:201], v20 offset:42496\n"
"v_mfma_f32_16x16x32_bf16 v[180:183], a[164:167], a[52:55], v[180:183]\n"
"buffer_load_dwordx4 v15, s[44:47], s74 offen offset:3072 lds\n"
"v_mfma_f32_16x16x32_bf16 v[176:179], a[164:167], a[48:51], v[176:179]\n"
"v_accvgpr_write_b32 a43, 0\n"
"v_mfma_f32_16x16x32_bf16 v[192:195], a[168:171], a[48:51], v[192:195]\n"
"ds_read_b64_tr_b16 a[202:203], v20 offset:42752\n"
"ds_read_b64_tr_b16 a[204:205], v20 offset:42504\n"
"v_mfma_f32_16x16x32_bf16 v[196:199], a[168:171], a[52:55], v[196:199]\n"
"v_accvgpr_write_b32 a44, 0\n"
"v_mfma_f32_16x16x32_bf16 v[200:203], a[168:171], a[56:59], v[200:203]\n"
"v_accvgpr_write_b32 a45, 0\n"
"v_mfma_f32_16x16x32_bf16 v[204:207], a[168:171], a[60:63], v[204:207]\n"
"ds_read_b64_tr_b16 a[206:207], v20 offset:42760\n"
"s_add_u32 s74, s74, 16384\n"
"v_mfma_f32_16x16x32_bf16 v[220:223], a[172:175], a[60:63], v[220:223]\n"
"v_accvgpr_write_b32 a46, 0\n"
"v_mfma_f32_16x16x32_bf16 v[216:219], a[172:175], a[56:59], v[216:219]\n"
"v_mfma_f32_16x16x32_bf16 v[212:215], a[172:175], a[52:55], v[212:215]\n"
"buffer_load_dword v253, v243, s[92:95], s87 offen\n"
"v_mfma_f32_16x16x32_bf16 v[208:211], a[172:175], a[48:51], v[208:211]\n"
"v_accvgpr_write_b32 a47, 0\n"
"s_add_u32 s87, s87, s7\n"
"s_waitcnt vmcnt(30) lgkmcnt(0)\n"
"v_mfma_f32_16x16x32_bf16 v[96:99], a[176:179], a[64:67], v[96:99]\n"
"s_waitcnt vmcnt(22)\n"
"s_barrier\n"
"v_mfma_f32_16x16x32_bf16 v[100:103], a[176:179], a[68:71], v[100:103]\n"
"v_accvgpr_write_b32 a48, 0\n"
"v_mfma_f32_16x16x32_bf16 v[104:107], a[176:179], a[72:75], v[104:107]\n"
"ds_read_b64_tr_b16 a[144:145], v20 offset:49152\n"
"s_add_u32 m0, s75, 32768\n"
"v_mfma_f32_16x16x32_bf16 v[108:111], a[176:179], a[76:79], v[108:111]\n"
"v_accvgpr_write_b32 a49, 0\n"
"v_mfma_f32_16x16x32_bf16 v[124:127], a[180:183], a[76:79], v[124:127]\n"
"ds_read_b64_tr_b16 a[146:147], v20 offset:49408\n"
"ds_read_b64_tr_b16 a[148:149], v20 offset:49160\n"
"v_mfma_f32_16x16x32_bf16 v[120:123], a[180:183], a[72:75], v[120:123]\n"
"v_accvgpr_write_b32 a50, 0\n"
"v_mfma_f32_16x16x32_bf16 v[116:119], a[180:183], a[68:71], v[116:119]\n"
"buffer_load_dwordx4 v12, s[44:47], s74 offen offset:0 lds\n"
"v_mfma_f32_16x16x32_bf16 v[112:115], a[180:183], a[64:67], v[112:115]\n"
"v_accvgpr_write_b32 a51, 0\n"
"v_mfma_f32_16x16x32_bf16 v[128:131], a[184:187], a[64:67], v[128:131]\n"
"ds_read_b64_tr_b16 a[150:151], v20 offset:49416\n"
"ds_read_b64_tr_b16 a[152:153], v20 offset:49664\n"
"v_mfma_f32_16x16x32_bf16 v[132:135], a[184:187], a[68:71], v[132:135]\n"
"buffer_load_dwordx4 v13, s[44:47], s74 offen offset:1024 lds\n"
"v_mfma_f32_16x16x32_bf16 v[136:139], a[184:187], a[72:75], v[136:139]\n"
"v_accvgpr_write_b32 a52, 0\n"
"v_accvgpr_write_b32 a53, 0\n"
"v_mfma_f32_16x16x32_bf16 v[140:143], a[184:187], a[76:79], v[140:143]\n"
"ds_read_b64_tr_b16 a[154:155], v20 offset:49920\n"
"ds_read_b64_tr_b16 a[156:157], v20 offset:49672\n"
"v_mfma_f32_16x16x32_bf16 v[156:159], a[188:191], a[76:79], v[156:159]\n"
"buffer_load_dwordx4 v14, s[44:47], s74 offen offset:2048 lds\n"
"v_mfma_f32_16x16x32_bf16 v[152:155], a[188:191], a[72:75], v[152:155]\n"
"buffer_load_dwordx4 v15, s[44:47], s74 offen offset:3072 lds\n"
"v_mfma_f32_16x16x32_bf16 v[148:151], a[188:191], a[68:71], v[148:151]\n"
"v_accvgpr_write_b32 a54, 0\n"
"v_accvgpr_write_b32 a55, 0\n"
"v_mfma_f32_16x16x32_bf16 v[144:147], a[188:191], a[64:67], v[144:147]\n"
"ds_read_b64_tr_b16 a[158:159], v20 offset:49928\n"
"ds_read_b64_tr_b16 a[160:161], v20 offset:50176\n"
"v_mfma_f32_16x16x32_bf16 v[160:163], a[192:195], a[64:67], v[160:163]\n"
"s_add_u32 s74, s74, 16384\n"
"v_accvgpr_write_b32 a56, 0\n"
"v_mfma_f32_16x16x32_bf16 v[164:167], a[192:195], a[68:71], v[164:167]\n"
"ds_read_b64_tr_b16 a[162:163], v20 offset:50432\n"
"s_add_u32 m0, s75, 49152\n"
"v_mfma_f32_16x16x32_bf16 v[168:171], a[192:195], a[72:75], v[168:171]\n"
"ds_read_b64_tr_b16 a[164:165], v20 offset:50184\n"
"v_mfma_f32_16x16x32_bf16 v[172:175], a[192:195], a[76:79], v[172:175]\n"
"v_accvgpr_write_b32 a57, 0\n"
"v_mfma_f32_16x16x32_bf16 v[188:191], a[196:199], a[76:79], v[188:191]\n"
"buffer_load_dwordx4 v16, s[48:51], s83 offen offset:0 lds\n"
"v_mfma_f32_16x16x32_bf16 v[184:187], a[196:199], a[72:75], v[184:187]\n"
"ds_read_b64_tr_b16 a[166:167], v20 offset:50440\n"
"ds_read_b64_tr_b16 a[168:169], v20 offset:50688\n"
"v_mfma_f32_16x16x32_bf16 v[180:183], a[196:199], a[68:71], v[180:183]\n"
"v_accvgpr_write_b32 a58, 0\n"
"v_accvgpr_write_b32 a59, 0\n"
"v_mfma_f32_16x16x32_bf16 v[176:179], a[196:199], a[64:67], v[176:179]\n"
"buffer_load_dwordx4 v17, s[48:51], s83 offen offset:1024 lds\n"
"v_mfma_f32_16x16x32_bf16 v[192:195], a[200:203], a[64:67], v[192:195]\n"
"ds_read_b64_tr_b16 a[170:171], v20 offset:50944\n"
"ds_read_b64_tr_b16 a[172:173], v20 offset:50696\n"
"v_mfma_f32_16x16x32_bf16 v[196:199], a[200:203], a[68:71], v[196:199]\n"
"v_accvgpr_write_b32 a60, 0\n"
"v_mfma_f32_16x16x32_bf16 v[200:203], a[200:203], a[72:75], v[200:203]\n"
"buffer_load_dwordx4 v18, s[48:51], s83 offen offset:2048 lds\n"
"ds_read_b64_tr_b16 a[174:175], v20 offset:50952\n"
"v_mfma_f32_16x16x32_bf16 v[204:207], a[200:203], a[76:79], v[204:207]\n"
"v_accvgpr_write_b32 a61, 0\n"
"v_mfma_f32_16x16x32_bf16 v[220:223], a[204:207], a[76:79], v[220:223]\n"
"buffer_load_dwordx4 v19, s[48:51], s83 offen offset:3072 lds\n"
"v_mfma_f32_16x16x32_bf16 v[216:219], a[204:207], a[72:75], v[216:219]\n"
"v_accvgpr_write_b32 a62, 0\n"
"s_add_u32 s83, s83, 16384\n"
"v_mfma_f32_16x16x32_bf16 v[212:215], a[204:207], a[68:71], v[212:215]\n"
"v_accvgpr_write_b32 a63, 0\n"
"v_mfma_f32_16x16x32_bf16 v[208:211], a[204:207], a[64:67], v[208:211]\n"
"s_waitcnt lgkmcnt(0)\n"
"v_mfma_f32_16x16x32_bf16 v[96:99], a[144:147], a[80:83], v[96:99]\n"
"ds_read_b64_tr_b16 a[176:177], v20 offset:57344\n"
"s_add_u32 m0, s75, 65536\n"
"v_mfma_f32_16x16x32_bf16 v[100:103], a[144:147], a[84:87], v[100:103]\n"
"v_accvgpr_write_b32 a64, 0\n"
"v_mfma_f32_16x16x32_bf16 v[104:107], a[144:147], a[88:91], v[104:107]\n"
"ds_read_b64_tr_b16 a[178:179], v20 offset:57600\n"
"v_mfma_f32_16x16x32_bf16 v[108:111], a[144:147], a[92:95], v[108:111]\n"
"v_accvgpr_write_b32 a65, 0\n"
"v_mfma_f32_16x16x32_bf16 v[124:127], a[148:151], a[92:95], v[124:127]\n"
"buffer_load_dwordx4 v12, s[44:47], s74 offen offset:0 lds\n"
"v_mfma_f32_16x16x32_bf16 v[120:123], a[148:151], a[88:91], v[120:123]\n"
"ds_read_b64_tr_b16 a[180:181], v20 offset:57352\n"
"ds_read_b64_tr_b16 a[182:183], v20 offset:57608\n"
"v_mfma_f32_16x16x32_bf16 v[116:119], a[148:151], a[84:87], v[116:119]\n"
"v_accvgpr_write_b32 a66, 0\n"
"v_accvgpr_write_b32 a67, 0\n"
"v_mfma_f32_16x16x32_bf16 v[112:115], a[148:151], a[80:83], v[112:115]\n"
"buffer_load_dwordx4 v13, s[44:47], s74 offen offset:1024 lds\n"
"v_mfma_f32_16x16x32_bf16 v[128:131], a[152:155], a[80:83], v[128:131]\n"
"ds_read_b64_tr_b16 a[184:185], v20 offset:57856\n"
"ds_read_b64_tr_b16 a[186:187], v20 offset:58112\n"
"v_mfma_f32_16x16x32_bf16 v[132:135], a[152:155], a[84:87], v[132:135]\n"
"buffer_load_dwordx4 v14, s[44:47], s74 offen offset:2048 lds\n"
"v_mfma_f32_16x16x32_bf16 v[136:139], a[152:155], a[88:91], v[136:139]\n"
"v_accvgpr_write_b32 a68, 0\n"
"v_accvgpr_write_b32 a69, 0\n"
"v_mfma_f32_16x16x32_bf16 v[140:143], a[152:155], a[92:95], v[140:143]\n"
"ds_read_b64_tr_b16 a[188:189], v20 offset:57864\n"
"v_mfma_f32_16x16x32_bf16 v[156:159], a[156:159], a[92:95], v[156:159]\n"
"buffer_load_dwordx4 v15, s[44:47], s74 offen offset:3072 lds\n"
"v_mfma_f32_16x16x32_bf16 v[152:155], a[156:159], a[88:91], v[152:155]\n"
"v_accvgpr_write_b32 a70, 0\n"
"s_add_u32 s74, s74, 16384\n"
"v_mfma_f32_16x16x32_bf16 v[148:151], a[156:159], a[84:87], v[148:151]\n"
"ds_read_b64_tr_b16 a[190:191], v20 offset:58120\n"
"ds_read_b64_tr_b16 a[192:193], v20 offset:58368\n"
"v_mfma_f32_16x16x32_bf16 v[144:147], a[156:159], a[80:83], v[144:147]\n"
"v_accvgpr_write_b32 a71, 0\n"
"v_mfma_f32_16x16x32_bf16 v[160:163], a[160:163], a[80:83], v[160:163]\n"
"ds_read_b64_tr_b16 a[194:195], v20 offset:58624\n"
"s_add_u32 m0, s75, 81920\n"
"v_mfma_f32_16x16x32_bf16 v[164:167], a[160:163], a[84:87], v[164:167]\n"
"v_accvgpr_write_b32 a72, 0\n"
"v_mfma_f32_16x16x32_bf16 v[168:171], a[160:163], a[88:91], v[168:171]\n"
"ds_read_b64_tr_b16 a[196:197], v20 offset:58376\n"
"v_mfma_f32_16x16x32_bf16 v[172:175], a[160:163], a[92:95], v[172:175]\n"
"v_accvgpr_write_b32 a73, 0\n"
"v_mfma_f32_16x16x32_bf16 v[188:191], a[164:167], a[92:95], v[188:191]\n"
"buffer_load_dwordx4 v16, s[48:51], s83 offen offset:0 lds\n"
"v_mfma_f32_16x16x32_bf16 v[184:187], a[164:167], a[88:91], v[184:187]\n"
"ds_read_b64_tr_b16 a[198:199], v20 offset:58632\n"
"ds_read_b64_tr_b16 a[200:201], v20 offset:58880\n"
"v_mfma_f32_16x16x32_bf16 v[180:183], a[164:167], a[84:87], v[180:183]\n"
"v_accvgpr_write_b32 a74, 0\n"
"v_accvgpr_write_b32 a75, 0\n"
"v_mfma_f32_16x16x32_bf16 v[176:179], a[164:167], a[80:83], v[176:179]\n"
"buffer_load_dwordx4 v17, s[48:51], s83 offen offset:1024 lds\n"
"v_mfma_f32_16x16x32_bf16 v[192:195], a[168:171], a[80:83], v[192:195]\n"
"ds_read_b64_tr_b16 a[202:203], v20 offset:59136\n"
"ds_read_b64_tr_b16 a[204:205], v20 offset:58888\n"
"v_mfma_f32_16x16x32_bf16 v[196:199], a[168:171], a[84:87], v[196:199]\n"
"buffer_load_dwordx4 v18, s[48:51], s83 offen offset:2048 lds\n"
"v_mfma_f32_16x16x32_bf16 v[200:203], a[168:171], a[88:91], v[200:203]\n"
"v_accvgpr_write_b32 a76, 0\n"
"v_accvgpr_write_b32 a77, 0\n"
"v_mfma_f32_16x16x32_bf16 v[204:207], a[168:171], a[92:95], v[204:207]\n"
"ds_read_b64_tr_b16 a[206:207], v20 offset:59144\n"
"v_mfma_f32_16x16x32_bf16 v[220:223], a[172:175], a[92:95], v[220:223]\n"
"buffer_load_dwordx4 v19, s[48:51], s83 offen offset:3072 lds\n"
"v_mfma_f32_16x16x32_bf16 v[216:219], a[172:175], a[88:91], v[216:219]\n"
"v_accvgpr_write_b32 a78, 0\n"
"s_add_u32 s83, s83, 16384\n"
"v_mfma_f32_16x16x32_bf16 v[212:215], a[172:175], a[84:87], v[212:215]\n"
"v_accvgpr_write_b32 a79, 0\n"
"v_mfma_f32_16x16x32_bf16 v[208:211], a[172:175], a[80:83], v[208:211]\n"
"s_waitcnt vmcnt(34) lgkmcnt(0)\n"
"v_mfma_f32_16x16x32_bf16 v[96:99], a[176:179], a[96:99], v[96:99]\n"
"s_waitcnt vmcnt(26)\n"
"s_barrier\n"
"v_mfma_f32_16x16x32_bf16 v[100:103], a[176:179], a[100:103], v[100:103]\n"
"v_accvgpr_write_b32 a80, 0\n"
"s_lshl2_add_u32 s3, s70, s71\n"
"v_mfma_f32_16x16x32_bf16 v[104:107], a[176:179], a[104:107], v[104:107]\n"
"ds_read_b64_tr_b16 a[144:145], v20 offset:0\n"
"s_lshl2_add_u32 s55, s3, s11\n"
"v_mfma_f32_16x16x32_bf16 v[108:111], a[176:179], a[108:111], v[108:111]\n"
"v_accvgpr_write_b32 a81, 0\n"
"s_lshl2_add_u32 s55, s55, s69\n"
"v_mfma_f32_16x16x32_bf16 v[124:127], a[180:183], a[108:111], v[124:127]\n"
"ds_read_b64_tr_b16 a[146:147], v20 offset:256\n"
"ds_read_b64_tr_b16 a[148:149], v20 offset:8\n"
"v_mfma_f32_16x16x32_bf16 v[120:123], a[180:183], a[104:107], v[120:123]\n"
"v_accvgpr_write_b32 a82, 0\n"
"s_lshl_b32 s55, s55, 2\n"
"v_mfma_f32_16x16x32_bf16 v[116:119], a[180:183], a[100:103], v[116:119]\n"
"v_accvgpr_write_b32 a83, 0\n"
"v_mfma_f32_16x16x32_bf16 v[112:115], a[180:183], a[96:99], v[112:115]\n"
"ds_read_b64_tr_b16 a[150:151], v20 offset:264\n"
"s_add_u32 s55, s55, 0x2ae4000\n"
"v_mfma_f32_16x16x32_bf16 v[128:131], a[184:187], a[96:99], v[128:131]\n"
"ds_read_b64_tr_b16 a[152:153], v20 offset:512\n"
"v_mfma_f32_16x16x32_bf16 v[132:135], a[184:187], a[100:103], v[132:135]\n"
"v_accvgpr_write_b32 a84, 0\n"
"v_mov_b32 v27, s79\n"
"v_mfma_f32_16x16x32_bf16 v[136:139], a[184:187], a[104:107], v[136:139]\n"
"ds_read_b64_tr_b16 a[154:155], v20 offset:768\n"
"v_mfma_f32_16x16x32_bf16 v[140:143], a[184:187], a[108:111], v[140:143]\n"
"v_accvgpr_write_b32 a85, 0\n"
"v_writelane_b32 v27, s55, 1\n"
"v_mfma_f32_16x16x32_bf16 v[156:159], a[188:191], a[108:111], v[156:159]\n"
"ds_read_b64_tr_b16 a[156:157], v20 offset:520\n"
"ds_read_b64_tr_b16 a[158:159], v20 offset:776\n"
"v_mfma_f32_16x16x32_bf16 v[152:155], a[188:191], a[104:107], v[152:155]\n"
"v_accvgpr_write_b32 a86, 0\n"
"s_lshl_b32 s3, s3, 17\n"
"v_mfma_f32_16x16x32_bf16 v[148:151], a[188:191], a[100:103], v[148:151]\n"
"v_accvgpr_write_b32 a87, 0\n"
"v_mfma_f32_16x16x32_bf16 v[144:147], a[188:191], a[96:99], v[144:147]\n"
"ds_read_b64_tr_b16 a[160:161], v20 offset:1024\n"
"s_lshl_b32 s55, s11, 13\n"
"v_mfma_f32_16x16x32_bf16 v[160:163], a[192:195], a[96:99], v[160:163]\n"
"v_accvgpr_write_b32 a88, 0\n"
"s_add_u32 s3, s3, s55\n"
"v_mfma_f32_16x16x32_bf16 v[164:167], a[192:195], a[100:103], v[164:167]\n"
"ds_read_b64_tr_b16 a[162:163], v20 offset:1280\n"
"ds_read_b64_tr_b16 a[164:165], v20 offset:1032\n"
"v_mfma_f32_16x16x32_bf16 v[168:171], a[192:195], a[104:107], v[168:171]\n"
"v_accvgpr_write_b32 a89, 0\n"
"s_add_u32 s3, s3, 0x22d8000\n"
"v_mfma_f32_16x16x32_bf16 v[172:175], a[192:195], a[108:111], v[172:175]\n"
"ds_read_b64_tr_b16 a[166:167], v20 offset:1288\n"
"v_mfma_f32_16x16x32_bf16 v[188:191], a[196:199], a[108:111], v[188:191]\n"
"v_accvgpr_write_b32 a90, 0\n"
"s_lshl_b32 s55, s69, 15\n"
"v_mfma_f32_16x16x32_bf16 v[184:187], a[196:199], a[104:107], v[184:187]\n"
"ds_read_b64_tr_b16 a[168:169], v20 offset:1536\n"
"v_mfma_f32_16x16x32_bf16 v[180:183], a[196:199], a[100:103], v[180:183]\n"
"v_accvgpr_write_b32 a91, 0\n"
"s_add_u32 s55, s55, s3\n"
"v_mfma_f32_16x16x32_bf16 v[176:179], a[196:199], a[96:99], v[176:179]\n"
"ds_read_b64_tr_b16 a[170:171], v20 offset:1792\n"
"v_mfma_f32_16x16x32_bf16 v[192:195], a[200:203], a[96:99], v[192:195]\n"
"v_accvgpr_write_b32 a92, 0\n"
"v_mov_b32 v224, -1\n"
"v_mfma_f32_16x16x32_bf16 v[196:199], a[200:203], a[100:103], v[196:199]\n"
"ds_read_b64_tr_b16 a[172:173], v20 offset:1544\n"
"v_mfma_f32_16x16x32_bf16 v[200:203], a[200:203], a[104:107], v[200:203]\n"
"v_accvgpr_write_b32 a93, 0\n"
"v_mov_b32 v225, -1\n"
"v_mfma_f32_16x16x32_bf16 v[204:207], a[200:203], a[108:111], v[204:207]\n"
"ds_read_b64_tr_b16 a[174:175], v20 offset:1800\n"
"v_mfma_f32_16x16x32_bf16 v[220:223], a[204:207], a[108:111], v[220:223]\n"
"v_accvgpr_write_b32 a94, 0\n"
"v_mov_b32 v226, -1\n"
"v_mfma_f32_16x16x32_bf16 v[216:219], a[204:207], a[104:107], v[216:219]\n"
"v_mfma_f32_16x16x32_bf16 v[212:215], a[204:207], a[100:103], v[212:215]\n"
"buffer_load_dword v253, v243, s[92:95], s87 offen\n"
"v_mfma_f32_16x16x32_bf16 v[208:211], a[204:207], a[96:99], v[208:211]\n"
"v_accvgpr_write_b32 a95, 0\n"
"v_mov_b32 v227, -1\n"
"s_add_u32 s87, s87, s7\n"
"s_waitcnt lgkmcnt(0)\n"
"v_mfma_f32_16x16x32_bf16 v[96:99], a[144:147], a[112:115], v[96:99]\n"
"ds_read_b64_tr_b16 a[176:177], v20 offset:8192\n"
"s_and_b32 s77, s52, 3\n"
"v_mfma_f32_16x16x32_bf16 v[100:103], a[144:147], a[116:119], v[100:103]\n"
"v_accvgpr_write_b32 a96, 0\n"
"s_lshr_b32 s78, s52, 2\n"
"v_mfma_f32_16x16x32_bf16 v[104:107], a[144:147], a[120:123], v[104:107]\n"
"ds_read_b64_tr_b16 a[178:179], v20 offset:8448\n"
"v_mfma_f32_16x16x32_bf16 v[108:111], a[144:147], a[124:127], v[108:111]\n"
"v_accvgpr_write_b32 a97, 0\n"
"s_lshl_b32 s78, s78, 2\n"
"v_mfma_f32_16x16x32_bf16 v[124:127], a[148:151], a[124:127], v[124:127]\n"
"buffer_store_dwordx4 v[224:227], v1, s[36:39], s55 offen sc1\n"
"v_mfma_f32_16x16x32_bf16 v[120:123], a[148:151], a[120:123], v[120:123]\n"
"ds_read_b64_tr_b16 a[180:181], v20 offset:8200\n"
"ds_read_b64_tr_b16 a[182:183], v20 offset:8456\n"
"v_mfma_f32_16x16x32_bf16 v[116:119], a[148:151], a[116:119], v[116:119]\n"
"v_accvgpr_write_b32 a98, 0\n"
"s_bfe_u32 s76, s53, 0x20002\n"
"v_mfma_f32_16x16x32_bf16 v[112:115], a[148:151], a[112:115], v[112:115]\n"
"v_accvgpr_write_b32 a99, 0\n"
"s_add_u32 s78, s78, s76\n"
"v_mfma_f32_16x16x32_bf16 v[128:131], a[152:155], a[112:115], v[128:131]\n"
"ds_read_b64_tr_b16 a[184:185], v20 offset:8704\n"
"ds_read_b64_tr_b16 a[186:187], v20 offset:8960\n"
"v_mfma_f32_16x16x32_bf16 v[132:135], a[152:155], a[116:119], v[132:135]\n"
"v_accvgpr_write_b32 a100, 0\n"
"s_and_b32 s76, s53, 3\n"
"v_mfma_f32_16x16x32_bf16 v[136:139], a[152:155], a[120:123], v[136:139]\n"
"v_accvgpr_write_b32 a101, 0\n"
"s_mul_i32 s72, s77, 4718592\n"
"v_mfma_f32_16x16x32_bf16 v[140:143], a[152:155], a[124:127], v[140:143]\n"
"ds_read_b64_tr_b16 a[188:189], v20 offset:8712\n"
"v_mfma_f32_16x16x32_bf16 v[156:159], a[156:159], a[124:127], v[156:159]\n"
"v_accvgpr_write_b32 a102, 0\n"
"v_mfma_f32_16x16x32_bf16 v[152:155], a[156:159], a[120:123], v[152:155]\n"
"ds_read_b64_tr_b16 a[190:191], v20 offset:8968\n"
"v_mfma_f32_16x16x32_bf16 v[148:151], a[156:159], a[116:119], v[148:151]\n"
"s_add_u32 s85, s26, s72\n"
"s_addc_u32 s86, s27, 0\n"
"v_mfma_f32_16x16x32_bf16 v[144:147], a[156:159], a[112:115], v[144:147]\n"
"ds_read_b64_tr_b16 a[192:193], v20 offset:9216\n"
"s_lshl2_add_u32 s76, s76, s11\n"
"v_mfma_f32_16x16x32_bf16 v[160:163], a[160:163], a[112:115], v[160:163]\n"
"v_accvgpr_write_b32 a103, 0\n"
"s_add_u32 s55, s55, 1024\n"
"v_mfma_f32_16x16x32_bf16 v[164:167], a[160:163], a[116:119], v[164:167]\n"
"ds_read_b64_tr_b16 a[194:195], v20 offset:9472\n"
"s_lshl_b32 s76, s76, 16\n"
"v_mfma_f32_16x16x32_bf16 v[168:171], a[160:163], a[120:123], v[168:171]\n"
"v_accvgpr_write_b32 a104, 0\n"
"s_lshl_b32 s78, s78, 9\n"
"v_mfma_f32_16x16x32_bf16 v[172:175], a[160:163], a[124:127], v[172:175]\n"
"ds_read_b64_tr_b16 a[196:197], v20 offset:9224\n"
"ds_read_b64_tr_b16 a[198:199], v20 offset:9480\n"
"v_mfma_f32_16x16x32_bf16 v[188:191], a[164:167], a[124:127], v[188:191]\n"
"v_accvgpr_write_b32 a105, 0\n"
"s_add_u32 s76, s76, s78\n"
"v_mfma_f32_16x16x32_bf16 v[184:187], a[164:167], a[120:123], v[184:187]\n"
"v_accvgpr_write_b32 a106, 0\n"
"s_sub_u32 s76, s76, 2097152\n"
"v_mfma_f32_16x16x32_bf16 v[180:183], a[164:167], a[116:119], v[180:183]\n"
"ds_read_b64_tr_b16 a[200:201], v20 offset:9728\n"
"ds_read_b64_tr_b16 a[202:203], v20 offset:9984\n"
"v_mfma_f32_16x16x32_bf16 v[176:179], a[164:167], a[112:115], v[176:179]\n"
"v_accvgpr_write_b32 a107, 0\n"
"s_lshl2_add_u32 s78, s53, s11\n"
"v_mfma_f32_16x16x32_bf16 v[192:195], a[168:171], a[112:115], v[192:195]\n"
"v_accvgpr_write_b32 a108, 0\n"
"s_bfe_u32 s77, s78, 0x40002\n"
"v_mfma_f32_16x16x32_bf16 v[196:199], a[168:171], a[116:119], v[196:199]\n"
"ds_read_b64_tr_b16 a[204:205], v20 offset:9736\n"
"v_mfma_f32_16x16x32_bf16 v[200:203], a[168:171], a[120:123], v[200:203]\n"
"v_accvgpr_write_b32 a109, 0\n"
"s_and_b32 s78, s78, 3\n"
"v_mfma_f32_16x16x32_bf16 v[204:207], a[168:171], a[124:127], v[204:207]\n"
"ds_read_b64_tr_b16 a[206:207], v20 offset:9992\n"
"v_mfma_f32_16x16x32_bf16 v[220:223], a[172:175], a[124:127], v[220:223]\n"
"v_accvgpr_write_b32 a110, 0\n"
"s_lshl_b32 s78, s78, 13\n"
"v_mfma_f32_16x16x32_bf16 v[216:219], a[172:175], a[120:123], v[216:219]\n"
"v_mfma_f32_16x16x32_bf16 v[212:215], a[172:175], a[116:119], v[212:215]\n"
"v_accvgpr_write_b32 a111, 0\n"
"s_sub_u32 s78, s78, 65536\n"
"v_mfma_f32_16x16x32_bf16 v[208:211], a[172:175], a[112:115], v[208:211]\n"
"s_waitcnt vmcnt(19) lgkmcnt(0)\n"
"s_barrier\n"
"s_nop 0\n"
"v_mfma_f32_16x16x32_bf16 v[96:99], a[176:179], a[128:131], v[96:99]\n"
"v_accvgpr_write_b32 a112, 0\n"
"v_accvgpr_write_b32 a113, 0\n"
"v_mfma_f32_16x16x32_bf16 v[100:103], a[176:179], a[132:135], v[100:103]\n"
"ds_read_b128 a[144:147], v21 offset:0\n"
"ds_read_b128 a[148:151], v21 offset:512\n"
"v_mfma_f32_16x16x32_bf16 v[104:107], a[176:179], a[136:139], v[104:107]\n"
"v_accvgpr_write_b32 a114, 0\n"
"v_accvgpr_write_b32 a115, 0\n"
"v_mfma_f32_16x16x32_bf16 v[108:111], a[176:179], a[140:143], v[108:111]\n"
"v_accvgpr_write_b32 a116, 0\n"
"v_accvgpr_write_b32 a117, 0\n"
"v_mfma_f32_16x16x32_bf16 v[124:127], a[180:183], a[140:143], v[124:127]\n"
"ds_read_b128 a[152:155], v21 offset:1024\n"
"ds_read_b128 a[156:159], v21 offset:1536\n"
"v_mfma_f32_16x16x32_bf16 v[120:123], a[180:183], a[136:139], v[120:123]\n"
"v_accvgpr_write_b32 a118, 0\n"
"v_accvgpr_write_b32 a119, 0\n"
"v_mfma_f32_16x16x32_bf16 v[116:119], a[180:183], a[132:135], v[116:119]\n"
"v_accvgpr_write_b32 a120, 0\n"
"v_accvgpr_write_b32 a121, 0\n"
"v_mfma_f32_16x16x32_bf16 v[112:115], a[180:183], a[128:131], v[112:115]\n"
"ds_read_b128 a[160:163], v21 offset:4096\n"
"ds_read_b128 a[164:167], v21 offset:4608\n"
"ds_read_b128 a[168:171], v21 offset:5120\n"
"v_mfma_f32_16x16x32_bf16 v[128:131], a[184:187], a[128:131], v[128:131]\n"
"v_accvgpr_write_b32 a122, 0\n"
"v_accvgpr_write_b32 a123, 0\n"
"ds_read_b128 a[172:175], v21 offset:5632\n"
"v_mfma_f32_16x16x32_bf16 v[132:135], a[184:187], a[132:135], v[132:135]\n"
"v_accvgpr_write_b32 a124, 0\n"
"v_accvgpr_write_b32 a125, 0\n"
"v_accvgpr_write_b32 a126, 0\n"
"v_accvgpr_write_b32 a127, 0\n"
"v_mfma_f32_16x16x32_bf16 v[136:139], a[184:187], a[136:139], v[136:139]\n"
"v_cvt_pk_bf16_f32 v32, v96, v97\n"
"v_cvt_pk_bf16_f32 v33, v98, v99\n"
"v_mfma_f32_16x16x32_bf16 v[140:143], a[184:187], a[140:143], v[140:143]\n"
"v_cvt_pk_bf16_f32 v34, v112, v113\n"
"v_cvt_pk_bf16_f32 v35, v114, v115\n"
"v_mfma_f32_16x16x32_bf16 v[156:159], a[188:191], a[140:143], v[156:159]\n"
"v_cvt_pk_bf16_f32 v48, v100, v101\n"
"v_cvt_pk_bf16_f32 v49, v102, v103\n"
"v_mfma_f32_16x16x32_bf16 v[152:155], a[188:191], a[136:139], v[152:155]\n"
"v_cvt_pk_bf16_f32 v50, v116, v117\n"
"v_cvt_pk_bf16_f32 v51, v118, v119\n"
"v_mfma_f32_16x16x32_bf16 v[148:151], a[188:191], a[132:135], v[148:151]\n"
"v_cvt_pk_bf16_f32 v64, v104, v105\n"
"v_cvt_pk_bf16_f32 v65, v106, v107\n"
"v_mfma_f32_16x16x32_bf16 v[144:147], a[188:191], a[128:131], v[144:147]\n"
"v_cvt_pk_bf16_f32 v66, v120, v121\n"
"v_mfma_f32_16x16x32_bf16 v[160:163], a[192:195], a[128:131], v[160:163]\n"
"v_cvt_pk_bf16_f32 v67, v122, v123\n"
"v_cvt_pk_bf16_f32 v80, v108, v109\n"
"v_mfma_f32_16x16x32_bf16 v[164:167], a[192:195], a[132:135], v[164:167]\n"
"v_cvt_pk_bf16_f32 v81, v110, v111\n"
"v_cvt_pk_bf16_f32 v82, v124, v125\n"
"v_mfma_f32_16x16x32_bf16 v[168:171], a[192:195], a[136:139], v[168:171]\n"
"v_cvt_pk_bf16_f32 v83, v126, v127\n"
"v_cvt_pk_bf16_f32 v36, v128, v129\n"
"v_mfma_f32_16x16x32_bf16 v[172:175], a[192:195], a[140:143], v[172:175]\n"
"v_cvt_pk_bf16_f32 v37, v130, v131\n"
"v_cvt_pk_bf16_f32 v38, v144, v145\n"
"v_mfma_f32_16x16x32_bf16 v[188:191], a[196:199], a[140:143], v[188:191]\n"
"v_cvt_pk_bf16_f32 v39, v146, v147\n"
"v_mfma_f32_16x16x32_bf16 v[184:187], a[196:199], a[136:139], v[184:187]\n"
"v_cvt_pk_bf16_f32 v52, v132, v133\n"
"v_cvt_pk_bf16_f32 v53, v134, v135\n"
"v_mfma_f32_16x16x32_bf16 v[180:183], a[196:199], a[132:135], v[180:183]\n"
"v_cvt_pk_bf16_f32 v54, v148, v149\n"
"v_cvt_pk_bf16_f32 v55, v150, v151\n"
"v_mfma_f32_16x16x32_bf16 v[176:179], a[196:199], a[128:131], v[176:179]\n"
"v_cvt_pk_bf16_f32 v68, v136, v137\n"
"v_cvt_pk_bf16_f32 v69, v138, v139\n"
"v_mfma_f32_16x16x32_bf16 v[192:195], a[200:203], a[128:131], v[192:195]\n"
"v_cvt_pk_bf16_f32 v70, v152, v153\n"
"v_cvt_pk_bf16_f32 v71, v154, v155\n"
"v_mfma_f32_16x16x32_bf16 v[196:199], a[200:203], a[132:135], v[196:199]\n"
"v_cvt_pk_bf16_f32 v84, v140, v141\n"
"v_cvt_pk_bf16_f32 v85, v142, v143\n"
"v_mfma_f32_16x16x32_bf16 v[200:203], a[200:203], a[136:139], v[200:203]\n"
"v_cvt_pk_bf16_f32 v86, v156, v157\n"
"v_mfma_f32_16x16x32_bf16 v[204:207], a[200:203], a[140:143], v[204:207]\n"
"v_cvt_pk_bf16_f32 v87, v158, v159\n"
"v_cvt_pk_bf16_f32 v40, v160, v161\n"
"v_mfma_f32_16x16x32_bf16 v[220:223], a[204:207], a[140:143], v[220:223]\n"
"v_cvt_pk_bf16_f32 v41, v162, v163\n"
"v_cvt_pk_bf16_f32 v42, v176, v177\n"
"v_mfma_f32_16x16x32_bf16 v[216:219], a[204:207], a[136:139], v[216:219]\n"
"v_cvt_pk_bf16_f32 v43, v178, v179\n"
"v_cvt_pk_bf16_f32 v56, v164, v165\n"
"v_mfma_f32_16x16x32_bf16 v[212:215], a[204:207], a[132:135], v[212:215]\n"
"v_cvt_pk_bf16_f32 v57, v166, v167\n"
"v_cvt_pk_bf16_f32 v58, v180, v181\n"
"v_mfma_f32_16x16x32_bf16 v[208:211], a[204:207], a[128:131], v[208:211]\n"
"v_cvt_pk_bf16_f32 v59, v182, v183\n"
"s_cmp_eq_u32 s54, 1\n"
"s_cbranch_scc1 L_firstB\n"
"v_cvt_pk_bf16_f32 v72, v168, v169\n"
"v_cvt_pk_bf16_f32 v73, v170, v171\n"
"v_cvt_pk_bf16_f32 v74, v184, v185\n"
"v_cvt_pk_bf16_f32 v75, v186, v187\n"
"v_cvt_pk_bf16_f32 v88, v172, v173\n"
"v_cvt_pk_bf16_f32 v89, v174, v175\n"
"v_cvt_pk_bf16_f32 v90, v188, v189\n"
"v_cvt_pk_bf16_f32 v91, v190, v191\n"
"s_waitcnt lgkmcnt(0)\n"
"v_mfma_f32_16x16x32_bf16 v[96:99], a[144:147], v[32:35], 0\n"
"v_cvt_pk_bf16_f32 v44, v192, v193\n"
"v_cvt_pk_bf16_f32 v45, v194, v195\n"
"v_mfma_f32_16x16x32_bf16 v[100:103], a[144:147], v[48:51], 0\n"
"v_cvt_pk_bf16_f32 v46, v208, v209\n"
"v_cvt_pk_bf16_f32 v47, v210, v211\n"
"v_mfma_f32_16x16x32_bf16 v[104:107], a[144:147], v[64:67], 0\n"
"v_cvt_pk_bf16_f32 v60, v196, v197\n"
"v_cvt_pk_bf16_f32 v61, v198, v199\n"
"v_mfma_f32_16x16x32_bf16 v[108:111], a[144:147], v[80:83], 0\n"
"v_cvt_pk_bf16_f32 v62, v212, v213\n"
"v_cvt_pk_bf16_f32 v63, v214, v215\n"
"v_mfma_f32_16x16x32_bf16 v[96:99], a[148:151], v[36:39], v[96:99]\n"
"v_cvt_pk_bf16_f32 v76, v200, v201\n"
"v_cvt_pk_bf16_f32 v77, v202, v203\n"
"v_mfma_f32_16x16x32_bf16 v[100:103], a[148:151], v[52:55], v[100:103]\n"
"v_cvt_pk_bf16_f32 v78, v216, v217\n"
"v_cvt_pk_bf16_f32 v79, v218, v219\n"
"v_mfma_f32_16x16x32_bf16 v[104:107], a[148:151], v[68:71], v[104:107]\n"
"v_cvt_pk_bf16_f32 v92, v204, v205\n"
"v_cvt_pk_bf16_f32 v93, v206, v207\n"
"v_mfma_f32_16x16x32_bf16 v[108:111], a[148:151], v[84:87], v[108:111]\n"
"v_cvt_pk_bf16_f32 v94, v220, v221\n"
"v_cvt_pk_bf16_f32 v95, v222, v223\n"
"v_mfma_f32_16x16x32_bf16 v[96:99], a[152:155], v[40:43], v[96:99]\n"
"v_mov_b32 v208, 0x3f803f80\n"
"v_mov_b32 v209, 0x3f803f80\n"
"v_mfma_f32_16x16x32_bf16 v[100:103], a[152:155], v[56:59], v[100:103]\n"
"v_mov_b32 v210, 0x3f803f80\n"
"v_mov_b32 v211, 0x3f803f80\n"
"v_mfma_f32_16x16x32_bf16 v[104:107], a[152:155], v[72:75], v[104:107]\n"
"v_accvgpr_write_b32 a128, 0\n"
"v_accvgpr_write_b32 a129, 0\n"
"v_mfma_f32_16x16x32_bf16 v[108:111], a[152:155], v[88:91], v[108:111]\n"
"v_accvgpr_write_b32 a130, 0\n"
"v_accvgpr_write_b32 a131, 0\n"
"v_mfma_f32_16x16x32_bf16 v[96:99], a[156:159], v[44:47], v[96:99]\n"
"v_accvgpr_write_b32 a132, 0\n"
"v_accvgpr_write_b32 a133, 0\n"
"v_mfma_f32_16x16x32_bf16 v[100:103], a[156:159], v[60:63], v[100:103]\n"
"v_accvgpr_write_b32 a134, 0\n"
"v_accvgpr_write_b32 a135, 0\n"
"v_mfma_f32_16x16x32_bf16 v[104:107], a[156:159], v[76:79], v[104:107]\n"
"v_accvgpr_write_b32 a136, 0\n"
"v_accvgpr_write_b32 a137, 0\n"
"v_mfma_f32_16x16x32_bf16 v[108:111], a[156:159], v[92:95], v[108:111]\n"
"v_accvgpr_write_b32 a138, 0\n"
"v_accvgpr_write_b32 a139, 0\n"
"v_mfma_f32_16x16x32_bf16 v[112:115], a[160:163], v[32:35], 0\n"
"v_accvgpr_write_b32 a140, 0\n"
"v_accvgpr_write_b32 a141, 0\n"
"v_mfma_f32_16x16x32_bf16 v[116:119], a[160:163], v[48:51], 0\n"
"v_accvgpr_write_b32 a142, 0\n"
"v_accvgpr_write_b32 a143, 0\n"
"v_mfma_f32_16x16x32_bf16 v[120:123], a[160:163], v[64:67], 0\n"
"v_max3_f32 v228, v96, v97, v98\n"
"v_max3_f32 v229, v100, v101, v102\n"
"v_mfma_f32_16x16x32_bf16 v[124:127], a[160:163], v[80:83], 0\n"
"v_max3_f32 v230, v104, v105, v106\n"
"v_max3_f32 v231, v108, v109, v110\n"
"v_mfma_f32_16x16x32_bf16 v[112:115], a[164:167], v[36:39], v[112:115]\n"
"v_max_f32 v228, v228, v99\n"
"v_max_f32 v229, v229, v103\n"
"v_mfma_f32_16x16x32_bf16 v[116:119], a[164:167], v[52:55], v[116:119]\n"
"v_max_f32 v230, v230, v107\n"
"v_max_f32 v231, v231, v111\n"
"v_mfma_f32_16x16x32_bf16 v[120:123], a[164:167], v[68:71], v[120:123]\n"
"v_mov_b32 v232, v228\n"
"v_mov_b32 v233, v229\n"
"v_mfma_f32_16x16x32_bf16 v[124:127], a[164:167], v[84:87], v[124:127]\n"
"v_mov_b32 v234, v230\n"
"v_mov_b32 v235, v231\n"
"v_mfma_f32_16x16x32_bf16 v[112:115], a[168:171], v[40:43], v[112:115]\n"
"v_permlane32_swap_b32 v232, v228\n"
"v_permlane32_swap_b32 v233, v229\n"
"v_mfma_f32_16x16x32_bf16 v[116:119], a[168:171], v[56:59], v[116:119]\n"
"v_permlane32_swap_b32 v234, v230\n"
"v_permlane32_swap_b32 v235, v231\n"
"v_mfma_f32_16x16x32_bf16 v[120:123], a[168:171], v[72:75], v[120:123]\n"
"v_max_f32 v228, v228, v232\n"
"v_max_f32 v229, v229, v233\n"
"v_mfma_f32_16x16x32_bf16 v[124:127], a[168:171], v[88:91], v[124:127]\n"
"v_max_f32 v230, v230, v234\n"
"v_max_f32 v231, v231, v235\n"
"v_mfma_f32_16x16x32_bf16 v[112:115], a[172:175], v[44:47], v[112:115]\n"
"v_mov_b32 v232, v228\n"
"v_mov_b32 v233, v229\n"
"v_mfma_f32_16x16x32_bf16 v[116:119], a[172:175], v[60:63], v[116:119]\n"
"v_mov_b32 v234, v230\n"
"v_mov_b32 v235, v231\n"
"v_mfma_f32_16x16x32_bf16 v[120:123], a[172:175], v[76:79], v[120:123]\n"
"v_permlane16_swap_b32 v232, v228\n"
"v_permlane16_swap_b32 v233, v229\n"
"v_mfma_f32_16x16x32_bf16 v[124:127], a[172:175], v[92:95], v[124:127]\n"
"v_permlane16_swap_b32 v234, v230\n"
"v_permlane16_swap_b32 v235, v231\n"
"v_max_f32 v228, v228, v232\n"
"v_max_f32 v229, v229, v233\n"
"v_max_f32 v230, v230, v234\n"
"v_max_f32 v231, v231, v235\n"
"v_sub_f32 v192, 0xc2480000, v228\n"
"v_sub_f32 v196, 0xc2480000, v229\n"
"v_sub_f32 v200, 0xc2480000, v230\n"
"v_sub_f32 v204, 0xc2480000, v231\n"
"ds_read_b128 a[144:147], v21 offset:8192\n"
"ds_read_b128 a[148:151], v21 offset:8704\n"
"ds_read_b128 a[152:155], v21 offset:9216\n"
"ds_read_b128 a[156:159], v21 offset:9728\n"
"ds_read_b128 a[160:163], v21 offset:12288\n"
"ds_read_b128 a[164:167], v21 offset:12800\n"
"ds_read_b128 a[168:171], v21 offset:13312\n"
"ds_read_b128 a[172:175], v21 offset:13824\n"
"v_mov_b32 v193, v192\n"
"v_mov_b32 v197, v196\n"
"v_mov_b32 v201, v200\n"
"v_mov_b32 v205, v204\n"
"v_mov_b32 v194, v192\n"
"v_mov_b32 v198, v196\n"
"v_mov_b32 v202, v200\n"
"v_mov_b32 v206, v204\n"
"v_mov_b32 v195, v192\n"
"v_mov_b32 v199, v196\n"
"v_mov_b32 v203, v200\n"
"v_mov_b32 v207, v204\n"
"s_mov_b32 s40, s20\n"
"s_and_b32 s41, s21, 0xffff\n"
"s_mov_b32 s42, 0x480000\n"
"s_mov_b32 s43, 0x20000\n"
"v_add_f32 v96, v96, v192\n"
"v_add_f32 v97, v97, v192\n"
"v_add_f32 v98, v98, v192\n"
"v_add_f32 v99, v99, v192\n"
"v_add_f32 v100, v100, v196\n"
"v_add_f32 v101, v101, v196\n"
"v_add_f32 v102, v102, v196\n"
"v_add_f32 v103, v103, v196\n"
"v_add_f32 v104, v104, v200\n"
"v_add_f32 v105, v105, v200\n"
"v_add_f32 v106, v106, v200\n"
"v_add_f32 v107, v107, v200\n"
"v_add_f32 v108, v108, v204\n"
"v_add_f32 v109, v109, v204\n"
"v_add_f32 v110, v110, v204\n"
"v_add_f32 v111, v111, v204\n"
"v_add_f32 v112, v112, v192\n"
"v_add_f32 v113, v113, v192\n"
"s_bfe_u64 s[60:61], s[58:59], 0x50000\n"
"s_mul_i32 s60, s60, 147456\n"
"s_add_u32 s73, s56, s60\n"
"s_add_u32 m0, s0, 0\n"
"s_nop 0\n"
"buffer_load_dwordx4 v28, s[40:43], s73 offen offset:0 lds\n"
"v_add_f32 v114, v114, v192\n"
"v_add_f32 v115, v115, v192\n"
"v_add_f32 v116, v116, v196\n"
"buffer_load_dwordx4 v29, s[40:43], s73 offen offset:1024 lds\n"
"v_add_f32 v117, v117, v196\n"
"v_add_f32 v118, v118, v196\n"
"v_add_f32 v119, v119, v196\n"
"buffer_load_dwordx4 v30, s[40:43], s73 offen offset:2048 lds\n"
"v_add_f32 v120, v120, v200\n"
"v_add_f32 v121, v121, v200\n"
"v_add_f32 v122, v122, v200\n"
"v_add_f32 v123, v123, v200\n"
"buffer_load_dwordx4 v31, s[40:43], s73 offen offset:3072 lds\n"
"v_add_f32 v124, v124, v204\n"
"v_add_f32 v125, v125, v204\n"
"v_add_f32 v126, v126, v204\n"
"s_add_u32 m0, s0, 4096\n"
"s_mov_b64 exec, s[96:97]\n"
"buffer_load_dwordx4 v240, s[40:43], s73 offen offset:0 lds\n"
"s_mov_b64 exec, -1\n"
"v_add_f32 v127, v127, v204\n"
"v_exp_f32 v96, v96\n"
"v_exp_f32 v97, v97\n"
"v_exp_f32 v98, v98\n"
"s_add_u32 s73, s73, 73728\n"
"s_add_u32 m0, s0, 18432\n"
"s_nop 0\n"
"buffer_load_dwordx4 v28, s[40:43], s73 offen offset:0 lds\n"
"v_exp_f32 v99, v99\n"
"v_cvt_pk_bf16_f32 v160, v96, v97\n"
"v_exp_f32 v100, v100\n"
"buffer_load_dwordx4 v29, s[40:43], s73 offen offset:1024 lds\n"
"v_exp_f32 v101, v101\n"
"v_cvt_pk_bf16_f32 v161, v98, v99\n"
"v_exp_f32 v102, v102\n"
"buffer_load_dwordx4 v30, s[40:43], s73 offen offset:2048 lds\n"
"v_exp_f32 v103, v103\n"
"v_cvt_pk_bf16_f32 v164, v100, v101\n"
"v_exp_f32 v104, v104\n"
"v_exp_f32 v105, v105\n"
"buffer_load_dwordx4 v31, s[40:43], s73 offen offset:3072 lds\n"
"v_cvt_pk_bf16_f32 v165, v102, v103\n"
"v_exp_f32 v106, v106\n"
"v_exp_f32 v107, v107\n"
"s_add_u32 m0, s0, 22528\n"
"s_mov_b64 exec, s[96:97]\n"
"buffer_load_dwordx4 v240, s[40:43], s73 offen offset:0 lds\n"
"s_mov_b64 exec, -1\n"
"v_cvt_pk_bf16_f32 v168, v104, v105\n"
"v_exp_f32 v108, v108\n"
"v_exp_f32 v109, v109\n"
"v_cvt_pk_bf16_f32 v169, v106, v107\n"
"s_add_u32 s73, s73, 73728\n"
"s_add_u32 m0, s0, 36864\n"
"s_nop 0\n"
"buffer_load_dwordx4 v28, s[40:43], s73 offen offset:0 lds\n"
"v_exp_f32 v110, v110\n"
"v_exp_f32 v111, v111\n"
"v_cvt_pk_bf16_f32 v172, v108, v109\n"
"buffer_load_dwordx4 v29, s[40:43], s73 offen offset:1024 lds\n"
"v_exp_f32 v112, v112\n"
"v_exp_f32 v113, v113\n"
"v_cvt_pk_bf16_f32 v173, v110, v111\n"
"buffer_load_dwordx4 v30, s[40:43], s73 offen offset:2048 lds\n"
"v_exp_f32 v114, v114\n"
"v_exp_f32 v115, v115\n"
"v_cvt_pk_bf16_f32 v162, v112, v113\n"
"v_exp_f32 v116, v116\n"
"buffer_load_dwordx4 v31, s[40:43], s73 offen offset:3072 lds\n"
"v_exp_f32 v117, v117\n"
"v_cvt_pk_bf16_f32 v163, v114, v115\n"
"v_exp_f32 v118, v118\n"
"s_add_u32 m0, s0, 40960\n"
"s_mov_b64 exec, s[96:97]\n"
"buffer_load_dwordx4 v240, s[40:43], s73 offen offset:0 lds\n"
"s_mov_b64 exec, -1\n"
"s_add_u32 s73, s73, 73728\n"
"v_exp_f32 v119, v119\n"
"v_cvt_pk_bf16_f32 v166, v116, v117\n"
"v_exp_f32 v120, v120\n"
"v_exp_f32 v121, v121\n"
"s_mov_b32 s72, 0x6229020\n"
"s_bitcmp1_b32 s52, 0\n"
"s_cselect_b32 s72, 0x16a6b128, s72\n"
"s_mov_b32 s80, 0x2b48c0e6\n"
"s_bitcmp1_b32 s52, 0\n"
"s_cselect_b32 s80, 0x3bcce1ee, s80\n"
"s_mov_b32 s81, 0xbda72\n"
"s_bitcmp1_b32 s52, 0\n"
"s_cselect_b32 s81, 0xffb7a, s81\n"
"s_cmp_ge_u32 s77, 6\n"
"s_cselect_b32 s72, s80, s72\n"
"s_cselect_b32 s80, 6, 0\n"
"s_cmp_ge_u32 s77, 12\n"
"s_cselect_b32 s72, s81, s72\n"
"s_cselect_b32 s80, 12, s80\n"
"s_sub_u32 s77, s77, s80\n"
"s_mul_i32 s77, s77, 5\n"
"s_lshr_b32 s72, s72, s77\n"
"s_and_b32 s72, s72, 31\n"
"s_mul_i32 s72, s72, 147456\n"
"s_add_u32 s80, s20, s72\n"
"s_addc_u32 s81, s21, 0\n"
"s_mov_b32 s94, 147456\n"
"v_lshrrev_b32 v243, 2, v0\n"
"v_lshlrev_b32 v243, 12, v243\n"
"v_lshl_add_u32 v243, v5, 7, v243\n"
"s_cmp_eq_u32 s54, 1\n"
"s_cselect_b32 s92, s85, s80\n"
"s_cselect_b32 s93, s86, s81\n"
"s_cselect_b32 s87, s76, s78\n"
"s_cselect_b32 s7, 20, 15\n"
"s_cselect_b64 vcc, -1, 0\n"
"s_cselect_b32 s94, 0x480000, s94\n"
"s_mov_b32 s95, 0x20000\n"
"s_and_b32 s93, s93, 0xffff\n"
"s_lshl_b32 s7, 1, s7\n"
"v_cndmask_b32 v243, v254, v243, vcc\n"
"s_mov_b32 s77, 32768\n"
"s_mov_b32 s78, 0\n"
"s_mov_b32 s76, 7\n"
"s_waitcnt vmcnt(25) lgkmcnt(0)\n"
"s_barrier\n"
"v_add_u32 v21, 32768, v24\n"
"s_branch L_att\n"
"L_firstB:\n"
"v_cvt_pk_bf16_f32 v72, v168, v169\n"
"v_cvt_pk_bf16_f32 v73, v170, v171\n"
"v_cvt_pk_bf16_f32 v74, v184, v185\n"
"v_cvt_pk_bf16_f32 v75, v186, v187\n"
"v_cvt_pk_bf16_f32 v88, v172, v173\n"
"v_cvt_pk_bf16_f32 v89, v174, v175\n"
"v_cvt_pk_bf16_f32 v90, v188, v189\n"
"v_cvt_pk_bf16_f32 v91, v190, v191\n"
"s_waitcnt lgkmcnt(0)\n"
"v_mfma_f32_16x16x32_bf16 v[96:99], a[144:147], v[32:35], 0\n"
"v_cvt_pk_bf16_f32 v44, v192, v193\n"
"v_cvt_pk_bf16_f32 v45, v194, v195\n"
"v_mfma_f32_16x16x32_bf16 v[100:103], a[144:147], v[48:51], 0\n"
"v_cvt_pk_bf16_f32 v46, v208, v209\n"
"v_cvt_pk_bf16_f32 v47, v210, v211\n"
"v_mfma_f32_16x16x32_bf16 v[104:107], a[144:147], v[64:67], 0\n"
"v_cvt_pk_bf16_f32 v60, v196, v197\n"
"v_cvt_pk_bf16_f32 v61, v198, v199\n"
"v_mfma_f32_16x16x32_bf16 v[108:111], a[144:147], v[80:83], 0\n"
"v_cvt_pk_bf16_f32 v62, v212, v213\n"
"v_cvt_pk_bf16_f32 v63, v214, v215\n"
"v_mfma_f32_16x16x32_bf16 v[96:99], a[148:151], v[36:39], v[96:99]\n"
"v_cvt_pk_bf16_f32 v76, v200, v201\n"
"v_cvt_pk_bf16_f32 v77, v202, v203\n"
"v_mfma_f32_16x16x32_bf16 v[100:103], a[148:151], v[52:55], v[100:103]\n"
"v_cvt_pk_bf16_f32 v78, v216, v217\n"
"v_cvt_pk_bf16_f32 v79, v218, v219\n"
"v_mfma_f32_16x16x32_bf16 v[104:107], a[148:151], v[68:71], v[104:107]\n"
"v_cvt_pk_bf16_f32 v92, v204, v205\n"
"v_cvt_pk_bf16_f32 v93, v206, v207\n"
"v_mfma_f32_16x16x32_bf16 v[108:111], a[148:151], v[84:87], v[108:111]\n"
"v_cvt_pk_bf16_f32 v94, v220, v221\n"
"v_cvt_pk_bf16_f32 v95, v222, v223\n"
"v_mfma_f32_16x16x32_bf16 v[96:99], a[152:155], v[40:43], v[96:99]\n"
"v_mov_b32 v208, 0x3f803f80\n"
"v_mov_b32 v209, 0x3f803f80\n"
"v_mfma_f32_16x16x32_bf16 v[100:103], a[152:155], v[56:59], v[100:103]\n"
"v_mov_b32 v210, 0x3f803f80\n"
"v_mov_b32 v211, 0x3f803f80\n"
"v_mfma_f32_16x16x32_bf16 v[104:107], a[152:155], v[72:75], v[104:107]\n"
"v_accvgpr_write_b32 a128, 0\n"
"v_accvgpr_write_b32 a129, 0\n"
"v_mfma_f32_16x16x32_bf16 v[108:111], a[152:155], v[88:91], v[108:111]\n"
"v_accvgpr_write_b32 a130, 0\n"
"v_accvgpr_write_b32 a131, 0\n"
"v_mfma_f32_16x16x32_bf16 v[96:99], a[156:159], v[44:47], v[96:99]\n"
"v_accvgpr_write_b32 a132, 0\n"
"v_accvgpr_write_b32 a133, 0\n"
"v_mfma_f32_16x16x32_bf16 v[100:103], a[156:159], v[60:63], v[100:103]\n"
"v_accvgpr_write_b32 a134, 0\n"
"v_accvgpr_write_b32 a135, 0\n"
"v_mfma_f32_16x16x32_bf16 v[104:107], a[156:159], v[76:79], v[104:107]\n"
"v_accvgpr_write_b32 a136, 0\n"
"v_accvgpr_write_b32 a137, 0\n"
"v_mfma_f32_16x16x32_bf16 v[108:111], a[156:159], v[92:95], v[108:111]\n"
"v_accvgpr_write_b32 a138, 0\n"
"v_accvgpr_write_b32 a139, 0\n"
"v_mfma_f32_16x16x32_bf16 v[112:115], a[160:163], v[32:35], 0\n"
"v_accvgpr_write_b32 a140, 0\n"
"v_accvgpr_write_b32 a141, 0\n"
"v_mfma_f32_16x16x32_bf16 v[116:119], a[160:163], v[48:51], 0\n"
"v_accvgpr_write_b32 a142, 0\n"
"v_accvgpr_write_b32 a143, 0\n"
"v_mfma_f32_16x16x32_bf16 v[120:123], a[160:163], v[64:67], 0\n"
"v_max3_f32 v228, v96, v97, v98\n"
"v_max3_f32 v229, v100, v101, v102\n"
"v_mfma_f32_16x16x32_bf16 v[124:127], a[160:163], v[80:83], 0\n"
"v_max3_f32 v230, v104, v105, v106\n"
"v_max3_f32 v231, v108, v109, v110\n"
"v_mfma_f32_16x16x32_bf16 v[112:115], a[164:167], v[36:39], v[112:115]\n"
"v_max_f32 v228, v228, v99\n"
"v_max_f32 v229, v229, v103\n"
"v_mfma_f32_16x16x32_bf16 v[116:119], a[164:167], v[52:55], v[116:119]\n"
"v_max_f32 v230, v230, v107\n"
"v_max_f32 v231, v231, v111\n"
"v_mfma_f32_16x16x32_bf16 v[120:123], a[164:167], v[68:71], v[120:123]\n"
"v_mov_b32 v232, v228\n"
"v_mov_b32 v233, v229\n"
"v_mfma_f32_16x16x32_bf16 v[124:127], a[164:167], v[84:87], v[124:127]\n"
"v_mov_b32 v234, v230\n"
"v_mov_b32 v235, v231\n"
"v_mfma_f32_16x16x32_bf16 v[112:115], a[168:171], v[40:43], v[112:115]\n"
"v_permlane32_swap_b32 v232, v228\n"
"v_permlane32_swap_b32 v233, v229\n"
"v_mfma_f32_16x16x32_bf16 v[116:119], a[168:171], v[56:59], v[116:119]\n"
"v_permlane32_swap_b32 v234, v230\n"
"v_permlane32_swap_b32 v235, v231\n"
"v_mfma_f32_16x16x32_bf16 v[120:123], a[168:171], v[72:75], v[120:123]\n"
"v_max_f32 v228, v228, v232\n"
"v_max_f32 v229, v229, v233\n"
"v_mfma_f32_16x16x32_bf16 v[124:127], a[168:171], v[88:91], v[124:127]\n"
"v_max_f32 v230, v230, v234\n"
"v_max_f32 v231, v231, v235\n"
"v_mfma_f32_16x16x32_bf16 v[112:115], a[172:175], v[44:47], v[112:115]\n"
"v_mov_b32 v232, v228\n"
"v_mov_b32 v233, v229\n"
"v_mfma_f32_16x16x32_bf16 v[116:119], a[172:175], v[60:63], v[116:119]\n"
"v_mov_b32 v234, v230\n"
"v_mov_b32 v235, v231\n"
"v_mfma_f32_16x16x32_bf16 v[120:123], a[172:175], v[76:79], v[120:123]\n"
"v_permlane16_swap_b32 v232, v228\n"
"v_permlane16_swap_b32 v233, v229\n"
"v_mfma_f32_16x16x32_bf16 v[124:127], a[172:175], v[92:95], v[124:127]\n"
"v_permlane16_swap_b32 v234, v230\n"
"v_permlane16_swap_b32 v235, v231\n"
"v_max_f32 v228, v228, v232\n"
"v_max_f32 v229, v229, v233\n"
"v_max_f32 v230, v230, v234\n"
"v_max_f32 v231, v231, v235\n"
"v_sub_f32 v192, 0xc2480000, v228\n"
"v_sub_f32 v196, 0xc2480000, v229\n"
"v_sub_f32 v200, 0xc2480000, v230\n"
"v_sub_f32 v204, 0xc2480000, v231\n"
"ds_read_b128 a[144:147], v21 offset:8192\n"
"ds_read_b128 a[148:151], v21 offset:8704\n"
"ds_read_b128 a[152:155], v21 offset:9216\n"
"ds_read_b128 a[156:159], v21 offset:9728\n"
"ds_read_b128 a[160:163], v21 offset:12288\n"
"ds_read_b128 a[164:167], v21 offset:12800\n"
"ds_read_b128 a[168:171], v21 offset:13312\n"
"ds_read_b128 a[172:175], v21 offset:13824\n"
"v_mov_b32 v193, v192\n"
"v_mov_b32 v197, v196\n"
"v_mov_b32 v201, v200\n"
"v_mov_b32 v205, v204\n"
"v_mov_b32 v194, v192\n"
"v_mov_b32 v198, v196\n"
"v_mov_b32 v202, v200\n"
"v_mov_b32 v206, v204\n"
"v_mov_b32 v195, v192\n"
"v_mov_b32 v199, v196\n"
"v_mov_b32 v203, v200\n"
"v_mov_b32 v207, v204\n"
"s_and_b32 s5, s52, 3\n"
"s_lshr_b32 s68, s52, 2\n"
"s_lshl_b32 s68, s68, 2\n"
"s_bfe_u32 s80, s53, 0x20002\n"
"s_add_u32 s68, s68, s80\n"
"s_and_b32 s80, s53, 3\n"
"s_mul_i32 s81, s5, 1152\n"
"s_lshl3_add_u32 s81, s11, s81\n"
"s_lshl_b32 s81, s81, 12\n"
"s_lshl_b32 s89, s68, 9\n"
"s_add_u32 s73, s81, s89\n"
"s_lshl_b32 s2, s11, 12\n"
"s_sub_u32 s40, s26, 0x1000\n"
"s_subb_u32 s41, s27, 0\n"
"s_and_b32 s41, s41, 0xffff\n"
"s_mov_b32 s42, 0x1201000\n"
"s_mov_b32 s43, 0x20000\n"
"v_bfe_u32 v6, v0, 2, 3\n"
"v_lshlrev_b32 v6, 12, v6\n"
"v_lshrrev_b32 v7, 5, v0\n"
"v_lshl_add_u32 v6, v7, 6, v6\n"
"v_bfe_u32 v7, v0, 4, 1\n"
"v_lshlrev_b32 v7, 1, v7\n"
"v_xor_b32 v7, v7, v5\n"
"v_lshl_add_u32 v6, v7, 4, v6\n"
"v_add_u32 v8, 4096, v6\n"
"v_add_u32 v9, 3200, v6\n"
"v_add_u32 v10, 2304, v6\n"
"v_add_u32 v11, 1408, v6\n"
"v_add_f32 v96, v96, v192\n"
"v_add_f32 v97, v97, v192\n"
"v_add_f32 v98, v98, v192\n"
"s_add_u32 m0, s2, 102400\n"
"s_nop 0\n"
"buffer_load_dwordx4 v8, s[40:43], s73 offen offset:0 lds\n"
"v_add_f32 v99, v99, v192\n"
"v_add_f32 v100, v100, v196\n"
"v_add_f32 v101, v101, v196\n"
"v_add_f32 v102, v102, v196\n"
"v_add_f32 v103, v103, v196\n"
"v_add_f32 v104, v104, v200\n"
"v_add_f32 v105, v105, v200\n"
"v_add_f32 v106, v106, v200\n"
"buffer_load_dwordx4 v9, s[40:43], s73 offen offset:1024 lds\n"
"v_add_f32 v107, v107, v200\n"
"v_add_f32 v108, v108, v204\n"
"v_add_f32 v109, v109, v204\n"
"v_add_f32 v110, v110, v204\n"
"v_add_f32 v111, v111, v204\n"
"v_add_f32 v112, v112, v192\n"
"v_add_f32 v113, v113, v192\n"
"v_add_f32 v114, v114, v192\n"
"buffer_load_dwordx4 v10, s[40:43], s73 offen offset:2048 lds\n"
"v_add_f32 v115, v115, v192\n"
"v_add_f32 v116, v116, v196\n"
"v_add_f32 v117, v117, v196\n"
"v_add_f32 v118, v118, v196\n"
"v_add_f32 v119, v119, v196\n"
"v_add_f32 v120, v120, v200\n"
"v_add_f32 v121, v121, v200\n"
"v_add_f32 v122, v122, v200\n"
"buffer_load_dwordx4 v11, s[40:43], s73 offen offset:3072 lds\n"
"v_add_f32 v123, v123, v200\n"
"v_add_f32 v124, v124, v204\n"
"v_add_f32 v125, v125, v204\n"
"v_add_f32 v126, v126, v204\n"
"v_add_f32 v127, v127, v204\n"
"v_exp_f32 v96, v96\n"
"v_exp_f32 v97, v97\n"
"v_exp_f32 v98, v98\n"
"v_exp_f32 v99, v99\n"
"s_add_u32 s73, s73, 131072\n"
"s_add_u32 m0, s2, 128000\n"
"s_nop 0\n"
"buffer_load_dwordx4 v8, s[40:43], s73 offen offset:0 lds\n"
"v_cvt_pk_bf16_f32 v160, v96, v97\n"
"v_exp_f32 v100, v100\n"
"v_exp_f32 v101, v101\n"
"v_cvt_pk_bf16_f32 v161, v98, v99\n"
"v_exp_f32 v102, v102\n"
"v_exp_f32 v103, v103\n"
"v_cvt_pk_bf16_f32 v164, v100, v101\n"
"v_exp_f32 v104, v104\n"
"buffer_load_dwordx4 v9, s[40:43], s73 offen offset:1024 lds\n"
"v_exp_f32 v105, v105\n"
"v_cvt_pk_bf16_f32 v165, v102, v103\n"
"v_exp_f32 v106, v106\n"
"v_exp_f32 v107, v107\n"
"v_cvt_pk_bf16_f32 v168, v104, v105\n"
"v_exp_f32 v108, v108\n"
"v_exp_f32 v109, v109\n"
"v_cvt_pk_bf16_f32 v169, v106, v107\n"
"buffer_load_dwordx4 v10, s[40:43], s73 offen offset:2048 lds\n"
"v_exp_f32 v110, v110\n"
"v_exp_f32 v111, v111\n"
"v_cvt_pk_bf16_f32 v172, v108, v109\n"
"v_exp_f32 v112, v112\n"
"v_exp_f32 v113, v113\n"
"v_cvt_pk_bf16_f32 v173, v110, v111\n"
"v_exp_f32 v114, v114\n"
"v_exp_f32 v115, v115\n"
"buffer_load_dwordx4 v11, s[40:43], s73 offen offset:3072 lds\n"
"s_add_u32 s73, s73, 131072\n"
"v_cvt_pk_bf16_f32 v162, v112, v113\n"
"v_exp_f32 v116, v116\n"
"v_exp_f32 v117, v117\n"
"v_cvt_pk_bf16_f32 v163, v114, v115\n"
"v_exp_f32 v118, v118\n"
"v_exp_f32 v119, v119\n"
"v_cvt_pk_bf16_f32 v166, v116, v117\n"
"v_exp_f32 v120, v120\n"
"v_exp_f32 v121, v121\n"
"s_sub_u32 s91, s53, 16\n"
"s_lshl2_add_u32 s91, s91, s11\n"
"L_tsl0:\n"
"s_cmp_lt_u32 s91, 32\n"
"s_cbranch_scc0 L_tsl1\n"
"s_add_u32 s90, s91, 0\n"
"s_mov_b32 s92, s18\n"
"s_and_b32 s93, s19, 0xffff\n"
"s_mov_b32 s94, 0x480000\n"
"s_mov_b32 s95, 0x20000\n"
"s_lshl_b32 s80, s90, 17\n"
"s_lshl_b32 s81, s52, 10\n"
"s_add_u32 s80, s80, s81\n"
"s_add_u32 s80, s80, 512\n"
"v_lshrrev_b32 v6, 2, v0\n"
"v_lshlrev_b32 v6, 13, v6\n"
"v_lshl_add_u32 v6, v5, 7, v6\n"
"s_branch L_tsle\n"
"L_tsl1:\n"
"s_sub_u32 s90, s91, 32\n"
"s_bitcmp1_b32 s90, 4\n"
"s_cselect_b32 s92, s16, s14\n"
"s_cselect_b32 s93, s17, s15\n"
"s_and_b32 s93, s93, 0xffff\n"
"s_mov_b32 s94, 0x800000\n"
"s_mov_b32 s95, 0x20000\n"
"s_and_b32 s80, s90, 15\n"
"s_lshl_b32 s80, s80, 13\n"
"s_lshl_b32 s81, s52, 20\n"
"s_add_u32 s80, s80, s81\n"
"s_add_u32 s80, s80, 0xe0000\n"
"v_mov_b32 v6, v254\n"
"L_tsle:\n"
"buffer_load_dword v253, v6, s[92:95], s80 offen\n"
"s_mov_b32 s72, 0x6229020\n"
"s_bitcmp1_b32 s52, 0\n"
"s_cselect_b32 s72, 0x16a6b128, s72\n"
"s_mov_b32 s80, 0x2b48c0e6\n"
"s_bitcmp1_b32 s52, 0\n"
"s_cselect_b32 s80, 0x3bcce1ee, s80\n"
"s_mov_b32 s81, 0xbda72\n"
"s_bitcmp1_b32 s52, 0\n"
"s_cselect_b32 s81, 0xffb7a, s81\n"
"s_cmp_ge_u32 s77, 6\n"
"s_cselect_b32 s72, s80, s72\n"
"s_cselect_b32 s80, 6, 0\n"
"s_cmp_ge_u32 s77, 12\n"
"s_cselect_b32 s72, s81, s72\n"
"s_cselect_b32 s80, 12, s80\n"
"s_sub_u32 s77, s77, s80\n"
"s_mul_i32 s77, s77, 5\n"
"s_lshr_b32 s72, s72, s77\n"
"s_and_b32 s72, s72, 31\n"
"s_mul_i32 s72, s72, 147456\n"
"s_add_u32 s80, s20, s72\n"
"s_addc_u32 s81, s21, 0\n"
"s_mov_b32 s94, 147456\n"
"v_lshrrev_b32 v243, 2, v0\n"
"v_lshlrev_b32 v243, 12, v243\n"
"v_lshl_add_u32 v243, v5, 7, v243\n"
"s_cmp_eq_u32 s54, 1\n"
"s_cselect_b32 s92, s85, s80\n"
"s_cselect_b32 s93, s86, s81\n"
"s_cselect_b32 s87, s76, s78\n"
"s_cselect_b32 s7, 20, 15\n"
"s_cselect_b64 vcc, -1, 0\n"
"s_cselect_b32 s94, 0x480000, s94\n"
"s_mov_b32 s95, 0x20000\n"
"s_and_b32 s93, s93, 0xffff\n"
"s_lshl_b32 s7, 1, s7\n"
"v_cndmask_b32 v243, v254, v243, vcc\n"
"s_mov_b32 s77, 32768\n"
"s_mov_b32 s78, 0\n"
"s_mov_b32 s76, 7\n"
"s_waitcnt vmcnt(19) lgkmcnt(0)\n"
"s_barrier\n"
"v_add_u32 v21, 32768, v24\n"
"L_att:\n"
"v_mfma_f32_16x16x32_bf16 v[128:131], a[144:147], v[32:35], v[192:195]\n"
"v_cvt_pk_bf16_f32 v167, v118, v119\n"
"v_mfma_f32_16x16x32_bf16 v[132:135], a[144:147], v[48:51], v[196:199]\n"
"v_exp_f32 v122, v122\n"
"v_mfma_f32_16x16x32_bf16 v[136:139], a[144:147], v[64:67], v[200:203]\n"
"ds_read_b64_tr_b16 a[176:177], v22 offset:0\n"
"ds_read_b64_tr_b16 a[178:179], v22 offset:4096\n"
"v_mfma_f32_16x16x32_bf16 v[140:143], a[144:147], v[80:83], v[204:207]\n"
"v_exp_f32 v123, v123\n"
"v_mfma_f32_16x16x32_bf16 v[128:131], a[148:151], v[36:39], v[128:131]\n"
"v_cvt_pk_bf16_f32 v170, v120, v121\n"
"v_mfma_f32_16x16x32_bf16 v[132:135], a[148:151], v[52:55], v[132:135]\n"
"v_exp_f32 v124, v124\n"
"v_mfma_f32_16x16x32_bf16 v[136:139], a[148:151], v[68:71], v[136:139]\n"
"ds_read_b64_tr_b16 a[180:181], v23 offset:0\n"
"ds_read_b64_tr_b16 a[182:183], v23 offset:4096\n"
"v_mfma_f32_16x16x32_bf16 v[140:143], a[148:151], v[84:87], v[140:143]\n"
"v_exp_f32 v125, v125\n"
"v_mfma_f32_16x16x32_bf16 v[128:131], a[152:155], v[40:43], v[128:131]\n"
"v_cvt_pk_bf16_f32 v171, v122, v123\n"
"v_mfma_f32_16x16x32_bf16 v[132:135], a[152:155], v[56:59], v[132:135]\n"
"v_exp_f32 v126, v126\n"
"v_mfma_f32_16x16x32_bf16 v[136:139], a[152:155], v[72:75], v[136:139]\n"
"ds_read_b64_tr_b16 a[184:185], v22 offset:512\n"
"ds_read_b64_tr_b16 a[186:187], v22 offset:4608\n"
"v_mfma_f32_16x16x32_bf16 v[140:143], a[152:155], v[88:91], v[140:143]\n"
"v_exp_f32 v127, v127\n"
"v_mfma_f32_16x16x32_bf16 v[128:131], a[156:159], v[44:47], v[128:131]\n"
"v_cvt_pk_bf16_f32 v174, v124, v125\n"
"v_mfma_f32_16x16x32_bf16 v[132:135], a[156:159], v[60:63], v[132:135]\n"
"ds_read_b64_tr_b16 a[188:189], v23 offset:512\n"
"ds_read_b64_tr_b16 a[190:191], v23 offset:4608\n"
"v_mfma_f32_16x16x32_bf16 v[136:139], a[156:159], v[76:79], v[136:139]\n"
"ds_read_b64_tr_b16 a[192:193], v22 offset:1024\n"
"ds_read_b64_tr_b16 a[194:195], v22 offset:5120\n"
"v_mfma_f32_16x16x32_bf16 v[140:143], a[156:159], v[92:95], v[140:143]\n"
"ds_read_b64_tr_b16 a[196:197], v23 offset:1024\n"
"ds_read_b64_tr_b16 a[198:199], v23 offset:5120\n"
"v_mfma_f32_16x16x32_bf16 v[144:147], a[160:163], v[32:35], v[192:195]\n"
"v_cvt_pk_bf16_f32 v175, v126, v127\n"
"v_mfma_f32_16x16x32_bf16 v[148:151], a[160:163], v[48:51], v[196:199]\n"
"v_exp_f32 v128, v128\n"
"v_mfma_f32_16x16x32_bf16 v[152:155], a[160:163], v[64:67], v[200:203]\n"
"ds_read_b64_tr_b16 a[200:201], v22 offset:1536\n"
"ds_read_b64_tr_b16 a[202:203], v22 offset:5632\n"
"v_mfma_f32_16x16x32_bf16 v[156:159], a[160:163], v[80:83], v[204:207]\n"
"v_exp_f32 v129, v129\n"
"v_mfma_f32_16x16x32_bf16 v[144:147], a[164:167], v[36:39], v[144:147]\n"
"ds_read_b64_tr_b16 a[204:205], v23 offset:1536\n"
"ds_read_b64_tr_b16 a[206:207], v23 offset:5632\n"
"v_mfma_f32_16x16x32_bf16 v[148:151], a[164:167], v[52:55], v[148:151]\n"
"v_exp_f32 v130, v130\n"
"v_mfma_f32_16x16x32_bf16 v[152:155], a[164:167], v[68:71], v[152:155]\n"
"v_mfma_f32_16x16x32_bf16 v[156:159], a[164:167], v[84:87], v[156:159]\n"
"v_exp_f32 v131, v131\n"
"v_mfma_f32_16x16x32_bf16 v[144:147], a[168:171], v[40:43], v[144:147]\n"
"v_cvt_pk_bf16_f32 v176, v128, v129\n"
"s_add_u32 m0, s78, s75\n"
"v_mfma_f32_16x16x32_bf16 v[148:151], a[168:171], v[56:59], v[148:151]\n"
"v_exp_f32 v132, v132\n"
"v_mfma_f32_16x16x32_bf16 v[152:155], a[168:171], v[72:75], v[152:155]\n"
"buffer_load_dwordx4 v12, s[44:47], s74 offen offset:0 lds\n"
"v_mfma_f32_16x16x32_bf16 v[156:159], a[168:171], v[88:91], v[156:159]\n"
"v_exp_f32 v133, v133\n"
"v_mfma_f32_16x16x32_bf16 v[144:147], a[172:175], v[44:47], v[144:147]\n"
"v_cvt_pk_bf16_f32 v177, v130, v131\n"
"s_waitcnt lgkmcnt(0)\n"
"v_mfma_f32_16x16x32_bf16 v[148:151], a[172:175], v[60:63], v[148:151]\n"
"v_exp_f32 v134, v134\n"
"v_mfma_f32_16x16x32_bf16 v[152:155], a[172:175], v[76:79], v[152:155]\n"
"buffer_load_dwordx4 v13, s[44:47], s74 offen offset:1024 lds\n"
"v_mfma_f32_16x16x32_bf16 v[156:159], a[172:175], v[92:95], v[156:159]\n"
"v_exp_f32 v135, v135\n"
"v_mfma_f32_16x16x32_bf16 a[0:3], a[176:179], v[160:163], a[0:3]\n"
"v_cvt_pk_bf16_f32 v180, v132, v133\n"
"v_mfma_f32_16x16x32_bf16 a[4:7], a[176:179], v[164:167], a[4:7]\n"
"v_exp_f32 v136, v136\n"
"v_mfma_f32_16x16x32_bf16 a[8:11], a[176:179], v[168:171], a[8:11]\n"
"ds_read_b128 a[144:147], v21 offset:0\n"
"ds_read_b128 a[148:151], v21 offset:512\n"
"v_mfma_f32_16x16x32_bf16 a[12:15], a[176:179], v[172:175], a[12:15]\n"
"v_exp_f32 v137, v137\n"
"v_mfma_f32_16x16x32_bf16 a[28:31], a[180:183], v[172:175], a[28:31]\n"
"v_cvt_pk_bf16_f32 v181, v134, v135\n"
"v_mfma_f32_16x16x32_bf16 a[24:27], a[180:183], v[168:171], a[24:27]\n"
"v_exp_f32 v138, v138\n"
"v_mfma_f32_16x16x32_bf16 a[20:23], a[180:183], v[164:167], a[20:23]\n"
"buffer_load_dwordx4 v14, s[44:47], s74 offen offset:2048 lds\n"
"v_mfma_f32_16x16x32_bf16 a[16:19], a[180:183], v[160:163], a[16:19]\n"
"v_exp_f32 v139, v139\n"
"v_mfma_f32_16x16x32_bf16 a[32:35], a[184:187], v[160:163], a[32:35]\n"
"v_cvt_pk_bf16_f32 v184, v136, v137\n"
"v_mfma_f32_16x16x32_bf16 a[36:39], a[184:187], v[164:167], a[36:39]\n"
"v_exp_f32 v140, v140\n"
"v_mfma_f32_16x16x32_bf16 a[40:43], a[184:187], v[168:171], a[40:43]\n"
"ds_read_b128 a[152:155], v21 offset:1024\n"
"ds_read_b128 a[156:159], v21 offset:1536\n"
"v_mfma_f32_16x16x32_bf16 a[44:47], a[184:187], v[172:175], a[44:47]\n"
"v_exp_f32 v141, v141\n"
"v_mfma_f32_16x16x32_bf16 a[60:63], a[188:191], v[172:175], a[60:63]\n"
"v_cvt_pk_bf16_f32 v185, v138, v139\n"
"v_mfma_f32_16x16x32_bf16 a[56:59], a[188:191], v[168:171], a[56:59]\n"
"v_exp_f32 v142, v142\n"
"v_mfma_f32_16x16x32_bf16 a[52:55], a[188:191], v[164:167], a[52:55]\n"
"buffer_load_dwordx4 v15, s[44:47], s74 offen offset:3072 lds\n"
"v_mfma_f32_16x16x32_bf16 a[48:51], a[188:191], v[160:163], a[48:51]\n"
"v_exp_f32 v143, v143\n"
"v_mfma_f32_16x16x32_bf16 a[64:67], a[192:195], v[160:163], a[64:67]\n"
"v_cvt_pk_bf16_f32 v188, v140, v141\n"
"s_add_u32 s74, s74, 16384\n"
"v_mfma_f32_16x16x32_bf16 a[68:71], a[192:195], v[164:167], a[68:71]\n"
"v_exp_f32 v144, v144\n"
"v_mfma_f32_16x16x32_bf16 a[72:75], a[192:195], v[168:171], a[72:75]\n"
"ds_read_b128 a[160:163], v21 offset:4096\n"
"ds_read_b128 a[164:167], v21 offset:4608\n"
"v_mfma_f32_16x16x32_bf16 a[76:79], a[192:195], v[172:175], a[76:79]\n"
"v_exp_f32 v145, v145\n"
"v_mfma_f32_16x16x32_bf16 a[92:95], a[196:199], v[172:175], a[92:95]\n"
"v_cvt_pk_bf16_f32 v189, v142, v143\n"
"s_add_u32 m0, m0, 16384\n"
"v_mfma_f32_16x16x32_bf16 a[88:91], a[196:199], v[168:171], a[88:91]\n"
"v_exp_f32 v146, v146\n"
"v_mfma_f32_16x16x32_bf16 a[84:87], a[196:199], v[164:167], a[84:87]\n"
"buffer_store_dwordx4 v[224:227], v1, s[36:39], s55 offen sc1\n"
"v_mfma_f32_16x16x32_bf16 a[80:83], a[196:199], v[160:163], a[80:83]\n"
"v_exp_f32 v147, v147\n"
"v_mfma_f32_16x16x32_bf16 a[96:99], a[200:203], v[160:163], a[96:99]\n"
"v_cvt_pk_bf16_f32 v178, v144, v145\n"
"s_add_u32 s55, s55, 1024\n"
"v_mfma_f32_16x16x32_bf16 a[100:103], a[200:203], v[164:167], a[100:103]\n"
"v_exp_f32 v148, v148\n"
"v_mfma_f32_16x16x32_bf16 a[104:107], a[200:203], v[168:171], a[104:107]\n"
"ds_read_b128 a[168:171], v21 offset:5120\n"
"ds_read_b128 a[172:175], v21 offset:5632\n"
"v_mfma_f32_16x16x32_bf16 a[108:111], a[200:203], v[172:175], a[108:111]\n"
"v_exp_f32 v149, v149\n"
"v_mfma_f32_16x16x32_bf16 a[124:127], a[204:207], v[172:175], a[124:127]\n"
"v_cvt_pk_bf16_f32 v179, v146, v147\n"
"v_mfma_f32_16x16x32_bf16 a[120:123], a[204:207], v[168:171], a[120:123]\n"
"v_exp_f32 v150, v150\n"
"v_mfma_f32_16x16x32_bf16 a[116:119], a[204:207], v[164:167], a[116:119]\n"
"v_mfma_f32_16x16x32_bf16 a[112:115], a[204:207], v[160:163], a[112:115]\n"
"v_exp_f32 v151, v151\n"
"v_mfma_f32_16x16x32_bf16 a[128:131], v[208:211], v[160:163], a[128:131]\n"
"v_cvt_pk_bf16_f32 v182, v148, v149\n"
"v_mfma_f32_16x16x32_bf16 a[132:135], v[208:211], v[164:167], a[132:135]\n"
"v_exp_f32 v152, v152\n"
"v_mfma_f32_16x16x32_bf16 a[136:139], v[208:211], v[168:171], a[136:139]\n"
"s_waitcnt lgkmcnt(0)\n"
"v_mfma_f32_16x16x32_bf16 a[140:143], v[208:211], v[172:175], a[140:143]\n"
"v_exp_f32 v153, v153\n"
"v_mfma_f32_16x16x32_bf16 v[96:99], a[144:147], v[32:35], v[192:195]\n"
"v_cvt_pk_bf16_f32 v183, v150, v151\n"
"s_mov_b32 s78, s77\n"
"v_mfma_f32_16x16x32_bf16 v[100:103], a[144:147], v[48:51], v[196:199]\n"
"v_exp_f32 v154, v154\n"
"v_mfma_f32_16x16x32_bf16 v[104:107], a[144:147], v[64:67], v[200:203]\n"
"ds_read_b64_tr_b16 a[176:177], v22 offset:8192\n"
"ds_read_b64_tr_b16 a[178:179], v22 offset:12288\n"
"v_mfma_f32_16x16x32_bf16 v[108:111], a[144:147], v[80:83], v[204:207]\n"
"v_exp_f32 v155, v155\n"
"v_mfma_f32_16x16x32_bf16 v[96:99], a[148:151], v[36:39], v[96:99]\n"
"v_cvt_pk_bf16_f32 v186, v152, v153\n"
"s_add_u32 s77, s77, 32768\n"
"v_mfma_f32_16x16x32_bf16 v[100:103], a[148:151], v[52:55], v[100:103]\n"
"v_exp_f32 v156, v156\n"
"v_mfma_f32_16x16x32_bf16 v[104:107], a[148:151], v[68:71], v[104:107]\n"
"ds_read_b64_tr_b16 a[180:181], v23 offset:8192\n"
"ds_read_b64_tr_b16 a[182:183], v23 offset:12288\n"
"v_mfma_f32_16x16x32_bf16 v[108:111], a[148:151], v[84:87], v[108:111]\n"
"v_exp_f32 v157, v157\n"
"v_mfma_f32_16x16x32_bf16 v[96:99], a[152:155], v[40:43], v[96:99]\n"
"v_cvt_pk_bf16_f32 v187, v154, v155\n"
"s_cmp_eq_u32 s77, 98304\n"
"v_mfma_f32_16x16x32_bf16 v[100:103], a[152:155], v[56:59], v[100:103]\n"
"v_exp_f32 v158, v158\n"
"v_mfma_f32_16x16x32_bf16 v[104:107], a[152:155], v[72:75], v[104:107]\n"
"ds_read_b64_tr_b16 a[184:185], v22 offset:8704\n"
"ds_read_b64_tr_b16 a[186:187], v22 offset:12800\n"
"v_mfma_f32_16x16x32_bf16 v[108:111], a[152:155], v[88:91], v[108:111]\n"
"v_exp_f32 v159, v159\n"
"v_mfma_f32_16x16x32_bf16 v[96:99], a[156:159], v[44:47], v[96:99]\n"
"v_cvt_pk_bf16_f32 v190, v156, v157\n"
"s_cselect_b32 s77, 0, s77\n"
"v_mfma_f32_16x16x32_bf16 v[100:103], a[156:159], v[60:63], v[100:103]\n"
"ds_read_b64_tr_b16 a[188:189], v23 offset:8704\n"
"ds_read_b64_tr_b16 a[190:191], v23 offset:12800\n"
"v_mfma_f32_16x16x32_bf16 v[104:107], a[156:159], v[76:79], v[104:107]\n"
"ds_read_b64_tr_b16 a[192:193], v22 offset:9216\n"
"ds_read_b64_tr_b16 a[194:195], v22 offset:13312\n"
"v_mfma_f32_16x16x32_bf16 v[108:111], a[156:159], v[92:95], v[108:111]\n"
"ds_read_b64_tr_b16 a[196:197], v23 offset:9216\n"
"ds_read_b64_tr_b16 a[198:199], v23 offset:13312\n"
"v_mfma_f32_16x16x32_bf16 v[112:115], a[160:163], v[32:35], v[192:195]\n"
"v_cvt_pk_bf16_f32 v191, v158, v159\n"
"v_mfma_f32_16x16x32_bf16 v[116:119], a[160:163], v[48:51], v[196:199]\n"
"v_exp_f32 v96, v96\n"
"v_mfma_f32_16x16x32_bf16 v[120:123], a[160:163], v[64:67], v[200:203]\n"
"ds_read_b64_tr_b16 a[200:201], v22 offset:9728\n"
"ds_read_b64_tr_b16 a[202:203], v22 offset:13824\n"
"v_mfma_f32_16x16x32_bf16 v[124:127], a[160:163], v[80:83], v[204:207]\n"
"v_exp_f32 v97, v97\n"
"v_mfma_f32_16x16x32_bf16 v[112:115], a[164:167], v[36:39], v[112:115]\n"
"ds_read_b64_tr_b16 a[204:205], v23 offset:9728\n"
"ds_read_b64_tr_b16 a[206:207], v23 offset:13824\n"
"v_mfma_f32_16x16x32_bf16 v[116:119], a[164:167], v[52:55], v[116:119]\n"
"v_exp_f32 v98, v98\n"
"v_mfma_f32_16x16x32_bf16 v[120:123], a[164:167], v[68:71], v[120:123]\n"
"v_add_u32 v22, s77, v25\n"
"v_add_u32 v23, s77, v26\n"
"v_mfma_f32_16x16x32_bf16 v[124:127], a[164:167], v[84:87], v[124:127]\n"
"v_exp_f32 v99, v99\n"
"v_mfma_f32_16x16x32_bf16 v[112:115], a[168:171], v[40:43], v[112:115]\n"
"v_cvt_pk_bf16_f32 v160, v96, v97\n"
"v_mfma_f32_16x16x32_bf16 v[116:119], a[168:171], v[56:59], v[116:119]\n"
"v_exp_f32 v100, v100\n"
"v_mfma_f32_16x16x32_bf16 v[120:123], a[168:171], v[72:75], v[120:123]\n"
"buffer_load_dwordx4 v16, s[48:51], s83 offen offset:0 lds\n"
"v_mfma_f32_16x16x32_bf16 v[124:127], a[168:171], v[88:91], v[124:127]\n"
"v_exp_f32 v101, v101\n"
"v_mfma_f32_16x16x32_bf16 v[112:115], a[172:175], v[44:47], v[112:115]\n"
"v_cvt_pk_bf16_f32 v161, v98, v99\n"
"s_waitcnt lgkmcnt(0)\n"
"v_mfma_f32_16x16x32_bf16 v[116:119], a[172:175], v[60:63], v[116:119]\n"
"v_exp_f32 v102, v102\n"
"v_mfma_f32_16x16x32_bf16 v[120:123], a[172:175], v[76:79], v[120:123]\n"
"buffer_load_dwordx4 v17, s[48:51], s83 offen offset:1024 lds\n"
"v_mfma_f32_16x16x32_bf16 v[124:127], a[172:175], v[92:95], v[124:127]\n"
"v_exp_f32 v103, v103\n"
"v_mfma_f32_16x16x32_bf16 a[0:3], a[176:179], v[176:179], a[0:3]\n"
"v_cvt_pk_bf16_f32 v164, v100, v101\n"
"v_mfma_f32_16x16x32_bf16 a[4:7], a[176:179], v[180:183], a[4:7]\n"
"v_exp_f32 v104, v104\n"
"v_mfma_f32_16x16x32_bf16 a[8:11], a[176:179], v[184:187], a[8:11]\n"
"ds_read_b128 a[144:147], v21 offset:8192\n"
"ds_read_b128 a[148:151], v21 offset:8704\n"
"v_mfma_f32_16x16x32_bf16 a[12:15], a[176:179], v[188:191], a[12:15]\n"
"v_exp_f32 v105, v105\n"
"v_mfma_f32_16x16x32_bf16 a[28:31], a[180:183], v[188:191], a[28:31]\n"
"v_cvt_pk_bf16_f32 v165, v102, v103\n"
"v_mfma_f32_16x16x32_bf16 a[24:27], a[180:183], v[184:187], a[24:27]\n"
"v_exp_f32 v106, v106\n"
"v_mfma_f32_16x16x32_bf16 a[20:23], a[180:183], v[180:183], a[20:23]\n"
"buffer_load_dwordx4 v18, s[48:51], s83 offen offset:2048 lds\n"
"v_mfma_f32_16x16x32_bf16 a[16:19], a[180:183], v[176:179], a[16:19]\n"
"v_exp_f32 v107, v107\n"
"v_mfma_f32_16x16x32_bf16 a[32:35], a[184:187], v[176:179], a[32:35]\n"
"v_cvt_pk_bf16_f32 v168, v104, v105\n"
"v_mfma_f32_16x16x32_bf16 a[36:39], a[184:187], v[180:183], a[36:39]\n"
"v_exp_f32 v108, v108\n"
"v_mfma_f32_16x16x32_bf16 a[40:43], a[184:187], v[184:187], a[40:43]\n"
"ds_read_b128 a[152:155], v21 offset:9216\n"
"ds_read_b128 a[156:159], v21 offset:9728\n"
"v_mfma_f32_16x16x32_bf16 a[44:47], a[184:187], v[188:191], a[44:47]\n"
"v_exp_f32 v109, v109\n"
"v_mfma_f32_16x16x32_bf16 a[60:63], a[188:191], v[188:191], a[60:63]\n"
"v_cvt_pk_bf16_f32 v169, v106, v107\n"
"v_mfma_f32_16x16x32_bf16 a[56:59], a[188:191], v[184:187], a[56:59]\n"
"v_exp_f32 v110, v110\n"
"v_mfma_f32_16x16x32_bf16 a[52:55], a[188:191], v[180:183], a[52:55]\n"
"buffer_load_dwordx4 v19, s[48:51], s83 offen offset:3072 lds\n"
"v_mfma_f32_16x16x32_bf16 a[48:51], a[188:191], v[176:179], a[48:51]\n"
"v_exp_f32 v111, v111\n"
"v_mfma_f32_16x16x32_bf16 a[64:67], a[192:195], v[176:179], a[64:67]\n"
"v_cvt_pk_bf16_f32 v172, v108, v109\n"
"s_add_u32 s83, s83, 16384\n"
"v_mfma_f32_16x16x32_bf16 a[68:71], a[192:195], v[180:183], a[68:71]\n"
"v_exp_f32 v112, v112\n"
"v_mfma_f32_16x16x32_bf16 a[72:75], a[192:195], v[184:187], a[72:75]\n"
"ds_read_b128 a[160:163], v21 offset:12288\n"
"ds_read_b128 a[164:167], v21 offset:12800\n"
"v_mfma_f32_16x16x32_bf16 a[76:79], a[192:195], v[188:191], a[76:79]\n"
"v_exp_f32 v113, v113\n"
"v_mfma_f32_16x16x32_bf16 a[92:95], a[196:199], v[188:191], a[92:95]\n"
"v_cvt_pk_bf16_f32 v173, v110, v111\n"
"v_mfma_f32_16x16x32_bf16 a[88:91], a[196:199], v[184:187], a[88:91]\n"
"v_exp_f32 v114, v114\n"
"v_mfma_f32_16x16x32_bf16 a[84:87], a[196:199], v[180:183], a[84:87]\n"
"buffer_load_dword v253, v243, s[92:95], s87 offen\n"
"v_mfma_f32_16x16x32_bf16 a[80:83], a[196:199], v[176:179], a[80:83]\n"
"v_exp_f32 v115, v115\n"
"v_mfma_f32_16x16x32_bf16 a[96:99], a[200:203], v[176:179], a[96:99]\n"
"v_cvt_pk_bf16_f32 v162, v112, v113\n"
"s_add_u32 s87, s87, s7\n"
"v_mfma_f32_16x16x32_bf16 a[100:103], a[200:203], v[180:183], a[100:103]\n"
"v_exp_f32 v116, v116\n"
"v_mfma_f32_16x16x32_bf16 a[104:107], a[200:203], v[184:187], a[104:107]\n"
"ds_read_b128 a[168:171], v21 offset:13312\n"
"ds_read_b128 a[172:175], v21 offset:13824\n"
"v_mfma_f32_16x16x32_bf16 a[108:111], a[200:203], v[188:191], a[108:111]\n"
"v_exp_f32 v117, v117\n"
"v_mfma_f32_16x16x32_bf16 a[124:127], a[204:207], v[188:191], a[124:127]\n"
"v_cvt_pk_bf16_f32 v163, v114, v115\n"
"v_mfma_f32_16x16x32_bf16 a[120:123], a[204:207], v[184:187], a[120:123]\n"
"v_exp_f32 v118, v118\n"
"v_mfma_f32_16x16x32_bf16 a[116:119], a[204:207], v[180:183], a[116:119]\n"
"v_add_u32 v21, s77, v24\n"
"s_sub_u32 s76, s76, 1\n"
"v_mfma_f32_16x16x32_bf16 a[112:115], a[204:207], v[176:179], a[112:115]\n"
"v_exp_f32 v119, v119\n"
"v_mfma_f32_16x16x32_bf16 a[128:131], v[208:211], v[176:179], a[128:131]\n"
"v_cvt_pk_bf16_f32 v166, v116, v117\n"
"s_cmp_lg_u32 s76, 0\n"
"v_mfma_f32_16x16x32_bf16 a[132:135], v[208:211], v[180:183], a[132:135]\n"
"v_exp_f32 v120, v120\n"
"v_mfma_f32_16x16x32_bf16 a[136:139], v[208:211], v[184:187], a[136:139]\n"
"s_waitcnt vmcnt(11) lgkmcnt(0)\n"
"s_barrier\n"
"v_mfma_f32_16x16x32_bf16 a[140:143], v[208:211], v[188:191], a[140:143]\n"
"v_exp_f32 v121, v121\n"
"s_cbranch_scc1 L_att\n"
"s_mov_b32 s90, s79\n"
"s_waitcnt vmcnt(1)\n"
"s_barrier\n"
"s_cmp_eq_u32 s54, 1\n"
"s_cbranch_scc1 L_tB0\n"
"s_lshr_b32 s68, s11, 1\n"
"s_lshl1_add_u32 s5, s70, s68\n"
"s_lshl_b32 s5, s5, 7\n"
"s_add_u32 s91, s5, 0x2ae0000\n"
"s_mov_b32 s98, 0x5\n"
"s_cmp_eq_u32 s69, 1\n"
"s_cselect_b32 s98, 0x50, s98\n"
"s_cmp_eq_u32 s69, 2\n"
"s_cselect_b32 s98, 0x500, s98\n"
"s_cmp_eq_u32 s69, 3\n"
"s_cselect_b32 s98, 0x5000, s98\n"
"s_mov_b32 s8, 0x5\n"
"s_cmp_eq_u32 s69, 1\n"
"s_cselect_b32 s8, 0x50, s8\n"
"s_cmp_eq_u32 s69, 2\n"
"s_cselect_b32 s8, 0x500, s8\n"
"s_cmp_eq_u32 s69, 3\n"
"s_cselect_b32 s8, 0x5000, s8\n"
"s_mov_b32 s99, 0\n"
"s_mov_b32 s9, 0\n"
"buffer_load_dwordx2 v[248:249], v251, s[36:39], s91 offen sc1\n"
"s_add_u32 s44, s32, 0x9d8000\n"
"s_addc_u32 s45, s33, 0\n"
"s_and_b32 s45, s45, 0xffff\n"
"s_mov_b32 s46, 0x1000000\n"
"s_mov_b32 s47, 0x20000\n"
"v_mfma_f32_16x16x32_bf16 v[128:131], a[144:147], v[32:35], v[192:195]\n"
"v_cvt_pk_bf16_f32 v167, v118, v119\n"
"v_mfma_f32_16x16x32_bf16 v[132:135], a[144:147], v[48:51], v[196:199]\n"
"v_exp_f32 v122, v122\n"
"v_mfma_f32_16x16x32_bf16 v[136:139], a[144:147], v[64:67], v[200:203]\n"
"ds_read_b64_tr_b16 a[176:177], v22 offset:0\n"
"ds_read_b64_tr_b16 a[178:179], v22 offset:4096\n"
"v_mfma_f32_16x16x32_bf16 v[140:143], a[144:147], v[80:83], v[204:207]\n"
"v_exp_f32 v123, v123\n"
"v_mfma_f32_16x16x32_bf16 v[128:131], a[148:151], v[36:39], v[128:131]\n"
"v_cvt_pk_bf16_f32 v170, v120, v121\n"
"v_mfma_f32_16x16x32_bf16 v[132:135], a[148:151], v[52:55], v[132:135]\n"
"v_exp_f32 v124, v124\n"
"v_mfma_f32_16x16x32_bf16 v[136:139], a[148:151], v[68:71], v[136:139]\n"
"ds_read_b64_tr_b16 a[180:181], v23 offset:0\n"
"ds_read_b64_tr_b16 a[182:183], v23 offset:4096\n"
"v_mfma_f32_16x16x32_bf16 v[140:143], a[148:151], v[84:87], v[140:143]\n"
"v_exp_f32 v125, v125\n"
"v_mfma_f32_16x16x32_bf16 v[128:131], a[152:155], v[40:43], v[128:131]\n"
"v_cvt_pk_bf16_f32 v171, v122, v123\n"
"v_mfma_f32_16x16x32_bf16 v[132:135], a[152:155], v[56:59], v[132:135]\n"
"v_exp_f32 v126, v126\n"
"v_mfma_f32_16x16x32_bf16 v[136:139], a[152:155], v[72:75], v[136:139]\n"
"ds_read_b64_tr_b16 a[184:185], v22 offset:512\n"
"ds_read_b64_tr_b16 a[186:187], v22 offset:4608\n"
"v_mfma_f32_16x16x32_bf16 v[140:143], a[152:155], v[88:91], v[140:143]\n"
"v_exp_f32 v127, v127\n"
"v_mfma_f32_16x16x32_bf16 v[128:131], a[156:159], v[44:47], v[128:131]\n"
"v_cvt_pk_bf16_f32 v174, v124, v125\n"
"v_mfma_f32_16x16x32_bf16 v[132:135], a[156:159], v[60:63], v[132:135]\n"
"ds_read_b64_tr_b16 a[188:189], v23 offset:512\n"
"ds_read_b64_tr_b16 a[190:191], v23 offset:4608\n"
"v_mfma_f32_16x16x32_bf16 v[136:139], a[156:159], v[76:79], v[136:139]\n"
"ds_read_b64_tr_b16 a[192:193], v22 offset:1024\n"
"ds_read_b64_tr_b16 a[194:195], v22 offset:5120\n"
"v_mfma_f32_16x16x32_bf16 v[140:143], a[156:159], v[92:95], v[140:143]\n"
"ds_read_b64_tr_b16 a[196:197], v23 offset:1024\n"
"ds_read_b64_tr_b16 a[198:199], v23 offset:5120\n"
"v_mfma_f32_16x16x32_bf16 v[144:147], a[160:163], v[32:35], v[192:195]\n"
"v_cvt_pk_bf16_f32 v175, v126, v127\n"
"s_add_u32 m0, s1, 0\n"
"v_mfma_f32_16x16x32_bf16 v[148:151], a[160:163], v[48:51], v[196:199]\n"
"v_exp_f32 v128, v128\n"
"v_mfma_f32_16x16x32_bf16 v[152:155], a[160:163], v[64:67], v[200:203]\n"
"ds_read_b64_tr_b16 a[200:201], v22 offset:1536\n"
"ds_read_b64_tr_b16 a[202:203], v22 offset:5632\n"
"buffer_load_dwordx4 v28, s[40:43], s73 offen offset:0 lds\n"
"v_mfma_f32_16x16x32_bf16 v[156:159], a[160:163], v[80:83], v[204:207]\n"
"v_exp_f32 v129, v129\n"
"v_mfma_f32_16x16x32_bf16 v[144:147], a[164:167], v[36:39], v[144:147]\n"
"ds_read_b64_tr_b16 a[204:205], v23 offset:1536\n"
"ds_read_b64_tr_b16 a[206:207], v23 offset:5632\n"
"v_mfma_f32_16x16x32_bf16 v[148:151], a[164:167], v[52:55], v[148:151]\n"
"v_exp_f32 v130, v130\n"
"v_mfma_f32_16x16x32_bf16 v[152:155], a[164:167], v[68:71], v[152:155]\n"
"buffer_load_dwordx4 v29, s[40:43], s73 offen offset:1024 lds\n"
"v_mfma_f32_16x16x32_bf16 v[156:159], a[164:167], v[84:87], v[156:159]\n"
"v_exp_f32 v131, v131\n"
"v_mfma_f32_16x16x32_bf16 v[144:147], a[168:171], v[40:43], v[144:147]\n"
"v_cvt_pk_bf16_f32 v176, v128, v129\n"
"v_mfma_f32_16x16x32_bf16 v[148:151], a[168:171], v[56:59], v[148:151]\n"
"v_exp_f32 v132, v132\n"
"v_mfma_f32_16x16x32_bf16 v[152:155], a[168:171], v[72:75], v[152:155]\n"
"buffer_load_dwordx4 v30, s[40:43], s73 offen offset:2048 lds\n"
"v_mfma_f32_16x16x32_bf16 v[156:159], a[168:171], v[88:91], v[156:159]\n"
"v_exp_f32 v133, v133\n"
"v_mfma_f32_16x16x32_bf16 v[144:147], a[172:175], v[44:47], v[144:147]\n"
"v_cvt_pk_bf16_f32 v177, v130, v131\n"
"s_waitcnt lgkmcnt(0)\n"
"v_mfma_f32_16x16x32_bf16 v[148:151], a[172:175], v[60:63], v[148:151]\n"
"v_exp_f32 v134, v134\n"
"v_mfma_f32_16x16x32_bf16 v[152:155], a[172:175], v[76:79], v[152:155]\n"
"buffer_load_dwordx4 v31, s[40:43], s73 offen offset:3072 lds\n"
"v_mfma_f32_16x16x32_bf16 v[156:159], a[172:175], v[92:95], v[156:159]\n"
"v_exp_f32 v135, v135\n"
"v_mfma_f32_16x16x32_bf16 a[0:3], a[176:179], v[160:163], a[0:3]\n"
"v_cvt_pk_bf16_f32 v180, v132, v133\n"
"s_add_u32 m0, s1, 4096\n"
"v_mfma_f32_16x16x32_bf16 a[4:7], a[176:179], v[164:167], a[4:7]\n"
"v_exp_f32 v136, v136\n"
"v_mfma_f32_16x16x32_bf16 a[8:11], a[176:179], v[168:171], a[8:11]\n"
"s_mov_b64 exec, s[96:97]\n"
"buffer_load_dwordx4 v240, s[40:43], s73 offen offset:0 lds\n"
"s_mov_b64 exec, -1\n"
"s_add_u32 s73, s73, 73728\n"
"v_mfma_f32_16x16x32_bf16 a[12:15], a[176:179], v[172:175], a[12:15]\n"
"v_exp_f32 v137, v137\n"
"s_waitcnt vmcnt(5)\n"
"v_cmp_eq_u32 vcc, s34, v248\n"
"s_nop 0\n"
"s_andn2_b64 s[88:89], s[98:99], vcc\n"
"v_cmp_eq_u32 vcc, s34, v249\n"
"s_nop 0\n"
"s_andn2_b64 vcc, s[8:9], vcc\n"
"s_or_b64 s[88:89], s[88:89], vcc\n"
"s_cbranch_scc0 L_pt_ok\n"
"s_mov_b32 s55, 0x30000\n"
"L_pt_re:\n"
"s_sleep 1\n"
"buffer_load_dwordx2 v[248:249], v251, s[36:39], s91 offen sc1\n"
"s_waitcnt vmcnt(0)\n"
"v_cmp_eq_u32 vcc, s34, v248\n"
"s_nop 0\n"
"s_andn2_b64 s[88:89], s[98:99], vcc\n"
"v_cmp_eq_u32 vcc, s34, v249\n"
"s_nop 0\n"
"s_andn2_b64 vcc, s[8:9], vcc\n"
"s_or_b64 s[88:89], s[88:89], vcc\n"
"s_cbranch_scc0 L_pt_ok\n"
"s_sub_u32 s55, s55, 1\n"
"s_cmp_lg_u32 s55, 0\n"
"s_cbranch_scc1 L_pt_re\n"
"L_pt_ok:\n"
"v_mfma_f32_16x16x32_bf16 a[28:31], a[180:183], v[172:175], a[28:31]\n"
"v_cvt_pk_bf16_f32 v181, v134, v135\n"
"s_bfe_u64 s[62:63], s[58:59], 0x50000\n"
"s_mul_i32 s62, s62, 8192\n"
"s_add_u32 s72, s57, s62\n"
"v_mfma_f32_16x16x32_bf16 a[24:27], a[180:183], v[168:171], a[24:27]\n"
"v_exp_f32 v138, v138\n"
"v_mfma_f32_16x16x32_bf16 a[20:23], a[180:183], v[164:167], a[20:23]\n"
"buffer_load_dwordx4 a[144:147], v1, s[44:47], s72 offen offset:0 sc1\n"
"v_mfma_f32_16x16x32_bf16 a[16:19], a[180:183], v[160:163], a[16:19]\n"
"v_exp_f32 v139, v139\n"
"v_mfma_f32_16x16x32_bf16 a[32:35], a[184:187], v[160:163], a[32:35]\n"
"v_cvt_pk_bf16_f32 v184, v136, v137\n"
"v_mfma_f32_16x16x32_bf16 a[36:39], a[184:187], v[164:167], a[36:39]\n"
"v_exp_f32 v140, v140\n"
"v_mfma_f32_16x16x32_bf16 a[40:43], a[184:187], v[168:171], a[40:43]\n"
"buffer_load_dwordx4 a[148:151], v1, s[44:47], s72 offen offset:1024 sc1\n"
"v_mfma_f32_16x16x32_bf16 a[44:47], a[184:187], v[172:175], a[44:47]\n"
"v_exp_f32 v141, v141\n"
"v_mfma_f32_16x16x32_bf16 a[60:63], a[188:191], v[172:175], a[60:63]\n"
"v_cvt_pk_bf16_f32 v185, v138, v139\n"
"s_add_u32 s72, s72, 2048\n"
"v_mfma_f32_16x16x32_bf16 a[56:59], a[188:191], v[168:171], a[56:59]\n"
"v_exp_f32 v142, v142\n"
"v_mfma_f32_16x16x32_bf16 a[52:55], a[188:191], v[164:167], a[52:55]\n"
"buffer_load_dwordx4 a[152:155], v1, s[44:47], s72 offen offset:0 sc1\n"
"v_mfma_f32_16x16x32_bf16 a[48:51], a[188:191], v[160:163], a[48:51]\n"
"v_exp_f32 v143, v143\n"
"v_mfma_f32_16x16x32_bf16 a[64:67], a[192:195], v[160:163], a[64:67]\n"
"v_cvt_pk_bf16_f32 v188, v140, v141\n"
"v_mfma_f32_16x16x32_bf16 a[68:71], a[192:195], v[164:167], a[68:71]\n"
"v_exp_f32 v144, v144\n"
"v_mfma_f32_16x16x32_bf16 a[72:75], a[192:195], v[168:171], a[72:75]\n"
"buffer_load_dwordx4 a[156:159], v1, s[44:47], s72 offen offset:1024 sc1\n"
"v_mfma_f32_16x16x32_bf16 a[76:79], a[192:195], v[172:175], a[76:79]\n"
"v_exp_f32 v145, v145\n"
"v_mfma_f32_16x16x32_bf16 a[92:95], a[196:199], v[172:175], a[92:95]\n"
"v_cvt_pk_bf16_f32 v189, v142, v143\n"
"s_add_u32 s72, s72, 2048\n"
"v_mfma_f32_16x16x32_bf16 a[88:91], a[196:199], v[168:171], a[88:91]\n"
"v_exp_f32 v146, v146\n"
"v_mfma_f32_16x16x32_bf16 a[84:87], a[196:199], v[164:167], a[84:87]\n"
"buffer_load_dwordx4 a[160:163], v1, s[44:47], s72 offen offset:0 sc1\n"
"v_mfma_f32_16x16x32_bf16 a[80:83], a[196:199], v[160:163], a[80:83]\n"
"v_exp_f32 v147, v147\n"
"v_mfma_f32_16x16x32_bf16 a[96:99], a[200:203], v[160:163], a[96:99]\n"
"v_cvt_pk_bf16_f32 v178, v144, v145\n"
"v_mfma_f32_16x16x32_bf16 a[100:103], a[200:203], v[164:167], a[100:103]\n"
"v_exp_f32 v148, v148\n"
"v_mfma_f32_16x16x32_bf16 a[104:107], a[200:203], v[168:171], a[104:107]\n"
"buffer_load_dwordx4 a[164:167], v1, s[44:47], s72 offen offset:1024 sc1\n"
"v_mfma_f32_16x16x32_bf16 a[108:111], a[200:203], v[172:175], a[108:111]\n"
"v_exp_f32 v149, v149\n"
"v_mfma_f32_16x16x32_bf16 a[124:127], a[204:207], v[172:175], a[124:127]\n"
"v_cvt_pk_bf16_f32 v179, v146, v147\n"
"s_add_u32 s72, s72, 2048\n"
"v_mfma_f32_16x16x32_bf16 a[120:123], a[204:207], v[168:171], a[120:123]\n"
"v_exp_f32 v150, v150\n"
"v_mfma_f32_16x16x32_bf16 a[116:119], a[204:207], v[164:167], a[116:119]\n"
"buffer_load_dwordx4 a[168:171], v1, s[44:47], s72 offen offset:0 sc1\n"
"v_mfma_f32_16x16x32_bf16 a[112:115], a[204:207], v[160:163], a[112:115]\n"
"v_exp_f32 v151, v151\n"
"v_mfma_f32_16x16x32_bf16 a[128:131], v[208:211], v[160:163], a[128:131]\n"
"v_cvt_pk_bf16_f32 v182, v148, v149\n"
"v_mfma_f32_16x16x32_bf16 a[132:135], v[208:211], v[164:167], a[132:135]\n"
"v_exp_f32 v152, v152\n"
"v_mfma_f32_16x16x32_bf16 a[136:139], v[208:211], v[168:171], a[136:139]\n"
"buffer_load_dwordx4 a[172:175], v1, s[44:47], s72 offen offset:1024 sc1\n"
"v_mfma_f32_16x16x32_bf16 a[140:143], v[208:211], v[172:175], a[140:143]\n"
"v_exp_f32 v153, v153\n"
"s_add_u32 s72, s72, 2048\n"
"buffer_load_dwordx4 a[208:211], v1, s[44:47], s72 offen offset:0 sc1\n"
"ds_read_b64_tr_b16 a[176:177], v22 offset:8192\n"
"ds_read_b64_tr_b16 a[178:179], v22 offset:12288\n"
"ds_read_b64_tr_b16 a[180:181], v23 offset:8192\n"
"ds_read_b64_tr_b16 a[182:183], v23 offset:12288\n"
"ds_read_b64_tr_b16 a[184:185], v22 offset:8704\n"
"ds_read_b64_tr_b16 a[186:187], v22 offset:12800\n"
"ds_read_b64_tr_b16 a[188:189], v23 offset:8704\n"
"buffer_load_dwordx4 a[212:215], v1, s[44:47], s72 offen offset:1024 sc1\n"
"ds_read_b64_tr_b16 a[190:191], v23 offset:12800\n"
"v_cvt_pk_bf16_f32 v183, v150, v151\n"
"v_cvt_pk_bf16_f32 v186, v152, v153\n"
"v_exp_f32 v154, v154\n"
"v_exp_f32 v155, v155\n"
"ds_read_b64_tr_b16 a[192:193], v22 offset:9216\n"
"ds_read_b64_tr_b16 a[194:195], v22 offset:13312\n"
"v_cvt_pk_bf16_f32 v187, v154, v155\n"
"s_add_u32 s72, s72, 2048\n"
"buffer_load_dwordx4 a[216:219], v1, s[44:47], s72 offen offset:0 sc1\n"
"v_exp_f32 v156, v156\n"
"v_exp_f32 v157, v157\n"
"ds_read_b64_tr_b16 a[196:197], v23 offset:9216\n"
"ds_read_b64_tr_b16 a[198:199], v23 offset:13312\n"
"v_cvt_pk_bf16_f32 v190, v156, v157\n"
"v_exp_f32 v158, v158\n"
"v_exp_f32 v159, v159\n"
"buffer_load_dwordx4 a[220:223], v1, s[44:47], s72 offen offset:1024 sc1\n"
"s_add_u32 s72, s72, 2048\n"
"ds_read_b64_tr_b16 a[200:201], v22 offset:9728\n"
"ds_read_b64_tr_b16 a[202:203], v22 offset:13824\n"
"v_cvt_pk_bf16_f32 v191, v158, v159\n"
"ds_read_b64_tr_b16 a[204:205], v23 offset:9728\n"
"ds_read_b64_tr_b16 a[206:207], v23 offset:13824\n"
"s_add_u32 s84, s82, 262144\n"
"s_mov_b32 s35, 1\n"
"s_waitcnt lgkmcnt(0)\n"
"L_pv15A:\n"
"v_mfma_f32_16x16x32_bf16 a[128:131], v[208:211], v[176:179], a[128:131]\n"
"v_mfma_f32_16x16x32_bf16 a[132:135], v[208:211], v[180:183], a[132:135]\n"
"v_mfma_f32_16x16x32_bf16 a[136:139], v[208:211], v[184:187], a[136:139]\n"
"v_mfma_f32_16x16x32_bf16 a[140:143], v[208:211], v[188:191], a[140:143]\n"
"v_mfma_f32_16x16x32_bf16 a[12:15], a[176:179], v[188:191], a[12:15]\n"
"v_mfma_f32_16x16x32_bf16 a[8:11], a[176:179], v[184:187], a[8:11]\n"
"v_mfma_f32_16x16x32_bf16 a[4:7], a[176:179], v[180:183], a[4:7]\n"
"s_mov_b32 s80, 0x53800000\n"
"v_mfma_f32_16x16x32_bf16 a[0:3], a[176:179], v[176:179], a[0:3]\n"
"v_accvgpr_read_b32 v22, a128\n"
"v_accvgpr_read_b32 v23, a132\n"
"v_mfma_f32_16x16x32_bf16 a[16:19], a[180:183], v[176:179], a[16:19]\n"
"v_accvgpr_read_b32 v24, a136\n"
"v_accvgpr_read_b32 v25, a140\n"
"v_mfma_f32_16x16x32_bf16 a[20:23], a[180:183], v[180:183], a[20:23]\n"
"v_add_f32 v6, v22, v23\n"
"v_add_f32 v7, v24, v25\n"
"v_mfma_f32_16x16x32_bf16 a[24:27], a[180:183], v[184:187], a[24:27]\n"
"v_add_f32 v6, v6, v7\n"
"s_cmp_eq_u32 s35, 1\n"
"v_mfma_f32_16x16x32_bf16 a[28:31], a[180:183], v[188:191], a[28:31]\n"
"v_cmp_nge_f32 vcc, s80, v6\n"
"v_mfma_f32_16x16x32_bf16 a[44:47], a[184:187], v[188:191], a[44:47]\n"
"s_cselect_b64 vcc, vcc, 0\n"
"s_cbranch_vccnz L_slow\n"
"v_mfma_f32_16x16x32_bf16 a[40:43], a[184:187], v[184:187], a[40:43]\n"
"v_rcp_f32 v22, v22\n"
"v_mfma_f32_16x16x32_bf16 a[36:39], a[184:187], v[180:183], a[36:39]\n"
"v_rcp_f32 v23, v23\n"
"v_mfma_f32_16x16x32_bf16 a[32:35], a[184:187], v[176:179], a[32:35]\n"
"v_rcp_f32 v24, v24\n"
"v_mfma_f32_16x16x32_bf16 a[48:51], a[188:191], v[176:179], a[48:51]\n"
"v_rcp_f32 v25, v25\n"
"v_mfma_f32_16x16x32_bf16 a[52:55], a[188:191], v[180:183], a[52:55]\n"
"v_accvgpr_read_b32 v96, a0\n"
"v_accvgpr_read_b32 v97, a1\n"
"v_mfma_f32_16x16x32_bf16 a[56:59], a[188:191], v[184:187], a[56:59]\n"
"v_accvgpr_read_b32 v98, a2\n"
"v_accvgpr_read_b32 v99, a3\n"
"v_mfma_f32_16x16x32_bf16 a[60:63], a[188:191], v[188:191], a[60:63]\n"
"v_accvgpr_read_b32 v100, a16\n"
"v_accvgpr_read_b32 v101, a17\n"
"v_mfma_f32_16x16x32_bf16 a[76:79], a[192:195], v[188:191], a[76:79]\n"
"v_accvgpr_read_b32 v102, a18\n"
"v_accvgpr_read_b32 v103, a19\n"
"v_mfma_f32_16x16x32_bf16 a[72:75], a[192:195], v[184:187], a[72:75]\n"
"v_accvgpr_read_b32 v104, a4\n"
"v_accvgpr_read_b32 v105, a5\n"
"v_mfma_f32_16x16x32_bf16 a[68:71], a[192:195], v[180:183], a[68:71]\n"
"v_accvgpr_read_b32 v106, a6\n"
"v_accvgpr_read_b32 v107, a7\n"
"v_mfma_f32_16x16x32_bf16 a[64:67], a[192:195], v[176:179], a[64:67]\n"
"v_accvgpr_read_b32 v108, a20\n"
"v_accvgpr_read_b32 v109, a21\n"
"v_mfma_f32_16x16x32_bf16 a[80:83], a[196:199], v[176:179], a[80:83]\n"
"v_accvgpr_read_b32 v110, a22\n"
"v_accvgpr_read_b32 v111, a23\n"
"v_mfma_f32_16x16x32_bf16 a[84:87], a[196:199], v[180:183], a[84:87]\n"
"v_accvgpr_read_b32 v112, a8\n"
"v_accvgpr_read_b32 v113, a9\n"
"v_mfma_f32_16x16x32_bf16 a[88:91], a[196:199], v[184:187], a[88:91]\n"
"v_accvgpr_read_b32 v114, a10\n"
"v_accvgpr_read_b32 v115, a11\n"
"v_mfma_f32_16x16x32_bf16 a[92:95], a[196:199], v[188:191], a[92:95]\n"
"v_accvgpr_read_b32 v116, a24\n"
"v_accvgpr_read_b32 v117, a25\n"
"v_mfma_f32_16x16x32_bf16 a[108:111], a[200:203], v[188:191], a[108:111]\n"
"v_accvgpr_read_b32 v118, a26\n"
"v_accvgpr_read_b32 v119, a27\n"
"v_mfma_f32_16x16x32_bf16 a[104:107], a[200:203], v[184:187], a[104:107]\n"
"v_accvgpr_read_b32 v120, a12\n"
"v_accvgpr_read_b32 v121, a13\n"
"v_mfma_f32_16x16x32_bf16 a[100:103], a[200:203], v[180:183], a[100:103]\n"
"v_accvgpr_read_b32 v122, a14\n"
"v_accvgpr_read_b32 v123, a15\n"
"v_mfma_f32_16x16x32_bf16 a[96:99], a[200:203], v[176:179], a[96:99]\n"
"v_accvgpr_read_b32 v124, a28\n"
"v_accvgpr_read_b32 v125, a29\n"
"v_mfma_f32_16x16x32_bf16 a[112:115], a[204:207], v[176:179], a[112:115]\n"
"v_accvgpr_read_b32 v126, a30\n"
"v_accvgpr_read_b32 v127, a31\n"
"v_mfma_f32_16x16x32_bf16 a[116:119], a[204:207], v[180:183], a[116:119]\n"
"v_pk_mul_f32 v[96:97], v[96:97], v[22:23] op_sel_hi:[1,0]\n"
"v_pk_mul_f32 v[98:99], v[98:99], v[22:23] op_sel_hi:[1,0]\n"
"v_mfma_f32_16x16x32_bf16 a[120:123], a[204:207], v[184:187], a[120:123]\n"
"v_pk_mul_f32 v[100:101], v[100:101], v[22:23] op_sel_hi:[1,0]\n"
"v_pk_mul_f32 v[102:103], v[102:103], v[22:23] op_sel_hi:[1,0]\n"
"v_mfma_f32_16x16x32_bf16 a[124:127], a[204:207], v[188:191], a[124:127]\n"
"v_pk_mul_f32 v[104:105], v[104:105], v[22:23] op_sel:[0,1] op_sel_hi:[1,1]\n"
"v_pk_mul_f32 v[106:107], v[106:107], v[22:23] op_sel:[0,1] op_sel_hi:[1,1]\n"
"v_pk_mul_f32 v[108:109], v[108:109], v[22:23] op_sel:[0,1] op_sel_hi:[1,1]\n"
"v_pk_mul_f32 v[110:111], v[110:111], v[22:23] op_sel:[0,1] op_sel_hi:[1,1]\n"
"v_pk_mul_f32 v[112:113], v[112:113], v[24:25] op_sel_hi:[1,0]\n"
"v_pk_mul_f32 v[114:115], v[114:115], v[24:25] op_sel_hi:[1,0]\n"
"v_pk_mul_f32 v[116:117], v[116:117], v[24:25] op_sel_hi:[1,0]\n"
"v_pk_mul_f32 v[118:119], v[118:119], v[24:25] op_sel_hi:[1,0]\n"
"v_pk_mul_f32 v[120:121], v[120:121], v[24:25] op_sel:[0,1] op_sel_hi:[1,1]\n"
"v_pk_mul_f32 v[122:123], v[122:123], v[24:25] op_sel:[0,1] op_sel_hi:[1,1]\n"
"v_pk_mul_f32 v[124:125], v[124:125], v[24:25] op_sel:[0,1] op_sel_hi:[1,1]\n"
"v_pk_mul_f32 v[126:127], v[126:127], v[24:25] op_sel:[0,1] op_sel_hi:[1,1]\n"
"v_cvt_pk_bf16_f32 v192, v96, v97\n"
"v_cvt_pk_bf16_f32 v193, v98, v99\n"
"v_cvt_pk_bf16_f32 v194, v100, v101\n"
"v_cvt_pk_bf16_f32 v195, v102, v103\n"
"buffer_store_dwordx4 v[192:195], v1, s[36:39], s82 offen offset:0 sc1\n"
"v_cvt_pk_bf16_f32 v196, v104, v105\n"
"v_cvt_pk_bf16_f32 v197, v106, v107\n"
"v_cvt_pk_bf16_f32 v198, v108, v109\n"
"v_cvt_pk_bf16_f32 v199, v110, v111\n"
"buffer_store_dwordx4 v[196:199], v1, s[36:39], s82 offen offset:1024 sc1\n"
"v_cvt_pk_bf16_f32 v200, v112, v113\n"
"v_cvt_pk_bf16_f32 v201, v114, v115\n"
"v_cvt_pk_bf16_f32 v202, v116, v117\n"
"v_cvt_pk_bf16_f32 v203, v118, v119\n"
"buffer_store_dwordx4 v[200:203], v1, s[36:39], s84 offen offset:0 sc1\n"
"v_cvt_pk_bf16_f32 v204, v120, v121\n"
"v_cvt_pk_bf16_f32 v205, v122, v123\n"
"v_cvt_pk_bf16_f32 v206, v124, v125\n"
"v_cvt_pk_bf16_f32 v207, v126, v127\n"
"buffer_store_dwordx4 v[204:207], v1, s[36:39], s84 offen offset:1024 sc1\n"
"v_accvgpr_read_b32 v128, a32\n"
"v_accvgpr_read_b32 v129, a33\n"
"v_accvgpr_read_b32 v130, a34\n"
"v_accvgpr_read_b32 v131, a35\n"
"v_accvgpr_read_b32 v132, a48\n"
"v_accvgpr_read_b32 v133, a49\n"
"v_accvgpr_read_b32 v134, a50\n"
"v_accvgpr_read_b32 v135, a51\n"
"v_accvgpr_read_b32 v136, a36\n"
"v_accvgpr_read_b32 v137, a37\n"
"v_accvgpr_read_b32 v138, a38\n"
"v_accvgpr_read_b32 v139, a39\n"
"v_accvgpr_read_b32 v140, a52\n"
"v_accvgpr_read_b32 v141, a53\n"
"v_accvgpr_read_b32 v142, a54\n"
"v_accvgpr_read_b32 v143, a55\n"
"v_accvgpr_read_b32 v144, a40\n"
"v_accvgpr_read_b32 v145, a41\n"
"v_accvgpr_read_b32 v146, a42\n"
"v_accvgpr_read_b32 v147, a43\n"
"v_accvgpr_read_b32 v148, a56\n"
"v_accvgpr_read_b32 v149, a57\n"
"v_accvgpr_read_b32 v150, a58\n"
"v_accvgpr_read_b32 v151, a59\n"
"v_accvgpr_read_b32 v152, a44\n"
"v_accvgpr_read_b32 v153, a45\n"
"v_accvgpr_read_b32 v154, a46\n"
"v_accvgpr_read_b32 v155, a47\n"
"v_accvgpr_read_b32 v156, a60\n"
"v_accvgpr_read_b32 v157, a61\n"
"v_accvgpr_read_b32 v158, a62\n"
"v_accvgpr_read_b32 v159, a63\n"
"v_pk_mul_f32 v[128:129], v[128:129], v[22:23] op_sel_hi:[1,0]\n"
"v_pk_mul_f32 v[130:131], v[130:131], v[22:23] op_sel_hi:[1,0]\n"
"v_pk_mul_f32 v[132:133], v[132:133], v[22:23] op_sel_hi:[1,0]\n"
"v_pk_mul_f32 v[134:135], v[134:135], v[22:23] op_sel_hi:[1,0]\n"
"v_pk_mul_f32 v[136:137], v[136:137], v[22:23] op_sel:[0,1] op_sel_hi:[1,1]\n"
"v_pk_mul_f32 v[138:139], v[138:139], v[22:23] op_sel:[0,1] op_sel_hi:[1,1]\n"
"v_pk_mul_f32 v[140:141], v[140:141], v[22:23] op_sel:[0,1] op_sel_hi:[1,1]\n"
"v_pk_mul_f32 v[142:143], v[142:143], v[22:23] op_sel:[0,1] op_sel_hi:[1,1]\n"
"v_pk_mul_f32 v[144:145], v[144:145], v[24:25] op_sel_hi:[1,0]\n"
"v_pk_mul_f32 v[146:147], v[146:147], v[24:25] op_sel_hi:[1,0]\n"
"v_pk_mul_f32 v[148:149], v[148:149], v[24:25] op_sel_hi:[1,0]\n"
"v_pk_mul_f32 v[150:151], v[150:151], v[24:25] op_sel_hi:[1,0]\n"
"v_pk_mul_f32 v[152:153], v[152:153], v[24:25] op_sel:[0,1] op_sel_hi:[1,1]\n"
"v_pk_mul_f32 v[154:155], v[154:155], v[24:25] op_sel:[0,1] op_sel_hi:[1,1]\n"
"v_pk_mul_f32 v[156:157], v[156:157], v[24:25] op_sel:[0,1] op_sel_hi:[1,1]\n"
"v_pk_mul_f32 v[158:159], v[158:159], v[24:25] op_sel:[0,1] op_sel_hi:[1,1]\n"
"v_cvt_pk_bf16_f32 v208, v128, v129\n"
"v_cvt_pk_bf16_f32 v209, v130, v131\n"
"v_cvt_pk_bf16_f32 v210, v132, v133\n"
"v_cvt_pk_bf16_f32 v211, v134, v135\n"
"buffer_store_dwordx4 v[208:211], v1, s[36:39], s82 offen offset:2048 sc1\n"
"v_cvt_pk_bf16_f32 v212, v136, v137\n"
"v_cvt_pk_bf16_f32 v213, v138, v139\n"
"v_cvt_pk_bf16_f32 v214, v140, v141\n"
"v_cvt_pk_bf16_f32 v215, v142, v143\n"
"buffer_store_dwordx4 v[212:215], v1, s[36:39], s82 offen offset:3072 sc1\n"
"v_cvt_pk_bf16_f32 v216, v144, v145\n"
"v_cvt_pk_bf16_f32 v217, v146, v147\n"
"v_cvt_pk_bf16_f32 v218, v148, v149\n"
"v_cvt_pk_bf16_f32 v219, v150, v151\n"
"buffer_store_dwordx4 v[216:219], v1, s[36:39], s84 offen offset:2048 sc1\n"
"v_cvt_pk_bf16_f32 v220, v152, v153\n"
"v_cvt_pk_bf16_f32 v221, v154, v155\n"
"v_cvt_pk_bf16_f32 v222, v156, v157\n"
"v_cvt_pk_bf16_f32 v223, v158, v159\n"
"buffer_store_dwordx4 v[220:223], v1, s[36:39], s84 offen offset:3072 sc1\n"
"v_accvgpr_read_b32 v96, a64\n"
"v_accvgpr_read_b32 v97, a65\n"
"v_accvgpr_read_b32 v98, a66\n"
"v_accvgpr_read_b32 v99, a67\n"
"v_accvgpr_read_b32 v100, a80\n"
"v_accvgpr_read_b32 v101, a81\n"
"v_accvgpr_read_b32 v102, a82\n"
"v_accvgpr_read_b32 v103, a83\n"
"v_accvgpr_read_b32 v104, a68\n"
"v_accvgpr_read_b32 v105, a69\n"
"v_accvgpr_read_b32 v106, a70\n"
"v_accvgpr_read_b32 v107, a71\n"
"v_accvgpr_read_b32 v108, a84\n"
"v_accvgpr_read_b32 v109, a85\n"
"v_accvgpr_read_b32 v110, a86\n"
"v_accvgpr_read_b32 v111, a87\n"
"v_accvgpr_read_b32 v112, a72\n"
"v_accvgpr_read_b32 v113, a73\n"
"v_accvgpr_read_b32 v114, a74\n"
"v_accvgpr_read_b32 v115, a75\n"
"v_accvgpr_read_b32 v116, a88\n"
"v_accvgpr_read_b32 v117, a89\n"
"v_accvgpr_read_b32 v118, a90\n"
"v_accvgpr_read_b32 v119, a91\n"
"v_accvgpr_read_b32 v120, a76\n"
"v_accvgpr_read_b32 v121, a77\n"
"v_accvgpr_read_b32 v122, a78\n"
"v_accvgpr_read_b32 v123, a79\n"
"v_accvgpr_read_b32 v124, a92\n"
"v_accvgpr_read_b32 v125, a93\n"
"v_accvgpr_read_b32 v126, a94\n"
"v_accvgpr_read_b32 v127, a95\n"
"v_pk_mul_f32 v[96:97], v[96:97], v[22:23] op_sel_hi:[1,0]\n"
"v_pk_mul_f32 v[98:99], v[98:99], v[22:23] op_sel_hi:[1,0]\n"
"v_pk_mul_f32 v[100:101], v[100:101], v[22:23] op_sel_hi:[1,0]\n"
"v_pk_mul_f32 v[102:103], v[102:103], v[22:23] op_sel_hi:[1,0]\n"
"v_pk_mul_f32 v[104:105], v[104:105], v[22:23] op_sel:[0,1] op_sel_hi:[1,1]\n"
"v_pk_mul_f32 v[106:107], v[106:107], v[22:23] op_sel:[0,1] op_sel_hi:[1,1]\n"
"v_pk_mul_f32 v[108:109], v[108:109], v[22:23] op_sel:[0,1] op_sel_hi:[1,1]\n"
"v_pk_mul_f32 v[110:111], v[110:111], v[22:23] op_sel:[0,1] op_sel_hi:[1,1]\n"
"v_pk_mul_f32 v[112:113], v[112:113], v[24:25] op_sel_hi:[1,0]\n"
"v_pk_mul_f32 v[114:115], v[114:115], v[24:25] op_sel_hi:[1,0]\n"
"v_pk_mul_f32 v[116:117], v[116:117], v[24:25] op_sel_hi:[1,0]\n"
"v_pk_mul_f32 v[118:119], v[118:119], v[24:25] op_sel_hi:[1,0]\n"
"v_pk_mul_f32 v[120:121], v[120:121], v[24:25] op_sel:[0,1] op_sel_hi:[1,1]\n"
"v_pk_mul_f32 v[122:123], v[122:123], v[24:25] op_sel:[0,1] op_sel_hi:[1,1]\n"
"v_pk_mul_f32 v[124:125], v[124:125], v[24:25] op_sel:[0,1] op_sel_hi:[1,1]\n"
"v_pk_mul_f32 v[126:127], v[126:127], v[24:25] op_sel:[0,1] op_sel_hi:[1,1]\n"
"v_cvt_pk_bf16_f32 v224, v96, v97\n"
"v_cvt_pk_bf16_f32 v225, v98, v99\n"
"v_cvt_pk_bf16_f32 v226, v100, v101\n"
"v_cvt_pk_bf16_f32 v227, v102, v103\n"
"s_add_u32 s82, s82, 4096\n"
"s_add_u32 s84, s84, 4096\n"
"buffer_store_dwordx4 v[224:227], v1, s[36:39], s82 offen offset:0 sc1\n"
"v_cvt_pk_bf16_f32 v228, v104, v105\n"
"v_cvt_pk_bf16_f32 v229, v106, v107\n"
"v_cvt_pk_bf16_f32 v230, v108, v109\n"
"v_cvt_pk_bf16_f32 v231, v110, v111\n"
"buffer_store_dwordx4 v[228:231], v1, s[36:39], s82 offen offset:1024 sc1\n"
"v_cvt_pk_bf16_f32 v232, v112, v113\n"
"v_cvt_pk_bf16_f32 v233, v114, v115\n"
"v_cvt_pk_bf16_f32 v234, v116, v117\n"
"v_cvt_pk_bf16_f32 v235, v118, v119\n"
"buffer_store_dwordx4 v[232:235], v1, s[36:39], s84 offen offset:0 sc1\n"
"v_cvt_pk_bf16_f32 v236, v120, v121\n"
"v_cvt_pk_bf16_f32 v237, v122, v123\n"
"v_cvt_pk_bf16_f32 v238, v124, v125\n"
"v_cvt_pk_bf16_f32 v239, v126, v127\n"
"buffer_store_dwordx4 v[236:239], v1, s[36:39], s84 offen offset:1024 sc1\n"
"v_accvgpr_read_b32 v128, a96\n"
"v_accvgpr_read_b32 v129, a97\n"
"v_accvgpr_read_b32 v130, a98\n"
"v_accvgpr_read_b32 v131, a99\n"
"v_accvgpr_read_b32 v132, a112\n"
"v_accvgpr_read_b32 v133, a113\n"
"v_accvgpr_read_b32 v134, a114\n"
"v_accvgpr_read_b32 v135, a115\n"
"v_accvgpr_read_b32 v136, a100\n"
"v_accvgpr_read_b32 v137, a101\n"
"v_accvgpr_read_b32 v138, a102\n"
"v_accvgpr_read_b32 v139, a103\n"
"v_accvgpr_read_b32 v140, a116\n"
"v_accvgpr_read_b32 v141, a117\n"
"v_accvgpr_read_b32 v142, a118\n"
"v_accvgpr_read_b32 v143, a119\n"
"v_accvgpr_read_b32 v144, a104\n"
"v_accvgpr_read_b32 v145, a105\n"
"v_accvgpr_read_b32 v146, a106\n"
"v_accvgpr_read_b32 v147, a107\n"
"v_accvgpr_read_b32 v148, a120\n"
"v_accvgpr_read_b32 v149, a121\n"
"v_accvgpr_read_b32 v150, a122\n"
"v_accvgpr_read_b32 v151, a123\n"
"v_accvgpr_read_b32 v152, a108\n"
"v_accvgpr_read_b32 v153, a109\n"
"v_accvgpr_read_b32 v154, a110\n"
"v_accvgpr_read_b32 v155, a111\n"
"v_accvgpr_read_b32 v156, a124\n"
"v_accvgpr_read_b32 v157, a125\n"
"v_accvgpr_read_b32 v158, a126\n"
"v_accvgpr_read_b32 v159, a127\n"
"v_pk_mul_f32 v[128:129], v[128:129], v[22:23] op_sel_hi:[1,0]\n"
"v_pk_mul_f32 v[130:131], v[130:131], v[22:23] op_sel_hi:[1,0]\n"
"v_pk_mul_f32 v[132:133], v[132:133], v[22:23] op_sel_hi:[1,0]\n"
"v_pk_mul_f32 v[134:135], v[134:135], v[22:23] op_sel_hi:[1,0]\n"
"v_pk_mul_f32 v[136:137], v[136:137], v[22:23] op_sel:[0,1] op_sel_hi:[1,1]\n"
"v_pk_mul_f32 v[138:139], v[138:139], v[22:23] op_sel:[0,1] op_sel_hi:[1,1]\n"
"v_pk_mul_f32 v[140:141], v[140:141], v[22:23] op_sel:[0,1] op_sel_hi:[1,1]\n"
"v_pk_mul_f32 v[142:143], v[142:143], v[22:23] op_sel:[0,1] op_sel_hi:[1,1]\n"
"v_pk_mul_f32 v[144:145], v[144:145], v[24:25] op_sel_hi:[1,0]\n"
"v_pk_mul_f32 v[146:147], v[146:147], v[24:25] op_sel_hi:[1,0]\n"
"v_pk_mul_f32 v[148:149], v[148:149], v[24:25] op_sel_hi:[1,0]\n"
"v_pk_mul_f32 v[150:151], v[150:151], v[24:25] op_sel_hi:[1,0]\n"
"v_pk_mul_f32 v[152:153], v[152:153], v[24:25] op_sel:[0,1] op_sel_hi:[1,1]\n"
"v_pk_mul_f32 v[154:155], v[154:155], v[24:25] op_sel:[0,1] op_sel_hi:[1,1]\n"
"v_pk_mul_f32 v[156:157], v[156:157], v[24:25] op_sel:[0,1] op_sel_hi:[1,1]\n"
"v_pk_mul_f32 v[158:159], v[158:159], v[24:25] op_sel:[0,1] op_sel_hi:[1,1]\n"
"v_cvt_pk_bf16_f32 v176, v128, v129\n"
"v_cvt_pk_bf16_f32 v177, v130, v131\n"
"v_cvt_pk_bf16_f32 v178, v132, v133\n"
"v_cvt_pk_bf16_f32 v179, v134, v135\n"
"buffer_store_dwordx4 v[176:179], v1, s[36:39], s82 offen offset:2048 sc1\n"
"v_cvt_pk_bf16_f32 v180, v136, v137\n"
"v_cvt_pk_bf16_f32 v181, v138, v139\n"
"v_cvt_pk_bf16_f32 v182, v140, v141\n"
"v_cvt_pk_bf16_f32 v183, v142, v143\n"
"buffer_store_dwordx4 v[180:183], v1, s[36:39], s82 offen offset:3072 sc1\n"
"v_cvt_pk_bf16_f32 v184, v144, v145\n"
"v_cvt_pk_bf16_f32 v185, v146, v147\n"
"v_cvt_pk_bf16_f32 v186, v148, v149\n"
"v_cvt_pk_bf16_f32 v187, v150, v151\n"
"buffer_store_dwordx4 v[184:187], v1, s[36:39], s84 offen offset:2048 sc1\n"
"v_cvt_pk_bf16_f32 v188, v152, v153\n"
"v_cvt_pk_bf16_f32 v189, v154, v155\n"
"v_cvt_pk_bf16_f32 v190, v156, v157\n"
"v_cvt_pk_bf16_f32 v191, v158, v159\n"
"buffer_store_dwordx4 v[188:191], v1, s[36:39], s84 offen offset:3072 sc1\n"
"s_branch L_g3A\n"
"L_tB0:\n"
"s_and_b32 s5, s52, 3\n"
"s_lshr_b32 s68, s52, 2\n"
"s_lshl_b32 s68, s68, 2\n"
"s_bfe_u32 s80, s53, 0x20002\n"
"s_add_u32 s68, s68, s80\n"
"s_and_b32 s80, s53, 3\n"
"s_mul_i32 s81, s5, 1327104\n"
"s_mul_i32 s89, s80, 9216\n"
"s_add_u32 s81, s81, s89\n"
"s_mul_i32 s79, s11, 2304\n"
"s_add_u32 s74, s81, s79\n"
"s_add_u32 s79, s79, 16384\n"
"s_lshl3_add_u32 s81, s5, s68\n"
"s_lshl2_add_u32 s81, s81, s80\n"
"s_lshl2_add_u32 s81, s81, s11\n"
"s_mul_i32 s81, s81, 18432\n"
"s_add_u32 s85, s81, 0x19d8000\n"
"s_lshl2_add_u32 s81, s68, s80\n"
"s_lshl2_add_u32 s81, s81, s11\n"
"s_lshl2_add_u32 s81, s81, s5\n"
"s_lshl_b32 s81, s81, 2\n"
"s_add_u32 s86, s81, 0x2ae2000\n"
"s_add_u32 s44, s32, 0x480000\n"
"s_addc_u32 s45, s33, 0\n"
"s_and_b32 s45, s45, 0xffff\n"
"s_mov_b32 s46, 0x510000\n"
"s_mov_b32 s47, 0x20000\n"
"v_mfma_f32_16x16x32_bf16 v[128:131], a[144:147], v[32:35], v[192:195]\n"
"v_cvt_pk_bf16_f32 v167, v118, v119\n"
"v_mfma_f32_16x16x32_bf16 v[132:135], a[144:147], v[48:51], v[196:199]\n"
"v_exp_f32 v122, v122\n"
"v_mfma_f32_16x16x32_bf16 v[136:139], a[144:147], v[64:67], v[200:203]\n"
"ds_read_b64_tr_b16 a[176:177], v22 offset:0\n"
"ds_read_b64_tr_b16 a[178:179], v22 offset:4096\n"
"v_mfma_f32_16x16x32_bf16 v[140:143], a[144:147], v[80:83], v[204:207]\n"
"v_exp_f32 v123, v123\n"
"v_mfma_f32_16x16x32_bf16 v[128:131], a[148:151], v[36:39], v[128:131]\n"
"v_cvt_pk_bf16_f32 v170, v120, v121\n"
"v_mfma_f32_16x16x32_bf16 v[132:135], a[148:151], v[52:55], v[132:135]\n"
"v_exp_f32 v124, v124\n"
"v_mfma_f32_16x16x32_bf16 v[136:139], a[148:151], v[68:71], v[136:139]\n"
"ds_read_b64_tr_b16 a[180:181], v23 offset:0\n"
"ds_read_b64_tr_b16 a[182:183], v23 offset:4096\n"
"v_mfma_f32_16x16x32_bf16 v[140:143], a[148:151], v[84:87], v[140:143]\n"
"v_exp_f32 v125, v125\n"
"v_mfma_f32_16x16x32_bf16 v[128:131], a[152:155], v[40:43], v[128:131]\n"
"v_cvt_pk_bf16_f32 v171, v122, v123\n"
"v_mfma_f32_16x16x32_bf16 v[132:135], a[152:155], v[56:59], v[132:135]\n"
"v_exp_f32 v126, v126\n"
"v_mfma_f32_16x16x32_bf16 v[136:139], a[152:155], v[72:75], v[136:139]\n"
"ds_read_b64_tr_b16 a[184:185], v22 offset:512\n"
"ds_read_b64_tr_b16 a[186:187], v22 offset:4608\n"
"v_mfma_f32_16x16x32_bf16 v[140:143], a[152:155], v[88:91], v[140:143]\n"
"v_exp_f32 v127, v127\n"
"v_mfma_f32_16x16x32_bf16 v[128:131], a[156:159], v[44:47], v[128:131]\n"
"v_cvt_pk_bf16_f32 v174, v124, v125\n"
"v_mfma_f32_16x16x32_bf16 v[132:135], a[156:159], v[60:63], v[132:135]\n"
"ds_read_b64_tr_b16 a[188:189], v23 offset:512\n"
"ds_read_b64_tr_b16 a[190:191], v23 offset:4608\n"
"v_mfma_f32_16x16x32_bf16 v[136:139], a[156:159], v[76:79], v[136:139]\n"
"ds_read_b64_tr_b16 a[192:193], v22 offset:1024\n"
"ds_read_b64_tr_b16 a[194:195], v22 offset:5120\n"
"v_mfma_f32_16x16x32_bf16 v[140:143], a[156:159], v[92:95], v[140:143]\n"
"ds_read_b64_tr_b16 a[196:197], v23 offset:1024\n"
"ds_read_b64_tr_b16 a[198:199], v23 offset:5120\n"
"v_mfma_f32_16x16x32_bf16 v[144:147], a[160:163], v[32:35], v[192:195]\n"
"v_cvt_pk_bf16_f32 v175, v126, v127\n"
"s_add_u32 m0, s2, 0\n"
"v_mfma_f32_16x16x32_bf16 v[148:151], a[160:163], v[48:51], v[196:199]\n"
"v_exp_f32 v128, v128\n"
"v_mfma_f32_16x16x32_bf16 v[152:155], a[160:163], v[64:67], v[200:203]\n"
"ds_read_b64_tr_b16 a[200:201], v22 offset:1536\n"
"ds_read_b64_tr_b16 a[202:203], v22 offset:5632\n"
"buffer_load_dwordx4 v8, s[40:43], s73 offen offset:0 lds\n"
"v_mfma_f32_16x16x32_bf16 v[156:159], a[160:163], v[80:83], v[204:207]\n"
"v_exp_f32 v129, v129\n"
"v_mfma_f32_16x16x32_bf16 v[144:147], a[164:167], v[36:39], v[144:147]\n"
"ds_read_b64_tr_b16 a[204:205], v23 offset:1536\n"
"ds_read_b64_tr_b16 a[206:207], v23 offset:5632\n"
"v_mfma_f32_16x16x32_bf16 v[148:151], a[164:167], v[52:55], v[148:151]\n"
"v_exp_f32 v130, v130\n"
"v_mfma_f32_16x16x32_bf16 v[152:155], a[164:167], v[68:71], v[152:155]\n"
"buffer_load_dwordx4 v9, s[40:43], s73 offen offset:1024 lds\n"
"v_mfma_f32_16x16x32_bf16 v[156:159], a[164:167], v[84:87], v[156:159]\n"
"v_exp_f32 v131, v131\n"
"v_mfma_f32_16x16x32_bf16 v[144:147], a[168:171], v[40:43], v[144:147]\n"
"v_cvt_pk_bf16_f32 v176, v128, v129\n"
"v_mfma_f32_16x16x32_bf16 v[148:151], a[168:171], v[56:59], v[148:151]\n"
"v_exp_f32 v132, v132\n"
"v_mfma_f32_16x16x32_bf16 v[152:155], a[168:171], v[72:75], v[152:155]\n"
"buffer_load_dwordx4 v10, s[40:43], s73 offen offset:2048 lds\n"
"v_mfma_f32_16x16x32_bf16 v[156:159], a[168:171], v[88:91], v[156:159]\n"
"v_exp_f32 v133, v133\n"
"v_mfma_f32_16x16x32_bf16 v[144:147], a[172:175], v[44:47], v[144:147]\n"
"v_cvt_pk_bf16_f32 v177, v130, v131\n"
"s_waitcnt lgkmcnt(0)\n"
"v_mfma_f32_16x16x32_bf16 v[148:151], a[172:175], v[60:63], v[148:151]\n"
"v_exp_f32 v134, v134\n"
"v_mfma_f32_16x16x32_bf16 v[152:155], a[172:175], v[76:79], v[152:155]\n"
"buffer_load_dwordx4 v11, s[40:43], s73 offen offset:3072 lds\n"
"v_mfma_f32_16x16x32_bf16 v[156:159], a[172:175], v[92:95], v[156:159]\n"
"v_exp_f32 v135, v135\n"
"v_mfma_f32_16x16x32_bf16 a[0:3], a[176:179], v[160:163], a[0:3]\n"
"v_cvt_pk_bf16_f32 v180, v132, v133\n"
"s_add_u32 s73, s73, 131072\n"
"s_add_u32 m0, s2, 25600\n"
"v_mfma_f32_16x16x32_bf16 a[4:7], a[176:179], v[164:167], a[4:7]\n"
"v_exp_f32 v136, v136\n"
"v_mfma_f32_16x16x32_bf16 a[8:11], a[176:179], v[168:171], a[8:11]\n"
"buffer_load_dwordx4 v8, s[40:43], s73 offen offset:0 lds\n"
"v_mfma_f32_16x16x32_bf16 a[12:15], a[176:179], v[172:175], a[12:15]\n"
"v_exp_f32 v137, v137\n"
"v_mfma_f32_16x16x32_bf16 a[28:31], a[180:183], v[172:175], a[28:31]\n"
"v_cvt_pk_bf16_f32 v181, v134, v135\n"
"v_mfma_f32_16x16x32_bf16 a[24:27], a[180:183], v[168:171], a[24:27]\n"
"v_exp_f32 v138, v138\n"
"v_mfma_f32_16x16x32_bf16 a[20:23], a[180:183], v[164:167], a[20:23]\n"
"buffer_load_dwordx4 v9, s[40:43], s73 offen offset:1024 lds\n"
"v_mfma_f32_16x16x32_bf16 a[16:19], a[180:183], v[160:163], a[16:19]\n"
"v_exp_f32 v139, v139\n"
"v_mfma_f32_16x16x32_bf16 a[32:35], a[184:187], v[160:163], a[32:35]\n"
"v_cvt_pk_bf16_f32 v184, v136, v137\n"
"v_mfma_f32_16x16x32_bf16 a[36:39], a[184:187], v[164:167], a[36:39]\n"
"v_exp_f32 v140, v140\n"
"v_mfma_f32_16x16x32_bf16 a[40:43], a[184:187], v[168:171], a[40:43]\n"
"buffer_load_dwordx4 v10, s[40:43], s73 offen offset:2048 lds\n"
"v_mfma_f32_16x16x32_bf16 a[44:47], a[184:187], v[172:175], a[44:47]\n"
"v_exp_f32 v141, v141\n"
"v_mfma_f32_16x16x32_bf16 a[60:63], a[188:191], v[172:175], a[60:63]\n"
"v_cvt_pk_bf16_f32 v185, v138, v139\n"
"v_mfma_f32_16x16x32_bf16 a[56:59], a[188:191], v[168:171], a[56:59]\n"
"v_exp_f32 v142, v142\n"
"v_mfma_f32_16x16x32_bf16 a[52:55], a[188:191], v[164:167], a[52:55]\n"
"buffer_load_dwordx4 v11, s[40:43], s73 offen offset:3072 lds\n"
"v_mfma_f32_16x16x32_bf16 a[48:51], a[188:191], v[160:163], a[48:51]\n"
"v_exp_f32 v143, v143\n"
"v_mfma_f32_16x16x32_bf16 a[64:67], a[192:195], v[160:163], a[64:67]\n"
"v_cvt_pk_bf16_f32 v188, v140, v141\n"
"s_add_u32 s73, s73, 131072\n"
"s_add_u32 m0, s2, 51200\n"
"v_mfma_f32_16x16x32_bf16 a[68:71], a[192:195], v[164:167], a[68:71]\n"
"v_exp_f32 v144, v144\n"
"v_mfma_f32_16x16x32_bf16 a[72:75], a[192:195], v[168:171], a[72:75]\n"
"buffer_load_dwordx4 v8, s[40:43], s73 offen offset:0 lds\n"
"v_mfma_f32_16x16x32_bf16 a[76:79], a[192:195], v[172:175], a[76:79]\n"
"v_exp_f32 v145, v145\n"
"v_mfma_f32_16x16x32_bf16 a[92:95], a[196:199], v[172:175], a[92:95]\n"
"v_cvt_pk_bf16_f32 v189, v142, v143\n"
"v_mfma_f32_16x16x32_bf16 a[88:91], a[196:199], v[168:171], a[88:91]\n"
"v_exp_f32 v146, v146\n"
"v_mfma_f32_16x16x32_bf16 a[84:87], a[196:199], v[164:167], a[84:87]\n"
"buffer_load_dwordx4 v9, s[40:43], s73 offen offset:1024 lds\n"
"v_mfma_f32_16x16x32_bf16 a[80:83], a[196:199], v[160:163], a[80:83]\n"
"v_exp_f32 v147, v147\n"
"v_mfma_f32_16x16x32_bf16 a[96:99], a[200:203], v[160:163], a[96:99]\n"
"v_cvt_pk_bf16_f32 v178, v144, v145\n"
"v_mfma_f32_16x16x32_bf16 a[100:103], a[200:203], v[164:167], a[100:103]\n"
"v_exp_f32 v148, v148\n"
"v_mfma_f32_16x16x32_bf16 a[104:107], a[200:203], v[168:171], a[104:107]\n"
"buffer_load_dwordx4 v10, s[40:43], s73 offen offset:2048 lds\n"
"v_mfma_f32_16x16x32_bf16 a[108:111], a[200:203], v[172:175], a[108:111]\n"
"v_exp_f32 v149, v149\n"
"v_mfma_f32_16x16x32_bf16 a[124:127], a[204:207], v[172:175], a[124:127]\n"
"v_cvt_pk_bf16_f32 v179, v146, v147\n"
"v_mfma_f32_16x16x32_bf16 a[120:123], a[204:207], v[168:171], a[120:123]\n"
"v_exp_f32 v150, v150\n"
"v_mfma_f32_16x16x32_bf16 a[116:119], a[204:207], v[164:167], a[116:119]\n"
"buffer_load_dwordx4 v11, s[40:43], s73 offen offset:3072 lds\n"
"s_add_u32 s73, s73, 131072\n"
"v_mfma_f32_16x16x32_bf16 a[112:115], a[204:207], v[160:163], a[112:115]\n"
"v_exp_f32 v151, v151\n"
"v_mfma_f32_16x16x32_bf16 a[128:131], v[208:211], v[160:163], a[128:131]\n"
"v_cvt_pk_bf16_f32 v182, v148, v149\n"
"v_mfma_f32_16x16x32_bf16 a[132:135], v[208:211], v[164:167], a[132:135]\n"
"v_exp_f32 v152, v152\n"
"v_mfma_f32_16x16x32_bf16 a[136:139], v[208:211], v[168:171], a[136:139]\n"
"v_mfma_f32_16x16x32_bf16 a[140:143], v[208:211], v[172:175], a[140:143]\n"
"v_exp_f32 v153, v153\n"
"s_lshl_b32 s91, s52, 10\n"
"s_add_u32 s91, s91, 0x2adc000\n"
"buffer_load_dwordx2 v[248:249], v251, s[36:39], s91 offen sc1\n"
"ds_read_b64_tr_b16 a[176:177], v22 offset:8192\n"
"ds_read_b64_tr_b16 a[178:179], v22 offset:12288\n"
"ds_read_b64_tr_b16 a[180:181], v23 offset:8192\n"
"ds_read_b64_tr_b16 a[182:183], v23 offset:12288\n"
"ds_read_b64_tr_b16 a[184:185], v22 offset:8704\n"
"ds_read_b64_tr_b16 a[186:187], v22 offset:12800\n"
"ds_read_b64_tr_b16 a[188:189], v23 offset:8704\n"
"ds_read_b64_tr_b16 a[190:191], v23 offset:12800\n"
"v_cvt_pk_bf16_f32 v183, v150, v151\n"
"v_cvt_pk_bf16_f32 v186, v152, v153\n"
"v_exp_f32 v154, v154\n"
"v_exp_f32 v155, v155\n"
"ds_read_b64_tr_b16 a[192:193], v22 offset:9216\n"
"ds_read_b64_tr_b16 a[194:195], v22 offset:13312\n"
"v_cvt_pk_bf16_f32 v187, v154, v155\n"
"v_exp_f32 v156, v156\n"
"v_exp_f32 v157, v157\n"
"ds_read_b64_tr_b16 a[196:197], v23 offset:9216\n"
"ds_read_b64_tr_b16 a[198:199], v23 offset:13312\n"
"v_cvt_pk_bf16_f32 v190, v156, v157\n"
"v_exp_f32 v158, v158\n"
"v_exp_f32 v159, v159\n"
"ds_read_b64_tr_b16 a[200:201], v22 offset:9728\n"
"ds_read_b64_tr_b16 a[202:203], v22 offset:13824\n"
"v_cvt_pk_bf16_f32 v191, v158, v159\n"
"ds_read_b64_tr_b16 a[204:205], v23 offset:9728\n"
"ds_read_b64_tr_b16 a[206:207], v23 offset:13824\n"
"s_add_u32 s84, s82, 262144\n"
"s_mov_b32 s35, 1\n"
"s_waitcnt lgkmcnt(0)\n"
"s_barrier\n"
"L_pv15B:\n"
"v_mfma_f32_16x16x32_bf16 a[128:131], v[208:211], v[176:179], a[128:131]\n"
"v_mfma_f32_16x16x32_bf16 a[132:135], v[208:211], v[180:183], a[132:135]\n"
"v_mfma_f32_16x16x32_bf16 a[136:139], v[208:211], v[184:187], a[136:139]\n"
"v_mfma_f32_16x16x32_bf16 a[140:143], v[208:211], v[188:191], a[140:143]\n"
"v_mfma_f32_16x16x32_bf16 a[12:15], a[176:179], v[188:191], a[12:15]\n"
"v_mfma_f32_16x16x32_bf16 a[8:11], a[176:179], v[184:187], a[8:11]\n"
"v_mfma_f32_16x16x32_bf16 a[4:7], a[176:179], v[180:183], a[4:7]\n"
"s_mov_b32 s80, 0x53800000\n"
"v_mfma_f32_16x16x32_bf16 a[0:3], a[176:179], v[176:179], a[0:3]\n"
"v_accvgpr_read_b32 v22, a128\n"
"v_accvgpr_read_b32 v23, a132\n"
"v_mfma_f32_16x16x32_bf16 a[16:19], a[180:183], v[176:179], a[16:19]\n"
"v_accvgpr_read_b32 v24, a136\n"
"v_accvgpr_read_b32 v25, a140\n"
"v_mfma_f32_16x16x32_bf16 a[20:23], a[180:183], v[180:183], a[20:23]\n"
"v_add_f32 v6, v22, v23\n"
"v_add_f32 v7, v24, v25\n"
"v_mfma_f32_16x16x32_bf16 a[24:27], a[180:183], v[184:187], a[24:27]\n"
"v_add_f32 v6, v6, v7\n"
"s_cmp_eq_u32 s35, 1\n"
"v_mfma_f32_16x16x32_bf16 a[28:31], a[180:183], v[188:191], a[28:31]\n"
"v_cmp_nge_f32 vcc, s80, v6\n"
"v_mfma_f32_16x16x32_bf16 a[44:47], a[184:187], v[188:191], a[44:47]\n"
"s_cselect_b64 vcc, vcc, 0\n"
"s_cbranch_vccnz L_slow\n"
"v_mfma_f32_16x16x32_bf16 a[40:43], a[184:187], v[184:187], a[40:43]\n"
"v_rcp_f32 v22, v22\n"
"v_mfma_f32_16x16x32_bf16 a[36:39], a[184:187], v[180:183], a[36:39]\n"
"v_rcp_f32 v23, v23\n"
"v_mfma_f32_16x16x32_bf16 a[32:35], a[184:187], v[176:179], a[32:35]\n"
"v_rcp_f32 v24, v24\n"
"v_mfma_f32_16x16x32_bf16 a[48:51], a[188:191], v[176:179], a[48:51]\n"
"v_rcp_f32 v25, v25\n"
"v_mfma_f32_16x16x32_bf16 a[52:55], a[188:191], v[180:183], a[52:55]\n"
"v_accvgpr_read_b32 v96, a0\n"
"v_accvgpr_read_b32 v97, a1\n"
"v_mfma_f32_16x16x32_bf16 a[56:59], a[188:191], v[184:187], a[56:59]\n"
"v_accvgpr_read_b32 v98, a2\n"
"v_accvgpr_read_b32 v99, a3\n"
"v_mfma_f32_16x16x32_bf16 a[60:63], a[188:191], v[188:191], a[60:63]\n"
"v_accvgpr_read_b32 v100, a16\n"
"v_accvgpr_read_b32 v101, a17\n"
"v_mfma_f32_16x16x32_bf16 a[76:79], a[192:195], v[188:191], a[76:79]\n"
"v_accvgpr_read_b32 v102, a18\n"
"v_accvgpr_read_b32 v103, a19\n"
"v_mfma_f32_16x16x32_bf16 a[72:75], a[192:195], v[184:187], a[72:75]\n"
"v_accvgpr_read_b32 v104, a4\n"
"v_accvgpr_read_b32 v105, a5\n"
"v_mfma_f32_16x16x32_bf16 a[68:71], a[192:195], v[180:183], a[68:71]\n"
"v_accvgpr_read_b32 v106, a6\n"
"v_accvgpr_read_b32 v107, a7\n"
"v_mfma_f32_16x16x32_bf16 a[64:67], a[192:195], v[176:179], a[64:67]\n"
"v_accvgpr_read_b32 v108, a20\n"
"v_accvgpr_read_b32 v109, a21\n"
"v_mfma_f32_16x16x32_bf16 a[80:83], a[196:199], v[176:179], a[80:83]\n"
"v_accvgpr_read_b32 v110, a22\n"
"v_accvgpr_read_b32 v111, a23\n"
"v_mfma_f32_16x16x32_bf16 a[84:87], a[196:199], v[180:183], a[84:87]\n"
"v_accvgpr_read_b32 v112, a8\n"
"v_accvgpr_read_b32 v113, a9\n"
"v_mfma_f32_16x16x32_bf16 a[88:91], a[196:199], v[184:187], a[88:91]\n"
"v_accvgpr_read_b32 v114, a10\n"
"v_accvgpr_read_b32 v115, a11\n"
"v_mfma_f32_16x16x32_bf16 a[92:95], a[196:199], v[188:191], a[92:95]\n"
"v_accvgpr_read_b32 v116, a24\n"
"v_accvgpr_read_b32 v117, a25\n"
"v_mfma_f32_16x16x32_bf16 a[108:111], a[200:203], v[188:191], a[108:111]\n"
"v_accvgpr_read_b32 v118, a26\n"
"v_accvgpr_read_b32 v119, a27\n"
"v_mfma_f32_16x16x32_bf16 a[104:107], a[200:203], v[184:187], a[104:107]\n"
"v_accvgpr_read_b32 v120, a12\n"
"v_accvgpr_read_b32 v121, a13\n"
"v_mfma_f32_16x16x32_bf16 a[100:103], a[200:203], v[180:183], a[100:103]\n"
"v_accvgpr_read_b32 v122, a14\n"
"v_accvgpr_read_b32 v123, a15\n"
"v_mfma_f32_16x16x32_bf16 a[96:99], a[200:203], v[176:179], a[96:99]\n"
"v_accvgpr_read_b32 v124, a28\n"
"v_accvgpr_read_b32 v125, a29\n"
"v_mfma_f32_16x16x32_bf16 a[112:115], a[204:207], v[176:179], a[112:115]\n"
"v_accvgpr_read_b32 v126, a30\n"
"v_accvgpr_read_b32 v127, a31\n"
"v_mfma_f32_16x16x32_bf16 a[116:119], a[204:207], v[180:183], a[116:119]\n"
"v_pk_mul_f32 v[96:97], v[96:97], v[22:23] op_sel_hi:[1,0]\n"
"v_pk_mul_f32 v[98:99], v[98:99], v[22:23] op_sel_hi:[1,0]\n"
"v_mfma_f32_16x16x32_bf16 a[120:123], a[204:207], v[184:187], a[120:123]\n"
"v_pk_mul_f32 v[100:101], v[100:101], v[22:23] op_sel_hi:[1,0]\n"
"v_pk_mul_f32 v[102:103], v[102:103], v[22:23] op_sel_hi:[1,0]\n"
"v_mfma_f32_16x16x32_bf16 a[124:127], a[204:207], v[188:191], a[124:127]\n"
"s_and_b32 s5, s52, 3\n"
"s_mul_i32 s68, s5, 72\n"
"s_mul_i32 s80, s68, 9363\n"
"s_lshr_b32 s80, s80, 16\n"
"s_add_u32 s68, s68, 71\n"
"s_mul_i32 s68, s68, 9363\n"
"s_lshr_b32 s68, s68, 16\n"
"s_mul_i32 s80, s80, 3\n"
"s_mul_i32 s68, s68, 3\n"
"s_add_u32 s68, s68, 2\n"
"v_lshlrev_b32 v6, 1, v0\n"
"v_add_u32 v7, 1, v6\n"
"v_cmp_le_u32 s[98:99], s80, v6\n"
"v_cmp_ge_u32 vcc, s68, v6\n"
"s_nop 0\n"
"s_and_b64 s[98:99], s[98:99], vcc\n"
"v_cmp_le_u32 s[8:9], s80, v7\n"
"v_cmp_ge_u32 vcc, s68, v7\n"
"s_nop 0\n"
"s_and_b64 s[8:9], s[8:9], vcc\n"
"s_waitcnt vmcnt(0)\n"
"v_cmp_eq_u32 vcc, s34, v248\n"
"s_nop 0\n"
"s_andn2_b64 s[88:89], s[98:99], vcc\n"
"v_cmp_eq_u32 vcc, s34, v249\n"
"s_nop 0\n"
"s_andn2_b64 vcc, s[8:9], vcc\n"
"s_or_b64 s[88:89], s[88:89], vcc\n"
"s_cbranch_scc0 L_pu_ok\n"
"s_mov_b32 s55, 0x30000\n"
"L_pu_re:\n"
"s_sleep 1\n"
"buffer_load_dwordx2 v[248:249], v251, s[36:39], s91 offen sc1\n"
"s_waitcnt vmcnt(0)\n"
"v_cmp_eq_u32 vcc, s34, v248\n"
"s_nop 0\n"
"s_andn2_b64 s[88:89], s[98:99], vcc\n"
"v_cmp_eq_u32 vcc, s34, v249\n"
"s_nop 0\n"
"s_andn2_b64 vcc, s[8:9], vcc\n"
"s_or_b64 s[88:89], s[88:89], vcc\n"
"s_cbranch_scc0 L_pu_ok\n"
"s_sub_u32 s55, s55, 1\n"
"s_cmp_lg_u32 s55, 0\n"
"s_cbranch_scc1 L_pu_re\n"
"L_pu_ok:\n"
"s_add_u32 m0, s79, 102400\n"
"s_nop 0\n"
"buffer_load_dwordx4 v1, s[44:47], s74 offen offset:0 sc1 lds\n"
"v_pk_mul_f32 v[104:105], v[104:105], v[22:23] op_sel:[0,1] op_sel_hi:[1,1]\n"
"v_pk_mul_f32 v[106:107], v[106:107], v[22:23] op_sel:[0,1] op_sel_hi:[1,1]\n"
"v_pk_mul_f32 v[108:109], v[108:109], v[22:23] op_sel:[0,1] op_sel_hi:[1,1]\n"
"v_pk_mul_f32 v[110:111], v[110:111], v[22:23] op_sel:[0,1] op_sel_hi:[1,1]\n"
"v_pk_mul_f32 v[112:113], v[112:113], v[24:25] op_sel_hi:[1,0]\n"
"v_pk_mul_f32 v[114:115], v[114:115], v[24:25] op_sel_hi:[1,0]\n"
"v_pk_mul_f32 v[116:117], v[116:117], v[24:25] op_sel_hi:[1,0]\n"
"v_pk_mul_f32 v[118:119], v[118:119], v[24:25] op_sel_hi:[1,0]\n"
"v_pk_mul_f32 v[120:121], v[120:121], v[24:25] op_sel:[0,1] op_sel_hi:[1,1]\n"
"buffer_load_dwordx4 v1, s[44:47], s74 offen offset:1024 sc1 lds\n"
"v_pk_mul_f32 v[122:123], v[122:123], v[24:25] op_sel:[0,1] op_sel_hi:[1,1]\n"
"v_pk_mul_f32 v[124:125], v[124:125], v[24:25] op_sel:[0,1] op_sel_hi:[1,1]\n"
"v_pk_mul_f32 v[126:127], v[126:127], v[24:25] op_sel:[0,1] op_sel_hi:[1,1]\n"
"v_cvt_pk_bf16_f32 v192, v96, v97\n"
"v_cvt_pk_bf16_f32 v193, v98, v99\n"
"v_cvt_pk_bf16_f32 v194, v100, v101\n"
"v_cvt_pk_bf16_f32 v195, v102, v103\n"
"v_cvt_pk_bf16_f32 v196, v104, v105\n"
"v_cvt_pk_bf16_f32 v197, v106, v107\n"
"s_mov_b64 exec, 0xffff\n"
"buffer_load_dwordx4 v1, s[44:47], s74 offen offset:2048 sc1 lds\n"
"s_mov_b64 exec, -1\n"
"v_cvt_pk_bf16_f32 v198, v108, v109\n"
"v_cvt_pk_bf16_f32 v199, v110, v111\n"
"v_cvt_pk_bf16_f32 v200, v112, v113\n"
"v_cvt_pk_bf16_f32 v201, v114, v115\n"
"v_cvt_pk_bf16_f32 v202, v116, v117\n"
"v_cvt_pk_bf16_f32 v203, v118, v119\n"
"v_cvt_pk_bf16_f32 v204, v120, v121\n"
"v_cvt_pk_bf16_f32 v205, v122, v123\n"
"v_cvt_pk_bf16_f32 v206, v124, v125\n"
"s_add_u32 s74, s74, 36864\n"
"s_add_u32 m0, s79, 128000\n"
"s_nop 0\n"
"buffer_load_dwordx4 v1, s[44:47], s74 offen offset:0 sc1 lds\n"
"v_cvt_pk_bf16_f32 v207, v126, v127\n"
"v_accvgpr_read_b32 v128, a32\n"
"v_accvgpr_read_b32 v129, a33\n"
"v_accvgpr_read_b32 v130, a34\n"
"v_accvgpr_read_b32 v131, a35\n"
"v_accvgpr_read_b32 v132, a48\n"
"v_accvgpr_read_b32 v133, a49\n"
"v_accvgpr_read_b32 v134, a50\n"
"v_accvgpr_read_b32 v135, a51\n"
"buffer_load_dwordx4 v1, s[44:47], s74 offen offset:1024 sc1 lds\n"
"v_accvgpr_read_b32 v136, a36\n"
"v_accvgpr_read_b32 v137, a37\n"
"v_accvgpr_read_b32 v138, a38\n"
"v_accvgpr_read_b32 v139, a39\n"
"v_accvgpr_read_b32 v140, a52\n"
"v_accvgpr_read_b32 v141, a53\n"
"v_accvgpr_read_b32 v142, a54\n"
"v_accvgpr_read_b32 v143, a55\n"
"v_accvgpr_read_b32 v144, a40\n"
"s_mov_b64 exec, 0xffff\n"
"buffer_load_dwordx4 v1, s[44:47], s74 offen offset:2048 sc1 lds\n"
"s_mov_b64 exec, -1\n"
"v_accvgpr_read_b32 v145, a41\n"
"v_accvgpr_read_b32 v146, a42\n"
"v_accvgpr_read_b32 v147, a43\n"
"v_accvgpr_read_b32 v148, a56\n"
"v_accvgpr_read_b32 v149, a57\n"
"v_accvgpr_read_b32 v150, a58\n"
"v_accvgpr_read_b32 v151, a59\n"
"v_accvgpr_read_b32 v152, a44\n"
"v_accvgpr_read_b32 v153, a45\n"
"s_add_u32 s74, s74, 36864\n"
"s_add_u32 m0, s79, 0\n"
"s_nop 0\n"
"buffer_load_dwordx4 v1, s[44:47], s74 offen offset:0 sc1 lds\n"
"v_accvgpr_read_b32 v154, a46\n"
"v_accvgpr_read_b32 v155, a47\n"
"v_accvgpr_read_b32 v156, a60\n"
"v_accvgpr_read_b32 v157, a61\n"
"v_accvgpr_read_b32 v158, a62\n"
"v_accvgpr_read_b32 v159, a63\n"
"v_pk_mul_f32 v[128:129], v[128:129], v[22:23] op_sel_hi:[1,0]\n"
"v_pk_mul_f32 v[130:131], v[130:131], v[22:23] op_sel_hi:[1,0]\n"
"v_pk_mul_f32 v[132:133], v[132:133], v[22:23] op_sel_hi:[1,0]\n"
"buffer_load_dwordx4 v1, s[44:47], s74 offen offset:1024 sc1 lds\n"
"v_pk_mul_f32 v[134:135], v[134:135], v[22:23] op_sel_hi:[1,0]\n"
"v_pk_mul_f32 v[136:137], v[136:137], v[22:23] op_sel:[0,1] op_sel_hi:[1,1]\n"
"v_pk_mul_f32 v[138:139], v[138:139], v[22:23] op_sel:[0,1] op_sel_hi:[1,1]\n"
"v_pk_mul_f32 v[140:141], v[140:141], v[22:23] op_sel:[0,1] op_sel_hi:[1,1]\n"
"v_pk_mul_f32 v[142:143], v[142:143], v[22:23] op_sel:[0,1] op_sel_hi:[1,1]\n"
"v_pk_mul_f32 v[144:145], v[144:145], v[24:25] op_sel_hi:[1,0]\n"
"v_pk_mul_f32 v[146:147], v[146:147], v[24:25] op_sel_hi:[1,0]\n"
"v_pk_mul_f32 v[148:149], v[148:149], v[24:25] op_sel_hi:[1,0]\n"
"v_pk_mul_f32 v[150:151], v[150:151], v[24:25] op_sel_hi:[1,0]\n"
"v_pk_mul_f32 v[152:153], v[152:153], v[24:25] op_sel:[0,1] op_sel_hi:[1,1]\n"
"s_mov_b64 exec, 0xffff\n"
"buffer_load_dwordx4 v1, s[44:47], s74 offen offset:2048 sc1 lds\n"
"s_mov_b64 exec, -1\n"
"v_pk_mul_f32 v[154:155], v[154:155], v[24:25] op_sel:[0,1] op_sel_hi:[1,1]\n"
"v_pk_mul_f32 v[156:157], v[156:157], v[24:25] op_sel:[0,1] op_sel_hi:[1,1]\n"
"v_pk_mul_f32 v[158:159], v[158:159], v[24:25] op_sel:[0,1] op_sel_hi:[1,1]\n"
"v_cvt_pk_bf16_f32 v208, v128, v129\n"
"v_cvt_pk_bf16_f32 v209, v130, v131\n"
"v_cvt_pk_bf16_f32 v210, v132, v133\n"
"v_cvt_pk_bf16_f32 v211, v134, v135\n"
"v_cvt_pk_bf16_f32 v212, v136, v137\n"
"v_cvt_pk_bf16_f32 v213, v138, v139\n"
"s_add_u32 s74, s74, 36864\n"
"s_add_u32 m0, s79, 25600\n"
"s_nop 0\n"
"buffer_load_dwordx4 v1, s[44:47], s74 offen offset:0 sc1 lds\n"
"v_cvt_pk_bf16_f32 v214, v140, v141\n"
"v_cvt_pk_bf16_f32 v215, v142, v143\n"
"v_cvt_pk_bf16_f32 v216, v144, v145\n"
"v_cvt_pk_bf16_f32 v217, v146, v147\n"
"v_cvt_pk_bf16_f32 v218, v148, v149\n"
"v_cvt_pk_bf16_f32 v219, v150, v151\n"
"v_cvt_pk_bf16_f32 v220, v152, v153\n"
"v_cvt_pk_bf16_f32 v221, v154, v155\n"
"v_cvt_pk_bf16_f32 v222, v156, v157\n"
"buffer_load_dwordx4 v1, s[44:47], s74 offen offset:1024 sc1 lds\n"
"v_cvt_pk_bf16_f32 v223, v158, v159\n"
"v_accvgpr_read_b32 v96, a64\n"
"v_accvgpr_read_b32 v97, a65\n"
"v_accvgpr_read_b32 v98, a66\n"
"v_accvgpr_read_b32 v99, a67\n"
"v_accvgpr_read_b32 v100, a80\n"
"v_accvgpr_read_b32 v101, a81\n"
"v_accvgpr_read_b32 v102, a82\n"
"v_accvgpr_read_b32 v103, a83\n"
"s_mov_b64 exec, 0xffff\n"
"buffer_load_dwordx4 v1, s[44:47], s74 offen offset:2048 sc1 lds\n"
"s_mov_b64 exec, -1\n"
"v_accvgpr_read_b32 v104, a68\n"
"v_accvgpr_read_b32 v105, a69\n"
"v_accvgpr_read_b32 v106, a70\n"
"v_accvgpr_read_b32 v107, a71\n"
"v_accvgpr_read_b32 v108, a84\n"
"v_accvgpr_read_b32 v109, a85\n"
"v_accvgpr_read_b32 v110, a86\n"
"v_accvgpr_read_b32 v111, a87\n"
"v_accvgpr_read_b32 v112, a72\n"
"s_add_u32 s74, s74, 36864\n"
"s_add_u32 m0, s79, 51200\n"
"s_nop 0\n"
"buffer_load_dwordx4 v1, s[44:47], s74 offen offset:0 sc1 lds\n"
"v_accvgpr_read_b32 v113, a73\n"
"v_accvgpr_read_b32 v114, a74\n"
"v_accvgpr_read_b32 v115, a75\n"
"v_accvgpr_read_b32 v116, a88\n"
"v_accvgpr_read_b32 v117, a89\n"
"v_accvgpr_read_b32 v118, a90\n"
"v_accvgpr_read_b32 v119, a91\n"
"v_accvgpr_read_b32 v120, a76\n"
"v_accvgpr_read_b32 v121, a77\n"
"buffer_load_dwordx4 v1, s[44:47], s74 offen offset:1024 sc1 lds\n"
"v_accvgpr_read_b32 v122, a78\n"
"v_accvgpr_read_b32 v123, a79\n"
"v_accvgpr_read_b32 v124, a92\n"
"v_accvgpr_read_b32 v125, a93\n"
"v_accvgpr_read_b32 v126, a94\n"
"v_accvgpr_read_b32 v127, a95\n"
"v_pk_mul_f32 v[96:97], v[96:97], v[22:23] op_sel_hi:[1,0]\n"
"v_pk_mul_f32 v[98:99], v[98:99], v[22:23] op_sel_hi:[1,0]\n"
"v_pk_mul_f32 v[100:101], v[100:101], v[22:23] op_sel_hi:[1,0]\n"
"s_mov_b64 exec, 0xffff\n"
"buffer_load_dwordx4 v1, s[44:47], s74 offen offset:2048 sc1 lds\n"
"s_mov_b64 exec, -1\n"
"v_pk_mul_f32 v[102:103], v[102:103], v[22:23] op_sel_hi:[1,0]\n"
"v_pk_mul_f32 v[104:105], v[104:105], v[22:23] op_sel:[0,1] op_sel_hi:[1,1]\n"
"v_pk_mul_f32 v[106:107], v[106:107], v[22:23] op_sel:[0,1] op_sel_hi:[1,1]\n"
"v_pk_mul_f32 v[108:109], v[108:109], v[22:23] op_sel:[0,1] op_sel_hi:[1,1]\n"
"v_pk_mul_f32 v[110:111], v[110:111], v[22:23] op_sel:[0,1] op_sel_hi:[1,1]\n"
"v_pk_mul_f32 v[112:113], v[112:113], v[24:25] op_sel_hi:[1,0]\n"
"v_pk_mul_f32 v[114:115], v[114:115], v[24:25] op_sel_hi:[1,0]\n"
"v_pk_mul_f32 v[116:117], v[116:117], v[24:25] op_sel_hi:[1,0]\n"
"v_pk_mul_f32 v[118:119], v[118:119], v[24:25] op_sel_hi:[1,0]\n"
"v_pk_mul_f32 v[120:121], v[120:121], v[24:25] op_sel:[0,1] op_sel_hi:[1,1]\n"
"s_add_u32 s74, s74, 36864\n"
"s_add_u32 m0, s79, 76800\n"
"s_nop 0\n"
"buffer_load_dwordx4 v1, s[44:47], s74 offen offset:0 sc1 lds\n"
"v_pk_mul_f32 v[122:123], v[122:123], v[24:25] op_sel:[0,1] op_sel_hi:[1,1]\n"
"v_pk_mul_f32 v[124:125], v[124:125], v[24:25] op_sel:[0,1] op_sel_hi:[1,1]\n"
"v_pk_mul_f32 v[126:127], v[126:127], v[24:25] op_sel:[0,1] op_sel_hi:[1,1]\n"
"v_cvt_pk_bf16_f32 v224, v96, v97\n"
"v_cvt_pk_bf16_f32 v225, v98, v99\n"
"v_cvt_pk_bf16_f32 v226, v100, v101\n"
"v_cvt_pk_bf16_f32 v227, v102, v103\n"
"v_cvt_pk_bf16_f32 v228, v104, v105\n"
"v_cvt_pk_bf16_f32 v229, v106, v107\n"
"buffer_load_dwordx4 v1, s[44:47], s74 offen offset:1024 sc1 lds\n"
"v_cvt_pk_bf16_f32 v230, v108, v109\n"
"v_cvt_pk_bf16_f32 v231, v110, v111\n"
"v_cvt_pk_bf16_f32 v232, v112, v113\n"
"v_cvt_pk_bf16_f32 v233, v114, v115\n"
"v_cvt_pk_bf16_f32 v234, v116, v117\n"
"v_cvt_pk_bf16_f32 v235, v118, v119\n"
"v_cvt_pk_bf16_f32 v236, v120, v121\n"
"v_cvt_pk_bf16_f32 v237, v122, v123\n"
"v_cvt_pk_bf16_f32 v238, v124, v125\n"
"s_mov_b64 exec, 0xffff\n"
"buffer_load_dwordx4 v1, s[44:47], s74 offen offset:2048 sc1 lds\n"
"s_mov_b64 exec, -1\n"
"v_cvt_pk_bf16_f32 v239, v126, v127\n"
"v_accvgpr_read_b32 v128, a96\n"
"v_accvgpr_read_b32 v129, a97\n"
"v_accvgpr_read_b32 v130, a98\n"
"v_accvgpr_read_b32 v131, a99\n"
"v_accvgpr_read_b32 v132, a112\n"
"v_accvgpr_read_b32 v133, a113\n"
"v_accvgpr_read_b32 v134, a114\n"
"v_accvgpr_read_b32 v135, a115\n"
"s_add_u32 s74, s74, 36864\n"
"s_add_u32 m0, s2, 76800\n"
"s_nop 0\n"
"buffer_load_dwordx4 v8, s[40:43], s73 offen offset:0 lds\n"
"v_accvgpr_read_b32 v136, a100\n"
"v_accvgpr_read_b32 v137, a101\n"
"v_accvgpr_read_b32 v138, a102\n"
"v_accvgpr_read_b32 v139, a103\n"
"v_accvgpr_read_b32 v140, a116\n"
"v_accvgpr_read_b32 v141, a117\n"
"v_accvgpr_read_b32 v142, a118\n"
"v_accvgpr_read_b32 v143, a119\n"
"v_accvgpr_read_b32 v144, a104\n"
"buffer_load_dwordx4 v9, s[40:43], s73 offen offset:1024 lds\n"
"v_accvgpr_read_b32 v145, a105\n"
"v_accvgpr_read_b32 v146, a106\n"
"v_accvgpr_read_b32 v147, a107\n"
"v_accvgpr_read_b32 v148, a120\n"
"v_accvgpr_read_b32 v149, a121\n"
"v_accvgpr_read_b32 v150, a122\n"
"v_accvgpr_read_b32 v151, a123\n"
"v_accvgpr_read_b32 v152, a108\n"
"v_accvgpr_read_b32 v153, a109\n"
"buffer_load_dwordx4 v10, s[40:43], s73 offen offset:2048 lds\n"
"v_accvgpr_read_b32 v154, a110\n"
"v_accvgpr_read_b32 v155, a111\n"
"v_accvgpr_read_b32 v156, a124\n"
"v_accvgpr_read_b32 v157, a125\n"
"v_accvgpr_read_b32 v158, a126\n"
"v_accvgpr_read_b32 v159, a127\n"
"v_pk_mul_f32 v[128:129], v[128:129], v[22:23] op_sel_hi:[1,0]\n"
"v_pk_mul_f32 v[130:131], v[130:131], v[22:23] op_sel_hi:[1,0]\n"
"v_pk_mul_f32 v[132:133], v[132:133], v[22:23] op_sel_hi:[1,0]\n"
"buffer_load_dwordx4 v11, s[40:43], s73 offen offset:3072 lds\n"
"s_add_u32 s73, s73, 131072\n"
"v_pk_mul_f32 v[134:135], v[134:135], v[22:23] op_sel_hi:[1,0]\n"
"v_pk_mul_f32 v[136:137], v[136:137], v[22:23] op_sel:[0,1] op_sel_hi:[1,1]\n"
"v_pk_mul_f32 v[138:139], v[138:139], v[22:23] op_sel:[0,1] op_sel_hi:[1,1]\n"
"v_pk_mul_f32 v[140:141], v[140:141], v[22:23] op_sel:[0,1] op_sel_hi:[1,1]\n"
"v_pk_mul_f32 v[142:143], v[142:143], v[22:23] op_sel:[0,1] op_sel_hi:[1,1]\n"
"v_pk_mul_f32 v[144:145], v[144:145], v[24:25] op_sel_hi:[1,0]\n"
"v_pk_mul_f32 v[146:147], v[146:147], v[24:25] op_sel_hi:[1,0]\n"
"v_pk_mul_f32 v[148:149], v[148:149], v[24:25] op_sel_hi:[1,0]\n"
"v_pk_mul_f32 v[150:151], v[150:151], v[24:25] op_sel_hi:[1,0]\n"
"v_pk_mul_f32 v[152:153], v[152:153], v[24:25] op_sel:[0,1] op_sel_hi:[1,1]\n"
"v_pk_mul_f32 v[154:155], v[154:155], v[24:25] op_sel:[0,1] op_sel_hi:[1,1]\n"
"v_pk_mul_f32 v[156:157], v[156:157], v[24:25] op_sel:[0,1] op_sel_hi:[1,1]\n"
"v_pk_mul_f32 v[158:159], v[158:159], v[24:25] op_sel:[0,1] op_sel_hi:[1,1]\n"
"v_cvt_pk_bf16_f32 v176, v128, v129\n"
"v_cvt_pk_bf16_f32 v177, v130, v131\n"
"v_cvt_pk_bf16_f32 v178, v132, v133\n"
"v_cvt_pk_bf16_f32 v179, v134, v135\n"
"v_cvt_pk_bf16_f32 v180, v136, v137\n"
"v_cvt_pk_bf16_f32 v181, v138, v139\n"
"v_cvt_pk_bf16_f32 v182, v140, v141\n"
"v_cvt_pk_bf16_f32 v183, v142, v143\n"
"v_cvt_pk_bf16_f32 v184, v144, v145\n"
"v_cvt_pk_bf16_f32 v185, v146, v147\n"
"v_cvt_pk_bf16_f32 v186, v148, v149\n"
"v_cvt_pk_bf16_f32 v187, v150, v151\n"
"v_cvt_pk_bf16_f32 v188, v152, v153\n"
"v_cvt_pk_bf16_f32 v189, v154, v155\n"
"v_cvt_pk_bf16_f32 v190, v156, v157\n"
"v_cvt_pk_bf16_f32 v191, v158, v159\n"
"v_lshrrev_b32 v24, 1, v2\n"
"v_lshlrev_b32 v24, 12, v24\n"
"v_and_b32 v25, 1, v2\n"
"v_lshl_add_u32 v26, v25, 2, v4\n"
"v_lshl_add_u32 v24, v26, 6, v24\n"
"v_lshl_add_u32 v24, v5, 3, v24\n"
"s_lshl_b32 s81, s11, 10\n"
"v_add_u32 v24, s81, v24\n"
"v_lshl_add_u32 v12, v25, 5, v24\n"
"v_xor_b32 v13, 32, v12\n"
"v_add_u32 v14, 51200, v12\n"
"v_add_u32 v15, 51200, v13\n"
"v_add_u32 v16, 102400, v12\n"
"v_add_u32 v17, 102400, v13\n"
"v_add_u32 v18, 16384, v251\n"
"v_add_u32 v19, 67584, v251\n"
"v_add_u32 v20, 118784, v251\n"
"s_waitcnt vmcnt(19)\n"
"s_barrier\n"
"ds_read2st64_b64 a[0:3], v20 offset0:0 offset1:1\n"
"ds_read_b64_tr_b16 a[72:73], v16 offset:0\n"
"ds_read_b64_tr_b16 a[74:75], v16 offset:8192\n"
"ds_read_b64_tr_b16 a[76:77], v17 offset:0\n"
"ds_read_b64_tr_b16 a[78:79], v17 offset:8192\n"
"ds_read_b64_tr_b16 a[80:81], v16 offset:512\n"
"ds_read_b64_tr_b16 a[82:83], v16 offset:8704\n"
"ds_read_b64_tr_b16 a[84:85], v17 offset:512\n"
"ds_read_b64_tr_b16 a[86:87], v17 offset:8704\n"
"ds_read2st64_b64 a[4:7], v20 offset0:2 offset1:3\n"
"ds_read2st64_b64 a[8:11], v20 offset0:4 offset1:5\n"
"ds_read2st64_b64 a[12:15], v20 offset0:6 offset1:7\n"
"ds_read2st64_b64 a[16:19], v20 offset0:8 offset1:9\n"
"ds_read2st64_b64 a[20:23], v20 offset0:10 offset1:11\n"
"ds_read2st64_b64 a[24:27], v20 offset0:12 offset1:13\n"
"ds_read2st64_b64 a[28:31], v20 offset0:14 offset1:15\n"
"ds_read2st64_b64 a[32:35], v20 offset0:16 offset1:17\n"
"s_waitcnt lgkmcnt(0)\n"
"v_mfma_f32_16x16x32_bf16 v[32:35], a[0:3], a[72:75], 0\n"
"s_waitcnt vmcnt(16)\n"
"s_barrier\n"
"v_mfma_f32_16x16x32_bf16 v[36:39], a[0:3], a[76:79], 0\n"
"s_add_u32 m0, s2, 102400\n"
"ds_read2st64_b64 a[36:39], v20 offset0:50 offset1:51\n"
"v_mfma_f32_16x16x32_bf16 v[40:43], a[0:3], a[80:83], 0\n"
"buffer_load_dwordx4 v8, s[40:43], s73 offen offset:0 lds\n"
"v_mfma_f32_16x16x32_bf16 v[44:47], a[0:3], a[84:87], 0\n"
"ds_read_b64_tr_b16 a[88:89], v16 offset:25600\n"
"v_mfma_f32_16x16x32_bf16 v[60:63], a[4:7], a[84:87], 0\n"
"ds_read_b64_tr_b16 a[90:91], v16 offset:33792\n"
"v_mfma_f32_16x16x32_bf16 v[56:59], a[4:7], a[80:83], 0\n"
"ds_read_b64_tr_b16 a[92:93], v17 offset:25600\n"
"v_mfma_f32_16x16x32_bf16 v[52:55], a[4:7], a[76:79], 0\n"
"buffer_store_dwordx4 v[192:195], v1, s[36:39], s82 offen offset:0 sc1\n"
"v_mfma_f32_16x16x32_bf16 v[48:51], a[4:7], a[72:75], 0\n"
"buffer_load_dwordx4 v9, s[40:43], s73 offen offset:1024 lds\n"
"v_mfma_f32_16x16x32_bf16 v[64:67], a[8:11], a[72:75], 0\n"
"ds_read_b64_tr_b16 a[94:95], v17 offset:33792\n"
"v_mfma_f32_16x16x32_bf16 v[68:71], a[8:11], a[76:79], 0\n"
"ds_read_b64_tr_b16 a[96:97], v16 offset:26112\n"
"v_mfma_f32_16x16x32_bf16 v[72:75], a[8:11], a[80:83], 0\n"
"ds_read_b64_tr_b16 a[98:99], v16 offset:34304\n"
"v_mfma_f32_16x16x32_bf16 v[76:79], a[8:11], a[84:87], 0\n"
"v_mfma_f32_16x16x32_bf16 v[92:95], a[12:15], a[84:87], 0\n"
"buffer_load_dwordx4 v10, s[40:43], s73 offen offset:2048 lds\n"
"v_mfma_f32_16x16x32_bf16 v[88:91], a[12:15], a[80:83], 0\n"
"ds_read_b64_tr_b16 a[100:101], v17 offset:26112\n"
"v_mfma_f32_16x16x32_bf16 v[84:87], a[12:15], a[76:79], 0\n"
"ds_read_b64_tr_b16 a[102:103], v17 offset:34304\n"
"v_mfma_f32_16x16x32_bf16 v[80:83], a[12:15], a[72:75], 0\n"
"ds_read2st64_b64 a[40:43], v20 offset0:52 offset1:53\n"
"v_mfma_f32_16x16x32_bf16 v[96:99], a[16:19], a[72:75], 0\n"
"v_mfma_f32_16x16x32_bf16 v[100:103], a[16:19], a[76:79], 0\n"
"buffer_load_dwordx4 v11, s[40:43], s73 offen offset:3072 lds\n"
"v_mfma_f32_16x16x32_bf16 v[104:107], a[16:19], a[80:83], 0\n"
"ds_read2st64_b64 a[44:47], v20 offset0:54 offset1:55\n"
"v_mfma_f32_16x16x32_bf16 v[108:111], a[16:19], a[84:87], 0\n"
"ds_read2st64_b64 a[48:51], v20 offset0:56 offset1:57\n"
"v_mfma_f32_16x16x32_bf16 v[124:127], a[20:23], a[84:87], 0\n"
"buffer_store_dwordx4 v[196:199], v1, s[36:39], s82 offen offset:1024 sc1\n"
"v_mfma_f32_16x16x32_bf16 v[120:123], a[20:23], a[80:83], 0\n"
"s_add_u32 s73, s73, 131072\n"
"s_add_u32 m0, s79, 102400\n"
"v_mfma_f32_16x16x32_bf16 v[116:119], a[20:23], a[76:79], 0\n"
"buffer_load_dwordx4 v1, s[44:47], s74 offen offset:0 sc1 lds\n"
"v_mfma_f32_16x16x32_bf16 v[112:115], a[20:23], a[72:75], 0\n"
"ds_read2st64_b64 a[52:55], v20 offset0:58 offset1:59\n"
"ds_read2st64_b64 a[56:59], v20 offset0:60 offset1:61\n"
"v_mfma_f32_16x16x32_bf16 v[128:131], a[24:27], a[72:75], 0\n"
"ds_read2st64_b64 a[60:63], v20 offset0:62 offset1:63\n"
"v_mfma_f32_16x16x32_bf16 v[132:135], a[24:27], a[76:79], 0\n"
"ds_read2st64_b64 a[64:67], v20 offset0:64 offset1:65\n"
"v_mfma_f32_16x16x32_bf16 v[136:139], a[24:27], a[80:83], 0\n"
"v_mfma_f32_16x16x32_bf16 v[140:143], a[24:27], a[84:87], 0\n"
"buffer_load_dwordx4 v1, s[44:47], s74 offen offset:1024 sc1 lds\n"
"v_mfma_f32_16x16x32_bf16 v[156:159], a[28:31], a[84:87], 0\n"
"ds_read2st64_b64 a[68:71], v20 offset0:66 offset1:67\n"
"v_mfma_f32_16x16x32_bf16 v[152:155], a[28:31], a[80:83], 0\n"
"v_mfma_f32_16x16x32_bf16 v[148:151], a[28:31], a[76:79], 0\n"
"v_mfma_f32_16x16x32_bf16 v[144:147], a[28:31], a[72:75], 0\n"
"v_mfma_f32_16x16x32_bf16 v[160:163], a[32:35], a[72:75], 0\n"
"s_mov_b64 exec, 0xffff\n"
"buffer_load_dwordx4 v1, s[44:47], s74 offen offset:2048 sc1 lds\n"
"s_mov_b64 exec, -1\n"
"v_mfma_f32_16x16x32_bf16 v[164:167], a[32:35], a[76:79], 0\n"
"s_add_u32 s74, s74, 36864\n"
"v_mfma_f32_16x16x32_bf16 v[168:171], a[32:35], a[80:83], 0\n"
"v_mfma_f32_16x16x32_bf16 v[172:175], a[32:35], a[84:87], 0\n"
"s_waitcnt lgkmcnt(0)\n"
"v_mfma_f32_16x16x32_bf16 v[32:35], a[36:39], a[88:91], v[32:35]\n"
"s_waitcnt vmcnt(22)\n"
"s_barrier\n"
"v_mfma_f32_16x16x32_bf16 v[36:39], a[36:39], a[92:95], v[36:39]\n"
"s_add_u32 m0, s2, 128000\n"
"ds_read2st64_b64 a[0:3], v18 offset0:0 offset1:1\n"
"v_mfma_f32_16x16x32_bf16 v[40:43], a[36:39], a[96:99], v[40:43]\n"
"buffer_load_dwordx4 v8, s[40:43], s73 offen offset:0 lds\n"
"v_mfma_f32_16x16x32_bf16 v[44:47], a[36:39], a[100:103], v[44:47]\n"
"ds_read_b64_tr_b16 a[72:73], v12 offset:0\n"
"v_mfma_f32_16x16x32_bf16 v[60:63], a[40:43], a[100:103], v[60:63]\n"
"ds_read_b64_tr_b16 a[74:75], v12 offset:8192\n"
"v_mfma_f32_16x16x32_bf16 v[56:59], a[40:43], a[96:99], v[56:59]\n"
"ds_read_b64_tr_b16 a[76:77], v13 offset:0\n"
"v_mfma_f32_16x16x32_bf16 v[52:55], a[40:43], a[92:95], v[52:55]\n"
"buffer_store_dwordx4 v[200:203], v1, s[36:39], s84 offen offset:0 sc1\n"
"v_mfma_f32_16x16x32_bf16 v[48:51], a[40:43], a[88:91], v[48:51]\n"
"buffer_load_dwordx4 v9, s[40:43], s73 offen offset:1024 lds\n"
"v_mfma_f32_16x16x32_bf16 v[64:67], a[44:47], a[88:91], v[64:67]\n"
"ds_read_b64_tr_b16 a[78:79], v13 offset:8192\n"
"v_mfma_f32_16x16x32_bf16 v[68:71], a[44:47], a[92:95], v[68:71]\n"
"ds_read_b64_tr_b16 a[80:81], v12 offset:512\n"
"v_mfma_f32_16x16x32_bf16 v[72:75], a[44:47], a[96:99], v[72:75]\n"
"ds_read_b64_tr_b16 a[82:83], v12 offset:8704\n"
"v_mfma_f32_16x16x32_bf16 v[76:79], a[44:47], a[100:103], v[76:79]\n"
"v_mfma_f32_16x16x32_bf16 v[92:95], a[48:51], a[100:103], v[92:95]\n"
"buffer_load_dwordx4 v10, s[40:43], s73 offen offset:2048 lds\n"
"v_mfma_f32_16x16x32_bf16 v[88:91], a[48:51], a[96:99], v[88:91]\n"
"ds_read_b64_tr_b16 a[84:85], v13 offset:512\n"
"v_mfma_f32_16x16x32_bf16 v[84:87], a[48:51], a[92:95], v[84:87]\n"
"ds_read_b64_tr_b16 a[86:87], v13 offset:8704\n"
"v_mfma_f32_16x16x32_bf16 v[80:83], a[48:51], a[88:91], v[80:83]\n"
"ds_read2st64_b64 a[4:7], v18 offset0:2 offset1:3\n"
"v_mfma_f32_16x16x32_bf16 v[96:99], a[52:55], a[88:91], v[96:99]\n"
"v_mfma_f32_16x16x32_bf16 v[100:103], a[52:55], a[92:95], v[100:103]\n"
"buffer_load_dwordx4 v11, s[40:43], s73 offen offset:3072 lds\n"
"v_mfma_f32_16x16x32_bf16 v[104:107], a[52:55], a[96:99], v[104:107]\n"
"ds_read2st64_b64 a[8:11], v18 offset0:4 offset1:5\n"
"v_mfma_f32_16x16x32_bf16 v[108:111], a[52:55], a[100:103], v[108:111]\n"
"ds_read2st64_b64 a[12:15], v18 offset0:6 offset1:7\n"
"v_mfma_f32_16x16x32_bf16 v[124:127], a[56:59], a[100:103], v[124:127]\n"
"ds_read2st64_b64 a[16:19], v18 offset0:8 offset1:9\n"
"v_mfma_f32_16x16x32_bf16 v[120:123], a[56:59], a[96:99], v[120:123]\n"
"s_add_u32 s73, s73, 131072\n"
"s_add_u32 m0, s79, 128000\n"
"v_mfma_f32_16x16x32_bf16 v[116:119], a[56:59], a[92:95], v[116:119]\n"
"buffer_load_dwordx4 v1, s[44:47], s74 offen offset:0 sc1 lds\n"
"v_mfma_f32_16x16x32_bf16 v[112:115], a[56:59], a[88:91], v[112:115]\n"
"ds_read2st64_b64 a[20:23], v18 offset0:10 offset1:11\n"
"v_mfma_f32_16x16x32_bf16 v[128:131], a[60:63], a[88:91], v[128:131]\n"
"ds_read2st64_b64 a[24:27], v18 offset0:12 offset1:13\n"
"v_mfma_f32_16x16x32_bf16 v[132:135], a[60:63], a[92:95], v[132:135]\n"
"ds_read2st64_b64 a[28:31], v18 offset0:14 offset1:15\n"
"v_mfma_f32_16x16x32_bf16 v[136:139], a[60:63], a[96:99], v[136:139]\n"
"v_mfma_f32_16x16x32_bf16 v[140:143], a[60:63], a[100:103], v[140:143]\n"
"buffer_load_dwordx4 v1, s[44:47], s74 offen offset:1024 sc1 lds\n"
"v_mfma_f32_16x16x32_bf16 v[156:159], a[64:67], a[100:103], v[156:159]\n"
"ds_read2st64_b64 a[32:35], v18 offset0:16 offset1:17\n"
"v_mfma_f32_16x16x32_bf16 v[152:155], a[64:67], a[96:99], v[152:155]\n"
"v_mfma_f32_16x16x32_bf16 v[148:151], a[64:67], a[92:95], v[148:151]\n"
"v_mfma_f32_16x16x32_bf16 v[144:147], a[64:67], a[88:91], v[144:147]\n"
"v_mfma_f32_16x16x32_bf16 v[160:163], a[68:71], a[88:91], v[160:163]\n"
"s_mov_b64 exec, 0xffff\n"
"buffer_load_dwordx4 v1, s[44:47], s74 offen offset:2048 sc1 lds\n"
"s_mov_b64 exec, -1\n"
"v_mfma_f32_16x16x32_bf16 v[164:167], a[68:71], a[92:95], v[164:167]\n"
"s_add_u32 s74, s74, 36864\n"
"v_mfma_f32_16x16x32_bf16 v[168:171], a[68:71], a[96:99], v[168:171]\n"
"v_mfma_f32_16x16x32_bf16 v[172:175], a[68:71], a[100:103], v[172:175]\n"
"s_waitcnt lgkmcnt(0)\n"
"v_mfma_f32_16x16x32_bf16 v[32:35], a[0:3], a[72:75], v[32:35]\n"
"s_waitcnt vmcnt(27)\n"
"s_barrier\n"
"v_mfma_f32_16x16x32_bf16 v[36:39], a[0:3], a[76:79], v[36:39]\n"
"s_add_u32 m0, s2, 0\n"
"ds_read2st64_b64 a[36:39], v18 offset0:50 offset1:51\n"
"v_mfma_f32_16x16x32_bf16 v[40:43], a[0:3], a[80:83], v[40:43]\n"
"buffer_load_dwordx4 v8, s[40:43], s73 offen offset:0 lds\n"
"v_mfma_f32_16x16x32_bf16 v[44:47], a[0:3], a[84:87], v[44:47]\n"
"ds_read_b64_tr_b16 a[88:89], v12 offset:25600\n"
"v_mfma_f32_16x16x32_bf16 v[60:63], a[4:7], a[84:87], v[60:63]\n"
"ds_read_b64_tr_b16 a[90:91], v12 offset:33792\n"
"v_mfma_f32_16x16x32_bf16 v[56:59], a[4:7], a[80:83], v[56:59]\n"
"ds_read_b64_tr_b16 a[92:93], v13 offset:25600\n"
"v_mfma_f32_16x16x32_bf16 v[52:55], a[4:7], a[76:79], v[52:55]\n"
"buffer_store_dwordx4 v[204:207], v1, s[36:39], s84 offen offset:1024 sc1\n"
"v_mfma_f32_16x16x32_bf16 v[48:51], a[4:7], a[72:75], v[48:51]\n"
"buffer_load_dwordx4 v9, s[40:43], s73 offen offset:1024 lds\n"
"v_mfma_f32_16x16x32_bf16 v[64:67], a[8:11], a[72:75], v[64:67]\n"
"ds_read_b64_tr_b16 a[94:95], v13 offset:33792\n"
"v_mfma_f32_16x16x32_bf16 v[68:71], a[8:11], a[76:79], v[68:71]\n"
"ds_read_b64_tr_b16 a[96:97], v12 offset:26112\n"
"v_mfma_f32_16x16x32_bf16 v[72:75], a[8:11], a[80:83], v[72:75]\n"
"ds_read_b64_tr_b16 a[98:99], v12 offset:34304\n"
"v_mfma_f32_16x16x32_bf16 v[76:79], a[8:11], a[84:87], v[76:79]\n"
"v_mfma_f32_16x16x32_bf16 v[92:95], a[12:15], a[84:87], v[92:95]\n"
"buffer_load_dwordx4 v10, s[40:43], s73 offen offset:2048 lds\n"
"v_mfma_f32_16x16x32_bf16 v[88:91], a[12:15], a[80:83], v[88:91]\n"
"ds_read_b64_tr_b16 a[100:101], v13 offset:26112\n"
"v_mfma_f32_16x16x32_bf16 v[84:87], a[12:15], a[76:79], v[84:87]\n"
"ds_read_b64_tr_b16 a[102:103], v13 offset:34304\n"
"v_mfma_f32_16x16x32_bf16 v[80:83], a[12:15], a[72:75], v[80:83]\n"
"ds_read2st64_b64 a[40:43], v18 offset0:52 offset1:53\n"
"v_mfma_f32_16x16x32_bf16 v[96:99], a[16:19], a[72:75], v[96:99]\n"
"v_mfma_f32_16x16x32_bf16 v[100:103], a[16:19], a[76:79], v[100:103]\n"
"buffer_load_dwordx4 v11, s[40:43], s73 offen offset:3072 lds\n"
"v_mfma_f32_16x16x32_bf16 v[104:107], a[16:19], a[80:83], v[104:107]\n"
"ds_read2st64_b64 a[44:47], v18 offset0:54 offset1:55\n"
"v_mfma_f32_16x16x32_bf16 v[108:111], a[16:19], a[84:87], v[108:111]\n"
"ds_read2st64_b64 a[48:51], v18 offset0:56 offset1:57\n"
"v_mfma_f32_16x16x32_bf16 v[124:127], a[20:23], a[84:87], v[124:127]\n"
"ds_read2st64_b64 a[52:55], v18 offset0:58 offset1:59\n"
"v_mfma_f32_16x16x32_bf16 v[120:123], a[20:23], a[80:83], v[120:123]\n"
"s_add_u32 s73, s73, 131072\n"
"s_add_u32 m0, s79, 0\n"
"v_mfma_f32_16x16x32_bf16 v[116:119], a[20:23], a[76:79], v[116:119]\n"
"buffer_load_dwordx4 v1, s[44:47], s74 offen offset:0 sc1 lds\n"
"v_mfma_f32_16x16x32_bf16 v[112:115], a[20:23], a[72:75], v[112:115]\n"
"ds_read2st64_b64 a[56:59], v18 offset0:60 offset1:61\n"
"v_mfma_f32_16x16x32_bf16 v[128:131], a[24:27], a[72:75], v[128:131]\n"
"ds_read2st64_b64 a[60:63], v18 offset0:62 offset1:63\n"
"v_mfma_f32_16x16x32_bf16 v[132:135], a[24:27], a[76:79], v[132:135]\n"
"ds_read2st64_b64 a[64:67], v18 offset0:64 offset1:65\n"
"v_mfma_f32_16x16x32_bf16 v[136:139], a[24:27], a[80:83], v[136:139]\n"
"v_mfma_f32_16x16x32_bf16 v[140:143], a[24:27], a[84:87], v[140:143]\n"
"buffer_load_dwordx4 v1, s[44:47], s74 offen offset:1024 sc1 lds\n"
"v_mfma_f32_16x16x32_bf16 v[156:159], a[28:31], a[84:87], v[156:159]\n"
"ds_read2st64_b64 a[68:71], v18 offset0:66 offset1:67\n"
"v_mfma_f32_16x16x32_bf16 v[152:155], a[28:31], a[80:83], v[152:155]\n"
"v_mfma_f32_16x16x32_bf16 v[148:151], a[28:31], a[76:79], v[148:151]\n"
"v_mfma_f32_16x16x32_bf16 v[144:147], a[28:31], a[72:75], v[144:147]\n"
"v_mfma_f32_16x16x32_bf16 v[160:163], a[32:35], a[72:75], v[160:163]\n"
"s_mov_b64 exec, 0xffff\n"
"buffer_load_dwordx4 v1, s[44:47], s74 offen offset:2048 sc1 lds\n"
"s_mov_b64 exec, -1\n"
"v_mfma_f32_16x16x32_bf16 v[164:167], a[32:35], a[76:79], v[164:167]\n"
"s_add_u32 s74, s74, 36864\n"
"v_mfma_f32_16x16x32_bf16 v[168:171], a[32:35], a[80:83], v[168:171]\n"
"v_mfma_f32_16x16x32_bf16 v[172:175], a[32:35], a[84:87], v[172:175]\n"
"s_waitcnt lgkmcnt(0)\n"
"v_mfma_f32_16x16x32_bf16 v[32:35], a[36:39], a[88:91], v[32:35]\n"
"s_waitcnt vmcnt(32)\n"
"s_barrier\n"
"v_mfma_f32_16x16x32_bf16 v[36:39], a[36:39], a[92:95], v[36:39]\n"
"s_add_u32 m0, s2, 25600\n"
"ds_read2st64_b64 a[0:3], v19 offset0:0 offset1:1\n"
"v_mfma_f32_16x16x32_bf16 v[40:43], a[36:39], a[96:99], v[40:43]\n"
"buffer_load_dwordx4 v8, s[40:43], s73 offen offset:0 lds\n"
"v_mfma_f32_16x16x32_bf16 v[44:47], a[36:39], a[100:103], v[44:47]\n"
"ds_read_b64_tr_b16 a[72:73], v14 offset:0\n"
"v_mfma_f32_16x16x32_bf16 v[60:63], a[40:43], a[100:103], v[60:63]\n"
"ds_read_b64_tr_b16 a[74:75], v14 offset:8192\n"
"v_mfma_f32_16x16x32_bf16 v[56:59], a[40:43], a[96:99], v[56:59]\n"
"ds_read_b64_tr_b16 a[76:77], v15 offset:0\n"
"v_mfma_f32_16x16x32_bf16 v[52:55], a[40:43], a[92:95], v[52:55]\n"
"buffer_store_dwordx4 v[208:211], v1, s[36:39], s82 offen offset:2048 sc1\n"
"v_mfma_f32_16x16x32_bf16 v[48:51], a[40:43], a[88:91], v[48:51]\n"
"buffer_load_dwordx4 v9, s[40:43], s73 offen offset:1024 lds\n"
"v_mfma_f32_16x16x32_bf16 v[64:67], a[44:47], a[88:91], v[64:67]\n"
"ds_read_b64_tr_b16 a[78:79], v15 offset:8192\n"
"v_mfma_f32_16x16x32_bf16 v[68:71], a[44:47], a[92:95], v[68:71]\n"
"ds_read_b64_tr_b16 a[80:81], v14 offset:512\n"
"v_mfma_f32_16x16x32_bf16 v[72:75], a[44:47], a[96:99], v[72:75]\n"
"ds_read_b64_tr_b16 a[82:83], v14 offset:8704\n"
"v_mfma_f32_16x16x32_bf16 v[76:79], a[44:47], a[100:103], v[76:79]\n"
"v_mfma_f32_16x16x32_bf16 v[92:95], a[48:51], a[100:103], v[92:95]\n"
"buffer_load_dwordx4 v10, s[40:43], s73 offen offset:2048 lds\n"
"v_mfma_f32_16x16x32_bf16 v[88:91], a[48:51], a[96:99], v[88:91]\n"
"ds_read_b64_tr_b16 a[84:85], v15 offset:512\n"
"v_mfma_f32_16x16x32_bf16 v[84:87], a[48:51], a[92:95], v[84:87]\n"
"ds_read_b64_tr_b16 a[86:87], v15 offset:8704\n"
"v_mfma_f32_16x16x32_bf16 v[80:83], a[48:51], a[88:91], v[80:83]\n"
"ds_read2st64_b64 a[4:7], v19 offset0:2 offset1:3\n"
"v_mfma_f32_16x16x32_bf16 v[96:99], a[52:55], a[88:91], v[96:99]\n"
"v_mfma_f32_16x16x32_bf16 v[100:103], a[52:55], a[92:95], v[100:103]\n"
"buffer_load_dwordx4 v11, s[40:43], s73 offen offset:3072 lds\n"
"v_mfma_f32_16x16x32_bf16 v[104:107], a[52:55], a[96:99], v[104:107]\n"
"ds_read2st64_b64 a[8:11], v19 offset0:4 offset1:5\n"
"v_mfma_f32_16x16x32_bf16 v[108:111], a[52:55], a[100:103], v[108:111]\n"
"ds_read2st64_b64 a[12:15], v19 offset0:6 offset1:7\n"
"v_mfma_f32_16x16x32_bf16 v[124:127], a[56:59], a[100:103], v[124:127]\n"
"buffer_store_dwordx4 v[212:215], v1, s[36:39], s82 offen offset:3072 sc1\n"
"v_mfma_f32_16x16x32_bf16 v[120:123], a[56:59], a[96:99], v[120:123]\n"
"s_add_u32 s73, s73, 131072\n"
"s_add_u32 m0, s79, 25600\n"
"v_mfma_f32_16x16x32_bf16 v[116:119], a[56:59], a[92:95], v[116:119]\n"
"buffer_load_dwordx4 v1, s[44:47], s74 offen offset:0 sc1 lds\n"
"v_mfma_f32_16x16x32_bf16 v[112:115], a[56:59], a[88:91], v[112:115]\n"
"ds_read2st64_b64 a[16:19], v19 offset0:8 offset1:9\n"
"ds_read2st64_b64 a[20:23], v19 offset0:10 offset1:11\n"
"v_mfma_f32_16x16x32_bf16 v[128:131], a[60:63], a[88:91], v[128:131]\n"
"ds_read2st64_b64 a[24:27], v19 offset0:12 offset1:13\n"
"v_mfma_f32_16x16x32_bf16 v[132:135], a[60:63], a[92:95], v[132:135]\n"
"ds_read2st64_b64 a[28:31], v19 offset0:14 offset1:15\n"
"v_mfma_f32_16x16x32_bf16 v[136:139], a[60:63], a[96:99], v[136:139]\n"
"v_mfma_f32_16x16x32_bf16 v[140:143], a[60:63], a[100:103], v[140:143]\n"
"buffer_load_dwordx4 v1, s[44:47], s74 offen offset:1024 sc1 lds\n"
"v_mfma_f32_16x16x32_bf16 v[156:159], a[64:67], a[100:103], v[156:159]\n"
"ds_read2st64_b64 a[32:35], v19 offset0:16 offset1:17\n"
"v_mfma_f32_16x16x32_bf16 v[152:155], a[64:67], a[96:99], v[152:155]\n"
"v_mfma_f32_16x16x32_bf16 v[148:151], a[64:67], a[92:95], v[148:151]\n"
"v_mfma_f32_16x16x32_bf16 v[144:147], a[64:67], a[88:91], v[144:147]\n"
"v_mfma_f32_16x16x32_bf16 v[160:163], a[68:71], a[88:91], v[160:163]\n"
"s_mov_b64 exec, 0xffff\n"
"buffer_load_dwordx4 v1, s[44:47], s74 offen offset:2048 sc1 lds\n"
"s_mov_b64 exec, -1\n"
"v_mfma_f32_16x16x32_bf16 v[164:167], a[68:71], a[92:95], v[164:167]\n"
"s_add_u32 s74, s74, 36864\n"
"v_mfma_f32_16x16x32_bf16 v[168:171], a[68:71], a[96:99], v[168:171]\n"
"v_mfma_f32_16x16x32_bf16 v[172:175], a[68:71], a[100:103], v[172:175]\n"
"s_waitcnt lgkmcnt(0)\n"
"v_mfma_f32_16x16x32_bf16 v[32:35], a[0:3], a[72:75], v[32:35]\n"
"s_waitcnt vmcnt(34)\n"
"s_barrier\n"
"v_mfma_f32_16x16x32_bf16 v[36:39], a[0:3], a[76:79], v[36:39]\n"
"s_add_u32 m0, s2, 51200\n"
"ds_read2st64_b64 a[36:39], v19 offset0:50 offset1:51\n"
"v_mfma_f32_16x16x32_bf16 v[40:43], a[0:3], a[80:83], v[40:43]\n"
"buffer_load_dwordx4 v8, s[40:43], s73 offen offset:0 lds\n"
"v_mfma_f32_16x16x32_bf16 v[44:47], a[0:3], a[84:87], v[44:47]\n"
"ds_read_b64_tr_b16 a[88:89], v14 offset:25600\n"
"v_mfma_f32_16x16x32_bf16 v[60:63], a[4:7], a[84:87], v[60:63]\n"
"ds_read_b64_tr_b16 a[90:91], v14 offset:33792\n"
"v_mfma_f32_16x16x32_bf16 v[56:59], a[4:7], a[80:83], v[56:59]\n"
"ds_read_b64_tr_b16 a[92:93], v15 offset:25600\n"
"v_mfma_f32_16x16x32_bf16 v[52:55], a[4:7], a[76:79], v[52:55]\n"
"buffer_store_dwordx4 v[216:219], v1, s[36:39], s84 offen offset:2048 sc1\n"
"v_mfma_f32_16x16x32_bf16 v[48:51], a[4:7], a[72:75], v[48:51]\n"
"buffer_load_dwordx4 v9, s[40:43], s73 offen offset:1024 lds\n"
"v_mfma_f32_16x16x32_bf16 v[64:67], a[8:11], a[72:75], v[64:67]\n"
"ds_read_b64_tr_b16 a[94:95], v15 offset:33792\n"
"v_mfma_f32_16x16x32_bf16 v[68:71], a[8:11], a[76:79], v[68:71]\n"
"ds_read_b64_tr_b16 a[96:97], v14 offset:26112\n"
"v_mfma_f32_16x16x32_bf16 v[72:75], a[8:11], a[80:83], v[72:75]\n"
"ds_read_b64_tr_b16 a[98:99], v14 offset:34304\n"
"v_mfma_f32_16x16x32_bf16 v[76:79], a[8:11], a[84:87], v[76:79]\n"
"v_mfma_f32_16x16x32_bf16 v[92:95], a[12:15], a[84:87], v[92:95]\n"
"buffer_load_dwordx4 v10, s[40:43], s73 offen offset:2048 lds\n"
"v_mfma_f32_16x16x32_bf16 v[88:91], a[12:15], a[80:83], v[88:91]\n"
"ds_read_b64_tr_b16 a[100:101], v15 offset:26112\n"
"v_mfma_f32_16x16x32_bf16 v[84:87], a[12:15], a[76:79], v[84:87]\n"
"ds_read_b64_tr_b16 a[102:103], v15 offset:34304\n"
"v_mfma_f32_16x16x32_bf16 v[80:83], a[12:15], a[72:75], v[80:83]\n"
"ds_read2st64_b64 a[40:43], v19 offset0:52 offset1:53\n"
"v_mfma_f32_16x16x32_bf16 v[96:99], a[16:19], a[72:75], v[96:99]\n"
"v_mfma_f32_16x16x32_bf16 v[100:103], a[16:19], a[76:79], v[100:103]\n"
"buffer_load_dwordx4 v11, s[40:43], s73 offen offset:3072 lds\n"
"v_mfma_f32_16x16x32_bf16 v[104:107], a[16:19], a[80:83], v[104:107]\n"
"ds_read2st64_b64 a[44:47], v19 offset0:54 offset1:55\n"
"v_mfma_f32_16x16x32_bf16 v[108:111], a[16:19], a[84:87], v[108:111]\n"
"ds_read2st64_b64 a[48:51], v19 offset0:56 offset1:57\n"
"v_mfma_f32_16x16x32_bf16 v[124:127], a[20:23], a[84:87], v[124:127]\n"
"ds_read2st64_b64 a[52:55], v19 offset0:58 offset1:59\n"
"v_mfma_f32_16x16x32_bf16 v[120:123], a[20:23], a[80:83], v[120:123]\n"
"s_add_u32 s73, s73, 131072\n"
"s_add_u32 m0, s79, 51200\n"
"v_mfma_f32_16x16x32_bf16 v[116:119], a[20:23], a[76:79], v[116:119]\n"
"buffer_load_dwordx4 v1, s[44:47], s74 offen offset:0 sc1 lds\n"
"v_mfma_f32_16x16x32_bf16 v[112:115], a[20:23], a[72:75], v[112:115]\n"
"ds_read2st64_b64 a[56:59], v19 offset0:60 offset1:61\n"
"v_mfma_f32_16x16x32_bf16 v[128:131], a[24:27], a[72:75], v[128:131]\n"
"ds_read2st64_b64 a[60:63], v19 offset0:62 offset1:63\n"
"v_mfma_f32_16x16x32_bf16 v[132:135], a[24:27], a[76:79], v[132:135]\n"
"ds_read2st64_b64 a[64:67], v19 offset0:64 offset1:65\n"
"v_mfma_f32_16x16x32_bf16 v[136:139], a[24:27], a[80:83], v[136:139]\n"
"v_mfma_f32_16x16x32_bf16 v[140:143], a[24:27], a[84:87], v[140:143]\n"
"buffer_load_dwordx4 v1, s[44:47], s74 offen offset:1024 sc1 lds\n"
"v_mfma_f32_16x16x32_bf16 v[156:159], a[28:31], a[84:87], v[156:159]\n"
"ds_read2st64_b64 a[68:71], v19 offset0:66 offset1:67\n"
"v_mfma_f32_16x16x32_bf16 v[152:155], a[28:31], a[80:83], v[152:155]\n"
"v_mfma_f32_16x16x32_bf16 v[148:151], a[28:31], a[76:79], v[148:151]\n"
"v_mfma_f32_16x16x32_bf16 v[144:147], a[28:31], a[72:75], v[144:147]\n"
"v_mfma_f32_16x16x32_bf16 v[160:163], a[32:35], a[72:75], v[160:163]\n"
"s_mov_b64 exec, 0xffff\n"
"buffer_load_dwordx4 v1, s[44:47], s74 offen offset:2048 sc1 lds\n"
"s_mov_b64 exec, -1\n"
"v_mfma_f32_16x16x32_bf16 v[164:167], a[32:35], a[76:79], v[164:167]\n"
"s_add_u32 s74, s74, 36864\n"
"v_mfma_f32_16x16x32_bf16 v[168:171], a[32:35], a[80:83], v[168:171]\n"
"v_mfma_f32_16x16x32_bf16 v[172:175], a[32:35], a[84:87], v[172:175]\n"
"s_waitcnt lgkmcnt(0)\n"
"v_mfma_f32_16x16x32_bf16 v[32:35], a[36:39], a[88:91], v[32:35]\n"
"s_waitcnt vmcnt(33)\n"
"s_barrier\n"
"v_mfma_f32_16x16x32_bf16 v[36:39], a[36:39], a[92:95], v[36:39]\n"
"s_add_u32 m0, s2, 76800\n"
"ds_read2st64_b64 a[0:3], v20 offset0:0 offset1:1\n"
"v_mfma_f32_16x16x32_bf16 v[40:43], a[36:39], a[96:99], v[40:43]\n"
"buffer_load_dwordx4 v8, s[40:43], s73 offen offset:0 lds\n"
"v_mfma_f32_16x16x32_bf16 v[44:47], a[36:39], a[100:103], v[44:47]\n"
"ds_read_b64_tr_b16 a[72:73], v16 offset:0\n"
"v_mfma_f32_16x16x32_bf16 v[60:63], a[40:43], a[100:103], v[60:63]\n"
"ds_read_b64_tr_b16 a[74:75], v16 offset:8192\n"
"v_mfma_f32_16x16x32_bf16 v[56:59], a[40:43], a[96:99], v[56:59]\n"
"ds_read_b64_tr_b16 a[76:77], v17 offset:0\n"
"v_mfma_f32_16x16x32_bf16 v[52:55], a[40:43], a[92:95], v[52:55]\n"
"buffer_store_dwordx4 v[220:223], v1, s[36:39], s84 offen offset:3072 sc1\n"
"v_mfma_f32_16x16x32_bf16 v[48:51], a[40:43], a[88:91], v[48:51]\n"
"buffer_load_dwordx4 v9, s[40:43], s73 offen offset:1024 lds\n"
"v_mfma_f32_16x16x32_bf16 v[64:67], a[44:47], a[88:91], v[64:67]\n"
"ds_read_b64_tr_b16 a[78:79], v17 offset:8192\n"
"v_mfma_f32_16x16x32_bf16 v[68:71], a[44:47], a[92:95], v[68:71]\n"
"ds_read_b64_tr_b16 a[80:81], v16 offset:512\n"
"v_mfma_f32_16x16x32_bf16 v[72:75], a[44:47], a[96:99], v[72:75]\n"
"ds_read_b64_tr_b16 a[82:83], v16 offset:8704\n"
"v_mfma_f32_16x16x32_bf16 v[76:79], a[44:47], a[100:103], v[76:79]\n"
"v_mfma_f32_16x16x32_bf16 v[92:95], a[48:51], a[100:103], v[92:95]\n"
"buffer_load_dwordx4 v10, s[40:43], s73 offen offset:2048 lds\n"
"v_mfma_f32_16x16x32_bf16 v[88:91], a[48:51], a[96:99], v[88:91]\n"
"ds_read_b64_tr_b16 a[84:85], v17 offset:512\n"
"v_mfma_f32_16x16x32_bf16 v[84:87], a[48:51], a[92:95], v[84:87]\n"
"ds_read_b64_tr_b16 a[86:87], v17 offset:8704\n"
"v_mfma_f32_16x16x32_bf16 v[80:83], a[48:51], a[88:91], v[80:83]\n"
"ds_read2st64_b64 a[4:7], v20 offset0:2 offset1:3\n"
"v_mfma_f32_16x16x32_bf16 v[96:99], a[52:55], a[88:91], v[96:99]\n"
"v_mfma_f32_16x16x32_bf16 v[100:103], a[52:55], a[92:95], v[100:103]\n"
"buffer_load_dwordx4 v11, s[40:43], s73 offen offset:3072 lds\n"
"v_mfma_f32_16x16x32_bf16 v[104:107], a[52:55], a[96:99], v[104:107]\n"
"ds_read2st64_b64 a[8:11], v20 offset0:4 offset1:5\n"
"v_mfma_f32_16x16x32_bf16 v[108:111], a[52:55], a[100:103], v[108:111]\n"
"ds_read2st64_b64 a[12:15], v20 offset0:6 offset1:7\n"
"v_mfma_f32_16x16x32_bf16 v[124:127], a[56:59], a[100:103], v[124:127]\n"
"ds_read2st64_b64 a[16:19], v20 offset0:8 offset1:9\n"
"v_mfma_f32_16x16x32_bf16 v[120:123], a[56:59], a[96:99], v[120:123]\n"
"s_add_u32 s73, s73, 131072\n"
"s_add_u32 m0, s79, 76800\n"
"v_mfma_f32_16x16x32_bf16 v[116:119], a[56:59], a[92:95], v[116:119]\n"
"buffer_load_dwordx4 v1, s[44:47], s74 offen offset:0 sc1 lds\n"
"v_mfma_f32_16x16x32_bf16 v[112:115], a[56:59], a[88:91], v[112:115]\n"
"ds_read2st64_b64 a[20:23], v20 offset0:10 offset1:11\n"
"v_mfma_f32_16x16x32_bf16 v[128:131], a[60:63], a[88:91], v[128:131]\n"
"ds_read2st64_b64 a[24:27], v20 offset0:12 offset1:13\n"
"v_mfma_f32_16x16x32_bf16 v[132:135], a[60:63], a[92:95], v[132:135]\n"
"ds_read2st64_b64 a[28:31], v20 offset0:14 offset1:15\n"
"v_mfma_f32_16x16x32_bf16 v[136:139], a[60:63], a[96:99], v[136:139]\n"
"v_mfma_f32_16x16x32_bf16 v[140:143], a[60:63], a[100:103], v[140:143]\n"
"buffer_load_dwordx4 v1, s[44:47], s74 offen offset:1024 sc1 lds\n"
"v_mfma_f32_16x16x32_bf16 v[156:159], a[64:67], a[100:103], v[156:159]\n"
"ds_read2st64_b64 a[32:35], v20 offset0:16 offset1:17\n"
"v_mfma_f32_16x16x32_bf16 v[152:155], a[64:67], a[96:99], v[152:155]\n"
"v_mfma_f32_16x16x32_bf16 v[148:151], a[64:67], a[92:95], v[148:151]\n"
"v_mfma_f32_16x16x32_bf16 v[144:147], a[64:67], a[88:91], v[144:147]\n"
"v_mfma_f32_16x16x32_bf16 v[160:163], a[68:71], a[88:91], v[160:163]\n"
"s_mov_b64 exec, 0xffff\n"
"buffer_load_dwordx4 v1, s[44:47], s74 offen offset:2048 sc1 lds\n"
"s_mov_b64 exec, -1\n"
"v_mfma_f32_16x16x32_bf16 v[164:167], a[68:71], a[92:95], v[164:167]\n"
"s_add_u32 s74, s74, 36864\n"
"v_mfma_f32_16x16x32_bf16 v[168:171], a[68:71], a[96:99], v[168:171]\n"
"v_mfma_f32_16x16x32_bf16 v[172:175], a[68:71], a[100:103], v[172:175]\n"
"s_waitcnt lgkmcnt(0)\n"
"v_mfma_f32_16x16x32_bf16 v[32:35], a[0:3], a[72:75], v[32:35]\n"
"s_waitcnt vmcnt(33)\n"
"s_barrier\n"
"v_mfma_f32_16x16x32_bf16 v[36:39], a[0:3], a[76:79], v[36:39]\n"
"s_add_u32 m0, s2, 102400\n"
"ds_read2st64_b64 a[36:39], v20 offset0:50 offset1:51\n"
"v_mfma_f32_16x16x32_bf16 v[40:43], a[0:3], a[80:83], v[40:43]\n"
"buffer_load_dwordx4 v8, s[40:43], s73 offen offset:0 lds\n"
"v_mfma_f32_16x16x32_bf16 v[44:47], a[0:3], a[84:87], v[44:47]\n"
"ds_read_b64_tr_b16 a[88:89], v16 offset:25600\n"
"v_mfma_f32_16x16x32_bf16 v[60:63], a[4:7], a[84:87], v[60:63]\n"
"ds_read_b64_tr_b16 a[90:91], v16 offset:33792\n"
"v_mfma_f32_16x16x32_bf16 v[56:59], a[4:7], a[80:83], v[56:59]\n"
"ds_read_b64_tr_b16 a[92:93], v17 offset:25600\n"
"v_mfma_f32_16x16x32_bf16 v[52:55], a[4:7], a[76:79], v[52:55]\n"
"s_add_u32 s82, s82, 4096\n"
"s_add_u32 s84, s84, 4096\n"
"buffer_store_dwordx4 v[224:227], v1, s[36:39], s82 offen offset:0 sc1\n"
"v_mfma_f32_16x16x32_bf16 v[48:51], a[4:7], a[72:75], v[48:51]\n"
"buffer_load_dwordx4 v9, s[40:43], s73 offen offset:1024 lds\n"
"v_mfma_f32_16x16x32_bf16 v[64:67], a[8:11], a[72:75], v[64:67]\n"
"ds_read_b64_tr_b16 a[94:95], v17 offset:33792\n"
"v_mfma_f32_16x16x32_bf16 v[68:71], a[8:11], a[76:79], v[68:71]\n"
"ds_read_b64_tr_b16 a[96:97], v16 offset:26112\n"
"v_mfma_f32_16x16x32_bf16 v[72:75], a[8:11], a[80:83], v[72:75]\n"
"ds_read_b64_tr_b16 a[98:99], v16 offset:34304\n"
"v_mfma_f32_16x16x32_bf16 v[76:79], a[8:11], a[84:87], v[76:79]\n"
"v_mfma_f32_16x16x32_bf16 v[92:95], a[12:15], a[84:87], v[92:95]\n"
"buffer_load_dwordx4 v10, s[40:43], s73 offen offset:2048 lds\n"
"v_mfma_f32_16x16x32_bf16 v[88:91], a[12:15], a[80:83], v[88:91]\n"
"ds_read_b64_tr_b16 a[100:101], v17 offset:26112\n"
"v_mfma_f32_16x16x32_bf16 v[84:87], a[12:15], a[76:79], v[84:87]\n"
"ds_read_b64_tr_b16 a[102:103], v17 offset:34304\n"
"v_mfma_f32_16x16x32_bf16 v[80:83], a[12:15], a[72:75], v[80:83]\n"
"ds_read2st64_b64 a[40:43], v20 offset0:52 offset1:53\n"
"v_mfma_f32_16x16x32_bf16 v[96:99], a[16:19], a[72:75], v[96:99]\n"
"v_mfma_f32_16x16x32_bf16 v[100:103], a[16:19], a[76:79], v[100:103]\n"
"buffer_load_dwordx4 v11, s[40:43], s73 offen offset:3072 lds\n"
"v_mfma_f32_16x16x32_bf16 v[104:107], a[16:19], a[80:83], v[104:107]\n"
"ds_read2st64_b64 a[44:47], v20 offset0:54 offset1:55\n"
"v_mfma_f32_16x16x32_bf16 v[108:111], a[16:19], a[84:87], v[108:111]\n"
"ds_read2st64_b64 a[48:51], v20 offset0:56 offset1:57\n"
"v_mfma_f32_16x16x32_bf16 v[124:127], a[20:23], a[84:87], v[124:127]\n"
"buffer_store_dwordx4 v[228:231], v1, s[36:39], s82 offen offset:1024 sc1\n"
"v_mfma_f32_16x16x32_bf16 v[120:123], a[20:23], a[80:83], v[120:123]\n"
"s_add_u32 s73, s73, 131072\n"
"s_add_u32 m0, s79, 102400\n"
"v_mfma_f32_16x16x32_bf16 v[116:119], a[20:23], a[76:79], v[116:119]\n"
"buffer_load_dwordx4 v1, s[44:47], s74 offen offset:0 sc1 lds\n"
"v_mfma_f32_16x16x32_bf16 v[112:115], a[20:23], a[72:75], v[112:115]\n"
"ds_read2st64_b64 a[52:55], v20 offset0:58 offset1:59\n"
"ds_read2st64_b64 a[56:59], v20 offset0:60 offset1:61\n"
"v_mfma_f32_16x16x32_bf16 v[128:131], a[24:27], a[72:75], v[128:131]\n"
"ds_read2st64_b64 a[60:63], v20 offset0:62 offset1:63\n"
"v_mfma_f32_16x16x32_bf16 v[132:135], a[24:27], a[76:79], v[132:135]\n"
"ds_read2st64_b64 a[64:67], v20 offset0:64 offset1:65\n"
"v_mfma_f32_16x16x32_bf16 v[136:139], a[24:27], a[80:83], v[136:139]\n"
"v_mfma_f32_16x16x32_bf16 v[140:143], a[24:27], a[84:87], v[140:143]\n"
"buffer_load_dwordx4 v1, s[44:47], s74 offen offset:1024 sc1 lds\n"
"v_mfma_f32_16x16x32_bf16 v[156:159], a[28:31], a[84:87], v[156:159]\n"
"ds_read2st64_b64 a[68:71], v20 offset0:66 offset1:67\n"
"v_mfma_f32_16x16x32_bf16 v[152:155], a[28:31], a[80:83], v[152:155]\n"
"v_mfma_f32_16x16x32_bf16 v[148:151], a[28:31], a[76:79], v[148:151]\n"
"v_mfma_f32_16x16x32_bf16 v[144:147], a[28:31], a[72:75], v[144:147]\n"
"v_mfma_f32_16x16x32_bf16 v[160:163], a[32:35], a[72:75], v[160:163]\n"
"s_mov_b64 exec, 0xffff\n"
"buffer_load_dwordx4 v1, s[44:47], s74 offen offset:2048 sc1 lds\n"
"s_mov_b64 exec, -1\n"
"v_mfma_f32_16x16x32_bf16 v[164:167], a[32:35], a[76:79], v[164:167]\n"
"s_add_u32 s74, s74, 36864\n"
"v_mfma_f32_16x16x32_bf16 v[168:171], a[32:35], a[80:83], v[168:171]\n"
"v_mfma_f32_16x16x32_bf16 v[172:175], a[32:35], a[84:87], v[172:175]\n"
"s_waitcnt lgkmcnt(0)\n"
"v_mfma_f32_16x16x32_bf16 v[32:35], a[36:39], a[88:91], v[32:35]\n"
"s_waitcnt vmcnt(34)\n"
"s_barrier\n"
"v_mfma_f32_16x16x32_bf16 v[36:39], a[36:39], a[92:95], v[36:39]\n"
"s_add_u32 m0, s2, 128000\n"
"ds_read2st64_b64 a[0:3], v18 offset0:0 offset1:1\n"
"v_mfma_f32_16x16x32_bf16 v[40:43], a[36:39], a[96:99], v[40:43]\n"
"buffer_load_dwordx4 v8, s[40:43], s73 offen offset:0 lds\n"
"v_mfma_f32_16x16x32_bf16 v[44:47], a[36:39], a[100:103], v[44:47]\n"
"ds_read_b64_tr_b16 a[72:73], v12 offset:0\n"
"v_mfma_f32_16x16x32_bf16 v[60:63], a[40:43], a[100:103], v[60:63]\n"
"ds_read_b64_tr_b16 a[74:75], v12 offset:8192\n"
"v_mfma_f32_16x16x32_bf16 v[56:59], a[40:43], a[96:99], v[56:59]\n"
"ds_read_b64_tr_b16 a[76:77], v13 offset:0\n"
"v_mfma_f32_16x16x32_bf16 v[52:55], a[40:43], a[92:95], v[52:55]\n"
"buffer_store_dwordx4 v[232:235], v1, s[36:39], s84 offen offset:0 sc1\n"
"v_mfma_f32_16x16x32_bf16 v[48:51], a[40:43], a[88:91], v[48:51]\n"
"buffer_load_dwordx4 v9, s[40:43], s73 offen offset:1024 lds\n"
"v_mfma_f32_16x16x32_bf16 v[64:67], a[44:47], a[88:91], v[64:67]\n"
"ds_read_b64_tr_b16 a[78:79], v13 offset:8192\n"
"v_mfma_f32_16x16x32_bf16 v[68:71], a[44:47], a[92:95], v[68:71]\n"
"ds_read_b64_tr_b16 a[80:81], v12 offset:512\n"
"v_mfma_f32_16x16x32_bf16 v[72:75], a[44:47], a[96:99], v[72:75]\n"
"ds_read_b64_tr_b16 a[82:83], v12 offset:8704\n"
"v_mfma_f32_16x16x32_bf16 v[76:79], a[44:47], a[100:103], v[76:79]\n"
"v_mfma_f32_16x16x32_bf16 v[92:95], a[48:51], a[100:103], v[92:95]\n"
"buffer_load_dwordx4 v10, s[40:43], s73 offen offset:2048 lds\n"
"v_mfma_f32_16x16x32_bf16 v[88:91], a[48:51], a[96:99], v[88:91]\n"
"ds_read_b64_tr_b16 a[84:85], v13 offset:512\n"
"v_mfma_f32_16x16x32_bf16 v[84:87], a[48:51], a[92:95], v[84:87]\n"
"ds_read_b64_tr_b16 a[86:87], v13 offset:8704\n"
"v_mfma_f32_16x16x32_bf16 v[80:83], a[48:51], a[88:91], v[80:83]\n"
"ds_read2st64_b64 a[4:7], v18 offset0:2 offset1:3\n"
"v_mfma_f32_16x16x32_bf16 v[96:99], a[52:55], a[88:91], v[96:99]\n"
"v_mfma_f32_16x16x32_bf16 v[100:103], a[52:55], a[92:95], v[100:103]\n"
"buffer_load_dwordx4 v11, s[40:43], s73 offen offset:3072 lds\n"
"v_mfma_f32_16x16x32_bf16 v[104:107], a[52:55], a[96:99], v[104:107]\n"
"ds_read2st64_b64 a[8:11], v18 offset0:4 offset1:5\n"
"v_mfma_f32_16x16x32_bf16 v[108:111], a[52:55], a[100:103], v[108:111]\n"
"ds_read2st64_b64 a[12:15], v18 offset0:6 offset1:7\n"
"v_mfma_f32_16x16x32_bf16 v[124:127], a[56:59], a[100:103], v[124:127]\n"
"ds_read2st64_b64 a[16:19], v18 offset0:8 offset1:9\n"
"v_mfma_f32_16x16x32_bf16 v[120:123], a[56:59], a[96:99], v[120:123]\n"
"s_add_u32 s73, s73, 131072\n"
"s_add_u32 m0, s79, 128000\n"
"v_mfma_f32_16x16x32_bf16 v[116:119], a[56:59], a[92:95], v[116:119]\n"
"buffer_load_dwordx4 v1, s[44:47], s74 offen offset:0 sc1 lds\n"
"v_mfma_f32_16x16x32_bf16 v[112:115], a[56:59], a[88:91], v[112:115]\n"
"ds_read2st64_b64 a[20:23], v18 offset0:10 offset1:11\n"
"v_mfma_f32_16x16x32_bf16 v[128:131], a[60:63], a[88:91], v[128:131]\n"
"ds_read2st64_b64 a[24:27], v18 offset0:12 offset1:13\n"
"v_mfma_f32_16x16x32_bf16 v[132:135], a[60:63], a[92:95], v[132:135]\n"
"ds_read2st64_b64 a[28:31], v18 offset0:14 offset1:15\n"
"v_mfma_f32_16x16x32_bf16 v[136:139], a[60:63], a[96:99], v[136:139]\n"
"v_mfma_f32_16x16x32_bf16 v[140:143], a[60:63], a[100:103], v[140:143]\n"
"buffer_load_dwordx4 v1, s[44:47], s74 offen offset:1024 sc1 lds\n"
"v_mfma_f32_16x16x32_bf16 v[156:159], a[64:67], a[100:103], v[156:159]\n"
"ds_read2st64_b64 a[32:35], v18 offset0:16 offset1:17\n"
"v_mfma_f32_16x16x32_bf16 v[152:155], a[64:67], a[96:99], v[152:155]\n"
"v_mfma_f32_16x16x32_bf16 v[148:151], a[64:67], a[92:95], v[148:151]\n"
"v_mfma_f32_16x16x32_bf16 v[144:147], a[64:67], a[88:91], v[144:147]\n"
"v_mfma_f32_16x16x32_bf16 v[160:163], a[68:71], a[88:91], v[160:163]\n"
"s_mov_b64 exec, 0xffff\n"
"buffer_load_dwordx4 v1, s[44:47], s74 offen offset:2048 sc1 lds\n"
"s_mov_b64 exec, -1\n"
"v_mfma_f32_16x16x32_bf16 v[164:167], a[68:71], a[92:95], v[164:167]\n"
"s_add_u32 s74, s74, 36864\n"
"v_mfma_f32_16x16x32_bf16 v[168:171], a[68:71], a[96:99], v[168:171]\n"
"v_mfma_f32_16x16x32_bf16 v[172:175], a[68:71], a[100:103], v[172:175]\n"
"s_waitcnt lgkmcnt(0)\n"
"v_mfma_f32_16x16x32_bf16 v[32:35], a[0:3], a[72:75], v[32:35]\n"
"s_waitcnt vmcnt(33)\n"
"s_barrier\n"
"v_mfma_f32_16x16x32_bf16 v[36:39], a[0:3], a[76:79], v[36:39]\n"
"s_add_u32 m0, s2, 0\n"
"ds_read2st64_b64 a[36:39], v18 offset0:50 offset1:51\n"
"v_mfma_f32_16x16x32_bf16 v[40:43], a[0:3], a[80:83], v[40:43]\n"
"buffer_load_dwordx4 v8, s[40:43], s73 offen offset:0 lds\n"
"v_mfma_f32_16x16x32_bf16 v[44:47], a[0:3], a[84:87], v[44:47]\n"
"ds_read_b64_tr_b16 a[88:89], v12 offset:25600\n"
"v_mfma_f32_16x16x32_bf16 v[60:63], a[4:7], a[84:87], v[60:63]\n"
"ds_read_b64_tr_b16 a[90:91], v12 offset:33792\n"
"v_mfma_f32_16x16x32_bf16 v[56:59], a[4:7], a[80:83], v[56:59]\n"
"ds_read_b64_tr_b16 a[92:93], v13 offset:25600\n"
"v_mfma_f32_16x16x32_bf16 v[52:55], a[4:7], a[76:79], v[52:55]\n"
"buffer_store_dwordx4 v[236:239], v1, s[36:39], s84 offen offset:1024 sc1\n"
"v_mfma_f32_16x16x32_bf16 v[48:51], a[4:7], a[72:75], v[48:51]\n"
"buffer_load_dwordx4 v9, s[40:43], s73 offen offset:1024 lds\n"
"v_mfma_f32_16x16x32_bf16 v[64:67], a[8:11], a[72:75], v[64:67]\n"
"ds_read_b64_tr_b16 a[94:95], v13 offset:33792\n"
"v_mfma_f32_16x16x32_bf16 v[68:71], a[8:11], a[76:79], v[68:71]\n"
"ds_read_b64_tr_b16 a[96:97], v12 offset:26112\n"
"v_mfma_f32_16x16x32_bf16 v[72:75], a[8:11], a[80:83], v[72:75]\n"
"ds_read_b64_tr_b16 a[98:99], v12 offset:34304\n"
"v_mfma_f32_16x16x32_bf16 v[76:79], a[8:11], a[84:87], v[76:79]\n"
"v_mfma_f32_16x16x32_bf16 v[92:95], a[12:15], a[84:87], v[92:95]\n"
"buffer_load_dwordx4 v10, s[40:43], s73 offen offset:2048 lds\n"
"v_mfma_f32_16x16x32_bf16 v[88:91], a[12:15], a[80:83], v[88:91]\n"
"ds_read_b64_tr_b16 a[100:101], v13 offset:26112\n"
"v_mfma_f32_16x16x32_bf16 v[84:87], a[12:15], a[76:79], v[84:87]\n"
"ds_read_b64_tr_b16 a[102:103], v13 offset:34304\n"
"v_mfma_f32_16x16x32_bf16 v[80:83], a[12:15], a[72:75], v[80:83]\n"
"ds_read2st64_b64 a[40:43], v18 offset0:52 offset1:53\n"
"v_mfma_f32_16x16x32_bf16 v[96:99], a[16:19], a[72:75], v[96:99]\n"
"v_mfma_f32_16x16x32_bf16 v[100:103], a[16:19], a[76:79], v[100:103]\n"
"buffer_load_dwordx4 v11, s[40:43], s73 offen offset:3072 lds\n"
"v_mfma_f32_16x16x32_bf16 v[104:107], a[16:19], a[80:83], v[104:107]\n"
"ds_read2st64_b64 a[44:47], v18 offset0:54 offset1:55\n"
"v_mfma_f32_16x16x32_bf16 v[108:111], a[16:19], a[84:87], v[108:111]\n"
"ds_read2st64_b64 a[48:51], v18 offset0:56 offset1:57\n"
"v_mfma_f32_16x16x32_bf16 v[124:127], a[20:23], a[84:87], v[124:127]\n"
"ds_read2st64_b64 a[52:55], v18 offset0:58 offset1:59\n"
"v_mfma_f32_16x16x32_bf16 v[120:123], a[20:23], a[80:83], v[120:123]\n"
"s_add_u32 s73, s73, 131072\n"
"s_add_u32 m0, s79, 0\n"
"v_mfma_f32_16x16x32_bf16 v[116:119], a[20:23], a[76:79], v[116:119]\n"
"buffer_load_dwordx4 v1, s[44:47], s74 offen offset:0 sc1 lds\n"
"v_mfma_f32_16x16x32_bf16 v[112:115], a[20:23], a[72:75], v[112:115]\n"
"ds_read2st64_b64 a[56:59], v18 offset0:60 offset1:61\n"
"v_mfma_f32_16x16x32_bf16 v[128:131], a[24:27], a[72:75], v[128:131]\n"
"ds_read2st64_b64 a[60:63], v18 offset0:62 offset1:63\n"
"v_mfma_f32_16x16x32_bf16 v[132:135], a[24:27], a[76:79], v[132:135]\n"
"ds_read2st64_b64 a[64:67], v18 offset0:64 offset1:65\n"
"v_mfma_f32_16x16x32_bf16 v[136:139], a[24:27], a[80:83], v[136:139]\n"
"v_mfma_f32_16x16x32_bf16 v[140:143], a[24:27], a[84:87], v[140:143]\n"
"buffer_load_dwordx4 v1, s[44:47], s74 offen offset:1024 sc1 lds\n"
"v_mfma_f32_16x16x32_bf16 v[156:159], a[28:31], a[84:87], v[156:159]\n"
"ds_read2st64_b64 a[68:71], v18 offset0:66 offset1:67\n"
"v_mfma_f32_16x16x32_bf16 v[152:155], a[28:31], a[80:83], v[152:155]\n"
"v_mfma_f32_16x16x32_bf16 v[148:151], a[28:31], a[76:79], v[148:151]\n"
"v_mfma_f32_16x16x32_bf16 v[144:147], a[28:31], a[72:75], v[144:147]\n"
"v_mfma_f32_16x16x32_bf16 v[160:163], a[32:35], a[72:75], v[160:163]\n"
"s_mov_b64 exec, 0xffff\n"
"buffer_load_dwordx4 v1, s[44:47], s74 offen offset:2048 sc1 lds\n"
"s_mov_b64 exec, -1\n"
"v_mfma_f32_16x16x32_bf16 v[164:167], a[32:35], a[76:79], v[164:167]\n"
"s_add_u32 s74, s74, 36864\n"
"v_mfma_f32_16x16x32_bf16 v[168:171], a[32:35], a[80:83], v[168:171]\n"
"v_mfma_f32_16x16x32_bf16 v[172:175], a[32:35], a[84:87], v[172:175]\n"
"s_waitcnt lgkmcnt(0)\n"
"v_mfma_f32_16x16x32_bf16 v[32:35], a[36:39], a[88:91], v[32:35]\n"
"s_waitcnt vmcnt(33)\n"
"s_barrier\n"
"v_mfma_f32_16x16x32_bf16 v[36:39], a[36:39], a[92:95], v[36:39]\n"
"s_add_u32 m0, s2, 25600\n"
"ds_read2st64_b64 a[0:3], v19 offset0:0 offset1:1\n"
"v_mfma_f32_16x16x32_bf16 v[40:43], a[36:39], a[96:99], v[40:43]\n"
"buffer_load_dwordx4 v8, s[40:43], s73 offen offset:0 lds\n"
"v_mfma_f32_16x16x32_bf16 v[44:47], a[36:39], a[100:103], v[44:47]\n"
"ds_read_b64_tr_b16 a[72:73], v14 offset:0\n"
"v_mfma_f32_16x16x32_bf16 v[60:63], a[40:43], a[100:103], v[60:63]\n"
"ds_read_b64_tr_b16 a[74:75], v14 offset:8192\n"
"v_mfma_f32_16x16x32_bf16 v[56:59], a[40:43], a[96:99], v[56:59]\n"
"ds_read_b64_tr_b16 a[76:77], v15 offset:0\n"
"v_mfma_f32_16x16x32_bf16 v[52:55], a[40:43], a[92:95], v[52:55]\n"
"buffer_store_dwordx4 v[176:179], v1, s[36:39], s82 offen offset:2048 sc1\n"
"v_mfma_f32_16x16x32_bf16 v[48:51], a[40:43], a[88:91], v[48:51]\n"
"buffer_load_dwordx4 v9, s[40:43], s73 offen offset:1024 lds\n"
"v_mfma_f32_16x16x32_bf16 v[64:67], a[44:47], a[88:91], v[64:67]\n"
"ds_read_b64_tr_b16 a[78:79], v15 offset:8192\n"
"v_mfma_f32_16x16x32_bf16 v[68:71], a[44:47], a[92:95], v[68:71]\n"
"ds_read_b64_tr_b16 a[80:81], v14 offset:512\n"
"v_mfma_f32_16x16x32_bf16 v[72:75], a[44:47], a[96:99], v[72:75]\n"
"ds_read_b64_tr_b16 a[82:83], v14 offset:8704\n"
"v_mfma_f32_16x16x32_bf16 v[76:79], a[44:47], a[100:103], v[76:79]\n"
"v_mfma_f32_16x16x32_bf16 v[92:95], a[48:51], a[100:103], v[92:95]\n"
"buffer_load_dwordx4 v10, s[40:43], s73 offen offset:2048 lds\n"
"v_mfma_f32_16x16x32_bf16 v[88:91], a[48:51], a[96:99], v[88:91]\n"
"ds_read_b64_tr_b16 a[84:85], v15 offset:512\n"
"v_mfma_f32_16x16x32_bf16 v[84:87], a[48:51], a[92:95], v[84:87]\n"
"ds_read_b64_tr_b16 a[86:87], v15 offset:8704\n"
"v_mfma_f32_16x16x32_bf16 v[80:83], a[48:51], a[88:91], v[80:83]\n"
"ds_read2st64_b64 a[4:7], v19 offset0:2 offset1:3\n"
"v_mfma_f32_16x16x32_bf16 v[96:99], a[52:55], a[88:91], v[96:99]\n"
"v_mfma_f32_16x16x32_bf16 v[100:103], a[52:55], a[92:95], v[100:103]\n"
"buffer_load_dwordx4 v11, s[40:43], s73 offen offset:3072 lds\n"
"v_mfma_f32_16x16x32_bf16 v[104:107], a[52:55], a[96:99], v[104:107]\n"
"ds_read2st64_b64 a[8:11], v19 offset0:4 offset1:5\n"
"v_mfma_f32_16x16x32_bf16 v[108:111], a[52:55], a[100:103], v[108:111]\n"
"ds_read2st64_b64 a[12:15], v19 offset0:6 offset1:7\n"
"v_mfma_f32_16x16x32_bf16 v[124:127], a[56:59], a[100:103], v[124:127]\n"
"buffer_store_dwordx4 v[180:183], v1, s[36:39], s82 offen offset:3072 sc1\n"
"v_mfma_f32_16x16x32_bf16 v[120:123], a[56:59], a[96:99], v[120:123]\n"
"s_add_u32 s73, s73, 131072\n"
"s_add_u32 m0, s79, 25600\n"
"v_mfma_f32_16x16x32_bf16 v[116:119], a[56:59], a[92:95], v[116:119]\n"
"buffer_load_dwordx4 v1, s[44:47], s74 offen offset:0 sc1 lds\n"
"v_mfma_f32_16x16x32_bf16 v[112:115], a[56:59], a[88:91], v[112:115]\n"
"ds_read2st64_b64 a[16:19], v19 offset0:8 offset1:9\n"
"ds_read2st64_b64 a[20:23], v19 offset0:10 offset1:11\n"
"v_mfma_f32_16x16x32_bf16 v[128:131], a[60:63], a[88:91], v[128:131]\n"
"ds_read2st64_b64 a[24:27], v19 offset0:12 offset1:13\n"
"v_mfma_f32_16x16x32_bf16 v[132:135], a[60:63], a[92:95], v[132:135]\n"
"ds_read2st64_b64 a[28:31], v19 offset0:14 offset1:15\n"
"v_mfma_f32_16x16x32_bf16 v[136:139], a[60:63], a[96:99], v[136:139]\n"
"v_mfma_f32_16x16x32_bf16 v[140:143], a[60:63], a[100:103], v[140:143]\n"
"buffer_load_dwordx4 v1, s[44:47], s74 offen offset:1024 sc1 lds\n"
"v_mfma_f32_16x16x32_bf16 v[156:159], a[64:67], a[100:103], v[156:159]\n"
"ds_read2st64_b64 a[32:35], v19 offset0:16 offset1:17\n"
"v_mfma_f32_16x16x32_bf16 v[152:155], a[64:67], a[96:99], v[152:155]\n"
"v_mfma_f32_16x16x32_bf16 v[148:151], a[64:67], a[92:95], v[148:151]\n"
"v_mfma_f32_16x16x32_bf16 v[144:147], a[64:67], a[88:91], v[144:147]\n"
"v_mfma_f32_16x16x32_bf16 v[160:163], a[68:71], a[88:91], v[160:163]\n"
"s_mov_b64 exec, 0xffff\n"
"buffer_load_dwordx4 v1, s[44:47], s74 offen offset:2048 sc1 lds\n"
"s_mov_b64 exec, -1\n"
"v_mfma_f32_16x16x32_bf16 v[164:167], a[68:71], a[92:95], v[164:167]\n"
"s_add_u32 s74, s74, 36864\n"
"v_mfma_f32_16x16x32_bf16 v[168:171], a[68:71], a[96:99], v[168:171]\n"
"v_mfma_f32_16x16x32_bf16 v[172:175], a[68:71], a[100:103], v[172:175]\n"
"s_waitcnt lgkmcnt(0)\n"
"v_mfma_f32_16x16x32_bf16 v[32:35], a[0:3], a[72:75], v[32:35]\n"
"s_waitcnt vmcnt(34)\n"
"s_barrier\n"
"v_mfma_f32_16x16x32_bf16 v[36:39], a[0:3], a[76:79], v[36:39]\n"
"s_add_u32 m0, s2, 51200\n"
"ds_read2st64_b64 a[36:39], v19 offset0:50 offset1:51\n"
"v_mfma_f32_16x16x32_bf16 v[40:43], a[0:3], a[80:83], v[40:43]\n"
"buffer_load_dwordx4 v8, s[40:43], s73 offen offset:0 lds\n"
"v_mfma_f32_16x16x32_bf16 v[44:47], a[0:3], a[84:87], v[44:47]\n"
"ds_read_b64_tr_b16 a[88:89], v14 offset:25600\n"
"v_mfma_f32_16x16x32_bf16 v[60:63], a[4:7], a[84:87], v[60:63]\n"
"ds_read_b64_tr_b16 a[90:91], v14 offset:33792\n"
"v_mfma_f32_16x16x32_bf16 v[56:59], a[4:7], a[80:83], v[56:59]\n"
"ds_read_b64_tr_b16 a[92:93], v15 offset:25600\n"
"v_mfma_f32_16x16x32_bf16 v[52:55], a[4:7], a[76:79], v[52:55]\n"
"buffer_store_dwordx4 v[184:187], v1, s[36:39], s84 offen offset:2048 sc1\n"
"v_mfma_f32_16x16x32_bf16 v[48:51], a[4:7], a[72:75], v[48:51]\n"
"buffer_load_dwordx4 v9, s[40:43], s73 offen offset:1024 lds\n"
"v_mfma_f32_16x16x32_bf16 v[64:67], a[8:11], a[72:75], v[64:67]\n"
"ds_read_b64_tr_b16 a[94:95], v15 offset:33792\n"
"v_mfma_f32_16x16x32_bf16 v[68:71], a[8:11], a[76:79], v[68:71]\n"
"ds_read_b64_tr_b16 a[96:97], v14 offset:26112\n"
"v_mfma_f32_16x16x32_bf16 v[72:75], a[8:11], a[80:83], v[72:75]\n"
"ds_read_b64_tr_b16 a[98:99], v14 offset:34304\n"
"v_mfma_f32_16x16x32_bf16 v[76:79], a[8:11], a[84:87], v[76:79]\n"
"v_mfma_f32_16x16x32_bf16 v[92:95], a[12:15], a[84:87], v[92:95]\n"
"buffer_load_dwordx4 v10, s[40:43], s73 offen offset:2048 lds\n"
"v_mfma_f32_16x16x32_bf16 v[88:91], a[12:15], a[80:83], v[88:91]\n"
"ds_read_b64_tr_b16 a[100:101], v15 offset:26112\n"
"v_mfma_f32_16x16x32_bf16 v[84:87], a[12:15], a[76:79], v[84:87]\n"
"ds_read_b64_tr_b16 a[102:103], v15 offset:34304\n"
"v_mfma_f32_16x16x32_bf16 v[80:83], a[12:15], a[72:75], v[80:83]\n"
"ds_read2st64_b64 a[40:43], v19 offset0:52 offset1:53\n"
"v_mfma_f32_16x16x32_bf16 v[96:99], a[16:19], a[72:75], v[96:99]\n"
"v_mfma_f32_16x16x32_bf16 v[100:103], a[16:19], a[76:79], v[100:103]\n"
"buffer_load_dwordx4 v11, s[40:43], s73 offen offset:3072 lds\n"
"v_mfma_f32_16x16x32_bf16 v[104:107], a[16:19], a[80:83], v[104:107]\n"
"ds_read2st64_b64 a[44:47], v19 offset0:54 offset1:55\n"
"v_mfma_f32_16x16x32_bf16 v[108:111], a[16:19], a[84:87], v[108:111]\n"
"ds_read2st64_b64 a[48:51], v19 offset0:56 offset1:57\n"
"v_mfma_f32_16x16x32_bf16 v[124:127], a[20:23], a[84:87], v[124:127]\n"
"ds_read2st64_b64 a[52:55], v19 offset0:58 offset1:59\n"
"v_mfma_f32_16x16x32_bf16 v[120:123], a[20:23], a[80:83], v[120:123]\n"
"s_add_u32 s73, s73, 131072\n"
"s_add_u32 m0, s79, 51200\n"
"v_mfma_f32_16x16x32_bf16 v[116:119], a[20:23], a[76:79], v[116:119]\n"
"buffer_load_dwordx4 v1, s[44:47], s74 offen offset:0 sc1 lds\n"
"v_mfma_f32_16x16x32_bf16 v[112:115], a[20:23], a[72:75], v[112:115]\n"
"ds_read2st64_b64 a[56:59], v19 offset0:60 offset1:61\n"
"v_mfma_f32_16x16x32_bf16 v[128:131], a[24:27], a[72:75], v[128:131]\n"
"ds_read2st64_b64 a[60:63], v19 offset0:62 offset1:63\n"
"v_mfma_f32_16x16x32_bf16 v[132:135], a[24:27], a[76:79], v[132:135]\n"
"ds_read2st64_b64 a[64:67], v19 offset0:64 offset1:65\n"
"v_mfma_f32_16x16x32_bf16 v[136:139], a[24:27], a[80:83], v[136:139]\n"
"v_mfma_f32_16x16x32_bf16 v[140:143], a[24:27], a[84:87], v[140:143]\n"
"buffer_load_dwordx4 v1, s[44:47], s74 offen offset:1024 sc1 lds\n"
"v_mfma_f32_16x16x32_bf16 v[156:159], a[28:31], a[84:87], v[156:159]\n"
"ds_read2st64_b64 a[68:71], v19 offset0:66 offset1:67\n"
"v_mfma_f32_16x16x32_bf16 v[152:155], a[28:31], a[80:83], v[152:155]\n"
"v_mfma_f32_16x16x32_bf16 v[148:151], a[28:31], a[76:79], v[148:151]\n"
"v_mfma_f32_16x16x32_bf16 v[144:147], a[28:31], a[72:75], v[144:147]\n"
"v_mfma_f32_16x16x32_bf16 v[160:163], a[32:35], a[72:75], v[160:163]\n"
"s_mov_b64 exec, 0xffff\n"
"buffer_load_dwordx4 v1, s[44:47], s74 offen offset:2048 sc1 lds\n"
"s_mov_b64 exec, -1\n"
"v_mfma_f32_16x16x32_bf16 v[164:167], a[32:35], a[76:79], v[164:167]\n"
"s_add_u32 s74, s74, 36864\n"
"v_mfma_f32_16x16x32_bf16 v[168:171], a[32:35], a[80:83], v[168:171]\n"
"v_mfma_f32_16x16x32_bf16 v[172:175], a[32:35], a[84:87], v[172:175]\n"
"s_waitcnt lgkmcnt(0)\n"
"v_mfma_f32_16x16x32_bf16 v[32:35], a[36:39], a[88:91], v[32:35]\n"
"s_waitcnt vmcnt(33)\n"
"s_barrier\n"
"v_mfma_f32_16x16x32_bf16 v[36:39], a[36:39], a[92:95], v[36:39]\n"
"s_add_u32 m0, s2, 76800\n"
"ds_read2st64_b64 a[0:3], v20 offset0:0 offset1:1\n"
"v_mfma_f32_16x16x32_bf16 v[40:43], a[36:39], a[96:99], v[40:43]\n"
"buffer_load_dwordx4 v8, s[40:43], s73 offen offset:0 lds\n"
"v_mfma_f32_16x16x32_bf16 v[44:47], a[36:39], a[100:103], v[44:47]\n"
"ds_read_b64_tr_b16 a[72:73], v16 offset:0\n"
"v_mfma_f32_16x16x32_bf16 v[60:63], a[40:43], a[100:103], v[60:63]\n"
"ds_read_b64_tr_b16 a[74:75], v16 offset:8192\n"
"v_mfma_f32_16x16x32_bf16 v[56:59], a[40:43], a[96:99], v[56:59]\n"
"ds_read_b64_tr_b16 a[76:77], v17 offset:0\n"
"v_mfma_f32_16x16x32_bf16 v[52:55], a[40:43], a[92:95], v[52:55]\n"
"buffer_store_dwordx4 v[188:191], v1, s[36:39], s84 offen offset:3072 sc1\n"
"v_mfma_f32_16x16x32_bf16 v[48:51], a[40:43], a[88:91], v[48:51]\n"
"buffer_load_dwordx4 v9, s[40:43], s73 offen offset:1024 lds\n"
"v_mfma_f32_16x16x32_bf16 v[64:67], a[44:47], a[88:91], v[64:67]\n"
"ds_read_b64_tr_b16 a[78:79], v17 offset:8192\n"
"v_mfma_f32_16x16x32_bf16 v[68:71], a[44:47], a[92:95], v[68:71]\n"
"ds_read_b64_tr_b16 a[80:81], v16 offset:512\n"
"v_mfma_f32_16x16x32_bf16 v[72:75], a[44:47], a[96:99], v[72:75]\n"
"ds_read_b64_tr_b16 a[82:83], v16 offset:8704\n"
"v_mfma_f32_16x16x32_bf16 v[76:79], a[44:47], a[100:103], v[76:79]\n"
"v_mfma_f32_16x16x32_bf16 v[92:95], a[48:51], a[100:103], v[92:95]\n"
"buffer_load_dwordx4 v10, s[40:43], s73 offen offset:2048 lds\n"
"v_mfma_f32_16x16x32_bf16 v[88:91], a[48:51], a[96:99], v[88:91]\n"
"ds_read_b64_tr_b16 a[84:85], v17 offset:512\n"
"v_mfma_f32_16x16x32_bf16 v[84:87], a[48:51], a[92:95], v[84:87]\n"
"ds_read_b64_tr_b16 a[86:87], v17 offset:8704\n"
"v_mfma_f32_16x16x32_bf16 v[80:83], a[48:51], a[88:91], v[80:83]\n"
"ds_read2st64_b64 a[4:7], v20 offset0:2 offset1:3\n"
"v_mfma_f32_16x16x32_bf16 v[96:99], a[52:55], a[88:91], v[96:99]\n"
"v_mfma_f32_16x16x32_bf16 v[100:103], a[52:55], a[92:95], v[100:103]\n"
"buffer_load_dwordx4 v11, s[40:43], s73 offen offset:3072 lds\n"
"v_mfma_f32_16x16x32_bf16 v[104:107], a[52:55], a[96:99], v[104:107]\n"
"ds_read2st64_b64 a[8:11], v20 offset0:4 offset1:5\n"
"v_mfma_f32_16x16x32_bf16 v[108:111], a[52:55], a[100:103], v[108:111]\n"
"ds_read2st64_b64 a[12:15], v20 offset0:6 offset1:7\n"
"v_mfma_f32_16x16x32_bf16 v[124:127], a[56:59], a[100:103], v[124:127]\n"
"ds_read2st64_b64 a[16:19], v20 offset0:8 offset1:9\n"
"v_mfma_f32_16x16x32_bf16 v[120:123], a[56:59], a[96:99], v[120:123]\n"
"s_add_u32 s73, s73, 131072\n"
"s_add_u32 m0, s79, 76800\n"
"v_mfma_f32_16x16x32_bf16 v[116:119], a[56:59], a[92:95], v[116:119]\n"
"buffer_load_dwordx4 v1, s[44:47], s74 offen offset:0 sc1 lds\n"
"v_mfma_f32_16x16x32_bf16 v[112:115], a[56:59], a[88:91], v[112:115]\n"
"ds_read2st64_b64 a[20:23], v20 offset0:10 offset1:11\n"
"v_mfma_f32_16x16x32_bf16 v[128:131], a[60:63], a[88:91], v[128:131]\n"
"ds_read2st64_b64 a[24:27], v20 offset0:12 offset1:13\n"
"v_mfma_f32_16x16x32_bf16 v[132:135], a[60:63], a[92:95], v[132:135]\n"
"ds_read2st64_b64 a[28:31], v20 offset0:14 offset1:15\n"
"v_mfma_f32_16x16x32_bf16 v[136:139], a[60:63], a[96:99], v[136:139]\n"
"v_mfma_f32_16x16x32_bf16 v[140:143], a[60:63], a[100:103], v[140:143]\n"
"buffer_load_dwordx4 v1, s[44:47], s74 offen offset:1024 sc1 lds\n"
"v_mfma_f32_16x16x32_bf16 v[156:159], a[64:67], a[100:103], v[156:159]\n"
"ds_read2st64_b64 a[32:35], v20 offset0:16 offset1:17\n"
"v_mfma_f32_16x16x32_bf16 v[152:155], a[64:67], a[96:99], v[152:155]\n"
"v_mfma_f32_16x16x32_bf16 v[148:151], a[64:67], a[92:95], v[148:151]\n"
"v_mfma_f32_16x16x32_bf16 v[144:147], a[64:67], a[88:91], v[144:147]\n"
"v_mfma_f32_16x16x32_bf16 v[160:163], a[68:71], a[88:91], v[160:163]\n"
"s_mov_b64 exec, 0xffff\n"
"buffer_load_dwordx4 v1, s[44:47], s74 offen offset:2048 sc1 lds\n"
"s_mov_b64 exec, -1\n"
"v_mfma_f32_16x16x32_bf16 v[164:167], a[68:71], a[92:95], v[164:167]\n"
"s_add_u32 s74, s74, 36864\n"
"v_mfma_f32_16x16x32_bf16 v[168:171], a[68:71], a[96:99], v[168:171]\n"
"v_mfma_f32_16x16x32_bf16 v[172:175], a[68:71], a[100:103], v[172:175]\n"
"s_waitcnt lgkmcnt(0)\n"
"v_mfma_f32_16x16x32_bf16 v[32:35], a[0:3], a[72:75], v[32:35]\n"
"s_waitcnt vmcnt(33)\n"
"s_barrier\n"
"v_mfma_f32_16x16x32_bf16 v[36:39], a[0:3], a[76:79], v[36:39]\n"
"s_add_u32 m0, s2, 102400\n"
"ds_read2st64_b64 a[36:39], v20 offset0:50 offset1:51\n"
"v_mfma_f32_16x16x32_bf16 v[40:43], a[0:3], a[80:83], v[40:43]\n"
"buffer_load_dwordx4 v8, s[40:43], s73 offen offset:0 lds\n"
"v_mfma_f32_16x16x32_bf16 v[44:47], a[0:3], a[84:87], v[44:47]\n"
"ds_read_b64_tr_b16 a[88:89], v16 offset:25600\n"
"v_mfma_f32_16x16x32_bf16 v[60:63], a[4:7], a[84:87], v[60:63]\n"
"ds_read_b64_tr_b16 a[90:91], v16 offset:33792\n"
"v_mfma_f32_16x16x32_bf16 v[56:59], a[4:7], a[80:83], v[56:59]\n"
"ds_read_b64_tr_b16 a[92:93], v17 offset:25600\n"
"v_mfma_f32_16x16x32_bf16 v[52:55], a[4:7], a[76:79], v[52:55]\n"
"v_mfma_f32_16x16x32_bf16 v[48:51], a[4:7], a[72:75], v[48:51]\n"
"buffer_load_dwordx4 v9, s[40:43], s73 offen offset:1024 lds\n"
"v_mfma_f32_16x16x32_bf16 v[64:67], a[8:11], a[72:75], v[64:67]\n"
"ds_read_b64_tr_b16 a[94:95], v17 offset:33792\n"
"v_mfma_f32_16x16x32_bf16 v[68:71], a[8:11], a[76:79], v[68:71]\n"
"ds_read_b64_tr_b16 a[96:97], v16 offset:26112\n"
"v_mfma_f32_16x16x32_bf16 v[72:75], a[8:11], a[80:83], v[72:75]\n"
"ds_read_b64_tr_b16 a[98:99], v16 offset:34304\n"
"v_mfma_f32_16x16x32_bf16 v[76:79], a[8:11], a[84:87], v[76:79]\n"
"v_mfma_f32_16x16x32_bf16 v[92:95], a[12:15], a[84:87], v[92:95]\n"
"buffer_load_dwordx4 v10, s[40:43], s73 offen offset:2048 lds\n"
"v_mfma_f32_16x16x32_bf16 v[88:91], a[12:15], a[80:83], v[88:91]\n"
"ds_read_b64_tr_b16 a[100:101], v17 offset:26112\n"
"v_mfma_f32_16x16x32_bf16 v[84:87], a[12:15], a[76:79], v[84:87]\n"
"ds_read_b64_tr_b16 a[102:103], v17 offset:34304\n"
"v_mfma_f32_16x16x32_bf16 v[80:83], a[12:15], a[72:75], v[80:83]\n"
"ds_read2st64_b64 a[40:43], v20 offset0:52 offset1:53\n"
"v_mfma_f32_16x16x32_bf16 v[96:99], a[16:19], a[72:75], v[96:99]\n"
"v_mfma_f32_16x16x32_bf16 v[100:103], a[16:19], a[76:79], v[100:103]\n"
"buffer_load_dwordx4 v11, s[40:43], s73 offen offset:3072 lds\n"
"v_mfma_f32_16x16x32_bf16 v[104:107], a[16:19], a[80:83], v[104:107]\n"
"ds_read2st64_b64 a[44:47], v20 offset0:54 offset1:55\n"
"v_mfma_f32_16x16x32_bf16 v[108:111], a[16:19], a[84:87], v[108:111]\n"
"ds_read2st64_b64 a[48:51], v20 offset0:56 offset1:57\n"
"v_mfma_f32_16x16x32_bf16 v[124:127], a[20:23], a[84:87], v[124:127]\n"
"ds_read2st64_b64 a[52:55], v20 offset0:58 offset1:59\n"
"v_mfma_f32_16x16x32_bf16 v[120:123], a[20:23], a[80:83], v[120:123]\n"
"s_add_u32 s73, s73, 131072\n"
"s_add_u32 m0, s79, 102400\n"
"v_mfma_f32_16x16x32_bf16 v[116:119], a[20:23], a[76:79], v[116:119]\n"
"buffer_load_dwordx4 v1, s[44:47], s74 offen offset:0 sc1 lds\n"
"v_mfma_f32_16x16x32_bf16 v[112:115], a[20:23], a[72:75], v[112:115]\n"
"ds_read2st64_b64 a[56:59], v20 offset0:60 offset1:61\n"
"v_mfma_f32_16x16x32_bf16 v[128:131], a[24:27], a[72:75], v[128:131]\n"
"ds_read2st64_b64 a[60:63], v20 offset0:62 offset1:63\n"
"v_mfma_f32_16x16x32_bf16 v[132:135], a[24:27], a[76:79], v[132:135]\n"
"ds_read2st64_b64 a[64:67], v20 offset0:64 offset1:65\n"
"v_mfma_f32_16x16x32_bf16 v[136:139], a[24:27], a[80:83], v[136:139]\n"
"v_mfma_f32_16x16x32_bf16 v[140:143], a[24:27], a[84:87], v[140:143]\n"
"buffer_load_dwordx4 v1, s[44:47], s74 offen offset:1024 sc1 lds\n"
"v_mfma_f32_16x16x32_bf16 v[156:159], a[28:31], a[84:87], v[156:159]\n"
"ds_read2st64_b64 a[68:71], v20 offset0:66 offset1:67\n"
"v_mfma_f32_16x16x32_bf16 v[152:155], a[28:31], a[80:83], v[152:155]\n"
"v_mfma_f32_16x16x32_bf16 v[148:151], a[28:31], a[76:79], v[148:151]\n"
"v_mfma_f32_16x16x32_bf16 v[144:147], a[28:31], a[72:75], v[144:147]\n"
"v_mfma_f32_16x16x32_bf16 v[160:163], a[32:35], a[72:75], v[160:163]\n"
"s_mov_b64 exec, 0xffff\n"
"buffer_load_dwordx4 v1, s[44:47], s74 offen offset:2048 sc1 lds\n"
"s_mov_b64 exec, -1\n"
"v_mfma_f32_16x16x32_bf16 v[164:167], a[32:35], a[76:79], v[164:167]\n"
"s_add_u32 s74, s74, 36864\n"
"v_mfma_f32_16x16x32_bf16 v[168:171], a[32:35], a[80:83], v[168:171]\n"
"v_mfma_f32_16x16x32_bf16 v[172:175], a[32:35], a[84:87], v[172:175]\n"
"s_waitcnt lgkmcnt(0)\n"
"v_mfma_f32_16x16x32_bf16 v[32:35], a[36:39], a[88:91], v[32:35]\n"
"s_waitcnt vmcnt(32)\n"
"s_barrier\n"
"v_mfma_f32_16x16x32_bf16 v[36:39], a[36:39], a[92:95], v[36:39]\n"
"s_add_u32 m0, s2, 128000\n"
"ds_read2st64_b64 a[0:3], v18 offset0:0 offset1:1\n"
"v_mfma_f32_16x16x32_bf16 v[40:43], a[36:39], a[96:99], v[40:43]\n"
"buffer_load_dwordx4 v8, s[40:43], s73 offen offset:0 lds\n"
"v_mfma_f32_16x16x32_bf16 v[44:47], a[36:39], a[100:103], v[44:47]\n"
"ds_read_b64_tr_b16 a[72:73], v12 offset:0\n"
"v_mfma_f32_16x16x32_bf16 v[60:63], a[40:43], a[100:103], v[60:63]\n"
"ds_read_b64_tr_b16 a[74:75], v12 offset:8192\n"
"v_mfma_f32_16x16x32_bf16 v[56:59], a[40:43], a[96:99], v[56:59]\n"
"ds_read_b64_tr_b16 a[76:77], v13 offset:0\n"
"v_mfma_f32_16x16x32_bf16 v[52:55], a[40:43], a[92:95], v[52:55]\n"
"v_mfma_f32_16x16x32_bf16 v[48:51], a[40:43], a[88:91], v[48:51]\n"
"buffer_load_dwordx4 v9, s[40:43], s73 offen offset:1024 lds\n"
"v_mfma_f32_16x16x32_bf16 v[64:67], a[44:47], a[88:91], v[64:67]\n"
"ds_read_b64_tr_b16 a[78:79], v13 offset:8192\n"
"v_mfma_f32_16x16x32_bf16 v[68:71], a[44:47], a[92:95], v[68:71]\n"
"ds_read_b64_tr_b16 a[80:81], v12 offset:512\n"
"v_mfma_f32_16x16x32_bf16 v[72:75], a[44:47], a[96:99], v[72:75]\n"
"ds_read_b64_tr_b16 a[82:83], v12 offset:8704\n"
"v_mfma_f32_16x16x32_bf16 v[76:79], a[44:47], a[100:103], v[76:79]\n"
"v_mfma_f32_16x16x32_bf16 v[92:95], a[48:51], a[100:103], v[92:95]\n"
"buffer_load_dwordx4 v10, s[40:43], s73 offen offset:2048 lds\n"
"v_mfma_f32_16x16x32_bf16 v[88:91], a[48:51], a[96:99], v[88:91]\n"
"ds_read_b64_tr_b16 a[84:85], v13 offset:512\n"
"v_mfma_f32_16x16x32_bf16 v[84:87], a[48:51], a[92:95], v[84:87]\n"
"ds_read_b64_tr_b16 a[86:87], v13 offset:8704\n"
"v_mfma_f32_16x16x32_bf16 v[80:83], a[48:51], a[88:91], v[80:83]\n"
"ds_read2st64_b64 a[4:7], v18 offset0:2 offset1:3\n"
"v_mfma_f32_16x16x32_bf16 v[96:99], a[52:55], a[88:91], v[96:99]\n"
"v_mfma_f32_16x16x32_bf16 v[100:103], a[52:55], a[92:95], v[100:103]\n"
"buffer_load_dwordx4 v11, s[40:43], s73 offen offset:3072 lds\n"
"v_mfma_f32_16x16x32_bf16 v[104:107], a[52:55], a[96:99], v[104:107]\n"
"ds_read2st64_b64 a[8:11], v18 offset0:4 offset1:5\n"
"v_mfma_f32_16x16x32_bf16 v[108:111], a[52:55], a[100:103], v[108:111]\n"
"ds_read2st64_b64 a[12:15], v18 offset0:6 offset1:7\n"
"v_mfma_f32_16x16x32_bf16 v[124:127], a[56:59], a[100:103], v[124:127]\n"
"ds_read2st64_b64 a[16:19], v18 offset0:8 offset1:9\n"
"v_mfma_f32_16x16x32_bf16 v[120:123], a[56:59], a[96:99], v[120:123]\n"
"s_add_u32 s73, s73, 131072\n"
"s_add_u32 m0, s79, 128000\n"
"v_mfma_f32_16x16x32_bf16 v[116:119], a[56:59], a[92:95], v[116:119]\n"
"buffer_load_dwordx4 v1, s[44:47], s74 offen offset:0 sc1 lds\n"
"v_mfma_f32_16x16x32_bf16 v[112:115], a[56:59], a[88:91], v[112:115]\n"
"ds_read2st64_b64 a[20:23], v18 offset0:10 offset1:11\n"
"v_mfma_f32_16x16x32_bf16 v[128:131], a[60:63], a[88:91], v[128:131]\n"
"ds_read2st64_b64 a[24:27], v18 offset0:12 offset1:13\n"
"v_mfma_f32_16x16x32_bf16 v[132:135], a[60:63], a[92:95], v[132:135]\n"
"ds_read2st64_b64 a[28:31], v18 offset0:14 offset1:15\n"
"v_mfma_f32_16x16x32_bf16 v[136:139], a[60:63], a[96:99], v[136:139]\n"
"v_mfma_f32_16x16x32_bf16 v[140:143], a[60:63], a[100:103], v[140:143]\n"
"buffer_load_dwordx4 v1, s[44:47], s74 offen offset:1024 sc1 lds\n"
"v_mfma_f32_16x16x32_bf16 v[156:159], a[64:67], a[100:103], v[156:159]\n"
"ds_read2st64_b64 a[32:35], v18 offset0:16 offset1:17\n"
"v_mfma_f32_16x16x32_bf16 v[152:155], a[64:67], a[96:99], v[152:155]\n"
"v_mfma_f32_16x16x32_bf16 v[148:151], a[64:67], a[92:95], v[148:151]\n"
"v_mfma_f32_16x16x32_bf16 v[144:147], a[64:67], a[88:91], v[144:147]\n"
"v_mfma_f32_16x16x32_bf16 v[160:163], a[68:71], a[88:91], v[160:163]\n"
"s_mov_b64 exec, 0xffff\n"
"buffer_load_dwordx4 v1, s[44:47], s74 offen offset:2048 sc1 lds\n"
"s_mov_b64 exec, -1\n"
"v_mfma_f32_16x16x32_bf16 v[164:167], a[68:71], a[92:95], v[164:167]\n"
"s_add_u32 s74, s74, 36864\n"
"v_mfma_f32_16x16x32_bf16 v[168:171], a[68:71], a[96:99], v[168:171]\n"
"v_mfma_f32_16x16x32_bf16 v[172:175], a[68:71], a[100:103], v[172:175]\n"
"s_waitcnt lgkmcnt(0)\n"
"v_mfma_f32_16x16x32_bf16 v[32:35], a[0:3], a[72:75], v[32:35]\n"
"s_waitcnt vmcnt(30)\n"
"s_barrier\n"
"v_mfma_f32_16x16x32_bf16 v[36:39], a[0:3], a[76:79], v[36:39]\n"
"s_add_u32 m0, s2, 0\n"
"ds_read2st64_b64 a[36:39], v18 offset0:50 offset1:51\n"
"v_mfma_f32_16x16x32_bf16 v[40:43], a[0:3], a[80:83], v[40:43]\n"
"buffer_load_dwordx4 v8, s[40:43], s73 offen offset:0 lds\n"
"v_mfma_f32_16x16x32_bf16 v[44:47], a[0:3], a[84:87], v[44:47]\n"
"ds_read_b64_tr_b16 a[88:89], v12 offset:25600\n"
"v_mfma_f32_16x16x32_bf16 v[60:63], a[4:7], a[84:87], v[60:63]\n"
"ds_read_b64_tr_b16 a[90:91], v12 offset:33792\n"
"v_mfma_f32_16x16x32_bf16 v[56:59], a[4:7], a[80:83], v[56:59]\n"
"ds_read_b64_tr_b16 a[92:93], v13 offset:25600\n"
"v_mfma_f32_16x16x32_bf16 v[52:55], a[4:7], a[76:79], v[52:55]\n"
"v_mfma_f32_16x16x32_bf16 v[48:51], a[4:7], a[72:75], v[48:51]\n"
"buffer_load_dwordx4 v9, s[40:43], s73 offen offset:1024 lds\n"
"v_mfma_f32_16x16x32_bf16 v[64:67], a[8:11], a[72:75], v[64:67]\n"
"ds_read_b64_tr_b16 a[94:95], v13 offset:33792\n"
"v_mfma_f32_16x16x32_bf16 v[68:71], a[8:11], a[76:79], v[68:71]\n"
"ds_read_b64_tr_b16 a[96:97], v12 offset:26112\n"
"v_mfma_f32_16x16x32_bf16 v[72:75], a[8:11], a[80:83], v[72:75]\n"
"ds_read_b64_tr_b16 a[98:99], v12 offset:34304\n"
"v_mfma_f32_16x16x32_bf16 v[76:79], a[8:11], a[84:87], v[76:79]\n"
"v_mfma_f32_16x16x32_bf16 v[92:95], a[12:15], a[84:87], v[92:95]\n"
"buffer_load_dwordx4 v10, s[40:43], s73 offen offset:2048 lds\n"
"v_mfma_f32_16x16x32_bf16 v[88:91], a[12:15], a[80:83], v[88:91]\n"
"ds_read_b64_tr_b16 a[100:101], v13 offset:26112\n"
"v_mfma_f32_16x16x32_bf16 v[84:87], a[12:15], a[76:79], v[84:87]\n"
"ds_read_b64_tr_b16 a[102:103], v13 offset:34304\n"
"v_mfma_f32_16x16x32_bf16 v[80:83], a[12:15], a[72:75], v[80:83]\n"
"ds_read2st64_b64 a[40:43], v18 offset0:52 offset1:53\n"
"v_mfma_f32_16x16x32_bf16 v[96:99], a[16:19], a[72:75], v[96:99]\n"
"v_mfma_f32_16x16x32_bf16 v[100:103], a[16:19], a[76:79], v[100:103]\n"
"buffer_load_dwordx4 v11, s[40:43], s73 offen offset:3072 lds\n"
"v_mfma_f32_16x16x32_bf16 v[104:107], a[16:19], a[80:83], v[104:107]\n"
"ds_read2st64_b64 a[44:47], v18 offset0:54 offset1:55\n"
"v_mfma_f32_16x16x32_bf16 v[108:111], a[16:19], a[84:87], v[108:111]\n"
"ds_read2st64_b64 a[48:51], v18 offset0:56 offset1:57\n"
"v_mfma_f32_16x16x32_bf16 v[124:127], a[20:23], a[84:87], v[124:127]\n"
"ds_read2st64_b64 a[52:55], v18 offset0:58 offset1:59\n"
"v_mfma_f32_16x16x32_bf16 v[120:123], a[20:23], a[80:83], v[120:123]\n"
"s_add_u32 s73, s73, 131072\n"
"s_add_u32 m0, s79, 0\n"
"v_mfma_f32_16x16x32_bf16 v[116:119], a[20:23], a[76:79], v[116:119]\n"
"buffer_load_dwordx4 v1, s[44:47], s74 offen offset:0 sc1 lds\n"
"v_mfma_f32_16x16x32_bf16 v[112:115], a[20:23], a[72:75], v[112:115]\n"
"ds_read2st64_b64 a[56:59], v18 offset0:60 offset1:61\n"
"v_mfma_f32_16x16x32_bf16 v[128:131], a[24:27], a[72:75], v[128:131]\n"
"ds_read2st64_b64 a[60:63], v18 offset0:62 offset1:63\n"
"v_mfma_f32_16x16x32_bf16 v[132:135], a[24:27], a[76:79], v[132:135]\n"
"ds_read2st64_b64 a[64:67], v18 offset0:64 offset1:65\n"
"v_mfma_f32_16x16x32_bf16 v[136:139], a[24:27], a[80:83], v[136:139]\n"
"v_mfma_f32_16x16x32_bf16 v[140:143], a[24:27], a[84:87], v[140:143]\n"
"buffer_load_dwordx4 v1, s[44:47], s74 offen offset:1024 sc1 lds\n"
"v_mfma_f32_16x16x32_bf16 v[156:159], a[28:31], a[84:87], v[156:159]\n"
"ds_read2st64_b64 a[68:71], v18 offset0:66 offset1:67\n"
"v_mfma_f32_16x16x32_bf16 v[152:155], a[28:31], a[80:83], v[152:155]\n"
"v_mfma_f32_16x16x32_bf16 v[148:151], a[28:31], a[76:79], v[148:151]\n"
"v_mfma_f32_16x16x32_bf16 v[144:147], a[28:31], a[72:75], v[144:147]\n"
"v_mfma_f32_16x16x32_bf16 v[160:163], a[32:35], a[72:75], v[160:163]\n"
"s_mov_b64 exec, 0xffff\n"
"buffer_load_dwordx4 v1, s[44:47], s74 offen offset:2048 sc1 lds\n"
"s_mov_b64 exec, -1\n"
"v_mfma_f32_16x16x32_bf16 v[164:167], a[32:35], a[76:79], v[164:167]\n"
"s_add_u32 s74, s74, 36864\n"
"v_mfma_f32_16x16x32_bf16 v[168:171], a[32:35], a[80:83], v[168:171]\n"
"v_mfma_f32_16x16x32_bf16 v[172:175], a[32:35], a[84:87], v[172:175]\n"
"s_waitcnt lgkmcnt(0)\n"
"v_mfma_f32_16x16x32_bf16 v[32:35], a[36:39], a[88:91], v[32:35]\n"
"s_waitcnt vmcnt(29)\n"
"s_barrier\n"
"v_mfma_f32_16x16x32_bf16 v[36:39], a[36:39], a[92:95], v[36:39]\n"
"s_add_u32 m0, s2, 25600\n"
"ds_read2st64_b64 a[0:3], v19 offset0:0 offset1:1\n"
"v_mfma_f32_16x16x32_bf16 v[40:43], a[36:39], a[96:99], v[40:43]\n"
"buffer_load_dwordx4 v8, s[40:43], s73 offen offset:0 lds\n"
"v_mfma_f32_16x16x32_bf16 v[44:47], a[36:39], a[100:103], v[44:47]\n"
"ds_read_b64_tr_b16 a[72:73], v14 offset:0\n"
"v_mfma_f32_16x16x32_bf16 v[60:63], a[40:43], a[100:103], v[60:63]\n"
"ds_read_b64_tr_b16 a[74:75], v14 offset:8192\n"
"v_mfma_f32_16x16x32_bf16 v[56:59], a[40:43], a[96:99], v[56:59]\n"
"ds_read_b64_tr_b16 a[76:77], v15 offset:0\n"
"v_mfma_f32_16x16x32_bf16 v[52:55], a[40:43], a[92:95], v[52:55]\n"
"v_mfma_f32_16x16x32_bf16 v[48:51], a[40:43], a[88:91], v[48:51]\n"
"buffer_load_dwordx4 v9, s[40:43], s73 offen offset:1024 lds\n"
"v_mfma_f32_16x16x32_bf16 v[64:67], a[44:47], a[88:91], v[64:67]\n"
"ds_read_b64_tr_b16 a[78:79], v15 offset:8192\n"
"v_mfma_f32_16x16x32_bf16 v[68:71], a[44:47], a[92:95], v[68:71]\n"
"ds_read_b64_tr_b16 a[80:81], v14 offset:512\n"
"v_mfma_f32_16x16x32_bf16 v[72:75], a[44:47], a[96:99], v[72:75]\n"
"ds_read_b64_tr_b16 a[82:83], v14 offset:8704\n"
"v_mfma_f32_16x16x32_bf16 v[76:79], a[44:47], a[100:103], v[76:79]\n"
"v_mfma_f32_16x16x32_bf16 v[92:95], a[48:51], a[100:103], v[92:95]\n"
"buffer_load_dwordx4 v10, s[40:43], s73 offen offset:2048 lds\n"
"v_mfma_f32_16x16x32_bf16 v[88:91], a[48:51], a[96:99], v[88:91]\n"
"ds_read_b64_tr_b16 a[84:85], v15 offset:512\n"
"v_mfma_f32_16x16x32_bf16 v[84:87], a[48:51], a[92:95], v[84:87]\n"
"ds_read_b64_tr_b16 a[86:87], v15 offset:8704\n"
"v_mfma_f32_16x16x32_bf16 v[80:83], a[48:51], a[88:91], v[80:83]\n"
"ds_read2st64_b64 a[4:7], v19 offset0:2 offset1:3\n"
"v_mfma_f32_16x16x32_bf16 v[96:99], a[52:55], a[88:91], v[96:99]\n"
"v_mfma_f32_16x16x32_bf16 v[100:103], a[52:55], a[92:95], v[100:103]\n"
"buffer_load_dwordx4 v11, s[40:43], s73 offen offset:3072 lds\n"
"v_mfma_f32_16x16x32_bf16 v[104:107], a[52:55], a[96:99], v[104:107]\n"
"ds_read2st64_b64 a[8:11], v19 offset0:4 offset1:5\n"
"v_mfma_f32_16x16x32_bf16 v[108:111], a[52:55], a[100:103], v[108:111]\n"
"ds_read2st64_b64 a[12:15], v19 offset0:6 offset1:7\n"
"v_mfma_f32_16x16x32_bf16 v[124:127], a[56:59], a[100:103], v[124:127]\n"
"ds_read2st64_b64 a[16:19], v19 offset0:8 offset1:9\n"
"v_mfma_f32_16x16x32_bf16 v[120:123], a[56:59], a[96:99], v[120:123]\n"
"s_add_u32 s73, s73, 131072\n"
"s_add_u32 m0, s79, 25600\n"
"v_mfma_f32_16x16x32_bf16 v[116:119], a[56:59], a[92:95], v[116:119]\n"
"buffer_load_dwordx4 v1, s[44:47], s74 offen offset:0 sc1 lds\n"
"v_mfma_f32_16x16x32_bf16 v[112:115], a[56:59], a[88:91], v[112:115]\n"
"ds_read2st64_b64 a[20:23], v19 offset0:10 offset1:11\n"
"v_mfma_f32_16x16x32_bf16 v[128:131], a[60:63], a[88:91], v[128:131]\n"
"ds_read2st64_b64 a[24:27], v19 offset0:12 offset1:13\n"
"v_mfma_f32_16x16x32_bf16 v[132:135], a[60:63], a[92:95], v[132:135]\n"
"ds_read2st64_b64 a[28:31], v19 offset0:14 offset1:15\n"
"v_mfma_f32_16x16x32_bf16 v[136:139], a[60:63], a[96:99], v[136:139]\n"
"v_mfma_f32_16x16x32_bf16 v[140:143], a[60:63], a[100:103], v[140:143]\n"
"buffer_load_dwordx4 v1, s[44:47], s74 offen offset:1024 sc1 lds\n"
"v_mfma_f32_16x16x32_bf16 v[156:159], a[64:67], a[100:103], v[156:159]\n"
"ds_read2st64_b64 a[32:35], v19 offset0:16 offset1:17\n"
"v_mfma_f32_16x16x32_bf16 v[152:155], a[64:67], a[96:99], v[152:155]\n"
"v_mfma_f32_16x16x32_bf16 v[148:151], a[64:67], a[92:95], v[148:151]\n"
"v_mfma_f32_16x16x32_bf16 v[144:147], a[64:67], a[88:91], v[144:147]\n"
"v_mfma_f32_16x16x32_bf16 v[160:163], a[68:71], a[88:91], v[160:163]\n"
"s_mov_b64 exec, 0xffff\n"
"buffer_load_dwordx4 v1, s[44:47], s74 offen offset:2048 sc1 lds\n"
"s_mov_b64 exec, -1\n"
"v_mfma_f32_16x16x32_bf16 v[164:167], a[68:71], a[92:95], v[164:167]\n"
"s_add_u32 s74, s74, 36864\n"
"v_mfma_f32_16x16x32_bf16 v[168:171], a[68:71], a[96:99], v[168:171]\n"
"v_mfma_f32_16x16x32_bf16 v[172:175], a[68:71], a[100:103], v[172:175]\n"
"s_waitcnt lgkmcnt(0)\n"
"v_mfma_f32_16x16x32_bf16 v[32:35], a[0:3], a[72:75], v[32:35]\n"
"s_waitcnt vmcnt(28)\n"
"s_barrier\n"
"v_mfma_f32_16x16x32_bf16 v[36:39], a[0:3], a[76:79], v[36:39]\n"
"s_add_u32 m0, s2, 51200\n"
"ds_read2st64_b64 a[36:39], v19 offset0:50 offset1:51\n"
"v_mfma_f32_16x16x32_bf16 v[40:43], a[0:3], a[80:83], v[40:43]\n"
"buffer_load_dwordx4 v8, s[40:43], s73 offen offset:0 lds\n"
"v_mfma_f32_16x16x32_bf16 v[44:47], a[0:3], a[84:87], v[44:47]\n"
"ds_read_b64_tr_b16 a[88:89], v14 offset:25600\n"
"v_mfma_f32_16x16x32_bf16 v[60:63], a[4:7], a[84:87], v[60:63]\n"
"ds_read_b64_tr_b16 a[90:91], v14 offset:33792\n"
"v_mfma_f32_16x16x32_bf16 v[56:59], a[4:7], a[80:83], v[56:59]\n"
"ds_read_b64_tr_b16 a[92:93], v15 offset:25600\n"
"v_mfma_f32_16x16x32_bf16 v[52:55], a[4:7], a[76:79], v[52:55]\n"
"v_mfma_f32_16x16x32_bf16 v[48:51], a[4:7], a[72:75], v[48:51]\n"
"buffer_load_dwordx4 v9, s[40:43], s73 offen offset:1024 lds\n"
"v_mfma_f32_16x16x32_bf16 v[64:67], a[8:11], a[72:75], v[64:67]\n"
"ds_read_b64_tr_b16 a[94:95], v15 offset:33792\n"
"v_mfma_f32_16x16x32_bf16 v[68:71], a[8:11], a[76:79], v[68:71]\n"
"ds_read_b64_tr_b16 a[96:97], v14 offset:26112\n"
"v_mfma_f32_16x16x32_bf16 v[72:75], a[8:11], a[80:83], v[72:75]\n"
"ds_read_b64_tr_b16 a[98:99], v14 offset:34304\n"
"v_mfma_f32_16x16x32_bf16 v[76:79], a[8:11], a[84:87], v[76:79]\n"
"v_mfma_f32_16x16x32_bf16 v[92:95], a[12:15], a[84:87], v[92:95]\n"
"buffer_load_dwordx4 v10, s[40:43], s73 offen offset:2048 lds\n"
"v_mfma_f32_16x16x32_bf16 v[88:91], a[12:15], a[80:83], v[88:91]\n"
"ds_read_b64_tr_b16 a[100:101], v15 offset:26112\n"
"v_mfma_f32_16x16x32_bf16 v[84:87], a[12:15], a[76:79], v[84:87]\n"
"ds_read_b64_tr_b16 a[102:103], v15 offset:34304\n"
"v_mfma_f32_16x16x32_bf16 v[80:83], a[12:15], a[72:75], v[80:83]\n"
"ds_read2st64_b64 a[40:43], v19 offset0:52 offset1:53\n"
"v_mfma_f32_16x16x32_bf16 v[96:99], a[16:19], a[72:75], v[96:99]\n"
"v_mfma_f32_16x16x32_bf16 v[100:103], a[16:19], a[76:79], v[100:103]\n"
"buffer_load_dwordx4 v11, s[40:43], s73 offen offset:3072 lds\n"
"v_mfma_f32_16x16x32_bf16 v[104:107], a[16:19], a[80:83], v[104:107]\n"
"ds_read2st64_b64 a[44:47], v19 offset0:54 offset1:55\n"
"v_mfma_f32_16x16x32_bf16 v[108:111], a[16:19], a[84:87], v[108:111]\n"
"ds_read2st64_b64 a[48:51], v19 offset0:56 offset1:57\n"
"v_mfma_f32_16x16x32_bf16 v[124:127], a[20:23], a[84:87], v[124:127]\n"
"ds_read2st64_b64 a[52:55], v19 offset0:58 offset1:59\n"
"v_mfma_f32_16x16x32_bf16 v[120:123], a[20:23], a[80:83], v[120:123]\n"
"s_add_u32 s73, s73, 131072\n"
"s_add_u32 m0, s79, 51200\n"
"v_mfma_f32_16x16x32_bf16 v[116:119], a[20:23], a[76:79], v[116:119]\n"
"buffer_load_dwordx4 v1, s[44:47], s74 offen offset:0 sc1 lds\n"
"v_mfma_f32_16x16x32_bf16 v[112:115], a[20:23], a[72:75], v[112:115]\n"
"ds_read2st64_b64 a[56:59], v19 offset0:60 offset1:61\n"
"v_mfma_f32_16x16x32_bf16 v[128:131], a[24:27], a[72:75], v[128:131]\n"
"ds_read2st64_b64 a[60:63], v19 offset0:62 offset1:63\n"
"v_mfma_f32_16x16x32_bf16 v[132:135], a[24:27], a[76:79], v[132:135]\n"
"ds_read2st64_b64 a[64:67], v19 offset0:64 offset1:65\n"
"v_mfma_f32_16x16x32_bf16 v[136:139], a[24:27], a[80:83], v[136:139]\n"
"v_mfma_f32_16x16x32_bf16 v[140:143], a[24:27], a[84:87], v[140:143]\n"
"buffer_load_dwordx4 v1, s[44:47], s74 offen offset:1024 sc1 lds\n"
"v_mfma_f32_16x16x32_bf16 v[156:159], a[28:31], a[84:87], v[156:159]\n"
"ds_read2st64_b64 a[68:71], v19 offset0:66 offset1:67\n"
"v_mfma_f32_16x16x32_bf16 v[152:155], a[28:31], a[80:83], v[152:155]\n"
"v_mfma_f32_16x16x32_bf16 v[148:151], a[28:31], a[76:79], v[148:151]\n"
"v_mfma_f32_16x16x32_bf16 v[144:147], a[28:31], a[72:75], v[144:147]\n"
"v_mfma_f32_16x16x32_bf16 v[160:163], a[32:35], a[72:75], v[160:163]\n"
"s_mov_b64 exec, 0xffff\n"
"buffer_load_dwordx4 v1, s[44:47], s74 offen offset:2048 sc1 lds\n"
"s_mov_b64 exec, -1\n"
"v_mfma_f32_16x16x32_bf16 v[164:167], a[32:35], a[76:79], v[164:167]\n"
"s_add_u32 s74, s74, 36864\n"
"v_mfma_f32_16x16x32_bf16 v[168:171], a[32:35], a[80:83], v[168:171]\n"
"v_mfma_f32_16x16x32_bf16 v[172:175], a[32:35], a[84:87], v[172:175]\n"
"s_waitcnt lgkmcnt(0)\n"
"v_mfma_f32_16x16x32_bf16 v[32:35], a[36:39], a[88:91], v[32:35]\n"
"s_waitcnt vmcnt(28)\n"
"s_barrier\n"
"v_mfma_f32_16x16x32_bf16 v[36:39], a[36:39], a[92:95], v[36:39]\n"
"s_add_u32 m0, s2, 76800\n"
"ds_read2st64_b64 a[0:3], v20 offset0:0 offset1:1\n"
"v_mfma_f32_16x16x32_bf16 v[40:43], a[36:39], a[96:99], v[40:43]\n"
"buffer_load_dwordx4 v8, s[40:43], s73 offen offset:0 lds\n"
"v_mfma_f32_16x16x32_bf16 v[44:47], a[36:39], a[100:103], v[44:47]\n"
"ds_read_b64_tr_b16 a[72:73], v16 offset:0\n"
"v_mfma_f32_16x16x32_bf16 v[60:63], a[40:43], a[100:103], v[60:63]\n"
"ds_read_b64_tr_b16 a[74:75], v16 offset:8192\n"
"v_mfma_f32_16x16x32_bf16 v[56:59], a[40:43], a[96:99], v[56:59]\n"
"ds_read_b64_tr_b16 a[76:77], v17 offset:0\n"
"v_mfma_f32_16x16x32_bf16 v[52:55], a[40:43], a[92:95], v[52:55]\n"
"v_mfma_f32_16x16x32_bf16 v[48:51], a[40:43], a[88:91], v[48:51]\n"
"buffer_load_dwordx4 v9, s[40:43], s73 offen offset:1024 lds\n"
"v_mfma_f32_16x16x32_bf16 v[64:67], a[44:47], a[88:91], v[64:67]\n"
"ds_read_b64_tr_b16 a[78:79], v17 offset:8192\n"
"v_mfma_f32_16x16x32_bf16 v[68:71], a[44:47], a[92:95], v[68:71]\n"
"ds_read_b64_tr_b16 a[80:81], v16 offset:512\n"
"v_mfma_f32_16x16x32_bf16 v[72:75], a[44:47], a[96:99], v[72:75]\n"
"ds_read_b64_tr_b16 a[82:83], v16 offset:8704\n"
"v_mfma_f32_16x16x32_bf16 v[76:79], a[44:47], a[100:103], v[76:79]\n"
"v_mfma_f32_16x16x32_bf16 v[92:95], a[48:51], a[100:103], v[92:95]\n"
"buffer_load_dwordx4 v10, s[40:43], s73 offen offset:2048 lds\n"
"v_mfma_f32_16x16x32_bf16 v[88:91], a[48:51], a[96:99], v[88:91]\n"
"ds_read_b64_tr_b16 a[84:85], v17 offset:512\n"
"v_mfma_f32_16x16x32_bf16 v[84:87], a[48:51], a[92:95], v[84:87]\n"
"ds_read_b64_tr_b16 a[86:87], v17 offset:8704\n"
"v_mfma_f32_16x16x32_bf16 v[80:83], a[48:51], a[88:91], v[80:83]\n"
"ds_read2st64_b64 a[4:7], v20 offset0:2 offset1:3\n"
"v_mfma_f32_16x16x32_bf16 v[96:99], a[52:55], a[88:91], v[96:99]\n"
"v_mfma_f32_16x16x32_bf16 v[100:103], a[52:55], a[92:95], v[100:103]\n"
"buffer_load_dwordx4 v11, s[40:43], s73 offen offset:3072 lds\n"
"v_mfma_f32_16x16x32_bf16 v[104:107], a[52:55], a[96:99], v[104:107]\n"
"ds_read2st64_b64 a[8:11], v20 offset0:4 offset1:5\n"
"v_mfma_f32_16x16x32_bf16 v[108:111], a[52:55], a[100:103], v[108:111]\n"
"ds_read2st64_b64 a[12:15], v20 offset0:6 offset1:7\n"
"v_mfma_f32_16x16x32_bf16 v[124:127], a[56:59], a[100:103], v[124:127]\n"
"ds_read2st64_b64 a[16:19], v20 offset0:8 offset1:9\n"
"v_mfma_f32_16x16x32_bf16 v[120:123], a[56:59], a[96:99], v[120:123]\n"
"s_add_u32 s73, s73, 131072\n"
"s_add_u32 m0, s79, 76800\n"
"v_mfma_f32_16x16x32_bf16 v[116:119], a[56:59], a[92:95], v[116:119]\n"
"buffer_load_dwordx4 v1, s[44:47], s74 offen offset:0 sc1 lds\n"
"v_mfma_f32_16x16x32_bf16 v[112:115], a[56:59], a[88:91], v[112:115]\n"
"ds_read2st64_b64 a[20:23], v20 offset0:10 offset1:11\n"
"v_mfma_f32_16x16x32_bf16 v[128:131], a[60:63], a[88:91], v[128:131]\n"
"ds_read2st64_b64 a[24:27], v20 offset0:12 offset1:13\n"
"v_mfma_f32_16x16x32_bf16 v[132:135], a[60:63], a[92:95], v[132:135]\n"
"ds_read2st64_b64 a[28:31], v20 offset0:14 offset1:15\n"
"v_mfma_f32_16x16x32_bf16 v[136:139], a[60:63], a[96:99], v[136:139]\n"
"v_mfma_f32_16x16x32_bf16 v[140:143], a[60:63], a[100:103], v[140:143]\n"
"buffer_load_dwordx4 v1, s[44:47], s74 offen offset:1024 sc1 lds\n"
"v_mfma_f32_16x16x32_bf16 v[156:159], a[64:67], a[100:103], v[156:159]\n"
"ds_read2st64_b64 a[32:35], v20 offset0:16 offset1:17\n"
"v_mfma_f32_16x16x32_bf16 v[152:155], a[64:67], a[96:99], v[152:155]\n"
"v_mfma_f32_16x16x32_bf16 v[148:151], a[64:67], a[92:95], v[148:151]\n"
"v_mfma_f32_16x16x32_bf16 v[144:147], a[64:67], a[88:91], v[144:147]\n"
"v_mfma_f32_16x16x32_bf16 v[160:163], a[68:71], a[88:91], v[160:163]\n"
"s_mov_b64 exec, 0xffff\n"
"buffer_load_dwordx4 v1, s[44:47], s74 offen offset:2048 sc1 lds\n"
"s_mov_b64 exec, -1\n"
"v_mfma_f32_16x16x32_bf16 v[164:167], a[68:71], a[92:95], v[164:167]\n"
"s_add_u32 s74, s74, 36864\n"
"v_mfma_f32_16x16x32_bf16 v[168:171], a[68:71], a[96:99], v[168:171]\n"
"v_mfma_f32_16x16x32_bf16 v[172:175], a[68:71], a[100:103], v[172:175]\n"
"s_waitcnt lgkmcnt(0)\n"
"v_mfma_f32_16x16x32_bf16 v[32:35], a[0:3], a[72:75], v[32:35]\n"
"s_waitcnt vmcnt(28)\n"
"s_barrier\n"
"v_mfma_f32_16x16x32_bf16 v[36:39], a[0:3], a[76:79], v[36:39]\n"
"s_add_u32 m0, s2, 102400\n"
"ds_read2st64_b64 a[36:39], v20 offset0:50 offset1:51\n"
"v_mfma_f32_16x16x32_bf16 v[40:43], a[0:3], a[80:83], v[40:43]\n"
"buffer_load_dwordx4 v8, s[40:43], s73 offen offset:0 lds\n"
"v_mfma_f32_16x16x32_bf16 v[44:47], a[0:3], a[84:87], v[44:47]\n"
"ds_read_b64_tr_b16 a[88:89], v16 offset:25600\n"
"v_mfma_f32_16x16x32_bf16 v[60:63], a[4:7], a[84:87], v[60:63]\n"
"s_mov_b64 exec, 3\n"
"buffer_store_dword v252, v27, s[36:39], 0 offen sc1\n"
"s_mov_b64 exec, -1\n"
"v_mfma_f32_16x16x32_bf16 v[56:59], a[4:7], a[80:83], v[56:59]\n"
"ds_read_b64_tr_b16 a[90:91], v16 offset:33792\n"
"s_mov_b32 s90, s86\n"
"v_mfma_f32_16x16x32_bf16 v[52:55], a[4:7], a[76:79], v[52:55]\n"
"ds_read_b64_tr_b16 a[92:93], v17 offset:25600\n"
"v_mfma_f32_16x16x32_bf16 v[48:51], a[4:7], a[72:75], v[48:51]\n"
"buffer_load_dwordx4 v9, s[40:43], s73 offen offset:1024 lds\n"
"v_mfma_f32_16x16x32_bf16 v[64:67], a[8:11], a[72:75], v[64:67]\n"
"ds_read_b64_tr_b16 a[94:95], v17 offset:33792\n"
"v_mfma_f32_16x16x32_bf16 v[68:71], a[8:11], a[76:79], v[68:71]\n"
"ds_read_b64_tr_b16 a[96:97], v16 offset:26112\n"
"v_mfma_f32_16x16x32_bf16 v[72:75], a[8:11], a[80:83], v[72:75]\n"
"ds_read_b64_tr_b16 a[98:99], v16 offset:34304\n"
"v_mfma_f32_16x16x32_bf16 v[76:79], a[8:11], a[84:87], v[76:79]\n"
"v_mfma_f32_16x16x32_bf16 v[92:95], a[12:15], a[84:87], v[92:95]\n"
"buffer_load_dwordx4 v10, s[40:43], s73 offen offset:2048 lds\n"
"v_mfma_f32_16x16x32_bf16 v[88:91], a[12:15], a[80:83], v[88:91]\n"
"ds_read_b64_tr_b16 a[100:101], v17 offset:26112\n"
"v_mfma_f32_16x16x32_bf16 v[84:87], a[12:15], a[76:79], v[84:87]\n"
"ds_read_b64_tr_b16 a[102:103], v17 offset:34304\n"
"v_mfma_f32_16x16x32_bf16 v[80:83], a[12:15], a[72:75], v[80:83]\n"
"ds_read2st64_b64 a[40:43], v20 offset0:52 offset1:53\n"
"v_mfma_f32_16x16x32_bf16 v[96:99], a[16:19], a[72:75], v[96:99]\n"
"v_mfma_f32_16x16x32_bf16 v[100:103], a[16:19], a[76:79], v[100:103]\n"
"buffer_load_dwordx4 v11, s[40:43], s73 offen offset:3072 lds\n"
"v_mfma_f32_16x16x32_bf16 v[104:107], a[16:19], a[80:83], v[104:107]\n"
"ds_read2st64_b64 a[44:47], v20 offset0:54 offset1:55\n"
"v_mfma_f32_16x16x32_bf16 v[108:111], a[16:19], a[84:87], v[108:111]\n"
"ds_read2st64_b64 a[48:51], v20 offset0:56 offset1:57\n"
"v_mfma_f32_16x16x32_bf16 v[124:127], a[20:23], a[84:87], v[124:127]\n"
"ds_read2st64_b64 a[52:55], v20 offset0:58 offset1:59\n"
"v_mfma_f32_16x16x32_bf16 v[120:123], a[20:23], a[80:83], v[120:123]\n"
"s_add_u32 s73, s73, 131072\n"
"s_add_u32 m0, s79, 102400\n"
"v_mfma_f32_16x16x32_bf16 v[116:119], a[20:23], a[76:79], v[116:119]\n"
"buffer_load_dwordx4 v1, s[44:47], s74 offen offset:0 sc1 lds\n"
"v_mfma_f32_16x16x32_bf16 v[112:115], a[20:23], a[72:75], v[112:115]\n"
"ds_read2st64_b64 a[56:59], v20 offset0:60 offset1:61\n"
"v_mfma_f32_16x16x32_bf16 v[128:131], a[24:27], a[72:75], v[128:131]\n"
"ds_read2st64_b64 a[60:63], v20 offset0:62 offset1:63\n"
"v_mfma_f32_16x16x32_bf16 v[132:135], a[24:27], a[76:79], v[132:135]\n"
"ds_read2st64_b64 a[64:67], v20 offset0:64 offset1:65\n"
"v_mfma_f32_16x16x32_bf16 v[136:139], a[24:27], a[80:83], v[136:139]\n"
"v_mfma_f32_16x16x32_bf16 v[140:143], a[24:27], a[84:87], v[140:143]\n"
"buffer_load_dwordx4 v1, s[44:47], s74 offen offset:1024 sc1 lds\n"
"v_mfma_f32_16x16x32_bf16 v[156:159], a[28:31], a[84:87], v[156:159]\n"
"ds_read2st64_b64 a[68:71], v20 offset0:66 offset1:67\n"
"v_mfma_f32_16x16x32_bf16 v[152:155], a[28:31], a[80:83], v[152:155]\n"
"v_mfma_f32_16x16x32_bf16 v[148:151], a[28:31], a[76:79], v[148:151]\n"
"v_mfma_f32_16x16x32_bf16 v[144:147], a[28:31], a[72:75], v[144:147]\n"
"v_mfma_f32_16x16x32_bf16 v[160:163], a[32:35], a[72:75], v[160:163]\n"
"s_mov_b64 exec, 0xffff\n"
"buffer_load_dwordx4 v1, s[44:47], s74 offen offset:2048 sc1 lds\n"
"s_mov_b64 exec, -1\n"
"v_mfma_f32_16x16x32_bf16 v[164:167], a[32:35], a[76:79], v[164:167]\n"
"s_add_u32 s74, s74, 36864\n"
"v_mfma_f32_16x16x32_bf16 v[168:171], a[32:35], a[80:83], v[168:171]\n"
"v_mfma_f32_16x16x32_bf16 v[172:175], a[32:35], a[84:87], v[172:175]\n"
"s_waitcnt lgkmcnt(0)\n"
"v_mfma_f32_16x16x32_bf16 v[32:35], a[36:39], a[88:91], v[32:35]\n"
"s_waitcnt vmcnt(29)\n"
"s_barrier\n"
"v_mfma_f32_16x16x32_bf16 v[36:39], a[36:39], a[92:95], v[36:39]\n"
"s_add_u32 m0, s2, 128000\n"
"ds_read2st64_b64 a[0:3], v18 offset0:0 offset1:1\n"
"v_mfma_f32_16x16x32_bf16 v[40:43], a[36:39], a[96:99], v[40:43]\n"
"buffer_load_dwordx4 v8, s[40:43], s73 offen offset:0 lds\n"
"v_mfma_f32_16x16x32_bf16 v[44:47], a[36:39], a[100:103], v[44:47]\n"
"ds_read_b64_tr_b16 a[72:73], v12 offset:0\n"
"v_mfma_f32_16x16x32_bf16 v[60:63], a[40:43], a[100:103], v[60:63]\n"
"ds_read_b64_tr_b16 a[74:75], v12 offset:8192\n"
"v_mfma_f32_16x16x32_bf16 v[56:59], a[40:43], a[96:99], v[56:59]\n"
"ds_read_b64_tr_b16 a[76:77], v13 offset:0\n"
"v_mfma_f32_16x16x32_bf16 v[52:55], a[40:43], a[92:95], v[52:55]\n"
"v_mfma_f32_16x16x32_bf16 v[48:51], a[40:43], a[88:91], v[48:51]\n"
"buffer_load_dwordx4 v9, s[40:43], s73 offen offset:1024 lds\n"
"v_mfma_f32_16x16x32_bf16 v[64:67], a[44:47], a[88:91], v[64:67]\n"
"ds_read_b64_tr_b16 a[78:79], v13 offset:8192\n"
"v_mfma_f32_16x16x32_bf16 v[68:71], a[44:47], a[92:95], v[68:71]\n"
"ds_read_b64_tr_b16 a[80:81], v12 offset:512\n"
"v_mfma_f32_16x16x32_bf16 v[72:75], a[44:47], a[96:99], v[72:75]\n"
"ds_read_b64_tr_b16 a[82:83], v12 offset:8704\n"
"v_mfma_f32_16x16x32_bf16 v[76:79], a[44:47], a[100:103], v[76:79]\n"
"v_mfma_f32_16x16x32_bf16 v[92:95], a[48:51], a[100:103], v[92:95]\n"
"buffer_load_dwordx4 v10, s[40:43], s73 offen offset:2048 lds\n"
"v_mfma_f32_16x16x32_bf16 v[88:91], a[48:51], a[96:99], v[88:91]\n"
"ds_read_b64_tr_b16 a[84:85], v13 offset:512\n"
"v_mfma_f32_16x16x32_bf16 v[84:87], a[48:51], a[92:95], v[84:87]\n"
"ds_read_b64_tr_b16 a[86:87], v13 offset:8704\n"
"v_mfma_f32_16x16x32_bf16 v[80:83], a[48:51], a[88:91], v[80:83]\n"
"ds_read2st64_b64 a[4:7], v18 offset0:2 offset1:3\n"
"v_mfma_f32_16x16x32_bf16 v[96:99], a[52:55], a[88:91], v[96:99]\n"
"v_mfma_f32_16x16x32_bf16 v[100:103], a[52:55], a[92:95], v[100:103]\n"
"buffer_load_dwordx4 v11, s[40:43], s73 offen offset:3072 lds\n"
"v_mfma_f32_16x16x32_bf16 v[104:107], a[52:55], a[96:99], v[104:107]\n"
"ds_read2st64_b64 a[8:11], v18 offset0:4 offset1:5\n"
"v_mfma_f32_16x16x32_bf16 v[108:111], a[52:55], a[100:103], v[108:111]\n"
"ds_read2st64_b64 a[12:15], v18 offset0:6 offset1:7\n"
"v_mfma_f32_16x16x32_bf16 v[124:127], a[56:59], a[100:103], v[124:127]\n"
"ds_read2st64_b64 a[16:19], v18 offset0:8 offset1:9\n"
"v_mfma_f32_16x16x32_bf16 v[120:123], a[56:59], a[96:99], v[120:123]\n"
"s_add_u32 s73, s73, 131072\n"
"s_add_u32 m0, s79, 128000\n"
"v_mfma_f32_16x16x32_bf16 v[116:119], a[56:59], a[92:95], v[116:119]\n"
"buffer_load_dwordx4 v1, s[44:47], s74 offen offset:0 sc1 lds\n"
"v_mfma_f32_16x16x32_bf16 v[112:115], a[56:59], a[88:91], v[112:115]\n"
"ds_read2st64_b64 a[20:23], v18 offset0:10 offset1:11\n"
"v_mfma_f32_16x16x32_bf16 v[128:131], a[60:63], a[88:91], v[128:131]\n"
"ds_read2st64_b64 a[24:27], v18 offset0:12 offset1:13\n"
"v_mfma_f32_16x16x32_bf16 v[132:135], a[60:63], a[92:95], v[132:135]\n"
"ds_read2st64_b64 a[28:31], v18 offset0:14 offset1:15\n"
"v_mfma_f32_16x16x32_bf16 v[136:139], a[60:63], a[96:99], v[136:139]\n"
"v_mfma_f32_16x16x32_bf16 v[140:143], a[60:63], a[100:103], v[140:143]\n"
"buffer_load_dwordx4 v1, s[44:47], s74 offen offset:1024 sc1 lds\n"
"v_mfma_f32_16x16x32_bf16 v[156:159], a[64:67], a[100:103], v[156:159]\n"
"ds_read2st64_b64 a[32:35], v18 offset0:16 offset1:17\n"
"v_mfma_f32_16x16x32_bf16 v[152:155], a[64:67], a[96:99], v[152:155]\n"
"v_mfma_f32_16x16x32_bf16 v[148:151], a[64:67], a[92:95], v[148:151]\n"
"v_mfma_f32_16x16x32_bf16 v[144:147], a[64:67], a[88:91], v[144:147]\n"
"v_mfma_f32_16x16x32_bf16 v[160:163], a[68:71], a[88:91], v[160:163]\n"
"s_mov_b64 exec, 0xffff\n"
"buffer_load_dwordx4 v1, s[44:47], s74 offen offset:2048 sc1 lds\n"
"s_mov_b64 exec, -1\n"
"v_mfma_f32_16x16x32_bf16 v[164:167], a[68:71], a[92:95], v[164:167]\n"
"s_add_u32 s74, s74, 36864\n"
"v_mfma_f32_16x16x32_bf16 v[168:171], a[68:71], a[96:99], v[168:171]\n"
"v_mfma_f32_16x16x32_bf16 v[172:175], a[68:71], a[100:103], v[172:175]\n"
"s_waitcnt lgkmcnt(0)\n"
"v_mfma_f32_16x16x32_bf16 v[32:35], a[0:3], a[72:75], v[32:35]\n"
"s_waitcnt vmcnt(29)\n"
"s_barrier\n"
"v_mfma_f32_16x16x32_bf16 v[36:39], a[0:3], a[76:79], v[36:39]\n"
"s_add_u32 m0, s2, 0\n"
"ds_read2st64_b64 a[36:39], v18 offset0:50 offset1:51\n"
"v_mfma_f32_16x16x32_bf16 v[40:43], a[0:3], a[80:83], v[40:43]\n"
"buffer_load_dwordx4 v8, s[40:43], s73 offen offset:0 lds\n"
"v_mfma_f32_16x16x32_bf16 v[44:47], a[0:3], a[84:87], v[44:47]\n"
"ds_read_b64_tr_b16 a[88:89], v12 offset:25600\n"
"v_mfma_f32_16x16x32_bf16 v[60:63], a[4:7], a[84:87], v[60:63]\n"
"ds_read_b64_tr_b16 a[90:91], v12 offset:33792\n"
"v_mfma_f32_16x16x32_bf16 v[56:59], a[4:7], a[80:83], v[56:59]\n"
"ds_read_b64_tr_b16 a[92:93], v13 offset:25600\n"
"v_mfma_f32_16x16x32_bf16 v[52:55], a[4:7], a[76:79], v[52:55]\n"
"v_mfma_f32_16x16x32_bf16 v[48:51], a[4:7], a[72:75], v[48:51]\n"
"buffer_load_dwordx4 v9, s[40:43], s73 offen offset:1024 lds\n"
"v_mfma_f32_16x16x32_bf16 v[64:67], a[8:11], a[72:75], v[64:67]\n"
"ds_read_b64_tr_b16 a[94:95], v13 offset:33792\n"
"v_mfma_f32_16x16x32_bf16 v[68:71], a[8:11], a[76:79], v[68:71]\n"
"ds_read_b64_tr_b16 a[96:97], v12 offset:26112\n"
"v_mfma_f32_16x16x32_bf16 v[72:75], a[8:11], a[80:83], v[72:75]\n"
"ds_read_b64_tr_b16 a[98:99], v12 offset:34304\n"
"v_mfma_f32_16x16x32_bf16 v[76:79], a[8:11], a[84:87], v[76:79]\n"
"v_mfma_f32_16x16x32_bf16 v[92:95], a[12:15], a[84:87], v[92:95]\n"
"buffer_load_dwordx4 v10, s[40:43], s73 offen offset:2048 lds\n"
"v_mfma_f32_16x16x32_bf16 v[88:91], a[12:15], a[80:83], v[88:91]\n"
"ds_read_b64_tr_b16 a[100:101], v13 offset:26112\n"
"v_mfma_f32_16x16x32_bf16 v[84:87], a[12:15], a[76:79], v[84:87]\n"
"ds_read_b64_tr_b16 a[102:103], v13 offset:34304\n"
"v_mfma_f32_16x16x32_bf16 v[80:83], a[12:15], a[72:75], v[80:83]\n"
"ds_read2st64_b64 a[40:43], v18 offset0:52 offset1:53\n"
"v_mfma_f32_16x16x32_bf16 v[96:99], a[16:19], a[72:75], v[96:99]\n"
"v_mfma_f32_16x16x32_bf16 v[100:103], a[16:19], a[76:79], v[100:103]\n"
"buffer_load_dwordx4 v11, s[40:43], s73 offen offset:3072 lds\n"
"v_mfma_f32_16x16x32_bf16 v[104:107], a[16:19], a[80:83], v[104:107]\n"
"ds_read2st64_b64 a[44:47], v18 offset0:54 offset1:55\n"
"v_mfma_f32_16x16x32_bf16 v[108:111], a[16:19], a[84:87], v[108:111]\n"
"ds_read2st64_b64 a[48:51], v18 offset0:56 offset1:57\n"
"v_mfma_f32_16x16x32_bf16 v[124:127], a[20:23], a[84:87], v[124:127]\n"
"ds_read2st64_b64 a[52:55], v18 offset0:58 offset1:59\n"
"v_mfma_f32_16x16x32_bf16 v[120:123], a[20:23], a[80:83], v[120:123]\n"
"s_add_u32 s73, s73, 131072\n"
"s_add_u32 m0, s79, 0\n"
"v_mfma_f32_16x16x32_bf16 v[116:119], a[20:23], a[76:79], v[116:119]\n"
"buffer_load_dwordx4 v1, s[44:47], s74 offen offset:0 sc1 lds\n"
"v_mfma_f32_16x16x32_bf16 v[112:115], a[20:23], a[72:75], v[112:115]\n"
"ds_read2st64_b64 a[56:59], v18 offset0:60 offset1:61\n"
"v_mfma_f32_16x16x32_bf16 v[128:131], a[24:27], a[72:75], v[128:131]\n"
"ds_read2st64_b64 a[60:63], v18 offset0:62 offset1:63\n"
"v_mfma_f32_16x16x32_bf16 v[132:135], a[24:27], a[76:79], v[132:135]\n"
"ds_read2st64_b64 a[64:67], v18 offset0:64 offset1:65\n"
"v_mfma_f32_16x16x32_bf16 v[136:139], a[24:27], a[80:83], v[136:139]\n"
"v_mfma_f32_16x16x32_bf16 v[140:143], a[24:27], a[84:87], v[140:143]\n"
"buffer_load_dwordx4 v1, s[44:47], s74 offen offset:1024 sc1 lds\n"
"v_mfma_f32_16x16x32_bf16 v[156:159], a[28:31], a[84:87], v[156:159]\n"
"ds_read2st64_b64 a[68:71], v18 offset0:66 offset1:67\n"
"v_mfma_f32_16x16x32_bf16 v[152:155], a[28:31], a[80:83], v[152:155]\n"
"v_mfma_f32_16x16x32_bf16 v[148:151], a[28:31], a[76:79], v[148:151]\n"
"v_mfma_f32_16x16x32_bf16 v[144:147], a[28:31], a[72:75], v[144:147]\n"
"v_mfma_f32_16x16x32_bf16 v[160:163], a[32:35], a[72:75], v[160:163]\n"
"s_mov_b64 exec, 0xffff\n"
"buffer_load_dwordx4 v1, s[44:47], s74 offen offset:2048 sc1 lds\n"
"s_mov_b64 exec, -1\n"
"v_mfma_f32_16x16x32_bf16 v[164:167], a[32:35], a[76:79], v[164:167]\n"
"s_add_u32 s74, s74, 36864\n"
"v_mfma_f32_16x16x32_bf16 v[168:171], a[32:35], a[80:83], v[168:171]\n"
"v_mfma_f32_16x16x32_bf16 v[172:175], a[32:35], a[84:87], v[172:175]\n"
"s_waitcnt lgkmcnt(0)\n"
"v_mfma_f32_16x16x32_bf16 v[32:35], a[36:39], a[88:91], v[32:35]\n"
"s_waitcnt vmcnt(29)\n"
"s_barrier\n"
"v_mfma_f32_16x16x32_bf16 v[36:39], a[36:39], a[92:95], v[36:39]\n"
"s_add_u32 m0, s2, 25600\n"
"ds_read2st64_b64 a[0:3], v19 offset0:0 offset1:1\n"
"v_mfma_f32_16x16x32_bf16 v[40:43], a[36:39], a[96:99], v[40:43]\n"
"buffer_load_dwordx4 v8, s[40:43], s73 offen offset:0 lds\n"
"v_mfma_f32_16x16x32_bf16 v[44:47], a[36:39], a[100:103], v[44:47]\n"
"ds_read_b64_tr_b16 a[72:73], v14 offset:0\n"
"v_mfma_f32_16x16x32_bf16 v[60:63], a[40:43], a[100:103], v[60:63]\n"
"ds_read_b64_tr_b16 a[74:75], v14 offset:8192\n"
"v_mfma_f32_16x16x32_bf16 v[56:59], a[40:43], a[96:99], v[56:59]\n"
"ds_read_b64_tr_b16 a[76:77], v15 offset:0\n"
"v_mfma_f32_16x16x32_bf16 v[52:55], a[40:43], a[92:95], v[52:55]\n"
"v_mfma_f32_16x16x32_bf16 v[48:51], a[40:43], a[88:91], v[48:51]\n"
"buffer_load_dwordx4 v9, s[40:43], s73 offen offset:1024 lds\n"
"v_mfma_f32_16x16x32_bf16 v[64:67], a[44:47], a[88:91], v[64:67]\n"
"ds_read_b64_tr_b16 a[78:79], v15 offset:8192\n"
"v_mfma_f32_16x16x32_bf16 v[68:71], a[44:47], a[92:95], v[68:71]\n"
"ds_read_b64_tr_b16 a[80:81], v14 offset:512\n"
"v_mfma_f32_16x16x32_bf16 v[72:75], a[44:47], a[96:99], v[72:75]\n"
"ds_read_b64_tr_b16 a[82:83], v14 offset:8704\n"
"v_mfma_f32_16x16x32_bf16 v[76:79], a[44:47], a[100:103], v[76:79]\n"
"v_mfma_f32_16x16x32_bf16 v[92:95], a[48:51], a[100:103], v[92:95]\n"
"buffer_load_dwordx4 v10, s[40:43], s73 offen offset:2048 lds\n"
"v_mfma_f32_16x16x32_bf16 v[88:91], a[48:51], a[96:99], v[88:91]\n"
"ds_read_b64_tr_b16 a[84:85], v15 offset:512\n"
"v_mfma_f32_16x16x32_bf16 v[84:87], a[48:51], a[92:95], v[84:87]\n"
"ds_read_b64_tr_b16 a[86:87], v15 offset:8704\n"
"v_mfma_f32_16x16x32_bf16 v[80:83], a[48:51], a[88:91], v[80:83]\n"
"ds_read2st64_b64 a[4:7], v19 offset0:2 offset1:3\n"
"v_mfma_f32_16x16x32_bf16 v[96:99], a[52:55], a[88:91], v[96:99]\n"
"v_mfma_f32_16x16x32_bf16 v[100:103], a[52:55], a[92:95], v[100:103]\n"
"buffer_load_dwordx4 v11, s[40:43], s73 offen offset:3072 lds\n"
"v_mfma_f32_16x16x32_bf16 v[104:107], a[52:55], a[96:99], v[104:107]\n"
"ds_read2st64_b64 a[8:11], v19 offset0:4 offset1:5\n"
"v_mfma_f32_16x16x32_bf16 v[108:111], a[52:55], a[100:103], v[108:111]\n"
"ds_read2st64_b64 a[12:15], v19 offset0:6 offset1:7\n"
"v_mfma_f32_16x16x32_bf16 v[124:127], a[56:59], a[100:103], v[124:127]\n"
"ds_read2st64_b64 a[16:19], v19 offset0:8 offset1:9\n"
"v_mfma_f32_16x16x32_bf16 v[120:123], a[56:59], a[96:99], v[120:123]\n"
"s_add_u32 s73, s73, 131072\n"
"s_add_u32 m0, s79, 25600\n"
"v_mfma_f32_16x16x32_bf16 v[116:119], a[56:59], a[92:95], v[116:119]\n"
"buffer_load_dwordx4 v1, s[44:47], s74 offen offset:0 sc1 lds\n"
"v_mfma_f32_16x16x32_bf16 v[112:115], a[56:59], a[88:91], v[112:115]\n"
"ds_read2st64_b64 a[20:23], v19 offset0:10 offset1:11\n"
"v_mfma_f32_16x16x32_bf16 v[128:131], a[60:63], a[88:91], v[128:131]\n"
"ds_read2st64_b64 a[24:27], v19 offset0:12 offset1:13\n"
"v_mfma_f32_16x16x32_bf16 v[132:135], a[60:63], a[92:95], v[132:135]\n"
"ds_read2st64_b64 a[28:31], v19 offset0:14 offset1:15\n"
"v_mfma_f32_16x16x32_bf16 v[136:139], a[60:63], a[96:99], v[136:139]\n"
"v_mfma_f32_16x16x32_bf16 v[140:143], a[60:63], a[100:103], v[140:143]\n"
"buffer_load_dwordx4 v1, s[44:47], s74 offen offset:1024 sc1 lds\n"
"v_mfma_f32_16x16x32_bf16 v[156:159], a[64:67], a[100:103], v[156:159]\n"
"ds_read2st64_b64 a[32:35], v19 offset0:16 offset1:17\n"
"v_mfma_f32_16x16x32_bf16 v[152:155], a[64:67], a[96:99], v[152:155]\n"
"v_mfma_f32_16x16x32_bf16 v[148:151], a[64:67], a[92:95], v[148:151]\n"
"v_mfma_f32_16x16x32_bf16 v[144:147], a[64:67], a[88:91], v[144:147]\n"
"v_mfma_f32_16x16x32_bf16 v[160:163], a[68:71], a[88:91], v[160:163]\n"
"s_mov_b64 exec, 0xffff\n"
"buffer_load_dwordx4 v1, s[44:47], s74 offen offset:2048 sc1 lds\n"
"s_mov_b64 exec, -1\n"
"v_mfma_f32_16x16x32_bf16 v[164:167], a[68:71], a[92:95], v[164:167]\n"
"s_add_u32 s74, s74, 36864\n"
"v_mfma_f32_16x16x32_bf16 v[168:171], a[68:71], a[96:99], v[168:171]\n"
"v_mfma_f32_16x16x32_bf16 v[172:175], a[68:71], a[100:103], v[172:175]\n"
"s_waitcnt lgkmcnt(0)\n"
"v_mfma_f32_16x16x32_bf16 v[32:35], a[0:3], a[72:75], v[32:35]\n"
"s_waitcnt vmcnt(29)\n"
"s_barrier\n"
"v_mfma_f32_16x16x32_bf16 v[36:39], a[0:3], a[76:79], v[36:39]\n"
"s_add_u32 m0, s2, 51200\n"
"ds_read2st64_b64 a[36:39], v19 offset0:50 offset1:51\n"
"v_mfma_f32_16x16x32_bf16 v[40:43], a[0:3], a[80:83], v[40:43]\n"
"buffer_load_dwordx4 v8, s[40:43], s73 offen offset:0 lds\n"
"v_mfma_f32_16x16x32_bf16 v[44:47], a[0:3], a[84:87], v[44:47]\n"
"ds_read_b64_tr_b16 a[88:89], v14 offset:25600\n"
"v_mfma_f32_16x16x32_bf16 v[60:63], a[4:7], a[84:87], v[60:63]\n"
"ds_read_b64_tr_b16 a[90:91], v14 offset:33792\n"
"v_mfma_f32_16x16x32_bf16 v[56:59], a[4:7], a[80:83], v[56:59]\n"
"ds_read_b64_tr_b16 a[92:93], v15 offset:25600\n"
"v_mfma_f32_16x16x32_bf16 v[52:55], a[4:7], a[76:79], v[52:55]\n"
"v_mfma_f32_16x16x32_bf16 v[48:51], a[4:7], a[72:75], v[48:51]\n"
"buffer_load_dwordx4 v9, s[40:43], s73 offen offset:1024 lds\n"
"v_mfma_f32_16x16x32_bf16 v[64:67], a[8:11], a[72:75], v[64:67]\n"
"ds_read_b64_tr_b16 a[94:95], v15 offset:33792\n"
"v_mfma_f32_16x16x32_bf16 v[68:71], a[8:11], a[76:79], v[68:71]\n"
"ds_read_b64_tr_b16 a[96:97], v14 offset:26112\n"
"v_mfma_f32_16x16x32_bf16 v[72:75], a[8:11], a[80:83], v[72:75]\n"
"ds_read_b64_tr_b16 a[98:99], v14 offset:34304\n"
"v_mfma_f32_16x16x32_bf16 v[76:79], a[8:11], a[84:87], v[76:79]\n"
"v_mfma_f32_16x16x32_bf16 v[92:95], a[12:15], a[84:87], v[92:95]\n"
"buffer_load_dwordx4 v10, s[40:43], s73 offen offset:2048 lds\n"
"v_mfma_f32_16x16x32_bf16 v[88:91], a[12:15], a[80:83], v[88:91]\n"
"ds_read_b64_tr_b16 a[100:101], v15 offset:26112\n"
"v_mfma_f32_16x16x32_bf16 v[84:87], a[12:15], a[76:79], v[84:87]\n"
"ds_read_b64_tr_b16 a[102:103], v15 offset:34304\n"
"v_mfma_f32_16x16x32_bf16 v[80:83], a[12:15], a[72:75], v[80:83]\n"
"ds_read2st64_b64 a[40:43], v19 offset0:52 offset1:53\n"
"v_mfma_f32_16x16x32_bf16 v[96:99], a[16:19], a[72:75], v[96:99]\n"
"v_mfma_f32_16x16x32_bf16 v[100:103], a[16:19], a[76:79], v[100:103]\n"
"buffer_load_dwordx4 v11, s[40:43], s73 offen offset:3072 lds\n"
"v_mfma_f32_16x16x32_bf16 v[104:107], a[16:19], a[80:83], v[104:107]\n"
"ds_read2st64_b64 a[44:47], v19 offset0:54 offset1:55\n"
"v_mfma_f32_16x16x32_bf16 v[108:111], a[16:19], a[84:87], v[108:111]\n"
"ds_read2st64_b64 a[48:51], v19 offset0:56 offset1:57\n"
"v_mfma_f32_16x16x32_bf16 v[124:127], a[20:23], a[84:87], v[124:127]\n"
"ds_read2st64_b64 a[52:55], v19 offset0:58 offset1:59\n"
"v_mfma_f32_16x16x32_bf16 v[120:123], a[20:23], a[80:83], v[120:123]\n"
"s_add_u32 s73, s73, 131072\n"
"s_add_u32 m0, s79, 51200\n"
"v_mfma_f32_16x16x32_bf16 v[116:119], a[20:23], a[76:79], v[116:119]\n"
"buffer_load_dwordx4 v1, s[44:47], s74 offen offset:0 sc1 lds\n"
"v_mfma_f32_16x16x32_bf16 v[112:115], a[20:23], a[72:75], v[112:115]\n"
"ds_read2st64_b64 a[56:59], v19 offset0:60 offset1:61\n"
"v_mfma_f32_16x16x32_bf16 v[128:131], a[24:27], a[72:75], v[128:131]\n"
"ds_read2st64_b64 a[60:63], v19 offset0:62 offset1:63\n"
"v_mfma_f32_16x16x32_bf16 v[132:135], a[24:27], a[76:79], v[132:135]\n"
"ds_read2st64_b64 a[64:67], v19 offset0:64 offset1:65\n"
"v_mfma_f32_16x16x32_bf16 v[136:139], a[24:27], a[80:83], v[136:139]\n"
"v_mfma_f32_16x16x32_bf16 v[140:143], a[24:27], a[84:87], v[140:143]\n"
"buffer_load_dwordx4 v1, s[44:47], s74 offen offset:1024 sc1 lds\n"
"v_mfma_f32_16x16x32_bf16 v[156:159], a[28:31], a[84:87], v[156:159]\n"
"ds_read2st64_b64 a[68:71], v19 offset0:66 offset1:67\n"
"v_mfma_f32_16x16x32_bf16 v[152:155], a[28:31], a[80:83], v[152:155]\n"
"v_mfma_f32_16x16x32_bf16 v[148:151], a[28:31], a[76:79], v[148:151]\n"
"v_mfma_f32_16x16x32_bf16 v[144:147], a[28:31], a[72:75], v[144:147]\n"
"v_mfma_f32_16x16x32_bf16 v[160:163], a[32:35], a[72:75], v[160:163]\n"
"s_mov_b64 exec, 0xffff\n"
"buffer_load_dwordx4 v1, s[44:47], s74 offen offset:2048 sc1 lds\n"
"s_mov_b64 exec, -1\n"
"v_mfma_f32_16x16x32_bf16 v[164:167], a[32:35], a[76:79], v[164:167]\n"
"s_add_u32 s74, s74, 36864\n"
"v_mfma_f32_16x16x32_bf16 v[168:171], a[32:35], a[80:83], v[168:171]\n"
"v_mfma_f32_16x16x32_bf16 v[172:175], a[32:35], a[84:87], v[172:175]\n"
"s_waitcnt lgkmcnt(0)\n"
"v_mfma_f32_16x16x32_bf16 v[32:35], a[36:39], a[88:91], v[32:35]\n"
"s_waitcnt vmcnt(28)\n"
"s_barrier\n"
"v_mfma_f32_16x16x32_bf16 v[36:39], a[36:39], a[92:95], v[36:39]\n"
"s_add_u32 m0, s2, 76800\n"
"ds_read2st64_b64 a[0:3], v20 offset0:0 offset1:1\n"
"v_mfma_f32_16x16x32_bf16 v[40:43], a[36:39], a[96:99], v[40:43]\n"
"buffer_load_dwordx4 v8, s[40:43], s73 offen offset:0 lds\n"
"v_mfma_f32_16x16x32_bf16 v[44:47], a[36:39], a[100:103], v[44:47]\n"
"ds_read_b64_tr_b16 a[72:73], v16 offset:0\n"
"v_mfma_f32_16x16x32_bf16 v[60:63], a[40:43], a[100:103], v[60:63]\n"
"ds_read_b64_tr_b16 a[74:75], v16 offset:8192\n"
"v_mfma_f32_16x16x32_bf16 v[56:59], a[40:43], a[96:99], v[56:59]\n"
"ds_read_b64_tr_b16 a[76:77], v17 offset:0\n"
"v_mfma_f32_16x16x32_bf16 v[52:55], a[40:43], a[92:95], v[52:55]\n"
"v_mfma_f32_16x16x32_bf16 v[48:51], a[40:43], a[88:91], v[48:51]\n"
"buffer_load_dwordx4 v9, s[40:43], s73 offen offset:1024 lds\n"
"v_mfma_f32_16x16x32_bf16 v[64:67], a[44:47], a[88:91], v[64:67]\n"
"ds_read_b64_tr_b16 a[78:79], v17 offset:8192\n"
"v_mfma_f32_16x16x32_bf16 v[68:71], a[44:47], a[92:95], v[68:71]\n"
"ds_read_b64_tr_b16 a[80:81], v16 offset:512\n"
"v_mfma_f32_16x16x32_bf16 v[72:75], a[44:47], a[96:99], v[72:75]\n"
"ds_read_b64_tr_b16 a[82:83], v16 offset:8704\n"
"v_mfma_f32_16x16x32_bf16 v[76:79], a[44:47], a[100:103], v[76:79]\n"
"v_mfma_f32_16x16x32_bf16 v[92:95], a[48:51], a[100:103], v[92:95]\n"
"buffer_load_dwordx4 v10, s[40:43], s73 offen offset:2048 lds\n"
"v_mfma_f32_16x16x32_bf16 v[88:91], a[48:51], a[96:99], v[88:91]\n"
"ds_read_b64_tr_b16 a[84:85], v17 offset:512\n"
"v_mfma_f32_16x16x32_bf16 v[84:87], a[48:51], a[92:95], v[84:87]\n"
"ds_read_b64_tr_b16 a[86:87], v17 offset:8704\n"
"v_mfma_f32_16x16x32_bf16 v[80:83], a[48:51], a[88:91], v[80:83]\n"
"ds_read2st64_b64 a[4:7], v20 offset0:2 offset1:3\n"
"v_mfma_f32_16x16x32_bf16 v[96:99], a[52:55], a[88:91], v[96:99]\n"
"v_mfma_f32_16x16x32_bf16 v[100:103], a[52:55], a[92:95], v[100:103]\n"
"buffer_load_dwordx4 v11, s[40:43], s73 offen offset:3072 lds\n"
"v_mfma_f32_16x16x32_bf16 v[104:107], a[52:55], a[96:99], v[104:107]\n"
"ds_read2st64_b64 a[8:11], v20 offset0:4 offset1:5\n"
"v_mfma_f32_16x16x32_bf16 v[108:111], a[52:55], a[100:103], v[108:111]\n"
"ds_read2st64_b64 a[12:15], v20 offset0:6 offset1:7\n"
"v_mfma_f32_16x16x32_bf16 v[124:127], a[56:59], a[100:103], v[124:127]\n"
"ds_read2st64_b64 a[16:19], v20 offset0:8 offset1:9\n"
"v_mfma_f32_16x16x32_bf16 v[120:123], a[56:59], a[96:99], v[120:123]\n"
"s_add_u32 s73, s73, 131072\n"
"s_add_u32 m0, s79, 76800\n"
"v_mfma_f32_16x16x32_bf16 v[116:119], a[56:59], a[92:95], v[116:119]\n"
"buffer_load_dwordx4 v1, s[44:47], s74 offen offset:0 sc1 lds\n"
"v_mfma_f32_16x16x32_bf16 v[112:115], a[56:59], a[88:91], v[112:115]\n"
"ds_read2st64_b64 a[20:23], v20 offset0:10 offset1:11\n"
"v_mfma_f32_16x16x32_bf16 v[128:131], a[60:63], a[88:91], v[128:131]\n"
"ds_read2st64_b64 a[24:27], v20 offset0:12 offset1:13\n"
"v_mfma_f32_16x16x32_bf16 v[132:135], a[60:63], a[92:95], v[132:135]\n"
"ds_read2st64_b64 a[28:31], v20 offset0:14 offset1:15\n"
"v_mfma_f32_16x16x32_bf16 v[136:139], a[60:63], a[96:99], v[136:139]\n"
"v_mfma_f32_16x16x32_bf16 v[140:143], a[60:63], a[100:103], v[140:143]\n"
"buffer_load_dwordx4 v1, s[44:47], s74 offen offset:1024 sc1 lds\n"
"v_mfma_f32_16x16x32_bf16 v[156:159], a[64:67], a[100:103], v[156:159]\n"
"ds_read2st64_b64 a[32:35], v20 offset0:16 offset1:17\n"
"v_mfma_f32_16x16x32_bf16 v[152:155], a[64:67], a[96:99], v[152:155]\n"
"v_mfma_f32_16x16x32_bf16 v[148:151], a[64:67], a[92:95], v[148:151]\n"
"v_mfma_f32_16x16x32_bf16 v[144:147], a[64:67], a[88:91], v[144:147]\n"
"v_mfma_f32_16x16x32_bf16 v[160:163], a[68:71], a[88:91], v[160:163]\n"
"s_mov_b64 exec, 0xffff\n"
"buffer_load_dwordx4 v1, s[44:47], s74 offen offset:2048 sc1 lds\n"
"s_mov_b64 exec, -1\n"
"v_mfma_f32_16x16x32_bf16 v[164:167], a[68:71], a[92:95], v[164:167]\n"
"s_add_u32 s74, s74, 36864\n"
"v_mfma_f32_16x16x32_bf16 v[168:171], a[68:71], a[96:99], v[168:171]\n"
"v_mfma_f32_16x16x32_bf16 v[172:175], a[68:71], a[100:103], v[172:175]\n"
"s_waitcnt lgkmcnt(0)\n"
"v_mfma_f32_16x16x32_bf16 v[32:35], a[0:3], a[72:75], v[32:35]\n"
"s_waitcnt vmcnt(28)\n"
"s_barrier\n"
"v_mfma_f32_16x16x32_bf16 v[36:39], a[0:3], a[76:79], v[36:39]\n"
"s_add_u32 m0, s2, 102400\n"
"ds_read2st64_b64 a[36:39], v20 offset0:50 offset1:51\n"
"v_mfma_f32_16x16x32_bf16 v[40:43], a[0:3], a[80:83], v[40:43]\n"
"buffer_load_dwordx4 v8, s[40:43], s73 offen offset:0 lds\n"
"v_mfma_f32_16x16x32_bf16 v[44:47], a[0:3], a[84:87], v[44:47]\n"
"ds_read_b64_tr_b16 a[88:89], v16 offset:25600\n"
"v_mfma_f32_16x16x32_bf16 v[60:63], a[4:7], a[84:87], v[60:63]\n"
"ds_read_b64_tr_b16 a[90:91], v16 offset:33792\n"
"v_mfma_f32_16x16x32_bf16 v[56:59], a[4:7], a[80:83], v[56:59]\n"
"ds_read_b64_tr_b16 a[92:93], v17 offset:25600\n"
"v_mfma_f32_16x16x32_bf16 v[52:55], a[4:7], a[76:79], v[52:55]\n"
"v_mfma_f32_16x16x32_bf16 v[48:51], a[4:7], a[72:75], v[48:51]\n"
"buffer_load_dwordx4 v9, s[40:43], s73 offen offset:1024 lds\n"
"v_mfma_f32_16x16x32_bf16 v[64:67], a[8:11], a[72:75], v[64:67]\n"
"ds_read_b64_tr_b16 a[94:95], v17 offset:33792\n"
"v_mfma_f32_16x16x32_bf16 v[68:71], a[8:11], a[76:79], v[68:71]\n"
"ds_read_b64_tr_b16 a[96:97], v16 offset:26112\n"
"v_mfma_f32_16x16x32_bf16 v[72:75], a[8:11], a[80:83], v[72:75]\n"
"ds_read_b64_tr_b16 a[98:99], v16 offset:34304\n"
"v_mfma_f32_16x16x32_bf16 v[76:79], a[8:11], a[84:87], v[76:79]\n"
"v_mfma_f32_16x16x32_bf16 v[92:95], a[12:15], a[84:87], v[92:95]\n"
"buffer_load_dwordx4 v10, s[40:43], s73 offen offset:2048 lds\n"
"v_mfma_f32_16x16x32_bf16 v[88:91], a[12:15], a[80:83], v[88:91]\n"
"ds_read_b64_tr_b16 a[100:101], v17 offset:26112\n"
"v_mfma_f32_16x16x32_bf16 v[84:87], a[12:15], a[76:79], v[84:87]\n"
"ds_read_b64_tr_b16 a[102:103], v17 offset:34304\n"
"v_mfma_f32_16x16x32_bf16 v[80:83], a[12:15], a[72:75], v[80:83]\n"
"ds_read2st64_b64 a[40:43], v20 offset0:52 offset1:53\n"
"v_mfma_f32_16x16x32_bf16 v[96:99], a[16:19], a[72:75], v[96:99]\n"
"v_mfma_f32_16x16x32_bf16 v[100:103], a[16:19], a[76:79], v[100:103]\n"
"buffer_load_dwordx4 v11, s[40:43], s73 offen offset:3072 lds\n"
"v_mfma_f32_16x16x32_bf16 v[104:107], a[16:19], a[80:83], v[104:107]\n"
"ds_read2st64_b64 a[44:47], v20 offset0:54 offset1:55\n"
"v_mfma_f32_16x16x32_bf16 v[108:111], a[16:19], a[84:87], v[108:111]\n"
"ds_read2st64_b64 a[48:51], v20 offset0:56 offset1:57\n"
"v_mfma_f32_16x16x32_bf16 v[124:127], a[20:23], a[84:87], v[124:127]\n"
"ds_read2st64_b64 a[52:55], v20 offset0:58 offset1:59\n"
"v_mfma_f32_16x16x32_bf16 v[120:123], a[20:23], a[80:83], v[120:123]\n"
"s_add_u32 s73, s73, 131072\n"
"s_add_u32 m0, s79, 102400\n"
"v_mfma_f32_16x16x32_bf16 v[116:119], a[20:23], a[76:79], v[116:119]\n"
"buffer_load_dwordx4 v1, s[44:47], s74 offen offset:0 sc1 lds\n"
"v_mfma_f32_16x16x32_bf16 v[112:115], a[20:23], a[72:75], v[112:115]\n"
"ds_read2st64_b64 a[56:59], v20 offset0:60 offset1:61\n"
"v_mfma_f32_16x16x32_bf16 v[128:131], a[24:27], a[72:75], v[128:131]\n"
"ds_read2st64_b64 a[60:63], v20 offset0:62 offset1:63\n"
"v_mfma_f32_16x16x32_bf16 v[132:135], a[24:27], a[76:79], v[132:135]\n"
"ds_read2st64_b64 a[64:67], v20 offset0:64 offset1:65\n"
"v_mfma_f32_16x16x32_bf16 v[136:139], a[24:27], a[80:83], v[136:139]\n"
"v_mfma_f32_16x16x32_bf16 v[140:143], a[24:27], a[84:87], v[140:143]\n"
"buffer_load_dwordx4 v1, s[44:47], s74 offen offset:1024 sc1 lds\n"
"v_mfma_f32_16x16x32_bf16 v[156:159], a[28:31], a[84:87], v[156:159]\n"
"ds_read2st64_b64 a[68:71], v20 offset0:66 offset1:67\n"
"v_mfma_f32_16x16x32_bf16 v[152:155], a[28:31], a[80:83], v[152:155]\n"
"v_mfma_f32_16x16x32_bf16 v[148:151], a[28:31], a[76:79], v[148:151]\n"
"v_mfma_f32_16x16x32_bf16 v[144:147], a[28:31], a[72:75], v[144:147]\n"
"v_mfma_f32_16x16x32_bf16 v[160:163], a[32:35], a[72:75], v[160:163]\n"
"s_mov_b64 exec, 0xffff\n"
"buffer_load_dwordx4 v1, s[44:47], s74 offen offset:2048 sc1 lds\n"
"s_mov_b64 exec, -1\n"
"v_mfma_f32_16x16x32_bf16 v[164:167], a[32:35], a[76:79], v[164:167]\n"
"s_add_u32 s74, s74, 36864\n"
"v_mfma_f32_16x16x32_bf16 v[168:171], a[32:35], a[80:83], v[168:171]\n"
"v_mfma_f32_16x16x32_bf16 v[172:175], a[32:35], a[84:87], v[172:175]\n"
"s_waitcnt lgkmcnt(0)\n"
"v_mfma_f32_16x16x32_bf16 v[32:35], a[36:39], a[88:91], v[32:35]\n"
"s_waitcnt vmcnt(28)\n"
"s_barrier\n"
"v_mfma_f32_16x16x32_bf16 v[36:39], a[36:39], a[92:95], v[36:39]\n"
"s_add_u32 m0, s2, 128000\n"
"ds_read2st64_b64 a[0:3], v18 offset0:0 offset1:1\n"
"v_mfma_f32_16x16x32_bf16 v[40:43], a[36:39], a[96:99], v[40:43]\n"
"buffer_load_dwordx4 v8, s[40:43], s73 offen offset:0 lds\n"
"v_mfma_f32_16x16x32_bf16 v[44:47], a[36:39], a[100:103], v[44:47]\n"
"ds_read_b64_tr_b16 a[72:73], v12 offset:0\n"
"v_mfma_f32_16x16x32_bf16 v[60:63], a[40:43], a[100:103], v[60:63]\n"
"ds_read_b64_tr_b16 a[74:75], v12 offset:8192\n"
"v_mfma_f32_16x16x32_bf16 v[56:59], a[40:43], a[96:99], v[56:59]\n"
"ds_read_b64_tr_b16 a[76:77], v13 offset:0\n"
"v_mfma_f32_16x16x32_bf16 v[52:55], a[40:43], a[92:95], v[52:55]\n"
"v_mfma_f32_16x16x32_bf16 v[48:51], a[40:43], a[88:91], v[48:51]\n"
"buffer_load_dwordx4 v9, s[40:43], s73 offen offset:1024 lds\n"
"v_mfma_f32_16x16x32_bf16 v[64:67], a[44:47], a[88:91], v[64:67]\n"
"ds_read_b64_tr_b16 a[78:79], v13 offset:8192\n"
"v_mfma_f32_16x16x32_bf16 v[68:71], a[44:47], a[92:95], v[68:71]\n"
"ds_read_b64_tr_b16 a[80:81], v12 offset:512\n"
"v_mfma_f32_16x16x32_bf16 v[72:75], a[44:47], a[96:99], v[72:75]\n"
"ds_read_b64_tr_b16 a[82:83], v12 offset:8704\n"
"v_mfma_f32_16x16x32_bf16 v[76:79], a[44:47], a[100:103], v[76:79]\n"
"v_mfma_f32_16x16x32_bf16 v[92:95], a[48:51], a[100:103], v[92:95]\n"
"buffer_load_dwordx4 v10, s[40:43], s73 offen offset:2048 lds\n"
"v_mfma_f32_16x16x32_bf16 v[88:91], a[48:51], a[96:99], v[88:91]\n"
"ds_read_b64_tr_b16 a[84:85], v13 offset:512\n"
"v_mfma_f32_16x16x32_bf16 v[84:87], a[48:51], a[92:95], v[84:87]\n"
"ds_read_b64_tr_b16 a[86:87], v13 offset:8704\n"
"v_mfma_f32_16x16x32_bf16 v[80:83], a[48:51], a[88:91], v[80:83]\n"
"ds_read2st64_b64 a[4:7], v18 offset0:2 offset1:3\n"
"v_mfma_f32_16x16x32_bf16 v[96:99], a[52:55], a[88:91], v[96:99]\n"
"v_mfma_f32_16x16x32_bf16 v[100:103], a[52:55], a[92:95], v[100:103]\n"
"buffer_load_dwordx4 v11, s[40:43], s73 offen offset:3072 lds\n"
"v_mfma_f32_16x16x32_bf16 v[104:107], a[52:55], a[96:99], v[104:107]\n"
"ds_read2st64_b64 a[8:11], v18 offset0:4 offset1:5\n"
"v_mfma_f32_16x16x32_bf16 v[108:111], a[52:55], a[100:103], v[108:111]\n"
"ds_read2st64_b64 a[12:15], v18 offset0:6 offset1:7\n"
"v_mfma_f32_16x16x32_bf16 v[124:127], a[56:59], a[100:103], v[124:127]\n"
"ds_read2st64_b64 a[16:19], v18 offset0:8 offset1:9\n"
"v_mfma_f32_16x16x32_bf16 v[120:123], a[56:59], a[96:99], v[120:123]\n"
"s_add_u32 s73, s73, 131072\n"
"s_add_u32 m0, s79, 128000\n"
"v_mfma_f32_16x16x32_bf16 v[116:119], a[56:59], a[92:95], v[116:119]\n"
"buffer_load_dwordx4 v1, s[44:47], s74 offen offset:0 sc1 lds\n"
"v_mfma_f32_16x16x32_bf16 v[112:115], a[56:59], a[88:91], v[112:115]\n"
"ds_read2st64_b64 a[20:23], v18 offset0:10 offset1:11\n"
"v_mfma_f32_16x16x32_bf16 v[128:131], a[60:63], a[88:91], v[128:131]\n"
"ds_read2st64_b64 a[24:27], v18 offset0:12 offset1:13\n"
"v_mfma_f32_16x16x32_bf16 v[132:135], a[60:63], a[92:95], v[132:135]\n"
"ds_read2st64_b64 a[28:31], v18 offset0:14 offset1:15\n"
"v_mfma_f32_16x16x32_bf16 v[136:139], a[60:63], a[96:99], v[136:139]\n"
"v_mfma_f32_16x16x32_bf16 v[140:143], a[60:63], a[100:103], v[140:143]\n"
"buffer_load_dwordx4 v1, s[44:47], s74 offen offset:1024 sc1 lds\n"
"v_mfma_f32_16x16x32_bf16 v[156:159], a[64:67], a[100:103], v[156:159]\n"
"ds_read2st64_b64 a[32:35], v18 offset0:16 offset1:17\n"
"v_mfma_f32_16x16x32_bf16 v[152:155], a[64:67], a[96:99], v[152:155]\n"
"v_mfma_f32_16x16x32_bf16 v[148:151], a[64:67], a[92:95], v[148:151]\n"
"v_mfma_f32_16x16x32_bf16 v[144:147], a[64:67], a[88:91], v[144:147]\n"
"v_mfma_f32_16x16x32_bf16 v[160:163], a[68:71], a[88:91], v[160:163]\n"
"s_mov_b64 exec, 0xffff\n"
"buffer_load_dwordx4 v1, s[44:47], s74 offen offset:2048 sc1 lds\n"
"s_mov_b64 exec, -1\n"
"v_mfma_f32_16x16x32_bf16 v[164:167], a[68:71], a[92:95], v[164:167]\n"
"s_add_u32 s74, s74, 36864\n"
"v_mfma_f32_16x16x32_bf16 v[168:171], a[68:71], a[96:99], v[168:171]\n"
"v_mfma_f32_16x16x32_bf16 v[172:175], a[68:71], a[100:103], v[172:175]\n"
"s_waitcnt lgkmcnt(0)\n"
"v_mfma_f32_16x16x32_bf16 v[32:35], a[0:3], a[72:75], v[32:35]\n"
"s_waitcnt vmcnt(28)\n"
"s_barrier\n"
"v_mfma_f32_16x16x32_bf16 v[36:39], a[0:3], a[76:79], v[36:39]\n"
"s_add_u32 m0, s2, 0\n"
"ds_read2st64_b64 a[36:39], v18 offset0:50 offset1:51\n"
"v_mfma_f32_16x16x32_bf16 v[40:43], a[0:3], a[80:83], v[40:43]\n"
"buffer_load_dwordx4 v8, s[40:43], s73 offen offset:0 lds\n"
"v_mfma_f32_16x16x32_bf16 v[44:47], a[0:3], a[84:87], v[44:47]\n"
"s_lshr_b32 s68, s11, 1\n"
"ds_read_b64_tr_b16 a[88:89], v12 offset:25600\n"
"v_mfma_f32_16x16x32_bf16 v[60:63], a[4:7], a[84:87], v[60:63]\n"
"ds_read_b64_tr_b16 a[90:91], v12 offset:33792\n"
"s_lshl1_add_u32 s5, s70, s68\n"
"v_mfma_f32_16x16x32_bf16 v[56:59], a[4:7], a[80:83], v[56:59]\n"
"ds_read_b64_tr_b16 a[92:93], v13 offset:25600\n"
"v_mfma_f32_16x16x32_bf16 v[52:55], a[4:7], a[76:79], v[52:55]\n"
"s_lshl_b32 s5, s5, 7\n"
"v_mfma_f32_16x16x32_bf16 v[48:51], a[4:7], a[72:75], v[48:51]\n"
"buffer_load_dwordx4 v9, s[40:43], s73 offen offset:1024 lds\n"
"v_mfma_f32_16x16x32_bf16 v[64:67], a[8:11], a[72:75], v[64:67]\n"
"ds_read_b64_tr_b16 a[94:95], v13 offset:33792\n"
"v_mfma_f32_16x16x32_bf16 v[68:71], a[8:11], a[76:79], v[68:71]\n"
"ds_read_b64_tr_b16 a[96:97], v12 offset:26112\n"
"s_add_u32 s91, s5, 0x2ae0000\n"
"v_mfma_f32_16x16x32_bf16 v[72:75], a[8:11], a[80:83], v[72:75]\n"
"ds_read_b64_tr_b16 a[98:99], v12 offset:34304\n"
"v_mfma_f32_16x16x32_bf16 v[76:79], a[8:11], a[84:87], v[76:79]\n"
"s_mov_b32 s98, 0x5\n"
"v_mfma_f32_16x16x32_bf16 v[92:95], a[12:15], a[84:87], v[92:95]\n"
"buffer_load_dwordx4 v10, s[40:43], s73 offen offset:2048 lds\n"
"v_mfma_f32_16x16x32_bf16 v[88:91], a[12:15], a[80:83], v[88:91]\n"
"ds_read_b64_tr_b16 a[100:101], v13 offset:26112\n"
"v_mfma_f32_16x16x32_bf16 v[84:87], a[12:15], a[76:79], v[84:87]\n"
"s_cmp_eq_u32 s69, 1\n"
"s_cselect_b32 s98, 0x50, s98\n"
"v_mfma_f32_16x16x32_bf16 v[80:83], a[12:15], a[72:75], v[80:83]\n"
"ds_read_b64_tr_b16 a[102:103], v13 offset:34304\n"
"ds_read2st64_b64 a[40:43], v18 offset0:52 offset1:53\n"
"v_mfma_f32_16x16x32_bf16 v[96:99], a[16:19], a[72:75], v[96:99]\n"
"s_cmp_eq_u32 s69, 2\n"
"s_cselect_b32 s98, 0x500, s98\n"
"v_mfma_f32_16x16x32_bf16 v[100:103], a[16:19], a[76:79], v[100:103]\n"
"buffer_load_dwordx4 v11, s[40:43], s73 offen offset:3072 lds\n"
"v_mfma_f32_16x16x32_bf16 v[104:107], a[16:19], a[80:83], v[104:107]\n"
"ds_read2st64_b64 a[44:47], v18 offset0:54 offset1:55\n"
"v_mfma_f32_16x16x32_bf16 v[108:111], a[16:19], a[84:87], v[108:111]\n"
"s_cmp_eq_u32 s69, 3\n"
"s_cselect_b32 s98, 0x5000, s98\n"
"v_mfma_f32_16x16x32_bf16 v[124:127], a[20:23], a[84:87], v[124:127]\n"
"ds_read2st64_b64 a[48:51], v18 offset0:56 offset1:57\n"
"ds_read2st64_b64 a[52:55], v18 offset0:58 offset1:59\n"
"v_mfma_f32_16x16x32_bf16 v[120:123], a[20:23], a[80:83], v[120:123]\n"
"s_add_u32 s73, s73, 131072\n"
"s_add_u32 m0, s79, 0\n"
"v_mfma_f32_16x16x32_bf16 v[116:119], a[20:23], a[76:79], v[116:119]\n"
"buffer_load_dwordx4 v1, s[44:47], s74 offen offset:0 sc1 lds\n"
"v_mfma_f32_16x16x32_bf16 v[112:115], a[20:23], a[72:75], v[112:115]\n"
"s_mov_b32 s8, 0x5\n"
"ds_read2st64_b64 a[56:59], v18 offset0:60 offset1:61\n"
"v_mfma_f32_16x16x32_bf16 v[128:131], a[24:27], a[72:75], v[128:131]\n"
"s_cmp_eq_u32 s69, 1\n"
"s_cselect_b32 s8, 0x50, s8\n"
"v_mfma_f32_16x16x32_bf16 v[132:135], a[24:27], a[76:79], v[132:135]\n"
"ds_read2st64_b64 a[60:63], v18 offset0:62 offset1:63\n"
"ds_read2st64_b64 a[64:67], v18 offset0:64 offset1:65\n"
"v_mfma_f32_16x16x32_bf16 v[136:139], a[24:27], a[80:83], v[136:139]\n"
"s_cmp_eq_u32 s69, 2\n"
"s_cselect_b32 s8, 0x500, s8\n"
"v_mfma_f32_16x16x32_bf16 v[140:143], a[24:27], a[84:87], v[140:143]\n"
"buffer_load_dwordx4 v1, s[44:47], s74 offen offset:1024 sc1 lds\n"
"v_mfma_f32_16x16x32_bf16 v[156:159], a[28:31], a[84:87], v[156:159]\n"
"ds_read2st64_b64 a[68:71], v18 offset0:66 offset1:67\n"
"v_mfma_f32_16x16x32_bf16 v[152:155], a[28:31], a[80:83], v[152:155]\n"
"s_cmp_eq_u32 s69, 3\n"
"s_cselect_b32 s8, 0x5000, s8\n"
"v_mfma_f32_16x16x32_bf16 v[148:151], a[28:31], a[76:79], v[148:151]\n"
"v_mfma_f32_16x16x32_bf16 v[144:147], a[28:31], a[72:75], v[144:147]\n"
"s_mov_b32 s99, 0\n"
"v_mfma_f32_16x16x32_bf16 v[160:163], a[32:35], a[72:75], v[160:163]\n"
"s_mov_b64 exec, 0xffff\n"
"buffer_load_dwordx4 v1, s[44:47], s74 offen offset:2048 sc1 lds\n"
"s_mov_b64 exec, -1\n"
"v_mfma_f32_16x16x32_bf16 v[164:167], a[32:35], a[76:79], v[164:167]\n"
"s_add_u32 s74, s74, 36864\n"
"s_mov_b32 s9, 0\n"
"v_mfma_f32_16x16x32_bf16 v[168:171], a[32:35], a[80:83], v[168:171]\n"
"v_mfma_f32_16x16x32_bf16 v[172:175], a[32:35], a[84:87], v[172:175]\n"
"s_waitcnt lgkmcnt(0)\n"
"v_mfma_f32_16x16x32_bf16 v[32:35], a[36:39], a[88:91], v[32:35]\n"
"s_waitcnt vmcnt(28)\n"
"s_barrier\n"
"v_mfma_f32_16x16x32_bf16 v[36:39], a[36:39], a[92:95], v[36:39]\n"
"s_add_u32 m0, s2, 25600\n"
"ds_read2st64_b64 a[0:3], v19 offset0:0 offset1:1\n"
"v_mfma_f32_16x16x32_bf16 v[40:43], a[36:39], a[96:99], v[40:43]\n"
"buffer_load_dwordx4 v8, s[40:43], s73 offen offset:0 lds\n"
"v_mfma_f32_16x16x32_bf16 v[44:47], a[36:39], a[100:103], v[44:47]\n"
"ds_read_b64_tr_b16 a[72:73], v14 offset:0\n"
"v_mfma_f32_16x16x32_bf16 v[60:63], a[40:43], a[100:103], v[60:63]\n"
"ds_read_b64_tr_b16 a[74:75], v14 offset:8192\n"
"v_mfma_f32_16x16x32_bf16 v[56:59], a[40:43], a[96:99], v[56:59]\n"
"ds_read_b64_tr_b16 a[76:77], v15 offset:0\n"
"v_mfma_f32_16x16x32_bf16 v[52:55], a[40:43], a[92:95], v[52:55]\n"
"v_mfma_f32_16x16x32_bf16 v[48:51], a[40:43], a[88:91], v[48:51]\n"
"buffer_load_dwordx4 v9, s[40:43], s73 offen offset:1024 lds\n"
"v_mfma_f32_16x16x32_bf16 v[64:67], a[44:47], a[88:91], v[64:67]\n"
"ds_read_b64_tr_b16 a[78:79], v15 offset:8192\n"
"v_mfma_f32_16x16x32_bf16 v[68:71], a[44:47], a[92:95], v[68:71]\n"
"ds_read_b64_tr_b16 a[80:81], v14 offset:512\n"
"v_mfma_f32_16x16x32_bf16 v[72:75], a[44:47], a[96:99], v[72:75]\n"
"ds_read_b64_tr_b16 a[82:83], v14 offset:8704\n"
"v_mfma_f32_16x16x32_bf16 v[76:79], a[44:47], a[100:103], v[76:79]\n"
"v_mfma_f32_16x16x32_bf16 v[92:95], a[48:51], a[100:103], v[92:95]\n"
"buffer_load_dwordx4 v10, s[40:43], s73 offen offset:2048 lds\n"
"v_mfma_f32_16x16x32_bf16 v[88:91], a[48:51], a[96:99], v[88:91]\n"
"ds_read_b64_tr_b16 a[84:85], v15 offset:512\n"
"v_mfma_f32_16x16x32_bf16 v[84:87], a[48:51], a[92:95], v[84:87]\n"
"ds_read_b64_tr_b16 a[86:87], v15 offset:8704\n"
"v_mfma_f32_16x16x32_bf16 v[80:83], a[48:51], a[88:91], v[80:83]\n"
"ds_read2st64_b64 a[4:7], v19 offset0:2 offset1:3\n"
"v_mfma_f32_16x16x32_bf16 v[96:99], a[52:55], a[88:91], v[96:99]\n"
"v_mfma_f32_16x16x32_bf16 v[100:103], a[52:55], a[92:95], v[100:103]\n"
"buffer_load_dwordx4 v11, s[40:43], s73 offen offset:3072 lds\n"
"v_mfma_f32_16x16x32_bf16 v[104:107], a[52:55], a[96:99], v[104:107]\n"
"ds_read2st64_b64 a[8:11], v19 offset0:4 offset1:5\n"
"v_mfma_f32_16x16x32_bf16 v[108:111], a[52:55], a[100:103], v[108:111]\n"
"ds_read2st64_b64 a[12:15], v19 offset0:6 offset1:7\n"
"v_mfma_f32_16x16x32_bf16 v[124:127], a[56:59], a[100:103], v[124:127]\n"
"ds_read2st64_b64 a[16:19], v19 offset0:8 offset1:9\n"
"v_mfma_f32_16x16x32_bf16 v[120:123], a[56:59], a[96:99], v[120:123]\n"
"s_add_u32 s73, s73, 131072\n"
"s_add_u32 m0, s79, 25600\n"
"v_mfma_f32_16x16x32_bf16 v[116:119], a[56:59], a[92:95], v[116:119]\n"
"buffer_load_dwordx4 v1, s[44:47], s74 offen offset:0 sc1 lds\n"
"v_mfma_f32_16x16x32_bf16 v[112:115], a[56:59], a[88:91], v[112:115]\n"
"ds_read2st64_b64 a[20:23], v19 offset0:10 offset1:11\n"
"v_mfma_f32_16x16x32_bf16 v[128:131], a[60:63], a[88:91], v[128:131]\n"
"ds_read2st64_b64 a[24:27], v19 offset0:12 offset1:13\n"
"v_mfma_f32_16x16x32_bf16 v[132:135], a[60:63], a[92:95], v[132:135]\n"
"ds_read2st64_b64 a[28:31], v19 offset0:14 offset1:15\n"
"v_mfma_f32_16x16x32_bf16 v[136:139], a[60:63], a[96:99], v[136:139]\n"
"v_mfma_f32_16x16x32_bf16 v[140:143], a[60:63], a[100:103], v[140:143]\n"
"buffer_load_dwordx4 v1, s[44:47], s74 offen offset:1024 sc1 lds\n"
"v_mfma_f32_16x16x32_bf16 v[156:159], a[64:67], a[100:103], v[156:159]\n"
"ds_read2st64_b64 a[32:35], v19 offset0:16 offset1:17\n"
"v_mfma_f32_16x16x32_bf16 v[152:155], a[64:67], a[96:99], v[152:155]\n"
"v_mfma_f32_16x16x32_bf16 v[148:151], a[64:67], a[92:95], v[148:151]\n"
"v_mfma_f32_16x16x32_bf16 v[144:147], a[64:67], a[88:91], v[144:147]\n"
"v_mfma_f32_16x16x32_bf16 v[160:163], a[68:71], a[88:91], v[160:163]\n"
"s_mov_b64 exec, 0xffff\n"
"buffer_load_dwordx4 v1, s[44:47], s74 offen offset:2048 sc1 lds\n"
"s_mov_b64 exec, -1\n"
"v_mfma_f32_16x16x32_bf16 v[164:167], a[68:71], a[92:95], v[164:167]\n"
"s_add_u32 s74, s74, 36864\n"
"v_mfma_f32_16x16x32_bf16 v[168:171], a[68:71], a[96:99], v[168:171]\n"
"buffer_load_dwordx2 v[248:249], v251, s[36:39], s91 offen sc1\n"
"v_mfma_f32_16x16x32_bf16 v[172:175], a[68:71], a[100:103], v[172:175]\n"
"s_waitcnt lgkmcnt(0)\n"
"v_mfma_f32_16x16x32_bf16 v[32:35], a[0:3], a[72:75], v[32:35]\n"
"s_waitcnt vmcnt(29)\n"
"s_barrier\n"
"v_mfma_f32_16x16x32_bf16 v[36:39], a[0:3], a[76:79], v[36:39]\n"
"s_add_u32 m0, s2, 51200\n"
"ds_read2st64_b64 a[36:39], v19 offset0:50 offset1:51\n"
"v_mfma_f32_16x16x32_bf16 v[40:43], a[0:3], a[80:83], v[40:43]\n"
"buffer_load_dwordx4 v8, s[40:43], s73 offen offset:0 lds\n"
"v_mfma_f32_16x16x32_bf16 v[44:47], a[0:3], a[84:87], v[44:47]\n"
"ds_read_b64_tr_b16 a[88:89], v14 offset:25600\n"
"v_mfma_f32_16x16x32_bf16 v[60:63], a[4:7], a[84:87], v[60:63]\n"
"ds_read_b64_tr_b16 a[90:91], v14 offset:33792\n"
"v_mfma_f32_16x16x32_bf16 v[56:59], a[4:7], a[80:83], v[56:59]\n"
"ds_read_b64_tr_b16 a[92:93], v15 offset:25600\n"
"v_mfma_f32_16x16x32_bf16 v[52:55], a[4:7], a[76:79], v[52:55]\n"
"v_mfma_f32_16x16x32_bf16 v[48:51], a[4:7], a[72:75], v[48:51]\n"
"buffer_load_dwordx4 v9, s[40:43], s73 offen offset:1024 lds\n"
"v_mfma_f32_16x16x32_bf16 v[64:67], a[8:11], a[72:75], v[64:67]\n"
"ds_read_b64_tr_b16 a[94:95], v15 offset:33792\n"
"v_mfma_f32_16x16x32_bf16 v[68:71], a[8:11], a[76:79], v[68:71]\n"
"ds_read_b64_tr_b16 a[96:97], v14 offset:26112\n"
"v_mfma_f32_16x16x32_bf16 v[72:75], a[8:11], a[80:83], v[72:75]\n"
"ds_read_b64_tr_b16 a[98:99], v14 offset:34304\n"
"v_mfma_f32_16x16x32_bf16 v[76:79], a[8:11], a[84:87], v[76:79]\n"
"v_mfma_f32_16x16x32_bf16 v[92:95], a[12:15], a[84:87], v[92:95]\n"
"buffer_load_dwordx4 v10, s[40:43], s73 offen offset:2048 lds\n"
"v_mfma_f32_16x16x32_bf16 v[88:91], a[12:15], a[80:83], v[88:91]\n"
"ds_read_b64_tr_b16 a[100:101], v15 offset:26112\n"
"v_mfma_f32_16x16x32_bf16 v[84:87], a[12:15], a[76:79], v[84:87]\n"
"ds_read_b64_tr_b16 a[102:103], v15 offset:34304\n"
"v_mfma_f32_16x16x32_bf16 v[80:83], a[12:15], a[72:75], v[80:83]\n"
"ds_read2st64_b64 a[40:43], v19 offset0:52 offset1:53\n"
"v_mfma_f32_16x16x32_bf16 v[96:99], a[16:19], a[72:75], v[96:99]\n"
"v_mfma_f32_16x16x32_bf16 v[100:103], a[16:19], a[76:79], v[100:103]\n"
"buffer_load_dwordx4 v11, s[40:43], s73 offen offset:3072 lds\n"
"v_mfma_f32_16x16x32_bf16 v[104:107], a[16:19], a[80:83], v[104:107]\n"
"ds_read2st64_b64 a[44:47], v19 offset0:54 offset1:55\n"
"v_mfma_f32_16x16x32_bf16 v[108:111], a[16:19], a[84:87], v[108:111]\n"
"ds_read2st64_b64 a[48:51], v19 offset0:56 offset1:57\n"
"v_mfma_f32_16x16x32_bf16 v[124:127], a[20:23], a[84:87], v[124:127]\n"
"ds_read2st64_b64 a[52:55], v19 offset0:58 offset1:59\n"
"v_mfma_f32_16x16x32_bf16 v[120:123], a[20:23], a[80:83], v[120:123]\n"
"s_add_u32 s73, s73, 131072\n"
"s_add_u32 m0, s79, 51200\n"
"v_mfma_f32_16x16x32_bf16 v[116:119], a[20:23], a[76:79], v[116:119]\n"
"buffer_load_dwordx4 v1, s[44:47], s74 offen offset:0 sc1 lds\n"
"v_mfma_f32_16x16x32_bf16 v[112:115], a[20:23], a[72:75], v[112:115]\n"
"ds_read2st64_b64 a[56:59], v19 offset0:60 offset1:61\n"
"v_mfma_f32_16x16x32_bf16 v[128:131], a[24:27], a[72:75], v[128:131]\n"
"ds_read2st64_b64 a[60:63], v19 offset0:62 offset1:63\n"
"v_mfma_f32_16x16x32_bf16 v[132:135], a[24:27], a[76:79], v[132:135]\n"
"ds_read2st64_b64 a[64:67], v19 offset0:64 offset1:65\n"
"v_mfma_f32_16x16x32_bf16 v[136:139], a[24:27], a[80:83], v[136:139]\n"
"v_mfma_f32_16x16x32_bf16 v[140:143], a[24:27], a[84:87], v[140:143]\n"
"buffer_load_dwordx4 v1, s[44:47], s74 offen offset:1024 sc1 lds\n"
"v_mfma_f32_16x16x32_bf16 v[156:159], a[28:31], a[84:87], v[156:159]\n"
"ds_read2st64_b64 a[68:71], v19 offset0:66 offset1:67\n"
"v_mfma_f32_16x16x32_bf16 v[152:155], a[28:31], a[80:83], v[152:155]\n"
"v_mfma_f32_16x16x32_bf16 v[148:151], a[28:31], a[76:79], v[148:151]\n"
"v_mfma_f32_16x16x32_bf16 v[144:147], a[28:31], a[72:75], v[144:147]\n"
"v_mfma_f32_16x16x32_bf16 v[160:163], a[32:35], a[72:75], v[160:163]\n"
"s_mov_b64 exec, 0xffff\n"
"buffer_load_dwordx4 v1, s[44:47], s74 offen offset:2048 sc1 lds\n"
"s_mov_b64 exec, -1\n"
"v_mfma_f32_16x16x32_bf16 v[164:167], a[32:35], a[76:79], v[164:167]\n"
"s_add_u32 s74, s74, 36864\n"
"v_mfma_f32_16x16x32_bf16 v[168:171], a[32:35], a[80:83], v[168:171]\n"
"v_mfma_f32_16x16x32_bf16 v[172:175], a[32:35], a[84:87], v[172:175]\n"
"s_waitcnt lgkmcnt(0)\n"
"v_mfma_f32_16x16x32_bf16 v[32:35], a[36:39], a[88:91], v[32:35]\n"
"s_waitcnt vmcnt(29)\n"
"s_barrier\n"
"v_mfma_f32_16x16x32_bf16 v[36:39], a[36:39], a[92:95], v[36:39]\n"
"s_add_u32 m0, s2, 76800\n"
"ds_read2st64_b64 a[0:3], v20 offset0:0 offset1:1\n"
"v_mfma_f32_16x16x32_bf16 v[40:43], a[36:39], a[96:99], v[40:43]\n"
"buffer_load_dwordx4 v8, s[40:43], s73 offen offset:0 lds\n"
"v_mfma_f32_16x16x32_bf16 v[44:47], a[36:39], a[100:103], v[44:47]\n"
"ds_read_b64_tr_b16 a[72:73], v16 offset:0\n"
"v_mfma_f32_16x16x32_bf16 v[60:63], a[40:43], a[100:103], v[60:63]\n"
"ds_read_b64_tr_b16 a[74:75], v16 offset:8192\n"
"v_mfma_f32_16x16x32_bf16 v[56:59], a[40:43], a[96:99], v[56:59]\n"
"ds_read_b64_tr_b16 a[76:77], v17 offset:0\n"
"v_mfma_f32_16x16x32_bf16 v[52:55], a[40:43], a[92:95], v[52:55]\n"
"v_mfma_f32_16x16x32_bf16 v[48:51], a[40:43], a[88:91], v[48:51]\n"
"buffer_load_dwordx4 v9, s[40:43], s73 offen offset:1024 lds\n"
"v_mfma_f32_16x16x32_bf16 v[64:67], a[44:47], a[88:91], v[64:67]\n"
"ds_read_b64_tr_b16 a[78:79], v17 offset:8192\n"
"v_mfma_f32_16x16x32_bf16 v[68:71], a[44:47], a[92:95], v[68:71]\n"
"ds_read_b64_tr_b16 a[80:81], v16 offset:512\n"
"v_mfma_f32_16x16x32_bf16 v[72:75], a[44:47], a[96:99], v[72:75]\n"
"ds_read_b64_tr_b16 a[82:83], v16 offset:8704\n"
"v_mfma_f32_16x16x32_bf16 v[76:79], a[44:47], a[100:103], v[76:79]\n"
"v_mfma_f32_16x16x32_bf16 v[92:95], a[48:51], a[100:103], v[92:95]\n"
"buffer_load_dwordx4 v10, s[40:43], s73 offen offset:2048 lds\n"
"v_mfma_f32_16x16x32_bf16 v[88:91], a[48:51], a[96:99], v[88:91]\n"
"ds_read_b64_tr_b16 a[84:85], v17 offset:512\n"
"v_mfma_f32_16x16x32_bf16 v[84:87], a[48:51], a[92:95], v[84:87]\n"
"ds_read_b64_tr_b16 a[86:87], v17 offset:8704\n"
"v_mfma_f32_16x16x32_bf16 v[80:83], a[48:51], a[88:91], v[80:83]\n"
"ds_read2st64_b64 a[4:7], v20 offset0:2 offset1:3\n"
"v_mfma_f32_16x16x32_bf16 v[96:99], a[52:55], a[88:91], v[96:99]\n"
"v_mfma_f32_16x16x32_bf16 v[100:103], a[52:55], a[92:95], v[100:103]\n"
"buffer_load_dwordx4 v11, s[40:43], s73 offen offset:3072 lds\n"
"v_mfma_f32_16x16x32_bf16 v[104:107], a[52:55], a[96:99], v[104:107]\n"
"ds_read2st64_b64 a[8:11], v20 offset0:4 offset1:5\n"
"v_mfma_f32_16x16x32_bf16 v[108:111], a[52:55], a[100:103], v[108:111]\n"
"ds_read2st64_b64 a[12:15], v20 offset0:6 offset1:7\n"
"v_mfma_f32_16x16x32_bf16 v[124:127], a[56:59], a[100:103], v[124:127]\n"
"ds_read2st64_b64 a[16:19], v20 offset0:8 offset1:9\n"
"v_mfma_f32_16x16x32_bf16 v[120:123], a[56:59], a[96:99], v[120:123]\n"
"s_add_u32 s73, s73, 131072\n"
"s_add_u32 m0, s79, 76800\n"
"v_mfma_f32_16x16x32_bf16 v[116:119], a[56:59], a[92:95], v[116:119]\n"
"buffer_load_dwordx4 v1, s[44:47], s74 offen offset:0 sc1 lds\n"
"v_mfma_f32_16x16x32_bf16 v[112:115], a[56:59], a[88:91], v[112:115]\n"
"ds_read2st64_b64 a[20:23], v20 offset0:10 offset1:11\n"
"v_mfma_f32_16x16x32_bf16 v[128:131], a[60:63], a[88:91], v[128:131]\n"
"ds_read2st64_b64 a[24:27], v20 offset0:12 offset1:13\n"
"v_mfma_f32_16x16x32_bf16 v[132:135], a[60:63], a[92:95], v[132:135]\n"
"ds_read2st64_b64 a[28:31], v20 offset0:14 offset1:15\n"
"v_mfma_f32_16x16x32_bf16 v[136:139], a[60:63], a[96:99], v[136:139]\n"
"v_mfma_f32_16x16x32_bf16 v[140:143], a[60:63], a[100:103], v[140:143]\n"
"buffer_load_dwordx4 v1, s[44:47], s74 offen offset:1024 sc1 lds\n"
"v_mfma_f32_16x16x32_bf16 v[156:159], a[64:67], a[100:103], v[156:159]\n"
"ds_read2st64_b64 a[32:35], v20 offset0:16 offset1:17\n"
"v_mfma_f32_16x16x32_bf16 v[152:155], a[64:67], a[96:99], v[152:155]\n"
"v_mfma_f32_16x16x32_bf16 v[148:151], a[64:67], a[92:95], v[148:151]\n"
"v_mfma_f32_16x16x32_bf16 v[144:147], a[64:67], a[88:91], v[144:147]\n"
"v_mfma_f32_16x16x32_bf16 v[160:163], a[68:71], a[88:91], v[160:163]\n"
"s_mov_b64 exec, 0xffff\n"
"buffer_load_dwordx4 v1, s[44:47], s74 offen offset:2048 sc1 lds\n"
"s_mov_b64 exec, -1\n"
"v_mfma_f32_16x16x32_bf16 v[164:167], a[68:71], a[92:95], v[164:167]\n"
"s_add_u32 s74, s74, 36864\n"
"v_mfma_f32_16x16x32_bf16 v[168:171], a[68:71], a[96:99], v[168:171]\n"
"v_mfma_f32_16x16x32_bf16 v[172:175], a[68:71], a[100:103], v[172:175]\n"
"s_waitcnt lgkmcnt(0)\n"
"v_mfma_f32_16x16x32_bf16 v[32:35], a[0:3], a[72:75], v[32:35]\n"
"s_waitcnt vmcnt(29)\n"
"s_barrier\n"
"v_mfma_f32_16x16x32_bf16 v[36:39], a[0:3], a[76:79], v[36:39]\n"
"ds_read2st64_b64 a[36:39], v20 offset0:50 offset1:51\n"
"s_mov_b32 s40, s20\n"
"v_mfma_f32_16x16x32_bf16 v[40:43], a[0:3], a[80:83], v[40:43]\n"
"ds_read_b64_tr_b16 a[88:89], v16 offset:25600\n"
"v_mfma_f32_16x16x32_bf16 v[44:47], a[0:3], a[84:87], v[44:47]\n"
"v_mfma_f32_16x16x32_bf16 v[60:63], a[4:7], a[84:87], v[60:63]\n"
"ds_read_b64_tr_b16 a[90:91], v16 offset:33792\n"
"v_mfma_f32_16x16x32_bf16 v[56:59], a[4:7], a[80:83], v[56:59]\n"
"s_and_b32 s41, s21, 0xffff\n"
"ds_read_b64_tr_b16 a[92:93], v17 offset:25600\n"
"v_mfma_f32_16x16x32_bf16 v[52:55], a[4:7], a[76:79], v[52:55]\n"
"v_mfma_f32_16x16x32_bf16 v[48:51], a[4:7], a[72:75], v[48:51]\n"
"ds_read_b64_tr_b16 a[94:95], v17 offset:33792\n"
"v_mfma_f32_16x16x32_bf16 v[64:67], a[8:11], a[72:75], v[64:67]\n"
"v_mfma_f32_16x16x32_bf16 v[68:71], a[8:11], a[76:79], v[68:71]\n"
"ds_read_b64_tr_b16 a[96:97], v16 offset:26112\n"
"s_mov_b32 s42, 0x480000\n"
"v_mfma_f32_16x16x32_bf16 v[72:75], a[8:11], a[80:83], v[72:75]\n"
"ds_read_b64_tr_b16 a[98:99], v16 offset:34304\n"
"v_mfma_f32_16x16x32_bf16 v[76:79], a[8:11], a[84:87], v[76:79]\n"
"v_mfma_f32_16x16x32_bf16 v[92:95], a[12:15], a[84:87], v[92:95]\n"
"ds_read_b64_tr_b16 a[100:101], v17 offset:26112\n"
"v_mfma_f32_16x16x32_bf16 v[88:91], a[12:15], a[80:83], v[88:91]\n"
"v_mfma_f32_16x16x32_bf16 v[84:87], a[12:15], a[76:79], v[84:87]\n"
"s_mov_b32 s43, 0x20000\n"
"ds_read_b64_tr_b16 a[102:103], v17 offset:34304\n"
"v_mfma_f32_16x16x32_bf16 v[80:83], a[12:15], a[72:75], v[80:83]\n"
"ds_read2st64_b64 a[40:43], v20 offset0:52 offset1:53\n"
"v_mfma_f32_16x16x32_bf16 v[96:99], a[16:19], a[72:75], v[96:99]\n"
"v_mfma_f32_16x16x32_bf16 v[100:103], a[16:19], a[76:79], v[100:103]\n"
"ds_read2st64_b64 a[44:47], v20 offset0:54 offset1:55\n"
"v_mfma_f32_16x16x32_bf16 v[104:107], a[16:19], a[80:83], v[104:107]\n"
"s_add_u32 s44, s32, 0x9d8000\n"
"s_addc_u32 s45, s33, 0\n"
"v_mfma_f32_16x16x32_bf16 v[108:111], a[16:19], a[84:87], v[108:111]\n"
"ds_read2st64_b64 a[48:51], v20 offset0:56 offset1:57\n"
"v_mfma_f32_16x16x32_bf16 v[124:127], a[20:23], a[84:87], v[124:127]\n"
"ds_read2st64_b64 a[52:55], v20 offset0:58 offset1:59\n"
"v_mfma_f32_16x16x32_bf16 v[120:123], a[20:23], a[80:83], v[120:123]\n"
"v_mfma_f32_16x16x32_bf16 v[116:119], a[20:23], a[76:79], v[116:119]\n"
"ds_read2st64_b64 a[56:59], v20 offset0:60 offset1:61\n"
"s_and_b32 s45, s45, 0xffff\n"
"v_mfma_f32_16x16x32_bf16 v[112:115], a[20:23], a[72:75], v[112:115]\n"
"v_mfma_f32_16x16x32_bf16 v[128:131], a[24:27], a[72:75], v[128:131]\n"
"ds_read2st64_b64 a[60:63], v20 offset0:62 offset1:63\n"
"v_mfma_f32_16x16x32_bf16 v[132:135], a[24:27], a[76:79], v[132:135]\n"
"ds_read2st64_b64 a[64:67], v20 offset0:64 offset1:65\n"
"v_mfma_f32_16x16x32_bf16 v[136:139], a[24:27], a[80:83], v[136:139]\n"
"v_mfma_f32_16x16x32_bf16 v[140:143], a[24:27], a[84:87], v[140:143]\n"
"s_mov_b32 s46, 0x1000000\n"
"ds_read2st64_b64 a[68:71], v20 offset0:66 offset1:67\n"
"v_mfma_f32_16x16x32_bf16 v[156:159], a[28:31], a[84:87], v[156:159]\n"
"v_mfma_f32_16x16x32_bf16 v[152:155], a[28:31], a[80:83], v[152:155]\n"
"v_mfma_f32_16x16x32_bf16 v[148:151], a[28:31], a[76:79], v[148:151]\n"
"v_mfma_f32_16x16x32_bf16 v[144:147], a[28:31], a[72:75], v[144:147]\n"
"s_mov_b32 s47, 0x20000\n"
"v_mfma_f32_16x16x32_bf16 v[160:163], a[32:35], a[72:75], v[160:163]\n"
"v_mfma_f32_16x16x32_bf16 v[164:167], a[32:35], a[76:79], v[164:167]\n"
"v_mfma_f32_16x16x32_bf16 v[168:171], a[32:35], a[80:83], v[168:171]\n"
"v_mfma_f32_16x16x32_bf16 v[172:175], a[32:35], a[84:87], v[172:175]\n"
"s_waitcnt vmcnt(14)\n"
"v_cmp_eq_u32 vcc, s34, v248\n"
"s_nop 0\n"
"s_andn2_b64 s[88:89], s[98:99], vcc\n"
"v_cmp_eq_u32 vcc, s34, v249\n"
"s_nop 0\n"
"s_andn2_b64 vcc, s[8:9], vcc\n"
"s_or_b64 s[88:89], s[88:89], vcc\n"
"s_cbranch_scc0 L_pe_ok\n"
"s_mov_b32 s55, 0x30000\n"
"L_pe_re:\n"
"s_sleep 1\n"
"buffer_load_dwordx2 v[248:249], v251, s[36:39], s91 offen sc1\n"
"s_waitcnt vmcnt(0)\n"
"v_cmp_eq_u32 vcc, s34, v248\n"
"s_nop 0\n"
"s_andn2_b64 s[88:89], s[98:99], vcc\n"
"v_cmp_eq_u32 vcc, s34, v249\n"
"s_nop 0\n"
"s_andn2_b64 vcc, s[8:9], vcc\n"
"s_or_b64 s[88:89], s[88:89], vcc\n"
"s_cbranch_scc0 L_pe_ok\n"
"s_sub_u32 s55, s55, 1\n"
"s_cmp_lg_u32 s55, 0\n"
"s_cbranch_scc1 L_pe_re\n"
"L_pe_ok:\n"
"s_waitcnt lgkmcnt(0)\n"
"v_mfma_f32_16x16x32_bf16 v[32:35], a[36:39], a[88:91], v[32:35]\n"
"s_nop 0\n"
"s_barrier\n"
"v_mfma_f32_16x16x32_bf16 v[36:39], a[36:39], a[92:95], v[36:39]\n"
"ds_read2st64_b64 a[0:3], v18 offset0:0 offset1:1\n"
"s_bfe_u64 s[60:61], s[58:59], 0x50000\n"
"v_mfma_f32_16x16x32_bf16 v[40:43], a[36:39], a[96:99], v[40:43]\n"
"ds_read_b64_tr_b16 a[72:73], v12 offset:0\n"
"v_mfma_f32_16x16x32_bf16 v[44:47], a[36:39], a[100:103], v[44:47]\n"
"s_mul_i32 s60, s60, 147456\n"
"v_mfma_f32_16x16x32_bf16 v[60:63], a[40:43], a[100:103], v[60:63]\n"
"ds_read_b64_tr_b16 a[74:75], v12 offset:8192\n"
"v_mfma_f32_16x16x32_bf16 v[56:59], a[40:43], a[96:99], v[56:59]\n"
"ds_read_b64_tr_b16 a[76:77], v13 offset:0\n"
"v_mfma_f32_16x16x32_bf16 v[52:55], a[40:43], a[92:95], v[52:55]\n"
"s_add_u32 s73, s56, s60\n"
"v_mfma_f32_16x16x32_bf16 v[48:51], a[40:43], a[88:91], v[48:51]\n"
"ds_read_b64_tr_b16 a[78:79], v13 offset:8192\n"
"v_mfma_f32_16x16x32_bf16 v[64:67], a[44:47], a[88:91], v[64:67]\n"
"s_add_u32 m0, s0, 0\n"
"v_mfma_f32_16x16x32_bf16 v[68:71], a[44:47], a[92:95], v[68:71]\n"
"ds_read_b64_tr_b16 a[80:81], v12 offset:512\n"
"v_mfma_f32_16x16x32_bf16 v[72:75], a[44:47], a[96:99], v[72:75]\n"
"ds_read_b64_tr_b16 a[82:83], v12 offset:8704\n"
"v_mfma_f32_16x16x32_bf16 v[76:79], a[44:47], a[100:103], v[76:79]\n"
"buffer_load_dwordx4 v28, s[40:43], s73 offen offset:0 lds\n"
"v_mfma_f32_16x16x32_bf16 v[92:95], a[48:51], a[100:103], v[92:95]\n"
"ds_read_b64_tr_b16 a[84:85], v13 offset:512\n"
"v_mfma_f32_16x16x32_bf16 v[88:91], a[48:51], a[96:99], v[88:91]\n"
"buffer_load_dwordx4 v29, s[40:43], s73 offen offset:1024 lds\n"
"v_mfma_f32_16x16x32_bf16 v[84:87], a[48:51], a[92:95], v[84:87]\n"
"ds_read_b64_tr_b16 a[86:87], v13 offset:8704\n"
"v_mfma_f32_16x16x32_bf16 v[80:83], a[48:51], a[88:91], v[80:83]\n"
"ds_read2st64_b64 a[4:7], v18 offset0:2 offset1:3\n"
"v_mfma_f32_16x16x32_bf16 v[96:99], a[52:55], a[88:91], v[96:99]\n"
"buffer_load_dwordx4 v30, s[40:43], s73 offen offset:2048 lds\n"
"v_mfma_f32_16x16x32_bf16 v[100:103], a[52:55], a[92:95], v[100:103]\n"
"ds_read2st64_b64 a[8:11], v18 offset0:4 offset1:5\n"
"v_mfma_f32_16x16x32_bf16 v[104:107], a[52:55], a[96:99], v[104:107]\n"
"buffer_load_dwordx4 v31, s[40:43], s73 offen offset:3072 lds\n"
"v_mfma_f32_16x16x32_bf16 v[108:111], a[52:55], a[100:103], v[108:111]\n"
"ds_read2st64_b64 a[12:15], v18 offset0:6 offset1:7\n"
"v_mfma_f32_16x16x32_bf16 v[124:127], a[56:59], a[100:103], v[124:127]\n"
"ds_read2st64_b64 a[16:19], v18 offset0:8 offset1:9\n"
"v_mfma_f32_16x16x32_bf16 v[120:123], a[56:59], a[96:99], v[120:123]\n"
"s_add_u32 m0, s0, 4096\n"
"v_mfma_f32_16x16x32_bf16 v[116:119], a[56:59], a[92:95], v[116:119]\n"
"ds_read2st64_b64 a[20:23], v18 offset0:10 offset1:11\n"
"v_mfma_f32_16x16x32_bf16 v[112:115], a[56:59], a[88:91], v[112:115]\n"
"s_mov_b64 exec, s[96:97]\n"
"buffer_load_dwordx4 v240, s[40:43], s73 offen offset:0 lds\n"
"s_mov_b64 exec, -1\n"
"v_mfma_f32_16x16x32_bf16 v[128:131], a[60:63], a[88:91], v[128:131]\n"
"ds_read2st64_b64 a[24:27], v18 offset0:12 offset1:13\n"
"v_mfma_f32_16x16x32_bf16 v[132:135], a[60:63], a[92:95], v[132:135]\n"
"ds_read2st64_b64 a[28:31], v18 offset0:14 offset1:15\n"
"v_mfma_f32_16x16x32_bf16 v[136:139], a[60:63], a[96:99], v[136:139]\n"
"s_add_u32 s73, s73, 73728\n"
"v_mfma_f32_16x16x32_bf16 v[140:143], a[60:63], a[100:103], v[140:143]\n"
"ds_read2st64_b64 a[32:35], v18 offset0:16 offset1:17\n"
"v_mfma_f32_16x16x32_bf16 v[156:159], a[64:67], a[100:103], v[156:159]\n"
"s_add_u32 m0, s0, 18432\n"
"v_mfma_f32_16x16x32_bf16 v[152:155], a[64:67], a[96:99], v[152:155]\n"
"v_mfma_f32_16x16x32_bf16 v[148:151], a[64:67], a[92:95], v[148:151]\n"
"v_mfma_f32_16x16x32_bf16 v[144:147], a[64:67], a[88:91], v[144:147]\n"
"buffer_load_dwordx4 v28, s[40:43], s73 offen offset:0 lds\n"
"v_mfma_f32_16x16x32_bf16 v[160:163], a[68:71], a[88:91], v[160:163]\n"
"v_mfma_f32_16x16x32_bf16 v[164:167], a[68:71], a[92:95], v[164:167]\n"
"buffer_load_dwordx4 v29, s[40:43], s73 offen offset:1024 lds\n"
"v_mfma_f32_16x16x32_bf16 v[168:171], a[68:71], a[96:99], v[168:171]\n"
"v_mfma_f32_16x16x32_bf16 v[172:175], a[68:71], a[100:103], v[172:175]\n"
"s_waitcnt lgkmcnt(0)\n"
"v_mfma_f32_16x16x32_bf16 v[32:35], a[0:3], a[72:75], v[32:35]\n"
"s_nop 0\n"
"s_barrier\n"
"v_mfma_f32_16x16x32_bf16 v[36:39], a[0:3], a[76:79], v[36:39]\n"
"ds_read2st64_b64 a[36:39], v18 offset0:50 offset1:51\n"
"v_mfma_f32_16x16x32_bf16 v[40:43], a[0:3], a[80:83], v[40:43]\n"
"buffer_load_dwordx4 v30, s[40:43], s73 offen offset:2048 lds\n"
"v_mfma_f32_16x16x32_bf16 v[44:47], a[0:3], a[84:87], v[44:47]\n"
"ds_read_b64_tr_b16 a[88:89], v12 offset:25600\n"
"v_mfma_f32_16x16x32_bf16 v[60:63], a[4:7], a[84:87], v[60:63]\n"
"buffer_load_dwordx4 v31, s[40:43], s73 offen offset:3072 lds\n"
"v_mfma_f32_16x16x32_bf16 v[56:59], a[4:7], a[80:83], v[56:59]\n"
"ds_read_b64_tr_b16 a[90:91], v12 offset:33792\n"
"ds_read_b64_tr_b16 a[92:93], v13 offset:25600\n"
"v_mfma_f32_16x16x32_bf16 v[52:55], a[4:7], a[76:79], v[52:55]\n"
"s_add_u32 m0, s0, 22528\n"
"v_mfma_f32_16x16x32_bf16 v[48:51], a[4:7], a[72:75], v[48:51]\n"
"ds_read_b64_tr_b16 a[94:95], v13 offset:33792\n"
"v_mfma_f32_16x16x32_bf16 v[64:67], a[8:11], a[72:75], v[64:67]\n"
"v_mfma_f32_16x16x32_bf16 v[68:71], a[8:11], a[76:79], v[68:71]\n"
"s_mov_b64 exec, s[96:97]\n"
"buffer_load_dwordx4 v240, s[40:43], s73 offen offset:0 lds\n"
"s_mov_b64 exec, -1\n"
"v_mfma_f32_16x16x32_bf16 v[72:75], a[8:11], a[80:83], v[72:75]\n"
"ds_read_b64_tr_b16 a[96:97], v12 offset:26112\n"
"ds_read_b64_tr_b16 a[98:99], v12 offset:34304\n"
"v_mfma_f32_16x16x32_bf16 v[76:79], a[8:11], a[84:87], v[76:79]\n"
"s_add_u32 s73, s73, 73728\n"
"v_mfma_f32_16x16x32_bf16 v[92:95], a[12:15], a[84:87], v[92:95]\n"
"ds_read_b64_tr_b16 a[100:101], v13 offset:26112\n"
"v_mfma_f32_16x16x32_bf16 v[88:91], a[12:15], a[80:83], v[88:91]\n"
"v_mfma_f32_16x16x32_bf16 v[84:87], a[12:15], a[76:79], v[84:87]\n"
"ds_read_b64_tr_b16 a[102:103], v13 offset:34304\n"
"s_add_u32 m0, s0, 36864\n"
"v_mfma_f32_16x16x32_bf16 v[80:83], a[12:15], a[72:75], v[80:83]\n"
"ds_read2st64_b64 a[40:43], v18 offset0:52 offset1:53\n"
"v_mfma_f32_16x16x32_bf16 v[96:99], a[16:19], a[72:75], v[96:99]\n"
"v_mfma_f32_16x16x32_bf16 v[100:103], a[16:19], a[76:79], v[100:103]\n"
"buffer_load_dwordx4 v28, s[40:43], s73 offen offset:0 lds\n"
"v_mfma_f32_16x16x32_bf16 v[104:107], a[16:19], a[80:83], v[104:107]\n"
"ds_read2st64_b64 a[44:47], v18 offset0:54 offset1:55\n"
"v_mfma_f32_16x16x32_bf16 v[108:111], a[16:19], a[84:87], v[108:111]\n"
"ds_read2st64_b64 a[48:51], v18 offset0:56 offset1:57\n"
"v_mfma_f32_16x16x32_bf16 v[124:127], a[20:23], a[84:87], v[124:127]\n"
"buffer_load_dwordx4 v29, s[40:43], s73 offen offset:1024 lds\n"
"v_mfma_f32_16x16x32_bf16 v[120:123], a[20:23], a[80:83], v[120:123]\n"
"ds_read2st64_b64 a[52:55], v18 offset0:58 offset1:59\n"
"v_mfma_f32_16x16x32_bf16 v[116:119], a[20:23], a[76:79], v[116:119]\n"
"ds_read2st64_b64 a[56:59], v18 offset0:60 offset1:61\n"
"v_mfma_f32_16x16x32_bf16 v[112:115], a[20:23], a[72:75], v[112:115]\n"
"buffer_load_dwordx4 v30, s[40:43], s73 offen offset:2048 lds\n"
"v_mfma_f32_16x16x32_bf16 v[128:131], a[24:27], a[72:75], v[128:131]\n"
"ds_read2st64_b64 a[60:63], v18 offset0:62 offset1:63\n"
"v_mfma_f32_16x16x32_bf16 v[132:135], a[24:27], a[76:79], v[132:135]\n"
"buffer_load_dwordx4 v31, s[40:43], s73 offen offset:3072 lds\n"
"v_mfma_f32_16x16x32_bf16 v[136:139], a[24:27], a[80:83], v[136:139]\n"
"ds_read2st64_b64 a[64:67], v18 offset0:64 offset1:65\n"
"v_mfma_f32_16x16x32_bf16 v[140:143], a[24:27], a[84:87], v[140:143]\n"
"ds_read2st64_b64 a[68:71], v18 offset0:66 offset1:67\n"
"s_add_u32 m0, s0, 40960\n"
"v_mfma_f32_16x16x32_bf16 v[156:159], a[28:31], a[84:87], v[156:159]\n"
"v_mfma_f32_16x16x32_bf16 v[152:155], a[28:31], a[80:83], v[152:155]\n"
"v_mfma_f32_16x16x32_bf16 v[148:151], a[28:31], a[76:79], v[148:151]\n"
"s_mov_b64 exec, s[96:97]\n"
"buffer_load_dwordx4 v240, s[40:43], s73 offen offset:0 lds\n"
"s_mov_b64 exec, -1\n"
"v_mfma_f32_16x16x32_bf16 v[144:147], a[28:31], a[72:75], v[144:147]\n"
"v_mfma_f32_16x16x32_bf16 v[160:163], a[32:35], a[72:75], v[160:163]\n"
"v_mfma_f32_16x16x32_bf16 v[164:167], a[32:35], a[76:79], v[164:167]\n"
"s_add_u32 s73, s73, 73728\n"
"v_mfma_f32_16x16x32_bf16 v[168:171], a[32:35], a[80:83], v[168:171]\n"
"v_mfma_f32_16x16x32_bf16 v[172:175], a[32:35], a[84:87], v[172:175]\n"
"s_waitcnt lgkmcnt(0)\n"
"v_mfma_f32_16x16x32_bf16 v[32:35], a[36:39], a[88:91], v[32:35]\n"
"s_waitcnt vmcnt(22)\n"
"s_barrier\n"
"v_mfma_f32_16x16x32_bf16 v[36:39], a[36:39], a[92:95], v[36:39]\n"
"ds_read2st64_b64 a[0:3], v19 offset0:0 offset1:1\n"
"s_add_u32 m0, s1, 0\n"
"v_mfma_f32_16x16x32_bf16 v[40:43], a[36:39], a[96:99], v[40:43]\n"
"ds_read_b64_tr_b16 a[72:73], v14 offset:0\n"
"v_mfma_f32_16x16x32_bf16 v[44:47], a[36:39], a[100:103], v[44:47]\n"
"buffer_load_dwordx4 v28, s[40:43], s73 offen offset:0 lds\n"
"v_mfma_f32_16x16x32_bf16 v[60:63], a[40:43], a[100:103], v[60:63]\n"
"ds_read_b64_tr_b16 a[74:75], v14 offset:8192\n"
"v_mfma_f32_16x16x32_bf16 v[56:59], a[40:43], a[96:99], v[56:59]\n"
"buffer_load_dwordx4 v29, s[40:43], s73 offen offset:1024 lds\n"
"v_mfma_f32_16x16x32_bf16 v[52:55], a[40:43], a[92:95], v[52:55]\n"
"ds_read_b64_tr_b16 a[76:77], v15 offset:0\n"
"v_mfma_f32_16x16x32_bf16 v[48:51], a[40:43], a[88:91], v[48:51]\n"
"buffer_load_dwordx4 v30, s[40:43], s73 offen offset:2048 lds\n"
"v_mfma_f32_16x16x32_bf16 v[64:67], a[44:47], a[88:91], v[64:67]\n"
"ds_read_b64_tr_b16 a[78:79], v15 offset:8192\n"
"v_mfma_f32_16x16x32_bf16 v[68:71], a[44:47], a[92:95], v[68:71]\n"
"buffer_load_dwordx4 v31, s[40:43], s73 offen offset:3072 lds\n"
"v_mfma_f32_16x16x32_bf16 v[72:75], a[44:47], a[96:99], v[72:75]\n"
"ds_read_b64_tr_b16 a[80:81], v14 offset:512\n"
"s_add_u32 m0, s1, 4096\n"
"v_mfma_f32_16x16x32_bf16 v[76:79], a[44:47], a[100:103], v[76:79]\n"
"ds_read_b64_tr_b16 a[82:83], v14 offset:8704\n"
"v_mfma_f32_16x16x32_bf16 v[92:95], a[48:51], a[100:103], v[92:95]\n"
"s_mov_b64 exec, s[96:97]\n"
"buffer_load_dwordx4 v240, s[40:43], s73 offen offset:0 lds\n"
"s_mov_b64 exec, -1\n"
"v_mfma_f32_16x16x32_bf16 v[88:91], a[48:51], a[96:99], v[88:91]\n"
"ds_read_b64_tr_b16 a[84:85], v15 offset:512\n"
"s_add_u32 s73, s73, 73728\n"
"v_mfma_f32_16x16x32_bf16 v[84:87], a[48:51], a[92:95], v[84:87]\n"
"ds_read_b64_tr_b16 a[86:87], v15 offset:8704\n"
"v_mfma_f32_16x16x32_bf16 v[80:83], a[48:51], a[88:91], v[80:83]\n"
"s_bfe_u64 s[62:63], s[58:59], 0x50000\n"
"ds_read2st64_b64 a[4:7], v19 offset0:2 offset1:3\n"
"v_mfma_f32_16x16x32_bf16 v[96:99], a[52:55], a[88:91], v[96:99]\n"
"s_mul_i32 s62, s62, 8192\n"
"v_mfma_f32_16x16x32_bf16 v[100:103], a[52:55], a[92:95], v[100:103]\n"
"ds_read2st64_b64 a[8:11], v19 offset0:4 offset1:5\n"
"v_mfma_f32_16x16x32_bf16 v[104:107], a[52:55], a[96:99], v[104:107]\n"
"s_add_u32 s72, s57, s62\n"
"v_mfma_f32_16x16x32_bf16 v[108:111], a[52:55], a[100:103], v[108:111]\n"
"ds_read2st64_b64 a[12:15], v19 offset0:6 offset1:7\n"
"v_mfma_f32_16x16x32_bf16 v[124:127], a[56:59], a[100:103], v[124:127]\n"
"buffer_load_dwordx4 a[144:147], v1, s[44:47], s72 offen offset:0 sc1\n"
"v_mfma_f32_16x16x32_bf16 v[120:123], a[56:59], a[96:99], v[120:123]\n"
"ds_read2st64_b64 a[16:19], v19 offset0:8 offset1:9\n"
"v_mfma_f32_16x16x32_bf16 v[116:119], a[56:59], a[92:95], v[116:119]\n"
"buffer_load_dwordx4 a[148:151], v1, s[44:47], s72 offen offset:1024 sc1\n"
"v_mfma_f32_16x16x32_bf16 v[112:115], a[56:59], a[88:91], v[112:115]\n"
"ds_read2st64_b64 a[20:23], v19 offset0:10 offset1:11\n"
"s_add_u32 s72, s72, 2048\n"
"v_mfma_f32_16x16x32_bf16 v[128:131], a[60:63], a[88:91], v[128:131]\n"
"ds_read2st64_b64 a[24:27], v19 offset0:12 offset1:13\n"
"v_mfma_f32_16x16x32_bf16 v[132:135], a[60:63], a[92:95], v[132:135]\n"
"buffer_load_dwordx4 a[152:155], v1, s[44:47], s72 offen offset:0 sc1\n"
"v_mfma_f32_16x16x32_bf16 v[136:139], a[60:63], a[96:99], v[136:139]\n"
"ds_read2st64_b64 a[28:31], v19 offset0:14 offset1:15\n"
"v_mfma_f32_16x16x32_bf16 v[140:143], a[60:63], a[100:103], v[140:143]\n"
"buffer_load_dwordx4 a[156:159], v1, s[44:47], s72 offen offset:1024 sc1\n"
"v_mfma_f32_16x16x32_bf16 v[156:159], a[64:67], a[100:103], v[156:159]\n"
"ds_read2st64_b64 a[32:35], v19 offset0:16 offset1:17\n"
"v_mfma_f32_16x16x32_bf16 v[152:155], a[64:67], a[96:99], v[152:155]\n"
"s_add_u32 s72, s72, 2048\n"
"v_mfma_f32_16x16x32_bf16 v[148:151], a[64:67], a[92:95], v[148:151]\n"
"buffer_load_dwordx4 a[160:163], v1, s[44:47], s72 offen offset:0 sc1\n"
"v_mfma_f32_16x16x32_bf16 v[144:147], a[64:67], a[88:91], v[144:147]\n"
"v_mfma_f32_16x16x32_bf16 v[160:163], a[68:71], a[88:91], v[160:163]\n"
"buffer_load_dwordx4 a[164:167], v1, s[44:47], s72 offen offset:1024 sc1\n"
"v_mfma_f32_16x16x32_bf16 v[164:167], a[68:71], a[92:95], v[164:167]\n"
"v_mfma_f32_16x16x32_bf16 v[168:171], a[68:71], a[96:99], v[168:171]\n"
"s_add_u32 s72, s72, 2048\n"
"v_mfma_f32_16x16x32_bf16 v[172:175], a[68:71], a[100:103], v[172:175]\n"
"s_waitcnt vmcnt(26) lgkmcnt(0)\n"
"s_barrier\n"
"v_mfma_f32_16x16x32_bf16 v[32:35], a[0:3], a[72:75], v[32:35]\n"
"ds_read_b64_tr_b16 a[88:89], v14 offset:25600\n"
"ds_read_b64_tr_b16 a[90:91], v14 offset:33792\n"
"v_mfma_f32_16x16x32_bf16 v[36:39], a[0:3], a[76:79], v[36:39]\n"
"ds_read_b64_tr_b16 a[92:93], v15 offset:25600\n"
"ds_read_b64_tr_b16 a[94:95], v15 offset:33792\n"
"v_mfma_f32_16x16x32_bf16 v[40:43], a[0:3], a[80:83], v[40:43]\n"
"ds_read_b64_tr_b16 a[96:97], v14 offset:26112\n"
"ds_read_b64_tr_b16 a[98:99], v14 offset:34304\n"
"v_mfma_f32_16x16x32_bf16 v[44:47], a[0:3], a[84:87], v[44:47]\n"
"ds_read_b64_tr_b16 a[100:101], v15 offset:26112\n"
"ds_read_b64_tr_b16 a[102:103], v15 offset:34304\n"
"v_mfma_f32_16x16x32_bf16 v[60:63], a[4:7], a[84:87], v[60:63]\n"
"ds_read2st64_b64 a[36:39], v19 offset0:50 offset1:51\n"
"ds_read2st64_b64 a[40:43], v19 offset0:52 offset1:53\n"
"v_mfma_f32_16x16x32_bf16 v[56:59], a[4:7], a[80:83], v[56:59]\n"
"ds_read2st64_b64 a[44:47], v19 offset0:54 offset1:55\n"
"v_mfma_f32_16x16x32_bf16 v[52:55], a[4:7], a[76:79], v[52:55]\n"
"ds_read2st64_b64 a[48:51], v19 offset0:56 offset1:57\n"
"v_mfma_f32_16x16x32_bf16 v[48:51], a[4:7], a[72:75], v[48:51]\n"
"ds_read2st64_b64 a[52:55], v19 offset0:58 offset1:59\n"
"s_waitcnt lgkmcnt(3)\n"
"v_mfma_f32_16x16x32_bf16 v[32:35], a[36:39], a[88:91], v[32:35]\n"
"buffer_load_dwordx4 a[168:171], v1, s[44:47], s72 offen offset:0 sc1\n"
"v_mfma_f32_16x16x32_bf16 v[36:39], a[36:39], a[92:95], v[36:39]\n"
"ds_read2st64_b64 a[56:59], v19 offset0:60 offset1:61\n"
"v_mfma_f32_16x16x32_bf16 v[40:43], a[36:39], a[96:99], v[40:43]\n"
"buffer_load_dwordx4 a[172:175], v1, s[44:47], s72 offen offset:1024 sc1\n"
"v_mfma_f32_16x16x32_bf16 v[44:47], a[36:39], a[100:103], v[44:47]\n"
"ds_read2st64_b64 a[60:63], v19 offset0:62 offset1:63\n"
"s_add_u32 s72, s72, 2048\n"
"v_mfma_f32_16x16x32_bf16 v[60:63], a[40:43], a[100:103], v[60:63]\n"
"ds_read2st64_b64 a[64:67], v19 offset0:64 offset1:65\n"
"v_mfma_f32_16x16x32_bf16 v[56:59], a[40:43], a[96:99], v[56:59]\n"
"ds_read2st64_b64 a[68:71], v19 offset0:66 offset1:67\n"
"v_mfma_f32_16x16x32_bf16 v[52:55], a[40:43], a[92:95], v[52:55]\n"
"buffer_load_dwordx4 a[208:211], v1, s[44:47], s72 offen offset:0 sc1\n"
"v_mfma_f32_16x16x32_bf16 v[48:51], a[40:43], a[88:91], v[48:51]\n"
"buffer_load_dwordx4 a[212:215], v1, s[44:47], s72 offen offset:1024 sc1\n"
"v_mfma_f32_16x16x32_bf16 v[64:67], a[8:11], a[72:75], v[64:67]\n"
"v_mfma_f32_16x16x32_bf16 v[68:71], a[8:11], a[76:79], v[68:71]\n"
"s_add_u32 s72, s72, 2048\n"
"v_mfma_f32_16x16x32_bf16 v[72:75], a[8:11], a[80:83], v[72:75]\n"
"v_mfma_f32_16x16x32_bf16 v[76:79], a[8:11], a[84:87], v[76:79]\n"
"s_waitcnt lgkmcnt(0)\n"
"v_cvt_pk_bf16_f32 v176, v32, v33\n"
"v_cvt_pk_bf16_f32 v177, v34, v35\n"
"v_mfma_f32_16x16x32_bf16 v[64:67], a[44:47], a[88:91], v[64:67]\n"
"buffer_load_dwordx4 a[216:219], v1, s[44:47], s72 offen offset:0 sc1\n"
"v_mfma_f32_16x16x32_bf16 v[68:71], a[44:47], a[92:95], v[68:71]\n"
"v_cvt_pk_bf16_f32 v178, v48, v49\n"
"v_cvt_pk_bf16_f32 v179, v50, v51\n"
"v_mfma_f32_16x16x32_bf16 v[72:75], a[44:47], a[96:99], v[72:75]\n"
"buffer_load_dwordx4 a[220:223], v1, s[44:47], s72 offen offset:1024 sc1\n"
"v_mfma_f32_16x16x32_bf16 v[76:79], a[44:47], a[100:103], v[76:79]\n"
"v_cvt_pk_bf16_f32 v180, v36, v37\n"
"v_cvt_pk_bf16_f32 v181, v38, v39\n"
"s_add_u32 s72, s72, 2048\n"
"v_mfma_f32_16x16x32_bf16 v[92:95], a[12:15], a[84:87], v[92:95]\n"
"v_cvt_pk_bf16_f32 v182, v52, v53\n"
"v_cvt_pk_bf16_f32 v183, v54, v55\n"
"v_mfma_f32_16x16x32_bf16 v[88:91], a[12:15], a[80:83], v[88:91]\n"
"v_cvt_pk_bf16_f32 v184, v40, v41\n"
"v_cvt_pk_bf16_f32 v185, v42, v43\n"
"v_mfma_f32_16x16x32_bf16 v[84:87], a[12:15], a[76:79], v[84:87]\n"
"v_cvt_pk_bf16_f32 v186, v56, v57\n"
"v_cvt_pk_bf16_f32 v187, v58, v59\n"
"v_mfma_f32_16x16x32_bf16 v[80:83], a[12:15], a[72:75], v[80:83]\n"
"v_cvt_pk_bf16_f32 v188, v44, v45\n"
"v_cvt_pk_bf16_f32 v189, v46, v47\n"
"v_cvt_pk_bf16_f32 v190, v60, v61\n"
"v_cvt_pk_bf16_f32 v191, v62, v63\n"
"v_mfma_f32_16x16x32_bf16 v[92:95], a[48:51], a[100:103], v[92:95]\n"
"s_add_u32 s68, s85, 4608\n"
"v_mfma_f32_16x16x32_bf16 v[88:91], a[48:51], a[96:99], v[88:91]\n"
"v_mfma_f32_16x16x32_bf16 v[84:87], a[48:51], a[92:95], v[84:87]\n"
"s_add_u32 s80, s85, 9216\n"
"v_mfma_f32_16x16x32_bf16 v[80:83], a[48:51], a[88:91], v[80:83]\n"
"v_mfma_f32_16x16x32_bf16 v[96:99], a[16:19], a[72:75], v[96:99]\n"
"s_add_u32 s81, s85, 13824\n"
"v_mfma_f32_16x16x32_bf16 v[100:103], a[16:19], a[76:79], v[100:103]\n"
"v_mfma_f32_16x16x32_bf16 v[104:107], a[16:19], a[80:83], v[104:107]\n"
"buffer_store_dwordx4 v[176:179], v1, s[36:39], s85 offen offset:0 sc1\n"
"v_mfma_f32_16x16x32_bf16 v[108:111], a[16:19], a[84:87], v[108:111]\n"
"v_cvt_pk_bf16_f32 v192, v64, v65\n"
"v_cvt_pk_bf16_f32 v193, v66, v67\n"
"v_mfma_f32_16x16x32_bf16 v[96:99], a[52:55], a[88:91], v[96:99]\n"
"v_cvt_pk_bf16_f32 v194, v80, v81\n"
"v_cvt_pk_bf16_f32 v195, v82, v83\n"
"v_mfma_f32_16x16x32_bf16 v[100:103], a[52:55], a[92:95], v[100:103]\n"
"buffer_store_dwordx4 v[180:183], v1, s[36:39], s68 offen offset:0 sc1\n"
"v_mfma_f32_16x16x32_bf16 v[104:107], a[52:55], a[96:99], v[104:107]\n"
"v_cvt_pk_bf16_f32 v196, v68, v69\n"
"v_cvt_pk_bf16_f32 v197, v70, v71\n"
"v_mfma_f32_16x16x32_bf16 v[108:111], a[52:55], a[100:103], v[108:111]\n"
"v_cvt_pk_bf16_f32 v198, v84, v85\n"
"v_cvt_pk_bf16_f32 v199, v86, v87\n"
"v_mfma_f32_16x16x32_bf16 v[124:127], a[20:23], a[84:87], v[124:127]\n"
"buffer_store_dwordx4 v[184:187], v1, s[36:39], s80 offen offset:0 sc1\n"
"v_mfma_f32_16x16x32_bf16 v[120:123], a[20:23], a[80:83], v[120:123]\n"
"v_cvt_pk_bf16_f32 v200, v72, v73\n"
"v_cvt_pk_bf16_f32 v201, v74, v75\n"
"v_mfma_f32_16x16x32_bf16 v[116:119], a[20:23], a[76:79], v[116:119]\n"
"v_cvt_pk_bf16_f32 v202, v88, v89\n"
"v_cvt_pk_bf16_f32 v203, v90, v91\n"
"v_cvt_pk_bf16_f32 v204, v76, v77\n"
"v_mfma_f32_16x16x32_bf16 v[112:115], a[20:23], a[72:75], v[112:115]\n"
"buffer_store_dwordx4 v[188:191], v1, s[36:39], s81 offen offset:0 sc1\n"
"v_cvt_pk_bf16_f32 v205, v78, v79\n"
"v_cvt_pk_bf16_f32 v206, v92, v93\n"
"v_cvt_pk_bf16_f32 v207, v94, v95\n"
"v_mfma_f32_16x16x32_bf16 v[124:127], a[56:59], a[100:103], v[124:127]\n"
"v_cvt_pk_bf16_f32 v208, v96, v97\n"
"v_cvt_pk_bf16_f32 v209, v98, v99\n"
"v_mfma_f32_16x16x32_bf16 v[120:123], a[56:59], a[96:99], v[120:123]\n"
"v_cvt_pk_bf16_f32 v210, v100, v101\n"
"v_cvt_pk_bf16_f32 v211, v102, v103\n"
"v_mfma_f32_16x16x32_bf16 v[116:119], a[56:59], a[92:95], v[116:119]\n"
"buffer_store_dwordx4 v[192:195], v1, s[36:39], s85 offen offset:1024 sc1\n"
"v_mfma_f32_16x16x32_bf16 v[112:115], a[56:59], a[88:91], v[112:115]\n"
"v_cvt_pk_bf16_f32 v212, v104, v105\n"
"v_cvt_pk_bf16_f32 v213, v106, v107\n"
"v_mfma_f32_16x16x32_bf16 v[128:131], a[24:27], a[72:75], v[128:131]\n"
"v_cvt_pk_bf16_f32 v214, v108, v109\n"
"v_cvt_pk_bf16_f32 v215, v110, v111\n"
"v_mfma_f32_16x16x32_bf16 v[132:135], a[24:27], a[76:79], v[132:135]\n"
"buffer_store_dwordx4 v[196:199], v1, s[36:39], s68 offen offset:1024 sc1\n"
"v_mfma_f32_16x16x32_bf16 v[136:139], a[24:27], a[80:83], v[136:139]\n"
"v_mfma_f32_16x16x32_bf16 v[140:143], a[24:27], a[84:87], v[140:143]\n"
"v_mfma_f32_16x16x32_bf16 v[128:131], a[60:63], a[88:91], v[128:131]\n"
"buffer_store_dwordx2 v[208:209], v251, s[36:39], s85 offen offset:2048 sc1\n"
"v_mfma_f32_16x16x32_bf16 v[132:135], a[60:63], a[92:95], v[132:135]\n"
"buffer_store_dwordx4 v[200:203], v1, s[36:39], s80 offen offset:1024 sc1\n"
"v_mfma_f32_16x16x32_bf16 v[136:139], a[60:63], a[96:99], v[136:139]\n"
"buffer_store_dwordx2 v[210:211], v251, s[36:39], s68 offen offset:2048 sc1\n"
"v_mfma_f32_16x16x32_bf16 v[140:143], a[60:63], a[100:103], v[140:143]\n"
"buffer_store_dwordx4 v[204:207], v1, s[36:39], s81 offen offset:1024 sc1\n"
"v_mfma_f32_16x16x32_bf16 v[156:159], a[28:31], a[84:87], v[156:159]\n"
"v_mfma_f32_16x16x32_bf16 v[152:155], a[28:31], a[80:83], v[152:155]\n"
"buffer_store_dwordx2 v[212:213], v251, s[36:39], s80 offen offset:2048 sc1\n"
"v_mfma_f32_16x16x32_bf16 v[148:151], a[28:31], a[76:79], v[148:151]\n"
"v_mfma_f32_16x16x32_bf16 v[144:147], a[28:31], a[72:75], v[144:147]\n"
"v_cvt_pk_bf16_f32 v216, v112, v113\n"
"v_cvt_pk_bf16_f32 v217, v114, v115\n"
"v_mfma_f32_16x16x32_bf16 v[156:159], a[64:67], a[100:103], v[156:159]\n"
"v_cvt_pk_bf16_f32 v218, v128, v129\n"
"v_cvt_pk_bf16_f32 v219, v130, v131\n"
"v_mfma_f32_16x16x32_bf16 v[152:155], a[64:67], a[96:99], v[152:155]\n"
"buffer_store_dwordx2 v[214:215], v251, s[36:39], s81 offen offset:2048 sc1\n"
"v_mfma_f32_16x16x32_bf16 v[148:151], a[64:67], a[92:95], v[148:151]\n"
"v_cvt_pk_bf16_f32 v220, v116, v117\n"
"v_cvt_pk_bf16_f32 v221, v118, v119\n"
"v_mfma_f32_16x16x32_bf16 v[144:147], a[64:67], a[88:91], v[144:147]\n"
"v_cvt_pk_bf16_f32 v222, v132, v133\n"
"v_cvt_pk_bf16_f32 v223, v134, v135\n"
"v_mfma_f32_16x16x32_bf16 v[160:163], a[32:35], a[72:75], v[160:163]\n"
"v_cvt_pk_bf16_f32 v224, v120, v121\n"
"v_cvt_pk_bf16_f32 v225, v122, v123\n"
"v_mfma_f32_16x16x32_bf16 v[164:167], a[32:35], a[76:79], v[164:167]\n"
"v_cvt_pk_bf16_f32 v226, v136, v137\n"
"v_cvt_pk_bf16_f32 v227, v138, v139\n"
"v_mfma_f32_16x16x32_bf16 v[168:171], a[32:35], a[80:83], v[168:171]\n"
"v_cvt_pk_bf16_f32 v228, v124, v125\n"
"v_cvt_pk_bf16_f32 v229, v126, v127\n"
"v_mfma_f32_16x16x32_bf16 v[172:175], a[32:35], a[84:87], v[172:175]\n"
"v_cvt_pk_bf16_f32 v230, v140, v141\n"
"v_cvt_pk_bf16_f32 v231, v142, v143\n"
"v_mfma_f32_16x16x32_bf16 v[160:163], a[68:71], a[88:91], v[160:163]\n"
"buffer_store_dwordx4 v[216:219], v1, s[36:39], s85 offen offset:2560 sc1\n"
"v_mfma_f32_16x16x32_bf16 v[164:167], a[68:71], a[92:95], v[164:167]\n"
"buffer_store_dwordx4 v[220:223], v1, s[36:39], s68 offen offset:2560 sc1\n"
"v_mfma_f32_16x16x32_bf16 v[168:171], a[68:71], a[96:99], v[168:171]\n"
"buffer_store_dwordx4 v[224:227], v1, s[36:39], s80 offen offset:2560 sc1\n"
"v_mfma_f32_16x16x32_bf16 v[172:175], a[68:71], a[100:103], v[172:175]\n"
"buffer_store_dwordx4 v[228:231], v1, s[36:39], s81 offen offset:2560 sc1\n"
"v_cvt_pk_bf16_f32 v232, v144, v145\n"
"v_cvt_pk_bf16_f32 v233, v146, v147\n"
"v_cvt_pk_bf16_f32 v234, v160, v161\n"
"v_cvt_pk_bf16_f32 v235, v162, v163\n"
"v_cvt_pk_bf16_f32 v236, v148, v149\n"
"v_cvt_pk_bf16_f32 v237, v150, v151\n"
"v_cvt_pk_bf16_f32 v238, v164, v165\n"
"v_cvt_pk_bf16_f32 v239, v166, v167\n"
"v_cvt_pk_bf16_f32 v104, v152, v153\n"
"v_cvt_pk_bf16_f32 v105, v154, v155\n"
"v_cvt_pk_bf16_f32 v106, v168, v169\n"
"v_cvt_pk_bf16_f32 v107, v170, v171\n"
"v_cvt_pk_bf16_f32 v108, v156, v157\n"
"v_cvt_pk_bf16_f32 v109, v158, v159\n"
"v_cvt_pk_bf16_f32 v110, v172, v173\n"
"v_cvt_pk_bf16_f32 v111, v174, v175\n"
"buffer_store_dwordx4 v[232:235], v1, s[36:39], s85 offen offset:3584 sc1\n"
"buffer_store_dwordx4 v[236:239], v1, s[36:39], s68 offen offset:3584 sc1\n"
"buffer_store_dwordx4 v[104:107], v1, s[36:39], s80 offen offset:3584 sc1\n"
"buffer_store_dwordx4 v[108:111], v1, s[36:39], s81 offen offset:3584 sc1\n"
"L_g3B:\n"
"s_waitcnt vmcnt(47)\n"
"s_barrier\n"
"ds_read_b64_tr_b16 a[0:1], v241 offset:0\n"
"ds_read_b64_tr_b16 a[2:3], v241 offset:4608\n"
"ds_read_b64_tr_b16 a[4:5], v242 offset:0\n"
"ds_read_b64_tr_b16 a[6:7], v242 offset:4608\n"
"ds_read_b64_tr_b16 a[8:9], v241 offset:64\n"
"ds_read_b64_tr_b16 a[10:11], v241 offset:4672\n"
"ds_read_b64_tr_b16 a[12:13], v242 offset:64\n"
"ds_read_b64_tr_b16 a[14:15], v242 offset:4672\n"
"ds_read_b64_tr_b16 a[16:17], v241 offset:128\n"
"ds_read_b64_tr_b16 a[18:19], v241 offset:4736\n"
"ds_read_b64_tr_b16 a[20:21], v242 offset:128\n"
"ds_read_b64_tr_b16 a[22:23], v242 offset:4736\n"
"ds_read_b64_tr_b16 a[24:25], v241 offset:192\n"
"ds_read_b64_tr_b16 a[26:27], v241 offset:4800\n"
"ds_read_b64_tr_b16 a[28:29], v242 offset:192\n"
"ds_read_b64_tr_b16 a[30:31], v242 offset:4800\n"
"ds_read_b64_tr_b16 a[32:33], v241 offset:256\n"
"ds_read_b64_tr_b16 a[34:35], v241 offset:4864\n"
"s_waitcnt vmcnt(30) lgkmcnt(0)\n"
"v_mfma_f32_16x16x32_bf16 v[32:35], a[0:3], a[144:147], 0\n"
"ds_read_b64_tr_b16 a[36:37], v241 offset:9216\n"
"s_sub_u32 s77, s0, s1\n"
"v_mfma_f32_16x16x32_bf16 v[36:39], a[0:3], a[148:151], 0\n"
"ds_read_b64_tr_b16 a[38:39], v241 offset:13824\n"
"ds_read_b64_tr_b16 a[40:41], v242 offset:9216\n"
"v_mfma_f32_16x16x32_bf16 v[44:47], a[4:7], a[148:151], 0\n"
"ds_read_b64_tr_b16 a[42:43], v242 offset:13824\n"
"v_mfma_f32_16x16x32_bf16 v[40:43], a[4:7], a[144:147], 0\n"
"buffer_load_dwordx4 a[224:227], v1, s[44:47], s72 offen offset:0 sc1\n"
"v_mfma_f32_16x16x32_bf16 v[48:51], a[8:11], a[144:147], 0\n"
"v_subrev_u32 v20, s77, v241\n"
"v_mfma_f32_16x16x32_bf16 v[52:55], a[8:11], a[148:151], 0\n"
"ds_read_b64_tr_b16 a[44:45], v241 offset:9280\n"
"ds_read_b64_tr_b16 a[46:47], v241 offset:13888\n"
"v_mfma_f32_16x16x32_bf16 v[60:63], a[12:15], a[148:151], 0\n"
"ds_read_b64_tr_b16 a[48:49], v242 offset:9280\n"
"ds_read_b64_tr_b16 a[50:51], v242 offset:13888\n"
"v_mfma_f32_16x16x32_bf16 v[56:59], a[12:15], a[144:147], 0\n"
"buffer_load_dwordx4 a[228:231], v1, s[44:47], s72 offen offset:1024 sc1\n"
"v_mfma_f32_16x16x32_bf16 v[64:67], a[16:19], a[144:147], 0\n"
"s_add_u32 s72, s72, 2048\n"
"v_subrev_u32 v21, s77, v242\n"
"v_mfma_f32_16x16x32_bf16 v[68:71], a[16:19], a[148:151], 0\n"
"ds_read_b64_tr_b16 a[52:53], v241 offset:9344\n"
"ds_read_b64_tr_b16 a[54:55], v241 offset:13952\n"
"v_mfma_f32_16x16x32_bf16 v[76:79], a[20:23], a[148:151], 0\n"
"s_lshl_b32 s77, s69, 2\n"
"ds_read_b64_tr_b16 a[56:57], v242 offset:9344\n"
"v_mfma_f32_16x16x32_bf16 v[72:75], a[20:23], a[144:147], 0\n"
"buffer_load_dwordx4 a[232:235], v1, s[44:47], s72 offen offset:0 sc1\n"
"v_mfma_f32_16x16x32_bf16 v[80:83], a[24:27], a[144:147], 0\n"
"ds_read_b64_tr_b16 a[58:59], v242 offset:13952\n"
"ds_read_b64_tr_b16 a[60:61], v241 offset:9408\n"
"v_mfma_f32_16x16x32_bf16 v[84:87], a[24:27], a[148:151], 0\n"
"s_lshl_b32 s98, 10, s77\n"
"ds_read_b64_tr_b16 a[62:63], v241 offset:14016\n"
"v_mfma_f32_16x16x32_bf16 v[92:95], a[28:31], a[148:151], 0\n"
"ds_read_b64_tr_b16 a[64:65], v242 offset:9408\n"
"ds_read_b64_tr_b16 a[66:67], v242 offset:14016\n"
"ds_read_b64_tr_b16 a[68:69], v241 offset:9472\n"
"ds_read_b64_tr_b16 a[70:71], v241 offset:14080\n"
"v_mfma_f32_16x16x32_bf16 v[88:91], a[28:31], a[144:147], 0\n"
"buffer_load_dwordx4 a[236:239], v1, s[44:47], s72 offen offset:1024 sc1\n"
"v_mfma_f32_16x16x32_bf16 v[96:99], a[32:35], a[144:147], 0\n"
"s_add_u32 s72, s72, 2048\n"
"s_mov_b32 s99, 0\n"
"v_mfma_f32_16x16x32_bf16 v[100:103], a[32:35], a[148:151], 0\n"
"s_mov_b64 s[8:9], s[98:99]\n"
"s_waitcnt vmcnt(32) lgkmcnt(0)\n"
"v_mfma_f32_16x16x32_bf16 v[32:35], a[36:39], a[152:155], v[32:35]\n"
"s_nop 0\n"
"s_barrier\n"
"v_mfma_f32_16x16x32_bf16 v[36:39], a[36:39], a[156:159], v[36:39]\n"
"s_bfe_u64 s[60:61], s[58:59], 0x5000a\n"
"s_mul_i32 s60, s60, 147456\n"
"s_add_u32 s73, s56, s60\n"
"s_add_u32 m0, s0, 0\n"
"v_mfma_f32_16x16x32_bf16 v[44:47], a[40:43], a[156:159], v[44:47]\n"
"buffer_load_dwordx4 v28, s[40:43], s73 offen offset:0 lds\n"
"v_mfma_f32_16x16x32_bf16 v[40:43], a[40:43], a[152:155], v[40:43]\n"
"ds_read_b64_tr_b16 a[0:1], v241 offset:18432\n"
"ds_read_b64_tr_b16 a[2:3], v241 offset:23040\n"
"v_mfma_f32_16x16x32_bf16 v[48:51], a[44:47], a[152:155], v[48:51]\n"
"ds_read_b64_tr_b16 a[4:5], v242 offset:18432\n"
"ds_read_b64_tr_b16 a[6:7], v242 offset:23040\n"
"v_mfma_f32_16x16x32_bf16 v[52:55], a[44:47], a[156:159], v[52:55]\n"
"buffer_load_dwordx4 v29, s[40:43], s73 offen offset:1024 lds\n"
"v_mfma_f32_16x16x32_bf16 v[60:63], a[48:51], a[156:159], v[60:63]\n"
"ds_read_b64_tr_b16 a[8:9], v241 offset:18496\n"
"ds_read_b64_tr_b16 a[10:11], v241 offset:23104\n"
"v_mfma_f32_16x16x32_bf16 v[56:59], a[48:51], a[152:155], v[56:59]\n"
"ds_read_b64_tr_b16 a[12:13], v242 offset:18496\n"
"ds_read_b64_tr_b16 a[14:15], v242 offset:23104\n"
"v_mfma_f32_16x16x32_bf16 v[64:67], a[52:55], a[152:155], v[64:67]\n"
"ds_read_b64_tr_b16 a[16:17], v241 offset:18560\n"
"ds_read_b64_tr_b16 a[18:19], v241 offset:23168\n"
"v_mfma_f32_16x16x32_bf16 v[68:71], a[52:55], a[156:159], v[68:71]\n"
"buffer_load_dwordx4 v30, s[40:43], s73 offen offset:2048 lds\n"
"v_mfma_f32_16x16x32_bf16 v[76:79], a[56:59], a[156:159], v[76:79]\n"
"ds_read_b64_tr_b16 a[20:21], v242 offset:18560\n"
"ds_read_b64_tr_b16 a[22:23], v242 offset:23168\n"
"v_mfma_f32_16x16x32_bf16 v[72:75], a[56:59], a[152:155], v[72:75]\n"
"ds_read_b64_tr_b16 a[24:25], v241 offset:18624\n"
"ds_read_b64_tr_b16 a[26:27], v241 offset:23232\n"
"v_mfma_f32_16x16x32_bf16 v[80:83], a[60:63], a[152:155], v[80:83]\n"
"ds_read_b64_tr_b16 a[28:29], v242 offset:18624\n"
"ds_read_b64_tr_b16 a[30:31], v242 offset:23232\n"
"v_mfma_f32_16x16x32_bf16 v[84:87], a[60:63], a[156:159], v[84:87]\n"
"buffer_load_dwordx4 v31, s[40:43], s73 offen offset:3072 lds\n"
"v_mfma_f32_16x16x32_bf16 v[92:95], a[64:67], a[156:159], v[92:95]\n"
"ds_read_b64_tr_b16 a[32:33], v241 offset:18688\n"
"ds_read_b64_tr_b16 a[34:35], v241 offset:23296\n"
"v_mfma_f32_16x16x32_bf16 v[88:91], a[64:67], a[152:155], v[88:91]\n"
"v_mfma_f32_16x16x32_bf16 v[96:99], a[68:71], a[152:155], v[96:99]\n"
"s_add_u32 m0, s0, 4096\n"
"v_mfma_f32_16x16x32_bf16 v[100:103], a[68:71], a[156:159], v[100:103]\n"
"s_mov_b64 exec, s[96:97]\n"
"buffer_load_dwordx4 v240, s[40:43], s73 offen offset:0 lds\n"
"s_mov_b64 exec, -1\n"
"s_add_u32 s73, s73, 73728\n"
"s_waitcnt vmcnt(35) lgkmcnt(0)\n"
"v_mfma_f32_16x16x32_bf16 v[32:35], a[0:3], a[160:163], v[32:35]\n"
"ds_read_b64_tr_b16 a[36:37], v241 offset:27648\n"
"ds_read_b64_tr_b16 a[38:39], v241 offset:32256\n"
"v_mfma_f32_16x16x32_bf16 v[36:39], a[0:3], a[164:167], v[36:39]\n"
"ds_read_b64_tr_b16 a[40:41], v242 offset:27648\n"
"v_mfma_f32_16x16x32_bf16 v[44:47], a[4:7], a[164:167], v[44:47]\n"
"s_bfe_u64 s[62:63], s[58:59], 0x5000a\n"
"s_mul_i32 s62, s62, 8192\n"
"s_add_u32 s72, s57, s62\n"
"v_mfma_f32_16x16x32_bf16 v[40:43], a[4:7], a[160:163], v[40:43]\n"
"buffer_load_dwordx4 a[144:147], v1, s[44:47], s72 offen offset:0 sc1\n"
"v_mfma_f32_16x16x32_bf16 v[48:51], a[8:11], a[160:163], v[48:51]\n"
"ds_read_b64_tr_b16 a[42:43], v242 offset:32256\n"
"ds_read_b64_tr_b16 a[44:45], v241 offset:27712\n"
"v_mfma_f32_16x16x32_bf16 v[52:55], a[8:11], a[164:167], v[52:55]\n"
"ds_read_b64_tr_b16 a[46:47], v241 offset:32320\n"
"ds_read_b64_tr_b16 a[48:49], v242 offset:27712\n"
"v_mfma_f32_16x16x32_bf16 v[60:63], a[12:15], a[164:167], v[60:63]\n"
"ds_read_b64_tr_b16 a[50:51], v242 offset:32320\n"
"ds_read_b64_tr_b16 a[52:53], v241 offset:27776\n"
"v_mfma_f32_16x16x32_bf16 v[56:59], a[12:15], a[160:163], v[56:59]\n"
"buffer_load_dwordx4 a[148:151], v1, s[44:47], s72 offen offset:1024 sc1\n"
"v_mfma_f32_16x16x32_bf16 v[64:67], a[16:19], a[160:163], v[64:67]\n"
"s_add_u32 s72, s72, 2048\n"
"ds_read_b64_tr_b16 a[54:55], v241 offset:32384\n"
"v_mfma_f32_16x16x32_bf16 v[68:71], a[16:19], a[164:167], v[68:71]\n"
"ds_read_b64_tr_b16 a[56:57], v242 offset:27776\n"
"ds_read_b64_tr_b16 a[58:59], v242 offset:32384\n"
"v_mfma_f32_16x16x32_bf16 v[76:79], a[20:23], a[164:167], v[76:79]\n"
"ds_read_b64_tr_b16 a[60:61], v241 offset:27840\n"
"ds_read_b64_tr_b16 a[62:63], v241 offset:32448\n"
"v_mfma_f32_16x16x32_bf16 v[72:75], a[20:23], a[160:163], v[72:75]\n"
"buffer_load_dwordx4 a[152:155], v1, s[44:47], s72 offen offset:0 sc1\n"
"v_mfma_f32_16x16x32_bf16 v[80:83], a[24:27], a[160:163], v[80:83]\n"
"ds_read_b64_tr_b16 a[64:65], v242 offset:27840\n"
"ds_read_b64_tr_b16 a[66:67], v242 offset:32448\n"
"v_mfma_f32_16x16x32_bf16 v[84:87], a[24:27], a[164:167], v[84:87]\n"
"ds_read_b64_tr_b16 a[68:69], v241 offset:27904\n"
"v_mfma_f32_16x16x32_bf16 v[92:95], a[28:31], a[164:167], v[92:95]\n"
"ds_read_b64_tr_b16 a[70:71], v241 offset:32512\n"
"v_mfma_f32_16x16x32_bf16 v[88:91], a[28:31], a[160:163], v[88:91]\n"
"buffer_load_dwordx4 a[156:159], v1, s[44:47], s72 offen offset:1024 sc1\n"
"v_mfma_f32_16x16x32_bf16 v[96:99], a[32:35], a[160:163], v[96:99]\n"
"s_add_u32 s72, s72, 2048\n"
"v_mfma_f32_16x16x32_bf16 v[100:103], a[32:35], a[164:167], v[100:103]\n"
"s_waitcnt vmcnt(37) lgkmcnt(0)\n"
"v_mfma_f32_16x16x32_bf16 v[32:35], a[36:39], a[168:171], v[32:35]\n"
"s_nop 0\n"
"s_barrier\n"
"v_mfma_f32_16x16x32_bf16 v[36:39], a[36:39], a[172:175], v[36:39]\n"
"s_add_u32 m0, s0, 18432\n"
"ds_read_b64_tr_b16 a[0:1], v241 offset:36864\n"
"v_mfma_f32_16x16x32_bf16 v[44:47], a[40:43], a[172:175], v[44:47]\n"
"buffer_load_dwordx4 v28, s[40:43], s73 offen offset:0 lds\n"
"v_mfma_f32_16x16x32_bf16 v[40:43], a[40:43], a[168:171], v[40:43]\n"
"ds_read_b64_tr_b16 a[2:3], v241 offset:41472\n"
"ds_read_b64_tr_b16 a[4:5], v242 offset:36864\n"
"v_mfma_f32_16x16x32_bf16 v[48:51], a[44:47], a[168:171], v[48:51]\n"
"ds_read_b64_tr_b16 a[6:7], v242 offset:41472\n"
"ds_read_b64_tr_b16 a[8:9], v241 offset:36928\n"
"v_mfma_f32_16x16x32_bf16 v[52:55], a[44:47], a[172:175], v[52:55]\n"
"buffer_load_dwordx4 v29, s[40:43], s73 offen offset:1024 lds\n"
"v_mfma_f32_16x16x32_bf16 v[60:63], a[48:51], a[172:175], v[60:63]\n"
"ds_read_b64_tr_b16 a[10:11], v241 offset:41536\n"
"ds_read_b64_tr_b16 a[12:13], v242 offset:36928\n"
"v_mfma_f32_16x16x32_bf16 v[56:59], a[48:51], a[168:171], v[56:59]\n"
"ds_read_b64_tr_b16 a[14:15], v242 offset:41536\n"
"ds_read_b64_tr_b16 a[16:17], v241 offset:36992\n"
"v_mfma_f32_16x16x32_bf16 v[64:67], a[52:55], a[168:171], v[64:67]\n"
"ds_read_b64_tr_b16 a[18:19], v241 offset:41600\n"
"ds_read_b64_tr_b16 a[20:21], v242 offset:36992\n"
"v_mfma_f32_16x16x32_bf16 v[68:71], a[52:55], a[172:175], v[68:71]\n"
"buffer_load_dwordx4 v30, s[40:43], s73 offen offset:2048 lds\n"
"v_mfma_f32_16x16x32_bf16 v[76:79], a[56:59], a[172:175], v[76:79]\n"
"ds_read_b64_tr_b16 a[22:23], v242 offset:41600\n"
"ds_read_b64_tr_b16 a[24:25], v241 offset:37056\n"
"v_mfma_f32_16x16x32_bf16 v[72:75], a[56:59], a[168:171], v[72:75]\n"
"ds_read_b64_tr_b16 a[26:27], v241 offset:41664\n"
"ds_read_b64_tr_b16 a[28:29], v242 offset:37056\n"
"v_mfma_f32_16x16x32_bf16 v[80:83], a[60:63], a[168:171], v[80:83]\n"
"ds_read_b64_tr_b16 a[30:31], v242 offset:41664\n"
"v_mfma_f32_16x16x32_bf16 v[84:87], a[60:63], a[172:175], v[84:87]\n"
"buffer_load_dwordx4 v31, s[40:43], s73 offen offset:3072 lds\n"
"v_mfma_f32_16x16x32_bf16 v[92:95], a[64:67], a[172:175], v[92:95]\n"
"ds_read_b64_tr_b16 a[32:33], v241 offset:37120\n"
"ds_read_b64_tr_b16 a[34:35], v241 offset:41728\n"
"v_mfma_f32_16x16x32_bf16 v[88:91], a[64:67], a[168:171], v[88:91]\n"
"v_mfma_f32_16x16x32_bf16 v[96:99], a[68:71], a[168:171], v[96:99]\n"
"s_add_u32 m0, s0, 22528\n"
"v_mfma_f32_16x16x32_bf16 v[100:103], a[68:71], a[172:175], v[100:103]\n"
"s_mov_b64 exec, s[96:97]\n"
"buffer_load_dwordx4 v240, s[40:43], s73 offen offset:0 lds\n"
"s_mov_b64 exec, -1\n"
"s_add_u32 s73, s73, 73728\n"
"s_waitcnt vmcnt(40) lgkmcnt(0)\n"
"v_mfma_f32_16x16x32_bf16 v[32:35], a[0:3], a[208:211], v[32:35]\n"
"ds_read_b64_tr_b16 a[36:37], v241 offset:46080\n"
"ds_read_b64_tr_b16 a[38:39], v241 offset:50688\n"
"v_mfma_f32_16x16x32_bf16 v[36:39], a[0:3], a[212:215], v[36:39]\n"
"ds_read_b64_tr_b16 a[40:41], v242 offset:46080\n"
"v_mfma_f32_16x16x32_bf16 v[44:47], a[4:7], a[212:215], v[44:47]\n"
"ds_read_b64_tr_b16 a[42:43], v242 offset:50688\n"
"v_mfma_f32_16x16x32_bf16 v[40:43], a[4:7], a[208:211], v[40:43]\n"
"buffer_load_dwordx4 a[160:163], v1, s[44:47], s72 offen offset:0 sc1\n"
"v_mfma_f32_16x16x32_bf16 v[48:51], a[8:11], a[208:211], v[48:51]\n"
"ds_read_b64_tr_b16 a[44:45], v241 offset:46144\n"
"ds_read_b64_tr_b16 a[46:47], v241 offset:50752\n"
"v_mfma_f32_16x16x32_bf16 v[52:55], a[8:11], a[212:215], v[52:55]\n"
"ds_read_b64_tr_b16 a[48:49], v242 offset:46144\n"
"ds_read_b64_tr_b16 a[50:51], v242 offset:50752\n"
"v_mfma_f32_16x16x32_bf16 v[60:63], a[12:15], a[212:215], v[60:63]\n"
"ds_read_b64_tr_b16 a[52:53], v241 offset:46208\n"
"v_mfma_f32_16x16x32_bf16 v[56:59], a[12:15], a[208:211], v[56:59]\n"
"buffer_load_dwordx4 a[164:167], v1, s[44:47], s72 offen offset:1024 sc1\n"
"v_mfma_f32_16x16x32_bf16 v[64:67], a[16:19], a[208:211], v[64:67]\n"
"s_add_u32 s72, s72, 2048\n"
"ds_read_b64_tr_b16 a[54:55], v241 offset:50816\n"
"v_mfma_f32_16x16x32_bf16 v[68:71], a[16:19], a[212:215], v[68:71]\n"
"ds_read_b64_tr_b16 a[56:57], v242 offset:46208\n"
"ds_read_b64_tr_b16 a[58:59], v242 offset:50816\n"
"v_mfma_f32_16x16x32_bf16 v[76:79], a[20:23], a[212:215], v[76:79]\n"
"ds_read_b64_tr_b16 a[60:61], v241 offset:46272\n"
"ds_read_b64_tr_b16 a[62:63], v241 offset:50880\n"
"v_mfma_f32_16x16x32_bf16 v[72:75], a[20:23], a[208:211], v[72:75]\n"
"buffer_load_dwordx4 a[168:171], v1, s[44:47], s72 offen offset:0 sc1\n"
"v_mfma_f32_16x16x32_bf16 v[80:83], a[24:27], a[208:211], v[80:83]\n"
"ds_read_b64_tr_b16 a[64:65], v242 offset:46272\n"
"ds_read_b64_tr_b16 a[66:67], v242 offset:50880\n"
"v_mfma_f32_16x16x32_bf16 v[84:87], a[24:27], a[212:215], v[84:87]\n"
"ds_read_b64_tr_b16 a[68:69], v241 offset:46336\n"
"v_mfma_f32_16x16x32_bf16 v[92:95], a[28:31], a[212:215], v[92:95]\n"
"ds_read_b64_tr_b16 a[70:71], v241 offset:50944\n"
"v_mfma_f32_16x16x32_bf16 v[88:91], a[28:31], a[208:211], v[88:91]\n"
"buffer_load_dwordx4 a[172:175], v1, s[44:47], s72 offen offset:1024 sc1\n"
"v_mfma_f32_16x16x32_bf16 v[96:99], a[32:35], a[208:211], v[96:99]\n"
"s_add_u32 s72, s72, 2048\n"
"v_mfma_f32_16x16x32_bf16 v[100:103], a[32:35], a[212:215], v[100:103]\n"
"s_waitcnt vmcnt(42) lgkmcnt(0)\n"
"v_mfma_f32_16x16x32_bf16 v[32:35], a[36:39], a[216:219], v[32:35]\n"
"s_nop 0\n"
"s_barrier\n"
"v_mfma_f32_16x16x32_bf16 v[36:39], a[36:39], a[220:223], v[36:39]\n"
"s_add_u32 m0, s0, 36864\n"
"ds_read_b64_tr_b16 a[0:1], v20 offset:0\n"
"v_mfma_f32_16x16x32_bf16 v[44:47], a[40:43], a[220:223], v[44:47]\n"
"buffer_load_dwordx4 v28, s[40:43], s73 offen offset:0 lds\n"
"v_mfma_f32_16x16x32_bf16 v[40:43], a[40:43], a[216:219], v[40:43]\n"
"ds_read_b64_tr_b16 a[2:3], v20 offset:4608\n"
"ds_read_b64_tr_b16 a[4:5], v21 offset:0\n"
"v_mfma_f32_16x16x32_bf16 v[48:51], a[44:47], a[216:219], v[48:51]\n"
"ds_read_b64_tr_b16 a[6:7], v21 offset:4608\n"
"ds_read_b64_tr_b16 a[8:9], v20 offset:64\n"
"v_mfma_f32_16x16x32_bf16 v[52:55], a[44:47], a[220:223], v[52:55]\n"
"buffer_load_dwordx4 v29, s[40:43], s73 offen offset:1024 lds\n"
"v_mfma_f32_16x16x32_bf16 v[60:63], a[48:51], a[220:223], v[60:63]\n"
"ds_read_b64_tr_b16 a[10:11], v20 offset:4672\n"
"ds_read_b64_tr_b16 a[12:13], v21 offset:64\n"
"v_mfma_f32_16x16x32_bf16 v[56:59], a[48:51], a[216:219], v[56:59]\n"
"ds_read_b64_tr_b16 a[14:15], v21 offset:4672\n"
"ds_read_b64_tr_b16 a[16:17], v20 offset:128\n"
"v_mfma_f32_16x16x32_bf16 v[64:67], a[52:55], a[216:219], v[64:67]\n"
"ds_read_b64_tr_b16 a[18:19], v20 offset:4736\n"
"ds_read_b64_tr_b16 a[20:21], v21 offset:128\n"
"v_mfma_f32_16x16x32_bf16 v[68:71], a[52:55], a[220:223], v[68:71]\n"
"buffer_load_dwordx4 v30, s[40:43], s73 offen offset:2048 lds\n"
"v_mfma_f32_16x16x32_bf16 v[76:79], a[56:59], a[220:223], v[76:79]\n"
"ds_read_b64_tr_b16 a[22:23], v21 offset:4736\n"
"ds_read_b64_tr_b16 a[24:25], v20 offset:192\n"
"v_mfma_f32_16x16x32_bf16 v[72:75], a[56:59], a[216:219], v[72:75]\n"
"ds_read_b64_tr_b16 a[26:27], v20 offset:4800\n"
"ds_read_b64_tr_b16 a[28:29], v21 offset:192\n"
"v_mfma_f32_16x16x32_bf16 v[80:83], a[60:63], a[216:219], v[80:83]\n"
"ds_read_b64_tr_b16 a[30:31], v21 offset:4800\n"
"v_mfma_f32_16x16x32_bf16 v[84:87], a[60:63], a[220:223], v[84:87]\n"
"buffer_load_dwordx4 v31, s[40:43], s73 offen offset:3072 lds\n"
"v_mfma_f32_16x16x32_bf16 v[92:95], a[64:67], a[220:223], v[92:95]\n"
"ds_read_b64_tr_b16 a[32:33], v20 offset:256\n"
"ds_read_b64_tr_b16 a[34:35], v20 offset:4864\n"
"v_mfma_f32_16x16x32_bf16 v[88:91], a[64:67], a[216:219], v[88:91]\n"
"v_mfma_f32_16x16x32_bf16 v[96:99], a[68:71], a[216:219], v[96:99]\n"
"s_add_u32 m0, s0, 40960\n"
"v_mfma_f32_16x16x32_bf16 v[100:103], a[68:71], a[220:223], v[100:103]\n"
"s_mov_b64 exec, s[96:97]\n"
"buffer_load_dwordx4 v240, s[40:43], s73 offen offset:0 lds\n"
"s_mov_b64 exec, -1\n"
"s_add_u32 s73, s73, 73728\n"
"s_waitcnt vmcnt(25) lgkmcnt(0)\n"
"v_mfma_f32_16x16x32_bf16 v[32:35], a[0:3], a[224:227], v[32:35]\n"
"ds_read_b64_tr_b16 a[36:37], v20 offset:9216\n"
"ds_read_b64_tr_b16 a[38:39], v20 offset:13824\n"
"v_mfma_f32_16x16x32_bf16 v[36:39], a[0:3], a[228:231], v[36:39]\n"
"ds_read_b64_tr_b16 a[40:41], v21 offset:9216\n"
"v_mfma_f32_16x16x32_bf16 v[44:47], a[4:7], a[228:231], v[44:47]\n"
"ds_read_b64_tr_b16 a[42:43], v21 offset:13824\n"
"v_mfma_f32_16x16x32_bf16 v[40:43], a[4:7], a[224:227], v[40:43]\n"
"buffer_load_dwordx4 a[208:211], v1, s[44:47], s72 offen offset:0 sc1\n"
"v_mfma_f32_16x16x32_bf16 v[48:51], a[8:11], a[224:227], v[48:51]\n"
"ds_read_b64_tr_b16 a[44:45], v20 offset:9280\n"
"ds_read_b64_tr_b16 a[46:47], v20 offset:13888\n"
"v_mfma_f32_16x16x32_bf16 v[52:55], a[8:11], a[228:231], v[52:55]\n"
"ds_read_b64_tr_b16 a[48:49], v21 offset:9280\n"
"ds_read_b64_tr_b16 a[50:51], v21 offset:13888\n"
"v_mfma_f32_16x16x32_bf16 v[60:63], a[12:15], a[228:231], v[60:63]\n"
"ds_read_b64_tr_b16 a[52:53], v20 offset:9344\n"
"v_mfma_f32_16x16x32_bf16 v[56:59], a[12:15], a[224:227], v[56:59]\n"
"buffer_load_dwordx4 a[212:215], v1, s[44:47], s72 offen offset:1024 sc1\n"
"v_mfma_f32_16x16x32_bf16 v[64:67], a[16:19], a[224:227], v[64:67]\n"
"s_add_u32 s72, s72, 2048\n"
"ds_read_b64_tr_b16 a[54:55], v20 offset:13952\n"
"v_mfma_f32_16x16x32_bf16 v[68:71], a[16:19], a[228:231], v[68:71]\n"
"ds_read_b64_tr_b16 a[56:57], v21 offset:9344\n"
"ds_read_b64_tr_b16 a[58:59], v21 offset:13952\n"
"v_mfma_f32_16x16x32_bf16 v[76:79], a[20:23], a[228:231], v[76:79]\n"
"ds_read_b64_tr_b16 a[60:61], v20 offset:9408\n"
"ds_read_b64_tr_b16 a[62:63], v20 offset:14016\n"
"v_mfma_f32_16x16x32_bf16 v[72:75], a[20:23], a[224:227], v[72:75]\n"
"buffer_load_dwordx4 a[216:219], v1, s[44:47], s72 offen offset:0 sc1\n"
"v_mfma_f32_16x16x32_bf16 v[80:83], a[24:27], a[224:227], v[80:83]\n"
"ds_read_b64_tr_b16 a[64:65], v21 offset:9408\n"
"ds_read_b64_tr_b16 a[66:67], v21 offset:14016\n"
"v_mfma_f32_16x16x32_bf16 v[84:87], a[24:27], a[228:231], v[84:87]\n"
"ds_read_b64_tr_b16 a[68:69], v20 offset:9472\n"
"v_mfma_f32_16x16x32_bf16 v[92:95], a[28:31], a[228:231], v[92:95]\n"
"ds_read_b64_tr_b16 a[70:71], v20 offset:14080\n"
"v_mfma_f32_16x16x32_bf16 v[88:91], a[28:31], a[224:227], v[88:91]\n"
"buffer_load_dwordx4 a[220:223], v1, s[44:47], s72 offen offset:1024 sc1\n"
"v_mfma_f32_16x16x32_bf16 v[96:99], a[32:35], a[224:227], v[96:99]\n"
"s_add_u32 s72, s72, 2048\n"
"v_mfma_f32_16x16x32_bf16 v[100:103], a[32:35], a[228:231], v[100:103]\n"
"buffer_load_dwordx2 v[248:249], v251, s[36:39], s91 offen sc1\n"
"s_waitcnt vmcnt(28) lgkmcnt(0)\n"
"v_mfma_f32_16x16x32_bf16 v[32:35], a[36:39], a[232:235], v[32:35]\n"
"s_waitcnt vmcnt(23)\n"
"s_barrier\n"
"v_mfma_f32_16x16x32_bf16 v[36:39], a[36:39], a[236:239], v[36:39]\n"
"s_add_u32 m0, s1, 0\n"
"ds_read_b64_tr_b16 a[0:1], v241 offset:0\n"
"v_mfma_f32_16x16x32_bf16 v[44:47], a[40:43], a[236:239], v[44:47]\n"
"buffer_load_dwordx4 v28, s[40:43], s73 offen offset:0 lds\n"
"v_mfma_f32_16x16x32_bf16 v[40:43], a[40:43], a[232:235], v[40:43]\n"
"ds_read_b64_tr_b16 a[2:3], v241 offset:4608\n"
"ds_read_b64_tr_b16 a[4:5], v242 offset:0\n"
"v_mfma_f32_16x16x32_bf16 v[48:51], a[44:47], a[232:235], v[48:51]\n"
"ds_read_b64_tr_b16 a[6:7], v242 offset:4608\n"
"ds_read_b64_tr_b16 a[8:9], v241 offset:64\n"
"v_mfma_f32_16x16x32_bf16 v[52:55], a[44:47], a[236:239], v[52:55]\n"
"buffer_load_dwordx4 v29, s[40:43], s73 offen offset:1024 lds\n"
"v_mfma_f32_16x16x32_bf16 v[60:63], a[48:51], a[236:239], v[60:63]\n"
"ds_read_b64_tr_b16 a[10:11], v241 offset:4672\n"
"ds_read_b64_tr_b16 a[12:13], v242 offset:64\n"
"v_mfma_f32_16x16x32_bf16 v[56:59], a[48:51], a[232:235], v[56:59]\n"
"ds_read_b64_tr_b16 a[14:15], v242 offset:4672\n"
"ds_read_b64_tr_b16 a[16:17], v241 offset:128\n"
"v_mfma_f32_16x16x32_bf16 v[64:67], a[52:55], a[232:235], v[64:67]\n"
"ds_read_b64_tr_b16 a[18:19], v241 offset:4736\n"
"ds_read_b64_tr_b16 a[20:21], v242 offset:128\n"
"v_mfma_f32_16x16x32_bf16 v[68:71], a[52:55], a[236:239], v[68:71]\n"
"buffer_load_dwordx4 v30, s[40:43], s73 offen offset:2048 lds\n"
"v_mfma_f32_16x16x32_bf16 v[76:79], a[56:59], a[236:239], v[76:79]\n"
"ds_read_b64_tr_b16 a[22:23], v242 offset:4736\n"
"ds_read_b64_tr_b16 a[24:25], v241 offset:192\n"
"v_mfma_f32_16x16x32_bf16 v[72:75], a[56:59], a[232:235], v[72:75]\n"
"ds_read_b64_tr_b16 a[26:27], v241 offset:4800\n"
"ds_read_b64_tr_b16 a[28:29], v242 offset:192\n"
"v_mfma_f32_16x16x32_bf16 v[80:83], a[60:63], a[232:235], v[80:83]\n"
"ds_read_b64_tr_b16 a[30:31], v242 offset:4800\n"
"v_mfma_f32_16x16x32_bf16 v[84:87], a[60:63], a[236:239], v[84:87]\n"
"buffer_load_dwordx4 v31, s[40:43], s73 offen offset:3072 lds\n"
"v_mfma_f32_16x16x32_bf16 v[92:95], a[64:67], a[236:239], v[92:95]\n"
"ds_read_b64_tr_b16 a[32:33], v241 offset:256\n"
"ds_read_b64_tr_b16 a[34:35], v241 offset:4864\n"
"v_mfma_f32_16x16x32_bf16 v[88:91], a[64:67], a[232:235], v[88:91]\n"
"v_mfma_f32_16x16x32_bf16 v[96:99], a[68:71], a[232:235], v[96:99]\n"
"s_add_u32 m0, s1, 4096\n"
"s_mov_b64 exec, 1\n"
"buffer_store_dword v252, v250, s[36:39], s90 offen sc1\n"
"s_mov_b64 exec, -1\n"
"v_mfma_f32_16x16x32_bf16 v[100:103], a[68:71], a[236:239], v[100:103]\n"
"s_mov_b64 exec, s[96:97]\n"
"buffer_load_dwordx4 v240, s[40:43], s73 offen offset:0 lds\n"
"s_mov_b64 exec, -1\n"
"s_add_u32 s73, s73, 73728\n"
"s_waitcnt vmcnt(27) lgkmcnt(0)\n"
"v_mfma_f32_16x16x32_bf16 v[32:35], a[0:3], a[144:147], v[32:35]\n"
"ds_read_b64_tr_b16 a[36:37], v241 offset:9216\n"
"ds_read_b64_tr_b16 a[38:39], v241 offset:13824\n"
"v_mfma_f32_16x16x32_bf16 v[36:39], a[0:3], a[148:151], v[36:39]\n"
"ds_read_b64_tr_b16 a[40:41], v242 offset:9216\n"
"v_mfma_f32_16x16x32_bf16 v[44:47], a[4:7], a[148:151], v[44:47]\n"
"ds_read_b64_tr_b16 a[42:43], v242 offset:13824\n"
"v_mfma_f32_16x16x32_bf16 v[40:43], a[4:7], a[144:147], v[40:43]\n"
"buffer_load_dwordx4 a[224:227], v1, s[44:47], s72 offen offset:0 sc1\n"
"v_mfma_f32_16x16x32_bf16 v[48:51], a[8:11], a[144:147], v[48:51]\n"
"ds_read_b64_tr_b16 a[44:45], v241 offset:9280\n"
"ds_read_b64_tr_b16 a[46:47], v241 offset:13888\n"
"v_mfma_f32_16x16x32_bf16 v[52:55], a[8:11], a[148:151], v[52:55]\n"
"ds_read_b64_tr_b16 a[48:49], v242 offset:9280\n"
"ds_read_b64_tr_b16 a[50:51], v242 offset:13888\n"
"v_mfma_f32_16x16x32_bf16 v[60:63], a[12:15], a[148:151], v[60:63]\n"
"ds_read_b64_tr_b16 a[52:53], v241 offset:9344\n"
"v_mfma_f32_16x16x32_bf16 v[56:59], a[12:15], a[144:147], v[56:59]\n"
"buffer_load_dwordx4 a[228:231], v1, s[44:47], s72 offen offset:1024 sc1\n"
"v_mfma_f32_16x16x32_bf16 v[64:67], a[16:19], a[144:147], v[64:67]\n"
"s_add_u32 s72, s72, 2048\n"
"ds_read_b64_tr_b16 a[54:55], v241 offset:13952\n"
"v_mfma_f32_16x16x32_bf16 v[68:71], a[16:19], a[148:151], v[68:71]\n"
"ds_read_b64_tr_b16 a[56:57], v242 offset:9344\n"
"ds_read_b64_tr_b16 a[58:59], v242 offset:13952\n"
"v_mfma_f32_16x16x32_bf16 v[76:79], a[20:23], a[148:151], v[76:79]\n"
"ds_read_b64_tr_b16 a[60:61], v241 offset:9408\n"
"ds_read_b64_tr_b16 a[62:63], v241 offset:14016\n"
"v_mfma_f32_16x16x32_bf16 v[72:75], a[20:23], a[144:147], v[72:75]\n"
"buffer_load_dwordx4 a[232:235], v1, s[44:47], s72 offen offset:0 sc1\n"
"v_mfma_f32_16x16x32_bf16 v[80:83], a[24:27], a[144:147], v[80:83]\n"
"ds_read_b64_tr_b16 a[64:65], v242 offset:9408\n"
"ds_read_b64_tr_b16 a[66:67], v242 offset:14016\n"
"v_mfma_f32_16x16x32_bf16 v[84:87], a[24:27], a[148:151], v[84:87]\n"
"ds_read_b64_tr_b16 a[68:69], v241 offset:9472\n"
"v_mfma_f32_16x16x32_bf16 v[92:95], a[28:31], a[148:151], v[92:95]\n"
"ds_read_b64_tr_b16 a[70:71], v241 offset:14080\n"
"v_mfma_f32_16x16x32_bf16 v[88:91], a[28:31], a[144:147], v[88:91]\n"
"buffer_load_dwordx4 a[236:239], v1, s[44:47], s72 offen offset:1024 sc1\n"
"v_mfma_f32_16x16x32_bf16 v[96:99], a[32:35], a[144:147], v[96:99]\n"
"s_add_u32 s72, s72, 2048\n"
"v_mfma_f32_16x16x32_bf16 v[100:103], a[32:35], a[148:151], v[100:103]\n"
"s_waitcnt vmcnt(29) lgkmcnt(0)\n"
"v_mfma_f32_16x16x32_bf16 v[32:35], a[36:39], a[152:155], v[32:35]\n"
"s_waitcnt vmcnt(24)\n"
"s_barrier\n"
"v_mfma_f32_16x16x32_bf16 v[36:39], a[36:39], a[156:159], v[36:39]\n"
"s_bfe_u64 s[60:61], s[58:59], 0x50014\n"
"s_mul_i32 s60, s60, 147456\n"
"s_add_u32 s73, s56, s60\n"
"s_add_u32 m0, s0, 0\n"
"v_mfma_f32_16x16x32_bf16 v[44:47], a[40:43], a[156:159], v[44:47]\n"
"buffer_load_dwordx4 v28, s[40:43], s73 offen offset:0 lds\n"
"v_mfma_f32_16x16x32_bf16 v[40:43], a[40:43], a[152:155], v[40:43]\n"
"ds_read_b64_tr_b16 a[0:1], v241 offset:18432\n"
"ds_read_b64_tr_b16 a[2:3], v241 offset:23040\n"
"v_mfma_f32_16x16x32_bf16 v[48:51], a[44:47], a[152:155], v[48:51]\n"
"ds_read_b64_tr_b16 a[4:5], v242 offset:18432\n"
"ds_read_b64_tr_b16 a[6:7], v242 offset:23040\n"
"v_mfma_f32_16x16x32_bf16 v[52:55], a[44:47], a[156:159], v[52:55]\n"
"buffer_load_dwordx4 v29, s[40:43], s73 offen offset:1024 lds\n"
"v_mfma_f32_16x16x32_bf16 v[60:63], a[48:51], a[156:159], v[60:63]\n"
"ds_read_b64_tr_b16 a[8:9], v241 offset:18496\n"
"ds_read_b64_tr_b16 a[10:11], v241 offset:23104\n"
"v_mfma_f32_16x16x32_bf16 v[56:59], a[48:51], a[152:155], v[56:59]\n"
"ds_read_b64_tr_b16 a[12:13], v242 offset:18496\n"
"ds_read_b64_tr_b16 a[14:15], v242 offset:23104\n"
"v_mfma_f32_16x16x32_bf16 v[64:67], a[52:55], a[152:155], v[64:67]\n"
"ds_read_b64_tr_b16 a[16:17], v241 offset:18560\n"
"ds_read_b64_tr_b16 a[18:19], v241 offset:23168\n"
"v_mfma_f32_16x16x32_bf16 v[68:71], a[52:55], a[156:159], v[68:71]\n"
"buffer_load_dwordx4 v30, s[40:43], s73 offen offset:2048 lds\n"
"v_mfma_f32_16x16x32_bf16 v[76:79], a[56:59], a[156:159], v[76:79]\n"
"ds_read_b64_tr_b16 a[20:21], v242 offset:18560\n"
"ds_read_b64_tr_b16 a[22:23], v242 offset:23168\n"
"v_mfma_f32_16x16x32_bf16 v[72:75], a[56:59], a[152:155], v[72:75]\n"
"ds_read_b64_tr_b16 a[24:25], v241 offset:18624\n"
"ds_read_b64_tr_b16 a[26:27], v241 offset:23232\n"
"v_mfma_f32_16x16x32_bf16 v[80:83], a[60:63], a[152:155], v[80:83]\n"
"ds_read_b64_tr_b16 a[28:29], v242 offset:18624\n"
"ds_read_b64_tr_b16 a[30:31], v242 offset:23232\n"
"v_mfma_f32_16x16x32_bf16 v[84:87], a[60:63], a[156:159], v[84:87]\n"
"buffer_load_dwordx4 v31, s[40:43], s73 offen offset:3072 lds\n"
"v_mfma_f32_16x16x32_bf16 v[92:95], a[64:67], a[156:159], v[92:95]\n"
"ds_read_b64_tr_b16 a[32:33], v241 offset:18688\n"
"ds_read_b64_tr_b16 a[34:35], v241 offset:23296\n"
"v_mfma_f32_16x16x32_bf16 v[88:91], a[64:67], a[152:155], v[88:91]\n"
"v_mfma_f32_16x16x32_bf16 v[96:99], a[68:71], a[152:155], v[96:99]\n"
"s_add_u32 m0, s0, 4096\n"
"v_mfma_f32_16x16x32_bf16 v[100:103], a[68:71], a[156:159], v[100:103]\n"
"s_mov_b64 exec, s[96:97]\n"
"buffer_load_dwordx4 v240, s[40:43], s73 offen offset:0 lds\n"
"s_mov_b64 exec, -1\n"
"s_add_u32 s73, s73, 73728\n"
"s_waitcnt vmcnt(15)\n"
"v_cmp_eq_u32 vcc, s34, v248\n"
"s_nop 0\n"
"s_andn2_b64 s[88:89], s[98:99], vcc\n"
"v_cmp_eq_u32 vcc, s34, v249\n"
"s_nop 0\n"
"s_andn2_b64 vcc, s[8:9], vcc\n"
"s_or_b64 s[88:89], s[88:89], vcc\n"
"s_cbranch_scc0 L_plB_ok\n"
"s_mov_b32 s55, 0x30000\n"
"L_plB_re:\n"
"s_sleep 1\n"
"buffer_load_dwordx2 v[248:249], v251, s[36:39], s91 offen sc1\n"
"s_waitcnt vmcnt(0)\n"
"v_cmp_eq_u32 vcc, s34, v248\n"
"s_nop 0\n"
"s_andn2_b64 s[88:89], s[98:99], vcc\n"
"v_cmp_eq_u32 vcc, s34, v249\n"
"s_nop 0\n"
"s_andn2_b64 vcc, s[8:9], vcc\n"
"s_or_b64 s[88:89], s[88:89], vcc\n"
"s_cbranch_scc0 L_plB_ok\n"
"s_sub_u32 s55, s55, 1\n"
"s_cmp_lg_u32 s55, 0\n"
"s_cbranch_scc1 L_plB_re\n"
"L_plB_ok:\n"
"s_waitcnt lgkmcnt(0)\n"
"v_mfma_f32_16x16x32_bf16 v[32:35], a[0:3], a[160:163], v[32:35]\n"
"ds_read_b64_tr_b16 a[36:37], v241 offset:27648\n"
"ds_read_b64_tr_b16 a[38:39], v241 offset:32256\n"
"v_mfma_f32_16x16x32_bf16 v[36:39], a[0:3], a[164:167], v[36:39]\n"
"ds_read_b64_tr_b16 a[40:41], v242 offset:27648\n"
"v_mfma_f32_16x16x32_bf16 v[44:47], a[4:7], a[164:167], v[44:47]\n"
"s_bfe_u64 s[62:63], s[58:59], 0x50014\n"
"s_mul_i32 s62, s62, 8192\n"
"s_add_u32 s72, s57, s62\n"
"v_mfma_f32_16x16x32_bf16 v[40:43], a[4:7], a[160:163], v[40:43]\n"
"buffer_load_dwordx4 a[144:147], v1, s[44:47], s72 offen offset:0 sc1\n"
"v_mfma_f32_16x16x32_bf16 v[48:51], a[8:11], a[160:163], v[48:51]\n"
"ds_read_b64_tr_b16 a[42:43], v242 offset:32256\n"
"ds_read_b64_tr_b16 a[44:45], v241 offset:27712\n"
"v_mfma_f32_16x16x32_bf16 v[52:55], a[8:11], a[164:167], v[52:55]\n"
"ds_read_b64_tr_b16 a[46:47], v241 offset:32320\n"
"ds_read_b64_tr_b16 a[48:49], v242 offset:27712\n"
"v_mfma_f32_16x16x32_bf16 v[60:63], a[12:15], a[164:167], v[60:63]\n"
"ds_read_b64_tr_b16 a[50:51], v242 offset:32320\n"
"ds_read_b64_tr_b16 a[52:53], v241 offset:27776\n"
"v_mfma_f32_16x16x32_bf16 v[56:59], a[12:15], a[160:163], v[56:59]\n"
"buffer_load_dwordx4 a[148:151], v1, s[44:47], s72 offen offset:1024 sc1\n"
"v_mfma_f32_16x16x32_bf16 v[64:67], a[16:19], a[160:163], v[64:67]\n"
"s_add_u32 s72, s72, 2048\n"
"ds_read_b64_tr_b16 a[54:55], v241 offset:32384\n"
"v_mfma_f32_16x16x32_bf16 v[68:71], a[16:19], a[164:167], v[68:71]\n"
"ds_read_b64_tr_b16 a[56:57], v242 offset:27776\n"
"ds_read_b64_tr_b16 a[58:59], v242 offset:32384\n"
"v_mfma_f32_16x16x32_bf16 v[76:79], a[20:23], a[164:167], v[76:79]\n"
"ds_read_b64_tr_b16 a[60:61], v241 offset:27840\n"
"ds_read_b64_tr_b16 a[62:63], v241 offset:32448\n"
"v_mfma_f32_16x16x32_bf16 v[72:75], a[20:23], a[160:163], v[72:75]\n"
"buffer_load_dwordx4 a[152:155], v1, s[44:47], s72 offen offset:0 sc1\n"
"v_mfma_f32_16x16x32_bf16 v[80:83], a[24:27], a[160:163], v[80:83]\n"
"ds_read_b64_tr_b16 a[64:65], v242 offset:27840\n"
"ds_read_b64_tr_b16 a[66:67], v242 offset:32448\n"
"v_mfma_f32_16x16x32_bf16 v[84:87], a[24:27], a[164:167], v[84:87]\n"
"ds_read_b64_tr_b16 a[68:69], v241 offset:27904\n"
"v_mfma_f32_16x16x32_bf16 v[92:95], a[28:31], a[164:167], v[92:95]\n"
"ds_read_b64_tr_b16 a[70:71], v241 offset:32512\n"
"v_mfma_f32_16x16x32_bf16 v[88:91], a[28:31], a[160:163], v[88:91]\n"
"buffer_load_dwordx4 a[156:159], v1, s[44:47], s72 offen offset:1024 sc1\n"
"v_mfma_f32_16x16x32_bf16 v[96:99], a[32:35], a[160:163], v[96:99]\n"
"s_add_u32 s72, s72, 2048\n"
"v_mfma_f32_16x16x32_bf16 v[100:103], a[32:35], a[164:167], v[100:103]\n"
"s_waitcnt lgkmcnt(0)\n"
"v_mfma_f32_16x16x32_bf16 v[32:35], a[36:39], a[168:171], v[32:35]\n"
"s_nop 0\n"
"s_barrier\n"
"v_mfma_f32_16x16x32_bf16 v[36:39], a[36:39], a[172:175], v[36:39]\n"
"s_add_u32 m0, s0, 18432\n"
"ds_read_b64_tr_b16 a[0:1], v241 offset:36864\n"
"v_mfma_f32_16x16x32_bf16 v[44:47], a[40:43], a[172:175], v[44:47]\n"
"buffer_load_dwordx4 v28, s[40:43], s73 offen offset:0 lds\n"
"v_mfma_f32_16x16x32_bf16 v[40:43], a[40:43], a[168:171], v[40:43]\n"
"ds_read_b64_tr_b16 a[2:3], v241 offset:41472\n"
"ds_read_b64_tr_b16 a[4:5], v242 offset:36864\n"
"v_mfma_f32_16x16x32_bf16 v[48:51], a[44:47], a[168:171], v[48:51]\n"
"ds_read_b64_tr_b16 a[6:7], v242 offset:41472\n"
"ds_read_b64_tr_b16 a[8:9], v241 offset:36928\n"
"v_mfma_f32_16x16x32_bf16 v[52:55], a[44:47], a[172:175], v[52:55]\n"
"buffer_load_dwordx4 v29, s[40:43], s73 offen offset:1024 lds\n"
"v_mfma_f32_16x16x32_bf16 v[60:63], a[48:51], a[172:175], v[60:63]\n"
"ds_read_b64_tr_b16 a[10:11], v241 offset:41536\n"
"ds_read_b64_tr_b16 a[12:13], v242 offset:36928\n"
"v_mfma_f32_16x16x32_bf16 v[56:59], a[48:51], a[168:171], v[56:59]\n"
"ds_read_b64_tr_b16 a[14:15], v242 offset:41536\n"
"ds_read_b64_tr_b16 a[16:17], v241 offset:36992\n"
"v_mfma_f32_16x16x32_bf16 v[64:67], a[52:55], a[168:171], v[64:67]\n"
"ds_read_b64_tr_b16 a[18:19], v241 offset:41600\n"
"ds_read_b64_tr_b16 a[20:21], v242 offset:36992\n"
"v_mfma_f32_16x16x32_bf16 v[68:71], a[52:55], a[172:175], v[68:71]\n"
"buffer_load_dwordx4 v30, s[40:43], s73 offen offset:2048 lds\n"
"v_mfma_f32_16x16x32_bf16 v[76:79], a[56:59], a[172:175], v[76:79]\n"
"ds_read_b64_tr_b16 a[22:23], v242 offset:41600\n"
"ds_read_b64_tr_b16 a[24:25], v241 offset:37056\n"
"v_mfma_f32_16x16x32_bf16 v[72:75], a[56:59], a[168:171], v[72:75]\n"
"ds_read_b64_tr_b16 a[26:27], v241 offset:41664\n"
"ds_read_b64_tr_b16 a[28:29], v242 offset:37056\n"
"v_mfma_f32_16x16x32_bf16 v[80:83], a[60:63], a[168:171], v[80:83]\n"
"ds_read_b64_tr_b16 a[30:31], v242 offset:41664\n"
"v_mfma_f32_16x16x32_bf16 v[84:87], a[60:63], a[172:175], v[84:87]\n"
"buffer_load_dwordx4 v31, s[40:43], s73 offen offset:3072 lds\n"
"v_mfma_f32_16x16x32_bf16 v[92:95], a[64:67], a[172:175], v[92:95]\n"
"ds_read_b64_tr_b16 a[32:33], v241 offset:37120\n"
"ds_read_b64_tr_b16 a[34:35], v241 offset:41728\n"
"v_mfma_f32_16x16x32_bf16 v[88:91], a[64:67], a[168:171], v[88:91]\n"
"v_mfma_f32_16x16x32_bf16 v[96:99], a[68:71], a[168:171], v[96:99]\n"
"s_add_u32 m0, s0, 22528\n"
"v_mfma_f32_16x16x32_bf16 v[100:103], a[68:71], a[172:175], v[100:103]\n"
"s_mov_b64 exec, s[96:97]\n"
"buffer_load_dwordx4 v240, s[40:43], s73 offen offset:0 lds\n"
"s_mov_b64 exec, -1\n"
"s_add_u32 s73, s73, 73728\n"
"s_waitcnt lgkmcnt(0)\n"
"v_mfma_f32_16x16x32_bf16 v[32:35], a[0:3], a[208:211], v[32:35]\n"
"ds_read_b64_tr_b16 a[36:37], v241 offset:46080\n"
"ds_read_b64_tr_b16 a[38:39], v241 offset:50688\n"
"v_mfma_f32_16x16x32_bf16 v[36:39], a[0:3], a[212:215], v[36:39]\n"
"ds_read_b64_tr_b16 a[40:41], v242 offset:46080\n"
"v_mfma_f32_16x16x32_bf16 v[44:47], a[4:7], a[212:215], v[44:47]\n"
"ds_read_b64_tr_b16 a[42:43], v242 offset:50688\n"
"v_mfma_f32_16x16x32_bf16 v[40:43], a[4:7], a[208:211], v[40:43]\n"
"buffer_load_dwordx4 a[160:163], v1, s[44:47], s72 offen offset:0 sc1\n"
"v_mfma_f32_16x16x32_bf16 v[48:51], a[8:11], a[208:211], v[48:51]\n"
"ds_read_b64_tr_b16 a[44:45], v241 offset:46144\n"
"ds_read_b64_tr_b16 a[46:47], v241 offset:50752\n"
"v_mfma_f32_16x16x32_bf16 v[52:55], a[8:11], a[212:215], v[52:55]\n"
"ds_read_b64_tr_b16 a[48:49], v242 offset:46144\n"
"ds_read_b64_tr_b16 a[50:51], v242 offset:50752\n"
"v_mfma_f32_16x16x32_bf16 v[60:63], a[12:15], a[212:215], v[60:63]\n"
"ds_read_b64_tr_b16 a[52:53], v241 offset:46208\n"
"v_mfma_f32_16x16x32_bf16 v[56:59], a[12:15], a[208:211], v[56:59]\n"
"buffer_load_dwordx4 a[164:167], v1, s[44:47], s72 offen offset:1024 sc1\n"
"v_mfma_f32_16x16x32_bf16 v[64:67], a[16:19], a[208:211], v[64:67]\n"
"s_add_u32 s72, s72, 2048\n"
"ds_read_b64_tr_b16 a[54:55], v241 offset:50816\n"
"v_mfma_f32_16x16x32_bf16 v[68:71], a[16:19], a[212:215], v[68:71]\n"
"ds_read_b64_tr_b16 a[56:57], v242 offset:46208\n"
"ds_read_b64_tr_b16 a[58:59], v242 offset:50816\n"
"v_mfma_f32_16x16x32_bf16 v[76:79], a[20:23], a[212:215], v[76:79]\n"
"ds_read_b64_tr_b16 a[60:61], v241 offset:46272\n"
"ds_read_b64_tr_b16 a[62:63], v241 offset:50880\n"
"v_mfma_f32_16x16x32_bf16 v[72:75], a[20:23], a[208:211], v[72:75]\n"
"buffer_load_dwordx4 a[168:171], v1, s[44:47], s72 offen offset:0 sc1\n"
"v_mfma_f32_16x16x32_bf16 v[80:83], a[24:27], a[208:211], v[80:83]\n"
"ds_read_b64_tr_b16 a[64:65], v242 offset:46272\n"
"ds_read_b64_tr_b16 a[66:67], v242 offset:50880\n"
"v_mfma_f32_16x16x32_bf16 v[84:87], a[24:27], a[212:215], v[84:87]\n"
"ds_read_b64_tr_b16 a[68:69], v241 offset:46336\n"
"v_mfma_f32_16x16x32_bf16 v[92:95], a[28:31], a[212:215], v[92:95]\n"
"ds_read_b64_tr_b16 a[70:71], v241 offset:50944\n"
"v_mfma_f32_16x16x32_bf16 v[88:91], a[28:31], a[208:211], v[88:91]\n"
"buffer_load_dwordx4 a[172:175], v1, s[44:47], s72 offen offset:1024 sc1\n"
"v_mfma_f32_16x16x32_bf16 v[96:99], a[32:35], a[208:211], v[96:99]\n"
"s_add_u32 s72, s72, 2048\n"
"v_mfma_f32_16x16x32_bf16 v[100:103], a[32:35], a[212:215], v[100:103]\n"
"s_waitcnt lgkmcnt(0)\n"
"v_mfma_f32_16x16x32_bf16 v[32:35], a[36:39], a[216:219], v[32:35]\n"
"s_waitcnt vmcnt(22)\n"
"s_barrier\n"
"v_mfma_f32_16x16x32_bf16 v[36:39], a[36:39], a[220:223], v[36:39]\n"
"s_add_u32 m0, s0, 36864\n"
"ds_read_b64_tr_b16 a[0:1], v20 offset:0\n"
"v_mfma_f32_16x16x32_bf16 v[44:47], a[40:43], a[220:223], v[44:47]\n"
"buffer_load_dwordx4 v28, s[40:43], s73 offen offset:0 lds\n"
"v_mfma_f32_16x16x32_bf16 v[40:43], a[40:43], a[216:219], v[40:43]\n"
"ds_read_b64_tr_b16 a[2:3], v20 offset:4608\n"
"ds_read_b64_tr_b16 a[4:5], v21 offset:0\n"
"v_mfma_f32_16x16x32_bf16 v[48:51], a[44:47], a[216:219], v[48:51]\n"
"ds_read_b64_tr_b16 a[6:7], v21 offset:4608\n"
"ds_read_b64_tr_b16 a[8:9], v20 offset:64\n"
"v_mfma_f32_16x16x32_bf16 v[52:55], a[44:47], a[220:223], v[52:55]\n"
"buffer_load_dwordx4 v29, s[40:43], s73 offen offset:1024 lds\n"
"v_mfma_f32_16x16x32_bf16 v[60:63], a[48:51], a[220:223], v[60:63]\n"
"ds_read_b64_tr_b16 a[10:11], v20 offset:4672\n"
"ds_read_b64_tr_b16 a[12:13], v21 offset:64\n"
"v_mfma_f32_16x16x32_bf16 v[56:59], a[48:51], a[216:219], v[56:59]\n"
"ds_read_b64_tr_b16 a[14:15], v21 offset:4672\n"
"ds_read_b64_tr_b16 a[16:17], v20 offset:128\n"
"v_mfma_f32_16x16x32_bf16 v[64:67], a[52:55], a[216:219], v[64:67]\n"
"ds_read_b64_tr_b16 a[18:19], v20 offset:4736\n"
"ds_read_b64_tr_b16 a[20:21], v21 offset:128\n"
"v_mfma_f32_16x16x32_bf16 v[68:71], a[52:55], a[220:223], v[68:71]\n"
"buffer_load_dwordx4 v30, s[40:43], s73 offen offset:2048 lds\n"
"v_mfma_f32_16x16x32_bf16 v[76:79], a[56:59], a[220:223], v[76:79]\n"
"ds_read_b64_tr_b16 a[22:23], v21 offset:4736\n"
"ds_read_b64_tr_b16 a[24:25], v20 offset:192\n"
"v_mfma_f32_16x16x32_bf16 v[72:75], a[56:59], a[216:219], v[72:75]\n"
"ds_read_b64_tr_b16 a[26:27], v20 offset:4800\n"
"ds_read_b64_tr_b16 a[28:29], v21 offset:192\n"
"v_mfma_f32_16x16x32_bf16 v[80:83], a[60:63], a[216:219], v[80:83]\n"
"ds_read_b64_tr_b16 a[30:31], v21 offset:4800\n"
"v_mfma_f32_16x16x32_bf16 v[84:87], a[60:63], a[220:223], v[84:87]\n"
"buffer_load_dwordx4 v31, s[40:43], s73 offen offset:3072 lds\n"
"v_mfma_f32_16x16x32_bf16 v[92:95], a[64:67], a[220:223], v[92:95]\n"
"ds_read_b64_tr_b16 a[32:33], v20 offset:256\n"
"ds_read_b64_tr_b16 a[34:35], v20 offset:4864\n"
"v_mfma_f32_16x16x32_bf16 v[88:91], a[64:67], a[216:219], v[88:91]\n"
"v_mfma_f32_16x16x32_bf16 v[96:99], a[68:71], a[216:219], v[96:99]\n"
"s_add_u32 m0, s0, 40960\n"
"v_mfma_f32_16x16x32_bf16 v[100:103], a[68:71], a[220:223], v[100:103]\n"
"s_mov_b64 exec, s[96:97]\n"
"buffer_load_dwordx4 v240, s[40:43], s73 offen offset:0 lds\n"
"s_mov_b64 exec, -1\n"
"s_add_u32 s73, s73, 73728\n"
"s_waitcnt vmcnt(25) lgkmcnt(0)\n"
"v_mfma_f32_16x16x32_bf16 v[32:35], a[0:3], a[224:227], v[32:35]\n"
"ds_read_b64_tr_b16 a[36:37], v20 offset:9216\n"
"ds_read_b64_tr_b16 a[38:39], v20 offset:13824\n"
"v_mfma_f32_16x16x32_bf16 v[36:39], a[0:3], a[228:231], v[36:39]\n"
"ds_read_b64_tr_b16 a[40:41], v21 offset:9216\n"
"v_mfma_f32_16x16x32_bf16 v[44:47], a[4:7], a[228:231], v[44:47]\n"
"ds_read_b64_tr_b16 a[42:43], v21 offset:13824\n"
"v_mfma_f32_16x16x32_bf16 v[40:43], a[4:7], a[224:227], v[40:43]\n"
"buffer_load_dwordx4 a[208:211], v1, s[44:47], s72 offen offset:0 sc1\n"
"v_mfma_f32_16x16x32_bf16 v[48:51], a[8:11], a[224:227], v[48:51]\n"
"ds_read_b64_tr_b16 a[44:45], v20 offset:9280\n"
"ds_read_b64_tr_b16 a[46:47], v20 offset:13888\n"
"v_mfma_f32_16x16x32_bf16 v[52:55], a[8:11], a[228:231], v[52:55]\n"
"ds_read_b64_tr_b16 a[48:49], v21 offset:9280\n"
"ds_read_b64_tr_b16 a[50:51], v21 offset:13888\n"
"v_mfma_f32_16x16x32_bf16 v[60:63], a[12:15], a[228:231], v[60:63]\n"
"ds_read_b64_tr_b16 a[52:53], v20 offset:9344\n"
"v_mfma_f32_16x16x32_bf16 v[56:59], a[12:15], a[224:227], v[56:59]\n"
"buffer_load_dwordx4 a[212:215], v1, s[44:47], s72 offen offset:1024 sc1\n"
"v_mfma_f32_16x16x32_bf16 v[64:67], a[16:19], a[224:227], v[64:67]\n"
"s_add_u32 s72, s72, 2048\n"
"ds_read_b64_tr_b16 a[54:55], v20 offset:13952\n"
"v_mfma_f32_16x16x32_bf16 v[68:71], a[16:19], a[228:231], v[68:71]\n"
"ds_read_b64_tr_b16 a[56:57], v21 offset:9344\n"
"ds_read_b64_tr_b16 a[58:59], v21 offset:13952\n"
"v_mfma_f32_16x16x32_bf16 v[76:79], a[20:23], a[228:231], v[76:79]\n"
"ds_read_b64_tr_b16 a[60:61], v20 offset:9408\n"
"ds_read_b64_tr_b16 a[62:63], v20 offset:14016\n"
"v_mfma_f32_16x16x32_bf16 v[72:75], a[20:23], a[224:227], v[72:75]\n"
"buffer_load_dwordx4 a[216:219], v1, s[44:47], s72 offen offset:0 sc1\n"
"v_mfma_f32_16x16x32_bf16 v[80:83], a[24:27], a[224:227], v[80:83]\n"
"ds_read_b64_tr_b16 a[64:65], v21 offset:9408\n"
"ds_read_b64_tr_b16 a[66:67], v21 offset:14016\n"
"v_mfma_f32_16x16x32_bf16 v[84:87], a[24:27], a[228:231], v[84:87]\n"
"ds_read_b64_tr_b16 a[68:69], v20 offset:9472\n"
"v_mfma_f32_16x16x32_bf16 v[92:95], a[28:31], a[228:231], v[92:95]\n"
"ds_read_b64_tr_b16 a[70:71], v20 offset:14080\n"
"v_mfma_f32_16x16x32_bf16 v[88:91], a[28:31], a[224:227], v[88:91]\n"
"buffer_load_dwordx4 a[220:223], v1, s[44:47], s72 offen offset:1024 sc1\n"
"v_mfma_f32_16x16x32_bf16 v[96:99], a[32:35], a[224:227], v[96:99]\n"
"s_add_u32 s72, s72, 2048\n"
"v_mfma_f32_16x16x32_bf16 v[100:103], a[32:35], a[228:231], v[100:103]\n"
"s_waitcnt vmcnt(27) lgkmcnt(0)\n"
"v_mfma_f32_16x16x32_bf16 v[32:35], a[36:39], a[232:235], v[32:35]\n"
"s_waitcnt vmcnt(22)\n"
"s_barrier\n"
"v_mfma_f32_16x16x32_bf16 v[36:39], a[36:39], a[236:239], v[36:39]\n"
"s_add_u32 m0, s1, 0\n"
"ds_read_b64_tr_b16 a[0:1], v241 offset:0\n"
"v_mfma_f32_16x16x32_bf16 v[44:47], a[40:43], a[236:239], v[44:47]\n"
"buffer_load_dwordx4 v28, s[40:43], s73 offen offset:0 lds\n"
"v_mfma_f32_16x16x32_bf16 v[40:43], a[40:43], a[232:235], v[40:43]\n"
"ds_read_b64_tr_b16 a[2:3], v241 offset:4608\n"
"ds_read_b64_tr_b16 a[4:5], v242 offset:0\n"
"v_mfma_f32_16x16x32_bf16 v[48:51], a[44:47], a[232:235], v[48:51]\n"
"ds_read_b64_tr_b16 a[6:7], v242 offset:4608\n"
"ds_read_b64_tr_b16 a[8:9], v241 offset:64\n"
"v_mfma_f32_16x16x32_bf16 v[52:55], a[44:47], a[236:239], v[52:55]\n"
"buffer_load_dwordx4 v29, s[40:43], s73 offen offset:1024 lds\n"
"v_mfma_f32_16x16x32_bf16 v[60:63], a[48:51], a[236:239], v[60:63]\n"
"ds_read_b64_tr_b16 a[10:11], v241 offset:4672\n"
"ds_read_b64_tr_b16 a[12:13], v242 offset:64\n"
"v_mfma_f32_16x16x32_bf16 v[56:59], a[48:51], a[232:235], v[56:59]\n"
"ds_read_b64_tr_b16 a[14:15], v242 offset:4672\n"
"ds_read_b64_tr_b16 a[16:17], v241 offset:128\n"
"v_mfma_f32_16x16x32_bf16 v[64:67], a[52:55], a[232:235], v[64:67]\n"
"ds_read_b64_tr_b16 a[18:19], v241 offset:4736\n"
"ds_read_b64_tr_b16 a[20:21], v242 offset:128\n"
"v_mfma_f32_16x16x32_bf16 v[68:71], a[52:55], a[236:239], v[68:71]\n"
"buffer_load_dwordx4 v30, s[40:43], s73 offen offset:2048 lds\n"
"v_mfma_f32_16x16x32_bf16 v[76:79], a[56:59], a[236:239], v[76:79]\n"
"ds_read_b64_tr_b16 a[22:23], v242 offset:4736\n"
"ds_read_b64_tr_b16 a[24:25], v241 offset:192\n"
"v_mfma_f32_16x16x32_bf16 v[72:75], a[56:59], a[232:235], v[72:75]\n"
"ds_read_b64_tr_b16 a[26:27], v241 offset:4800\n"
"ds_read_b64_tr_b16 a[28:29], v242 offset:192\n"
"v_mfma_f32_16x16x32_bf16 v[80:83], a[60:63], a[232:235], v[80:83]\n"
"ds_read_b64_tr_b16 a[30:31], v242 offset:4800\n"
"v_mfma_f32_16x16x32_bf16 v[84:87], a[60:63], a[236:239], v[84:87]\n"
"buffer_load_dwordx4 v31, s[40:43], s73 offen offset:3072 lds\n"
"v_mfma_f32_16x16x32_bf16 v[92:95], a[64:67], a[236:239], v[92:95]\n"
"ds_read_b64_tr_b16 a[32:33], v241 offset:256\n"
"ds_read_b64_tr_b16 a[34:35], v241 offset:4864\n"
"v_mfma_f32_16x16x32_bf16 v[88:91], a[64:67], a[232:235], v[88:91]\n"
"v_mfma_f32_16x16x32_bf16 v[96:99], a[68:71], a[232:235], v[96:99]\n"
"s_add_u32 m0, s1, 4096\n"
"v_mfma_f32_16x16x32_bf16 v[100:103], a[68:71], a[236:239], v[100:103]\n"
"s_mov_b64 exec, s[96:97]\n"
"buffer_load_dwordx4 v240, s[40:43], s73 offen offset:0 lds\n"
"s_mov_b64 exec, -1\n"
"s_add_u32 s73, s73, 73728\n"
"s_waitcnt vmcnt(25) lgkmcnt(0)\n"
"v_mfma_f32_16x16x32_bf16 v[32:35], a[0:3], a[144:147], v[32:35]\n"
"ds_read_b64_tr_b16 a[36:37], v241 offset:9216\n"
"ds_read_b64_tr_b16 a[38:39], v241 offset:13824\n"
"v_mfma_f32_16x16x32_bf16 v[36:39], a[0:3], a[148:151], v[36:39]\n"
"ds_read_b64_tr_b16 a[40:41], v242 offset:9216\n"
"v_mfma_f32_16x16x32_bf16 v[44:47], a[4:7], a[148:151], v[44:47]\n"
"ds_read_b64_tr_b16 a[42:43], v242 offset:13824\n"
"v_mfma_f32_16x16x32_bf16 v[40:43], a[4:7], a[144:147], v[40:43]\n"
"buffer_load_dwordx4 a[224:227], v1, s[44:47], s72 offen offset:0 sc1\n"
"v_mfma_f32_16x16x32_bf16 v[48:51], a[8:11], a[144:147], v[48:51]\n"
"ds_read_b64_tr_b16 a[44:45], v241 offset:9280\n"
"ds_read_b64_tr_b16 a[46:47], v241 offset:13888\n"
"v_mfma_f32_16x16x32_bf16 v[52:55], a[8:11], a[148:151], v[52:55]\n"
"ds_read_b64_tr_b16 a[48:49], v242 offset:9280\n"
"ds_read_b64_tr_b16 a[50:51], v242 offset:13888\n"
"v_mfma_f32_16x16x32_bf16 v[60:63], a[12:15], a[148:151], v[60:63]\n"
"ds_read_b64_tr_b16 a[52:53], v241 offset:9344\n"
"v_mfma_f32_16x16x32_bf16 v[56:59], a[12:15], a[144:147], v[56:59]\n"
"buffer_load_dwordx4 a[228:231], v1, s[44:47], s72 offen offset:1024 sc1\n"
"v_mfma_f32_16x16x32_bf16 v[64:67], a[16:19], a[144:147], v[64:67]\n"
"s_add_u32 s72, s72, 2048\n"
"ds_read_b64_tr_b16 a[54:55], v241 offset:13952\n"
"v_mfma_f32_16x16x32_bf16 v[68:71], a[16:19], a[148:151], v[68:71]\n"
"ds_read_b64_tr_b16 a[56:57], v242 offset:9344\n"
"ds_read_b64_tr_b16 a[58:59], v242 offset:13952\n"
"v_mfma_f32_16x16x32_bf16 v[76:79], a[20:23], a[148:151], v[76:79]\n"
"ds_read_b64_tr_b16 a[60:61], v241 offset:9408\n"
"ds_read_b64_tr_b16 a[62:63], v241 offset:14016\n"
"v_mfma_f32_16x16x32_bf16 v[72:75], a[20:23], a[144:147], v[72:75]\n"
"buffer_load_dwordx4 a[232:235], v1, s[44:47], s72 offen offset:0 sc1\n"
"v_mfma_f32_16x16x32_bf16 v[80:83], a[24:27], a[144:147], v[80:83]\n"
"ds_read_b64_tr_b16 a[64:65], v242 offset:9408\n"
"ds_read_b64_tr_b16 a[66:67], v242 offset:14016\n"
"v_mfma_f32_16x16x32_bf16 v[84:87], a[24:27], a[148:151], v[84:87]\n"
"ds_read_b64_tr_b16 a[68:69], v241 offset:9472\n"
"v_mfma_f32_16x16x32_bf16 v[92:95], a[28:31], a[148:151], v[92:95]\n"
"ds_read_b64_tr_b16 a[70:71], v241 offset:14080\n"
"v_mfma_f32_16x16x32_bf16 v[88:91], a[28:31], a[144:147], v[88:91]\n"
"buffer_load_dwordx4 a[236:239], v1, s[44:47], s72 offen offset:1024 sc1\n"
"v_mfma_f32_16x16x32_bf16 v[96:99], a[32:35], a[144:147], v[96:99]\n"
"s_add_u32 s72, s72, 2048\n"
"v_mfma_f32_16x16x32_bf16 v[100:103], a[32:35], a[148:151], v[100:103]\n"
"s_waitcnt vmcnt(27) lgkmcnt(0)\n"
"v_mfma_f32_16x16x32_bf16 v[32:35], a[36:39], a[152:155], v[32:35]\n"
"s_waitcnt vmcnt(22)\n"
"s_barrier\n"
"v_mfma_f32_16x16x32_bf16 v[36:39], a[36:39], a[156:159], v[36:39]\n"
"s_bfe_u64 s[60:61], s[58:59], 0x5001e\n"
"s_mul_i32 s60, s60, 147456\n"
"s_add_u32 s73, s56, s60\n"
"s_add_u32 m0, s0, 0\n"
"v_mfma_f32_16x16x32_bf16 v[44:47], a[40:43], a[156:159], v[44:47]\n"
"buffer_load_dwordx4 v28, s[40:43], s73 offen offset:0 lds\n"
"v_mfma_f32_16x16x32_bf16 v[40:43], a[40:43], a[152:155], v[40:43]\n"
"ds_read_b64_tr_b16 a[0:1], v241 offset:18432\n"
"ds_read_b64_tr_b16 a[2:3], v241 offset:23040\n"
"v_mfma_f32_16x16x32_bf16 v[48:51], a[44:47], a[152:155], v[48:51]\n"
"ds_read_b64_tr_b16 a[4:5], v242 offset:18432\n"
"ds_read_b64_tr_b16 a[6:7], v242 offset:23040\n"
"v_mfma_f32_16x16x32_bf16 v[52:55], a[44:47], a[156:159], v[52:55]\n"
"buffer_load_dwordx4 v29, s[40:43], s73 offen offset:1024 lds\n"
"v_mfma_f32_16x16x32_bf16 v[60:63], a[48:51], a[156:159], v[60:63]\n"
"ds_read_b64_tr_b16 a[8:9], v241 offset:18496\n"
"ds_read_b64_tr_b16 a[10:11], v241 offset:23104\n"
"v_mfma_f32_16x16x32_bf16 v[56:59], a[48:51], a[152:155], v[56:59]\n"
"ds_read_b64_tr_b16 a[12:13], v242 offset:18496\n"
"ds_read_b64_tr_b16 a[14:15], v242 offset:23104\n"
"v_mfma_f32_16x16x32_bf16 v[64:67], a[52:55], a[152:155], v[64:67]\n"
"ds_read_b64_tr_b16 a[16:17], v241 offset:18560\n"
"ds_read_b64_tr_b16 a[18:19], v241 offset:23168\n"
"v_mfma_f32_16x16x32_bf16 v[68:71], a[52:55], a[156:159], v[68:71]\n"
"buffer_load_dwordx4 v30, s[40:43], s73 offen offset:2048 lds\n"
"v_mfma_f32_16x16x32_bf16 v[76:79], a[56:59], a[156:159], v[76:79]\n"
"ds_read_b64_tr_b16 a[20:21], v242 offset:18560\n"
"ds_read_b64_tr_b16 a[22:23], v242 offset:23168\n"
"v_mfma_f32_16x16x32_bf16 v[72:75], a[56:59], a[152:155], v[72:75]\n"
"ds_read_b64_tr_b16 a[24:25], v241 offset:18624\n"
"ds_read_b64_tr_b16 a[26:27], v241 offset:23232\n"
"v_mfma_f32_16x16x32_bf16 v[80:83], a[60:63], a[152:155], v[80:83]\n"
"ds_read_b64_tr_b16 a[28:29], v242 offset:18624\n"
"ds_read_b64_tr_b16 a[30:31], v242 offset:23232\n"
"v_mfma_f32_16x16x32_bf16 v[84:87], a[60:63], a[156:159], v[84:87]\n"
"buffer_load_dwordx4 v31, s[40:43], s73 offen offset:3072 lds\n"
"v_mfma_f32_16x16x32_bf16 v[92:95], a[64:67], a[156:159], v[92:95]\n"
"ds_read_b64_tr_b16 a[32:33], v241 offset:18688\n"
"ds_read_b64_tr_b16 a[34:35], v241 offset:23296\n"
"v_mfma_f32_16x16x32_bf16 v[88:91], a[64:67], a[152:155], v[88:91]\n"
"v_mfma_f32_16x16x32_bf16 v[96:99], a[68:71], a[152:155], v[96:99]\n"
"s_add_u32 m0, s0, 4096\n"
"v_mfma_f32_16x16x32_bf16 v[100:103], a[68:71], a[156:159], v[100:103]\n"
"s_mov_b64 exec, s[96:97]\n"
"buffer_load_dwordx4 v240, s[40:43], s73 offen offset:0 lds\n"
"s_mov_b64 exec, -1\n"
"s_add_u32 s73, s73, 73728\n"
"s_waitcnt vmcnt(25) lgkmcnt(0)\n"
"v_mfma_f32_16x16x32_bf16 v[32:35], a[0:3], a[160:163], v[32:35]\n"
"ds_read_b64_tr_b16 a[36:37], v241 offset:27648\n"
"ds_read_b64_tr_b16 a[38:39], v241 offset:32256\n"
"v_mfma_f32_16x16x32_bf16 v[36:39], a[0:3], a[164:167], v[36:39]\n"
"ds_read_b64_tr_b16 a[40:41], v242 offset:27648\n"
"v_mfma_f32_16x16x32_bf16 v[44:47], a[4:7], a[164:167], v[44:47]\n"
"s_bfe_u64 s[62:63], s[58:59], 0x5001e\n"
"s_mul_i32 s62, s62, 8192\n"
"s_add_u32 s72, s57, s62\n"
"v_mfma_f32_16x16x32_bf16 v[40:43], a[4:7], a[160:163], v[40:43]\n"
"buffer_load_dwordx4 a[144:147], v1, s[44:47], s72 offen offset:0 sc1\n"
"v_mfma_f32_16x16x32_bf16 v[48:51], a[8:11], a[160:163], v[48:51]\n"
"ds_read_b64_tr_b16 a[42:43], v242 offset:32256\n"
"ds_read_b64_tr_b16 a[44:45], v241 offset:27712\n"
"v_mfma_f32_16x16x32_bf16 v[52:55], a[8:11], a[164:167], v[52:55]\n"
"ds_read_b64_tr_b16 a[46:47], v241 offset:32320\n"
"ds_read_b64_tr_b16 a[48:49], v242 offset:27712\n"
"v_mfma_f32_16x16x32_bf16 v[60:63], a[12:15], a[164:167], v[60:63]\n"
"ds_read_b64_tr_b16 a[50:51], v242 offset:32320\n"
"ds_read_b64_tr_b16 a[52:53], v241 offset:27776\n"
"v_mfma_f32_16x16x32_bf16 v[56:59], a[12:15], a[160:163], v[56:59]\n"
"buffer_load_dwordx4 a[148:151], v1, s[44:47], s72 offen offset:1024 sc1\n"
"v_mfma_f32_16x16x32_bf16 v[64:67], a[16:19], a[160:163], v[64:67]\n"
"s_add_u32 s72, s72, 2048\n"
"ds_read_b64_tr_b16 a[54:55], v241 offset:32384\n"
"v_mfma_f32_16x16x32_bf16 v[68:71], a[16:19], a[164:167], v[68:71]\n"
"ds_read_b64_tr_b16 a[56:57], v242 offset:27776\n"
"ds_read_b64_tr_b16 a[58:59], v242 offset:32384\n"
"v_mfma_f32_16x16x32_bf16 v[76:79], a[20:23], a[164:167], v[76:79]\n"
"ds_read_b64_tr_b16 a[60:61], v241 offset:27840\n"
"ds_read_b64_tr_b16 a[62:63], v241 offset:32448\n"
"v_mfma_f32_16x16x32_bf16 v[72:75], a[20:23], a[160:163], v[72:75]\n"
"buffer_load_dwordx4 a[152:155], v1, s[44:47], s72 offen offset:0 sc1\n"
"v_mfma_f32_16x16x32_bf16 v[80:83], a[24:27], a[160:163], v[80:83]\n"
"ds_read_b64_tr_b16 a[64:65], v242 offset:27840\n"
"ds_read_b64_tr_b16 a[66:67], v242 offset:32448\n"
"v_mfma_f32_16x16x32_bf16 v[84:87], a[24:27], a[164:167], v[84:87]\n"
"ds_read_b64_tr_b16 a[68:69], v241 offset:27904\n"
"v_mfma_f32_16x16x32_bf16 v[92:95], a[28:31], a[164:167], v[92:95]\n"
"ds_read_b64_tr_b16 a[70:71], v241 offset:32512\n"
"v_mfma_f32_16x16x32_bf16 v[88:91], a[28:31], a[160:163], v[88:91]\n"
"buffer_load_dwordx4 a[156:159], v1, s[44:47], s72 offen offset:1024 sc1\n"
"v_mfma_f32_16x16x32_bf16 v[96:99], a[32:35], a[160:163], v[96:99]\n"
"s_add_u32 s72, s72, 2048\n"
"v_mfma_f32_16x16x32_bf16 v[100:103], a[32:35], a[164:167], v[100:103]\n"
"s_waitcnt vmcnt(27) lgkmcnt(0)\n"
"v_mfma_f32_16x16x32_bf16 v[32:35], a[36:39], a[168:171], v[32:35]\n"
"s_waitcnt vmcnt(22)\n"
"s_barrier\n"
"v_mfma_f32_16x16x32_bf16 v[36:39], a[36:39], a[172:175], v[36:39]\n"
"s_add_u32 m0, s0, 18432\n"
"s_lshr_b32 s77, s70, 1\n"
"v_mfma_f32_16x16x32_bf16 v[44:47], a[40:43], a[172:175], v[44:47]\n"
"buffer_load_dwordx4 v28, s[40:43], s73 offen offset:0 lds\n"
"v_mfma_f32_16x16x32_bf16 v[40:43], a[40:43], a[168:171], v[40:43]\n"
"ds_read_b64_tr_b16 a[0:1], v241 offset:36864\n"
"s_lshl2_add_u32 s77, s77, s71\n"
"v_mfma_f32_16x16x32_bf16 v[48:51], a[44:47], a[168:171], v[48:51]\n"
"ds_read_b64_tr_b16 a[2:3], v241 offset:41472\n"
"s_and_b32 s78, s70, 1\n"
"v_mfma_f32_16x16x32_bf16 v[52:55], a[44:47], a[172:175], v[52:55]\n"
"buffer_load_dwordx4 v29, s[40:43], s73 offen offset:1024 lds\n"
"v_mfma_f32_16x16x32_bf16 v[60:63], a[48:51], a[172:175], v[60:63]\n"
"ds_read_b64_tr_b16 a[4:5], v242 offset:36864\n"
"s_lshl1_add_u32 s77, s77, s78\n"
"v_mfma_f32_16x16x32_bf16 v[56:59], a[48:51], a[168:171], v[56:59]\n"
"ds_read_b64_tr_b16 a[6:7], v242 offset:41472\n"
"s_lshr_b32 s78, s11, 1\n"
"v_mfma_f32_16x16x32_bf16 v[64:67], a[52:55], a[168:171], v[64:67]\n"
"ds_read_b64_tr_b16 a[8:9], v241 offset:36928\n"
"ds_read_b64_tr_b16 a[10:11], v241 offset:41536\n"
"v_mfma_f32_16x16x32_bf16 v[68:71], a[52:55], a[172:175], v[68:71]\n"
"buffer_load_dwordx4 v30, s[40:43], s73 offen offset:2048 lds\n"
"v_mfma_f32_16x16x32_bf16 v[76:79], a[56:59], a[172:175], v[76:79]\n"
"s_lshl1_add_u32 s77, s77, s78\n"
"ds_read_b64_tr_b16 a[12:13], v242 offset:36928\n"
"v_mfma_f32_16x16x32_bf16 v[72:75], a[56:59], a[168:171], v[72:75]\n"
"s_lshl_b32 s83, s77, 4\n"
"ds_read_b64_tr_b16 a[14:15], v242 offset:41536\n"
"v_mfma_f32_16x16x32_bf16 v[80:83], a[60:63], a[168:171], v[80:83]\n"
"s_add_u32 s83, s83, 0x2ae2000\n"
"ds_read_b64_tr_b16 a[16:17], v241 offset:36992\n"
"v_mfma_f32_16x16x32_bf16 v[84:87], a[60:63], a[172:175], v[84:87]\n"
"buffer_load_dwordx4 v31, s[40:43], s73 offen offset:3072 lds\n"
"v_mfma_f32_16x16x32_bf16 v[92:95], a[64:67], a[172:175], v[92:95]\n"
"ds_read_b64_tr_b16 a[18:19], v241 offset:41600\n"
"s_mov_b64 s[98:99], 15\n"
"ds_read_b64_tr_b16 a[20:21], v242 offset:36992\n"
"ds_read_b64_tr_b16 a[22:23], v242 offset:41600\n"
"ds_read_b64_tr_b16 a[24:25], v241 offset:37056\n"
"ds_read_b64_tr_b16 a[26:27], v241 offset:41664\n"
"ds_read_b64_tr_b16 a[28:29], v242 offset:37056\n"
"ds_read_b64_tr_b16 a[30:31], v242 offset:41664\n"
"ds_read_b64_tr_b16 a[32:33], v241 offset:37120\n"
"ds_read_b64_tr_b16 a[34:35], v241 offset:41728\n"
"v_mfma_f32_16x16x32_bf16 v[88:91], a[64:67], a[168:171], v[88:91]\n"
"s_mov_b64 s[8:9], 15\n"
"s_lshl2_add_u32 s78, s70, s71\n"
"v_mfma_f32_16x16x32_bf16 v[96:99], a[68:71], a[168:171], v[96:99]\n"
"s_add_u32 m0, s0, 22528\n"
"s_lshl2_add_u32 s78, s78, s11\n"
"s_lshl_b32 s78, s78, 4\n"
"s_add_u32 s78, s78, 0x2ae3ff0\n"
"v_add_u32 v6, s83, v251\n"
"v_add_u32 v7, s78, v251\n"
"v_mfma_f32_16x16x32_bf16 v[100:103], a[68:71], a[172:175], v[100:103]\n"
"s_mov_b64 exec, s[96:97]\n"
"buffer_load_dwordx4 v240, s[40:43], s73 offen offset:0 lds\n"
"s_mov_b64 exec, -1\n"
"s_add_u32 s73, s73, 73728\n"
"v_cmp_gt_u32 vcc, 2, v0\n"
"s_nop 1\n"
"v_cndmask_b32 v6, v7, v6, vcc\n"
"s_mov_b32 s83, 0\n"
"s_waitcnt vmcnt(25) lgkmcnt(0)\n"
"v_mfma_f32_16x16x32_bf16 v[32:35], a[0:3], a[208:211], v[32:35]\n"
"ds_read_b64_tr_b16 a[36:37], v241 offset:46080\n"
"ds_read_b64_tr_b16 a[38:39], v241 offset:50688\n"
"v_mfma_f32_16x16x32_bf16 v[36:39], a[0:3], a[212:215], v[36:39]\n"
"buffer_load_dwordx2 v[248:249], v6, s[36:39], s83 offen sc1\n"
"v_mfma_f32_16x16x32_bf16 v[44:47], a[4:7], a[212:215], v[44:47]\n"
"ds_read_b64_tr_b16 a[40:41], v242 offset:46080\n"
"ds_read_b64_tr_b16 a[42:43], v242 offset:50688\n"
"v_mfma_f32_16x16x32_bf16 v[40:43], a[4:7], a[208:211], v[40:43]\n"
"buffer_load_dwordx4 a[160:163], v1, s[44:47], s72 offen offset:0 sc1\n"
"v_mfma_f32_16x16x32_bf16 v[48:51], a[8:11], a[208:211], v[48:51]\n"
"s_mul_i32 s77, s77, 18432\n"
"ds_read_b64_tr_b16 a[44:45], v241 offset:46144\n"
"v_mfma_f32_16x16x32_bf16 v[52:55], a[8:11], a[212:215], v[52:55]\n"
"ds_read_b64_tr_b16 a[46:47], v241 offset:50752\n"
"s_and_b32 s78, s11, 1\n"
"v_mfma_f32_16x16x32_bf16 v[60:63], a[12:15], a[212:215], v[60:63]\n"
"ds_read_b64_tr_b16 a[48:49], v242 offset:46144\n"
"s_and_b32 s84, s69, 1\n"
"v_mfma_f32_16x16x32_bf16 v[56:59], a[12:15], a[208:211], v[56:59]\n"
"buffer_load_dwordx4 a[164:167], v1, s[44:47], s72 offen offset:1024 sc1\n"
"v_mfma_f32_16x16x32_bf16 v[64:67], a[16:19], a[208:211], v[64:67]\n"
"s_add_u32 s72, s72, 2048\n"
"ds_read_b64_tr_b16 a[50:51], v242 offset:50752\n"
"v_mfma_f32_16x16x32_bf16 v[68:71], a[16:19], a[212:215], v[68:71]\n"
"ds_read_b64_tr_b16 a[52:53], v241 offset:46208\n"
"s_lshl1_add_u32 s78, s78, s84\n"
"v_mfma_f32_16x16x32_bf16 v[76:79], a[20:23], a[212:215], v[76:79]\n"
"ds_read_b64_tr_b16 a[54:55], v241 offset:50816\n"
"s_mul_i32 s78, s78, 4608\n"
"v_mfma_f32_16x16x32_bf16 v[72:75], a[20:23], a[208:211], v[72:75]\n"
"buffer_load_dwordx4 a[168:171], v1, s[44:47], s72 offen offset:0 sc1\n"
"v_mfma_f32_16x16x32_bf16 v[80:83], a[24:27], a[208:211], v[80:83]\n"
"ds_read_b64_tr_b16 a[56:57], v242 offset:46208\n"
"ds_read_b64_tr_b16 a[58:59], v242 offset:50816\n"
"v_mfma_f32_16x16x32_bf16 v[84:87], a[24:27], a[212:215], v[84:87]\n"
"s_add_u32 s77, s77, s78\n"
"ds_read_b64_tr_b16 a[60:61], v241 offset:46272\n"
"v_mfma_f32_16x16x32_bf16 v[92:95], a[28:31], a[212:215], v[92:95]\n"
"s_lshr_b32 s78, s69, 1\n"
"ds_read_b64_tr_b16 a[62:63], v241 offset:50880\n"
"ds_read_b64_tr_b16 a[64:65], v242 offset:46272\n"
"ds_read_b64_tr_b16 a[66:67], v242 offset:50880\n"
"ds_read_b64_tr_b16 a[68:69], v241 offset:46336\n"
"ds_read_b64_tr_b16 a[70:71], v241 offset:50944\n"
"v_mfma_f32_16x16x32_bf16 v[88:91], a[28:31], a[208:211], v[88:91]\n"
"buffer_load_dwordx4 a[172:175], v1, s[44:47], s72 offen offset:1024 sc1\n"
"v_mfma_f32_16x16x32_bf16 v[96:99], a[32:35], a[208:211], v[96:99]\n"
"s_add_u32 s72, s72, 2048\n"
"s_mul_i32 s78, s78, 2560\n"
"v_mfma_f32_16x16x32_bf16 v[100:103], a[32:35], a[212:215], v[100:103]\n"
"s_add_u32 s77, s77, s78\n"
"s_add_u32 s82, s77, 0x19d8000\n"
"s_lshl2_add_u32 s77, s70, s11\n"
"s_lshl1_add_u32 s77, s77, s84\n"
"s_waitcnt vmcnt(28) lgkmcnt(0)\n"
"v_mfma_f32_16x16x32_bf16 v[32:35], a[36:39], a[216:219], v[32:35]\n"
"s_waitcnt vmcnt(23)\n"
"s_barrier\n"
"v_mfma_f32_16x16x32_bf16 v[36:39], a[36:39], a[220:223], v[36:39]\n"
"s_add_u32 m0, s0, 36864\n"
"s_lshl_b32 s74, s77, 5\n"
"v_mfma_f32_16x16x32_bf16 v[44:47], a[40:43], a[220:223], v[44:47]\n"
"buffer_load_dwordx4 v28, s[40:43], s73 offen offset:0 lds\n"
"v_mfma_f32_16x16x32_bf16 v[40:43], a[40:43], a[216:219], v[40:43]\n"
"ds_read_b64_tr_b16 a[0:1], v20 offset:0\n"
"ds_read_b64_tr_b16 a[2:3], v20 offset:4608\n"
"v_mfma_f32_16x16x32_bf16 v[48:51], a[44:47], a[216:219], v[48:51]\n"
"s_mul_i32 s84, s77, 18432\n"
"ds_read_b64_tr_b16 a[4:5], v21 offset:0\n"
"v_mfma_f32_16x16x32_bf16 v[52:55], a[44:47], a[220:223], v[52:55]\n"
"buffer_load_dwordx4 v29, s[40:43], s73 offen offset:1024 lds\n"
"v_mfma_f32_16x16x32_bf16 v[60:63], a[48:51], a[220:223], v[60:63]\n"
"ds_read_b64_tr_b16 a[6:7], v21 offset:4608\n"
"s_mul_i32 s77, s71, 288\n"
"v_mfma_f32_16x16x32_bf16 v[56:59], a[48:51], a[216:219], v[56:59]\n"
"ds_read_b64_tr_b16 a[8:9], v20 offset:64\n"
"ds_read_b64_tr_b16 a[10:11], v20 offset:4672\n"
"v_mfma_f32_16x16x32_bf16 v[64:67], a[52:55], a[216:219], v[64:67]\n"
"s_add_u32 s84, s84, s77\n"
"ds_read_b64_tr_b16 a[12:13], v21 offset:64\n"
"v_mfma_f32_16x16x32_bf16 v[68:71], a[52:55], a[220:223], v[68:71]\n"
"buffer_load_dwordx4 v30, s[40:43], s73 offen offset:2048 lds\n"
"v_mfma_f32_16x16x32_bf16 v[76:79], a[56:59], a[220:223], v[76:79]\n"
"ds_read_b64_tr_b16 a[14:15], v21 offset:4672\n"
"s_lshr_b32 s77, s69, 1\n"
"v_mfma_f32_16x16x32_bf16 v[72:75], a[56:59], a[216:219], v[72:75]\n"
"ds_read_b64_tr_b16 a[16:17], v20 offset:128\n"
"ds_read_b64_tr_b16 a[18:19], v20 offset:4736\n"
"v_mfma_f32_16x16x32_bf16 v[80:83], a[60:63], a[216:219], v[80:83]\n"
"ds_read_b64_tr_b16 a[20:21], v21 offset:128\n"
"s_mul_i32 s77, s77, 160\n"
"v_mfma_f32_16x16x32_bf16 v[84:87], a[60:63], a[220:223], v[84:87]\n"
"buffer_load_dwordx4 v31, s[40:43], s73 offen offset:3072 lds\n"
"v_mfma_f32_16x16x32_bf16 v[92:95], a[64:67], a[220:223], v[92:95]\n"
"ds_read_b64_tr_b16 a[22:23], v21 offset:4736\n"
"ds_read_b64_tr_b16 a[24:25], v20 offset:192\n"
"ds_read_b64_tr_b16 a[26:27], v20 offset:4800\n"
"ds_read_b64_tr_b16 a[28:29], v21 offset:192\n"
"ds_read_b64_tr_b16 a[30:31], v21 offset:4800\n"
"ds_read_b64_tr_b16 a[32:33], v20 offset:256\n"
"ds_read_b64_tr_b16 a[34:35], v20 offset:4864\n"
"v_mfma_f32_16x16x32_bf16 v[88:91], a[64:67], a[216:219], v[88:91]\n"
"s_add_u32 s84, s84, s77\n"
"v_mul_u32_u24 v16, 1152, v3\n"
"v_mfma_f32_16x16x32_bf16 v[96:99], a[68:71], a[216:219], v[96:99]\n"
"s_add_u32 m0, s0, 40960\n"
"v_lshl_add_u32 v16, v2, 3, v16\n"
"v_lshlrev_b32 v17, 1, v3\n"
"v_mfma_f32_16x16x32_bf16 v[100:103], a[68:71], a[220:223], v[100:103]\n"
"s_mov_b64 exec, s[96:97]\n"
"buffer_load_dwordx4 v240, s[40:43], s73 offen offset:0 lds\n"
"s_mov_b64 exec, -1\n"
"s_add_u32 s73, s73, 73728\n"
"s_waitcnt vmcnt(26) lgkmcnt(0)\n"
"v_mfma_f32_16x16x32_bf16 v[32:35], a[0:3], a[224:227], v[32:35]\n"
"ds_read_b64_tr_b16 a[36:37], v20 offset:9216\n"
"ds_read_b64_tr_b16 a[38:39], v20 offset:13824\n"
"v_mfma_f32_16x16x32_bf16 v[36:39], a[0:3], a[228:231], v[36:39]\n"
"s_cmp_lt_u32 s69, 2\n"
"s_cselect_b32 s87, s82, 0x80000000\n"
"v_mfma_f32_16x16x32_bf16 v[44:47], a[4:7], a[228:231], v[44:47]\n"
"ds_read_b64_tr_b16 a[40:41], v21 offset:9216\n"
"s_mov_b32 s48, s28\n"
"v_mfma_f32_16x16x32_bf16 v[40:43], a[4:7], a[224:227], v[40:43]\n"
"buffer_load_dwordx4 a[208:211], v1, s[44:47], s72 offen offset:0 sc1\n"
"v_mfma_f32_16x16x32_bf16 v[48:51], a[8:11], a[224:227], v[48:51]\n"
"ds_read_b64_tr_b16 a[42:43], v21 offset:13824\n"
"ds_read_b64_tr_b16 a[44:45], v20 offset:9280\n"
"v_mfma_f32_16x16x32_bf16 v[52:55], a[8:11], a[228:231], v[52:55]\n"
"s_and_b32 s49, s29, 0xffff\n"
"ds_read_b64_tr_b16 a[46:47], v20 offset:13888\n"
"v_mfma_f32_16x16x32_bf16 v[60:63], a[12:15], a[228:231], v[60:63]\n"
"ds_read_b64_tr_b16 a[48:49], v21 offset:9280\n"
"ds_read_b64_tr_b16 a[50:51], v21 offset:13888\n"
"v_mfma_f32_16x16x32_bf16 v[56:59], a[12:15], a[224:227], v[56:59]\n"
"buffer_load_dwordx4 a[212:215], v1, s[44:47], s72 offen offset:1024 sc1\n"
"v_mfma_f32_16x16x32_bf16 v[64:67], a[16:19], a[224:227], v[64:67]\n"
"s_add_u32 s72, s72, 2048\n"
"s_mov_b32 s50, 0x1000\n"
"v_mfma_f32_16x16x32_bf16 v[68:71], a[16:19], a[228:231], v[68:71]\n"
"ds_read_b64_tr_b16 a[52:53], v20 offset:9344\n"
"ds_read_b64_tr_b16 a[54:55], v20 offset:13952\n"
"v_mfma_f32_16x16x32_bf16 v[76:79], a[20:23], a[228:231], v[76:79]\n"
"s_mov_b32 s51, 0x20000\n"
"ds_read_b64_tr_b16 a[56:57], v21 offset:9344\n"
"v_mfma_f32_16x16x32_bf16 v[72:75], a[20:23], a[224:227], v[72:75]\n"
"buffer_load_dwordx4 a[216:219], v1, s[44:47], s72 offen offset:0 sc1\n"
"v_mfma_f32_16x16x32_bf16 v[80:83], a[24:27], a[224:227], v[80:83]\n"
"ds_read_b64_tr_b16 a[58:59], v21 offset:13952\n"
"ds_read_b64_tr_b16 a[60:61], v20 offset:9408\n"
"v_mfma_f32_16x16x32_bf16 v[84:87], a[24:27], a[228:231], v[84:87]\n"
"s_mov_b32 s92, s12\n"
"ds_read_b64_tr_b16 a[62:63], v20 offset:14016\n"
"v_mfma_f32_16x16x32_bf16 v[92:95], a[28:31], a[228:231], v[92:95]\n"
"ds_read_b64_tr_b16 a[64:65], v21 offset:9408\n"
"s_and_b32 s93, s13, 0xffff\n"
"ds_read_b64_tr_b16 a[66:67], v21 offset:14016\n"
"ds_read_b64_tr_b16 a[68:69], v20 offset:9472\n"
"ds_read_b64_tr_b16 a[70:71], v20 offset:14080\n"
"v_mfma_f32_16x16x32_bf16 v[88:91], a[28:31], a[224:227], v[88:91]\n"
"buffer_load_dwordx4 a[220:223], v1, s[44:47], s72 offen offset:1024 sc1\n"
"v_mfma_f32_16x16x32_bf16 v[96:99], a[32:35], a[224:227], v[96:99]\n"
"s_add_u32 s72, s72, 2048\n"
"s_mov_b32 s94, 0x240000\n"
"v_mfma_f32_16x16x32_bf16 v[100:103], a[32:35], a[228:231], v[100:103]\n"
"s_mov_b32 s95, 0x20000\n"
"s_waitcnt vmcnt(28) lgkmcnt(0)\n"
"v_mfma_f32_16x16x32_bf16 v[32:35], a[36:39], a[232:235], v[32:35]\n"
"s_waitcnt vmcnt(23)\n"
"s_barrier\n"
"v_mfma_f32_16x16x32_bf16 v[36:39], a[36:39], a[236:239], v[36:39]\n"
"s_add_u32 m0, s1, 0\n"
"ds_read_b64_tr_b16 a[0:1], v241 offset:0\n"
"v_mfma_f32_16x16x32_bf16 v[44:47], a[40:43], a[236:239], v[44:47]\n"
"buffer_load_dwordx4 v28, s[40:43], s73 offen offset:0 lds\n"
"v_mfma_f32_16x16x32_bf16 v[40:43], a[40:43], a[232:235], v[40:43]\n"
"ds_read_b64_tr_b16 a[2:3], v241 offset:4608\n"
"ds_read_b64_tr_b16 a[4:5], v242 offset:0\n"
"v_mfma_f32_16x16x32_bf16 v[48:51], a[44:47], a[232:235], v[48:51]\n"
"ds_read_b64_tr_b16 a[6:7], v242 offset:4608\n"
"ds_read_b64_tr_b16 a[8:9], v241 offset:64\n"
"v_mfma_f32_16x16x32_bf16 v[52:55], a[44:47], a[236:239], v[52:55]\n"
"buffer_load_dwordx4 v29, s[40:43], s73 offen offset:1024 lds\n"
"v_mfma_f32_16x16x32_bf16 v[60:63], a[48:51], a[236:239], v[60:63]\n"
"ds_read_b64_tr_b16 a[10:11], v241 offset:4672\n"
"ds_read_b64_tr_b16 a[12:13], v242 offset:64\n"
"v_mfma_f32_16x16x32_bf16 v[56:59], a[48:51], a[232:235], v[56:59]\n"
"ds_read_b64_tr_b16 a[14:15], v242 offset:4672\n"
"ds_read_b64_tr_b16 a[16:17], v241 offset:128\n"
"v_mfma_f32_16x16x32_bf16 v[64:67], a[52:55], a[232:235], v[64:67]\n"
"ds_read_b64_tr_b16 a[18:19], v241 offset:4736\n"
"ds_read_b64_tr_b16 a[20:21], v242 offset:128\n"
"v_mfma_f32_16x16x32_bf16 v[68:71], a[52:55], a[236:239], v[68:71]\n"
"buffer_load_dwordx4 v30, s[40:43], s73 offen offset:2048 lds\n"
"v_mfma_f32_16x16x32_bf16 v[76:79], a[56:59], a[236:239], v[76:79]\n"
"ds_read_b64_tr_b16 a[22:23], v242 offset:4736\n"
"ds_read_b64_tr_b16 a[24:25], v241 offset:192\n"
"v_mfma_f32_16x16x32_bf16 v[72:75], a[56:59], a[232:235], v[72:75]\n"
"ds_read_b64_tr_b16 a[26:27], v241 offset:4800\n"
"ds_read_b64_tr_b16 a[28:29], v242 offset:192\n"
"v_mfma_f32_16x16x32_bf16 v[80:83], a[60:63], a[232:235], v[80:83]\n"
"ds_read_b64_tr_b16 a[30:31], v242 offset:4800\n"
"v_mfma_f32_16x16x32_bf16 v[84:87], a[60:63], a[236:239], v[84:87]\n"
"buffer_load_dwordx4 v31, s[40:43], s73 offen offset:3072 lds\n"
"v_mfma_f32_16x16x32_bf16 v[92:95], a[64:67], a[236:239], v[92:95]\n"
"ds_read_b64_tr_b16 a[32:33], v241 offset:256\n"
"ds_read_b64_tr_b16 a[34:35], v241 offset:4864\n"
"v_mfma_f32_16x16x32_bf16 v[88:91], a[64:67], a[232:235], v[88:91]\n"
"v_mfma_f32_16x16x32_bf16 v[96:99], a[68:71], a[232:235], v[96:99]\n"
"s_add_u32 m0, s1, 4096\n"
"v_mfma_f32_16x16x32_bf16 v[100:103], a[68:71], a[236:239], v[100:103]\n"
"s_mov_b64 exec, s[96:97]\n"
"buffer_load_dwordx4 v240, s[40:43], s73 offen offset:0 lds\n"
"s_mov_b64 exec, -1\n"
"s_add_u32 s73, s73, 73728\n"
"s_branch L_g3z\n"
"L_g3A:\n"
"s_nop 0\n"
"s_barrier\n"
"ds_read_b64_tr_b16 a[0:1], v241 offset:0\n"
"ds_read_b64_tr_b16 a[2:3], v241 offset:4608\n"
"ds_read_b64_tr_b16 a[4:5], v242 offset:0\n"
"ds_read_b64_tr_b16 a[6:7], v242 offset:4608\n"
"ds_read_b64_tr_b16 a[8:9], v241 offset:64\n"
"ds_read_b64_tr_b16 a[10:11], v241 offset:4672\n"
"ds_read_b64_tr_b16 a[12:13], v242 offset:64\n"
"ds_read_b64_tr_b16 a[14:15], v242 offset:4672\n"
"ds_read_b64_tr_b16 a[16:17], v241 offset:128\n"
"ds_read_b64_tr_b16 a[18:19], v241 offset:4736\n"
"ds_read_b64_tr_b16 a[20:21], v242 offset:128\n"
"ds_read_b64_tr_b16 a[22:23], v242 offset:4736\n"
"ds_read_b64_tr_b16 a[24:25], v241 offset:192\n"
"ds_read_b64_tr_b16 a[26:27], v241 offset:4800\n"
"ds_read_b64_tr_b16 a[28:29], v242 offset:192\n"
"ds_read_b64_tr_b16 a[30:31], v242 offset:4800\n"
"ds_read_b64_tr_b16 a[32:33], v241 offset:256\n"
"ds_read_b64_tr_b16 a[34:35], v241 offset:4864\n"
"s_waitcnt vmcnt(26) lgkmcnt(0)\n"
"v_mfma_f32_16x16x32_bf16 v[32:35], a[0:3], a[144:147], 0\n"
"ds_read_b64_tr_b16 a[36:37], v241 offset:9216\n"
"s_sub_u32 s77, s0, s1\n"
"v_mfma_f32_16x16x32_bf16 v[36:39], a[0:3], a[148:151], 0\n"
"ds_read_b64_tr_b16 a[38:39], v241 offset:13824\n"
"ds_read_b64_tr_b16 a[40:41], v242 offset:9216\n"
"v_mfma_f32_16x16x32_bf16 v[44:47], a[4:7], a[148:151], 0\n"
"ds_read_b64_tr_b16 a[42:43], v242 offset:13824\n"
"v_mfma_f32_16x16x32_bf16 v[40:43], a[4:7], a[144:147], 0\n"
"buffer_load_dwordx4 a[224:227], v1, s[44:47], s72 offen offset:0 sc1\n"
"v_mfma_f32_16x16x32_bf16 v[48:51], a[8:11], a[144:147], 0\n"
"v_subrev_u32 v20, s77, v241\n"
"v_mfma_f32_16x16x32_bf16 v[52:55], a[8:11], a[148:151], 0\n"
"ds_read_b64_tr_b16 a[44:45], v241 offset:9280\n"
"ds_read_b64_tr_b16 a[46:47], v241 offset:13888\n"
"v_mfma_f32_16x16x32_bf16 v[60:63], a[12:15], a[148:151], 0\n"
"ds_read_b64_tr_b16 a[48:49], v242 offset:9280\n"
"ds_read_b64_tr_b16 a[50:51], v242 offset:13888\n"
"v_mfma_f32_16x16x32_bf16 v[56:59], a[12:15], a[144:147], 0\n"
"buffer_load_dwordx4 a[228:231], v1, s[44:47], s72 offen offset:1024 sc1\n"
"v_mfma_f32_16x16x32_bf16 v[64:67], a[16:19], a[144:147], 0\n"
"s_add_u32 s72, s72, 2048\n"
"v_subrev_u32 v21, s77, v242\n"
"v_mfma_f32_16x16x32_bf16 v[68:71], a[16:19], a[148:151], 0\n"
"ds_read_b64_tr_b16 a[52:53], v241 offset:9344\n"
"ds_read_b64_tr_b16 a[54:55], v241 offset:13952\n"
"v_mfma_f32_16x16x32_bf16 v[76:79], a[20:23], a[148:151], 0\n"
"s_lshl_b32 s77, s69, 2\n"
"ds_read_b64_tr_b16 a[56:57], v242 offset:9344\n"
"v_mfma_f32_16x16x32_bf16 v[72:75], a[20:23], a[144:147], 0\n"
"buffer_load_dwordx4 a[232:235], v1, s[44:47], s72 offen offset:0 sc1\n"
"v_mfma_f32_16x16x32_bf16 v[80:83], a[24:27], a[144:147], 0\n"
"ds_read_b64_tr_b16 a[58:59], v242 offset:13952\n"
"ds_read_b64_tr_b16 a[60:61], v241 offset:9408\n"
"v_mfma_f32_16x16x32_bf16 v[84:87], a[24:27], a[148:151], 0\n"
"s_lshl_b32 s98, 10, s77\n"
"ds_read_b64_tr_b16 a[62:63], v241 offset:14016\n"
"v_mfma_f32_16x16x32_bf16 v[92:95], a[28:31], a[148:151], 0\n"
"ds_read_b64_tr_b16 a[64:65], v242 offset:9408\n"
"ds_read_b64_tr_b16 a[66:67], v242 offset:14016\n"
"ds_read_b64_tr_b16 a[68:69], v241 offset:9472\n"
"ds_read_b64_tr_b16 a[70:71], v241 offset:14080\n"
"v_mfma_f32_16x16x32_bf16 v[88:91], a[28:31], a[144:147], 0\n"
"buffer_load_dwordx4 a[236:239], v1, s[44:47], s72 offen offset:1024 sc1\n"
"v_mfma_f32_16x16x32_bf16 v[96:99], a[32:35], a[144:147], 0\n"
"s_add_u32 s72, s72, 2048\n"
"s_mov_b32 s99, 0\n"
"v_mfma_f32_16x16x32_bf16 v[100:103], a[32:35], a[148:151], 0\n"
"s_mov_b64 s[8:9], s[98:99]\n"
"s_waitcnt vmcnt(28) lgkmcnt(0)\n"
"v_mfma_f32_16x16x32_bf16 v[32:35], a[36:39], a[152:155], v[32:35]\n"
"s_nop 0\n"
"s_barrier\n"
"v_mfma_f32_16x16x32_bf16 v[36:39], a[36:39], a[156:159], v[36:39]\n"
"s_bfe_u64 s[60:61], s[58:59], 0x5000a\n"
"s_mul_i32 s60, s60, 147456\n"
"s_add_u32 s73, s56, s60\n"
"s_add_u32 m0, s0, 0\n"
"v_mfma_f32_16x16x32_bf16 v[44:47], a[40:43], a[156:159], v[44:47]\n"
"buffer_load_dwordx4 v28, s[40:43], s73 offen offset:0 lds\n"
"v_mfma_f32_16x16x32_bf16 v[40:43], a[40:43], a[152:155], v[40:43]\n"
"ds_read_b64_tr_b16 a[0:1], v241 offset:18432\n"
"ds_read_b64_tr_b16 a[2:3], v241 offset:23040\n"
"v_mfma_f32_16x16x32_bf16 v[48:51], a[44:47], a[152:155], v[48:51]\n"
"ds_read_b64_tr_b16 a[4:5], v242 offset:18432\n"
"ds_read_b64_tr_b16 a[6:7], v242 offset:23040\n"
"v_mfma_f32_16x16x32_bf16 v[52:55], a[44:47], a[156:159], v[52:55]\n"
"buffer_load_dwordx4 v29, s[40:43], s73 offen offset:1024 lds\n"
"v_mfma_f32_16x16x32_bf16 v[60:63], a[48:51], a[156:159], v[60:63]\n"
"ds_read_b64_tr_b16 a[8:9], v241 offset:18496\n"
"ds_read_b64_tr_b16 a[10:11], v241 offset:23104\n"
"v_mfma_f32_16x16x32_bf16 v[56:59], a[48:51], a[152:155], v[56:59]\n"
"ds_read_b64_tr_b16 a[12:13], v242 offset:18496\n"
"ds_read_b64_tr_b16 a[14:15], v242 offset:23104\n"
"v_mfma_f32_16x16x32_bf16 v[64:67], a[52:55], a[152:155], v[64:67]\n"
"ds_read_b64_tr_b16 a[16:17], v241 offset:18560\n"
"ds_read_b64_tr_b16 a[18:19], v241 offset:23168\n"
"v_mfma_f32_16x16x32_bf16 v[68:71], a[52:55], a[156:159], v[68:71]\n"
"buffer_load_dwordx4 v30, s[40:43], s73 offen offset:2048 lds\n"
"v_mfma_f32_16x16x32_bf16 v[76:79], a[56:59], a[156:159], v[76:79]\n"
"ds_read_b64_tr_b16 a[20:21], v242 offset:18560\n"
"ds_read_b64_tr_b16 a[22:23], v242 offset:23168\n"
"v_mfma_f32_16x16x32_bf16 v[72:75], a[56:59], a[152:155], v[72:75]\n"
"ds_read_b64_tr_b16 a[24:25], v241 offset:18624\n"
"ds_read_b64_tr_b16 a[26:27], v241 offset:23232\n"
"v_mfma_f32_16x16x32_bf16 v[80:83], a[60:63], a[152:155], v[80:83]\n"
"ds_read_b64_tr_b16 a[28:29], v242 offset:18624\n"
"ds_read_b64_tr_b16 a[30:31], v242 offset:23232\n"
"v_mfma_f32_16x16x32_bf16 v[84:87], a[60:63], a[156:159], v[84:87]\n"
"buffer_load_dwordx4 v31, s[40:43], s73 offen offset:3072 lds\n"
"v_mfma_f32_16x16x32_bf16 v[92:95], a[64:67], a[156:159], v[92:95]\n"
"ds_read_b64_tr_b16 a[32:33], v241 offset:18688\n"
"ds_read_b64_tr_b16 a[34:35], v241 offset:23296\n"
"v_mfma_f32_16x16x32_bf16 v[88:91], a[64:67], a[152:155], v[88:91]\n"
"v_mfma_f32_16x16x32_bf16 v[96:99], a[68:71], a[152:155], v[96:99]\n"
"s_add_u32 m0, s0, 4096\n"
"v_mfma_f32_16x16x32_bf16 v[100:103], a[68:71], a[156:159], v[100:103]\n"
"s_mov_b64 exec, s[96:97]\n"
"buffer_load_dwordx4 v240, s[40:43], s73 offen offset:0 lds\n"
"s_mov_b64 exec, -1\n"
"s_add_u32 s73, s73, 73728\n"
"s_waitcnt vmcnt(31) lgkmcnt(0)\n"
"v_mfma_f32_16x16x32_bf16 v[32:35], a[0:3], a[160:163], v[32:35]\n"
"ds_read_b64_tr_b16 a[36:37], v241 offset:27648\n"
"ds_read_b64_tr_b16 a[38:39], v241 offset:32256\n"
"v_mfma_f32_16x16x32_bf16 v[36:39], a[0:3], a[164:167], v[36:39]\n"
"ds_read_b64_tr_b16 a[40:41], v242 offset:27648\n"
"v_mfma_f32_16x16x32_bf16 v[44:47], a[4:7], a[164:167], v[44:47]\n"
"s_bfe_u64 s[62:63], s[58:59], 0x5000a\n"
"s_mul_i32 s62, s62, 8192\n"
"s_add_u32 s72, s57, s62\n"
"v_mfma_f32_16x16x32_bf16 v[40:43], a[4:7], a[160:163], v[40:43]\n"
"buffer_load_dwordx4 a[144:147], v1, s[44:47], s72 offen offset:0 sc1\n"
"v_mfma_f32_16x16x32_bf16 v[48:51], a[8:11], a[160:163], v[48:51]\n"
"ds_read_b64_tr_b16 a[42:43], v242 offset:32256\n"
"ds_read_b64_tr_b16 a[44:45], v241 offset:27712\n"
"v_mfma_f32_16x16x32_bf16 v[52:55], a[8:11], a[164:167], v[52:55]\n"
"ds_read_b64_tr_b16 a[46:47], v241 offset:32320\n"
"ds_read_b64_tr_b16 a[48:49], v242 offset:27712\n"
"v_mfma_f32_16x16x32_bf16 v[60:63], a[12:15], a[164:167], v[60:63]\n"
"ds_read_b64_tr_b16 a[50:51], v242 offset:32320\n"
"ds_read_b64_tr_b16 a[52:53], v241 offset:27776\n"
"v_mfma_f32_16x16x32_bf16 v[56:59], a[12:15], a[160:163], v[56:59]\n"
"buffer_load_dwordx4 a[148:151], v1, s[44:47], s72 offen offset:1024 sc1\n"
"v_mfma_f32_16x16x32_bf16 v[64:67], a[16:19], a[160:163], v[64:67]\n"
"s_add_u32 s72, s72, 2048\n"
"ds_read_b64_tr_b16 a[54:55], v241 offset:32384\n"
"v_mfma_f32_16x16x32_bf16 v[68:71], a[16:19], a[164:167], v[68:71]\n"
"ds_read_b64_tr_b16 a[56:57], v242 offset:27776\n"
"ds_read_b64_tr_b16 a[58:59], v242 offset:32384\n"
"v_mfma_f32_16x16x32_bf16 v[76:79], a[20:23], a[164:167], v[76:79]\n"
"ds_read_b64_tr_b16 a[60:61], v241 offset:27840\n"
"ds_read_b64_tr_b16 a[62:63], v241 offset:32448\n"
"v_mfma_f32_16x16x32_bf16 v[72:75], a[20:23], a[160:163], v[72:75]\n"
"buffer_load_dwordx4 a[152:155], v1, s[44:47], s72 offen offset:0 sc1\n"
"v_mfma_f32_16x16x32_bf16 v[80:83], a[24:27], a[160:163], v[80:83]\n"
"ds_read_b64_tr_b16 a[64:65], v242 offset:27840\n"
"ds_read_b64_tr_b16 a[66:67], v242 offset:32448\n"
"v_mfma_f32_16x16x32_bf16 v[84:87], a[24:27], a[164:167], v[84:87]\n"
"ds_read_b64_tr_b16 a[68:69], v241 offset:27904\n"
"v_mfma_f32_16x16x32_bf16 v[92:95], a[28:31], a[164:167], v[92:95]\n"
"ds_read_b64_tr_b16 a[70:71], v241 offset:32512\n"
"v_mfma_f32_16x16x32_bf16 v[88:91], a[28:31], a[160:163], v[88:91]\n"
"buffer_load_dwordx4 a[156:159], v1, s[44:47], s72 offen offset:1024 sc1\n"
"v_mfma_f32_16x16x32_bf16 v[96:99], a[32:35], a[160:163], v[96:99]\n"
"s_add_u32 s72, s72, 2048\n"
"v_mfma_f32_16x16x32_bf16 v[100:103], a[32:35], a[164:167], v[100:103]\n"
"s_waitcnt vmcnt(13)\n"
"s_mov_b64 exec, 3\n"
"buffer_store_dword v252, v27, s[36:39], 0 offen sc1\n"
"s_mov_b64 exec, -1\n"
"s_waitcnt lgkmcnt(0)\n"
"v_mfma_f32_16x16x32_bf16 v[32:35], a[36:39], a[168:171], v[32:35]\n"
"s_nop 0\n"
"s_barrier\n"
"v_mfma_f32_16x16x32_bf16 v[36:39], a[36:39], a[172:175], v[36:39]\n"
"s_add_u32 m0, s0, 18432\n"
"ds_read_b64_tr_b16 a[0:1], v241 offset:36864\n"
"v_mfma_f32_16x16x32_bf16 v[44:47], a[40:43], a[172:175], v[44:47]\n"
"buffer_load_dwordx4 v28, s[40:43], s73 offen offset:0 lds\n"
"v_mfma_f32_16x16x32_bf16 v[40:43], a[40:43], a[168:171], v[40:43]\n"
"ds_read_b64_tr_b16 a[2:3], v241 offset:41472\n"
"ds_read_b64_tr_b16 a[4:5], v242 offset:36864\n"
"v_mfma_f32_16x16x32_bf16 v[48:51], a[44:47], a[168:171], v[48:51]\n"
"ds_read_b64_tr_b16 a[6:7], v242 offset:41472\n"
"ds_read_b64_tr_b16 a[8:9], v241 offset:36928\n"
"v_mfma_f32_16x16x32_bf16 v[52:55], a[44:47], a[172:175], v[52:55]\n"
"buffer_load_dwordx4 v29, s[40:43], s73 offen offset:1024 lds\n"
"v_mfma_f32_16x16x32_bf16 v[60:63], a[48:51], a[172:175], v[60:63]\n"
"ds_read_b64_tr_b16 a[10:11], v241 offset:41536\n"
"ds_read_b64_tr_b16 a[12:13], v242 offset:36928\n"
"v_mfma_f32_16x16x32_bf16 v[56:59], a[48:51], a[168:171], v[56:59]\n"
"ds_read_b64_tr_b16 a[14:15], v242 offset:41536\n"
"ds_read_b64_tr_b16 a[16:17], v241 offset:36992\n"
"v_mfma_f32_16x16x32_bf16 v[64:67], a[52:55], a[168:171], v[64:67]\n"
"ds_read_b64_tr_b16 a[18:19], v241 offset:41600\n"
"ds_read_b64_tr_b16 a[20:21], v242 offset:36992\n"
"v_mfma_f32_16x16x32_bf16 v[68:71], a[52:55], a[172:175], v[68:71]\n"
"buffer_load_dwordx4 v30, s[40:43], s73 offen offset:2048 lds\n"
"v_mfma_f32_16x16x32_bf16 v[76:79], a[56:59], a[172:175], v[76:79]\n"
"ds_read_b64_tr_b16 a[22:23], v242 offset:41600\n"
"ds_read_b64_tr_b16 a[24:25], v241 offset:37056\n"
"v_mfma_f32_16x16x32_bf16 v[72:75], a[56:59], a[168:171], v[72:75]\n"
"ds_read_b64_tr_b16 a[26:27], v241 offset:41664\n"
"ds_read_b64_tr_b16 a[28:29], v242 offset:37056\n"
"v_mfma_f32_16x16x32_bf16 v[80:83], a[60:63], a[168:171], v[80:83]\n"
"ds_read_b64_tr_b16 a[30:31], v242 offset:41664\n"
"v_mfma_f32_16x16x32_bf16 v[84:87], a[60:63], a[172:175], v[84:87]\n"
"buffer_load_dwordx4 v31, s[40:43], s73 offen offset:3072 lds\n"
"v_mfma_f32_16x16x32_bf16 v[92:95], a[64:67], a[172:175], v[92:95]\n"
"ds_read_b64_tr_b16 a[32:33], v241 offset:37120\n"
"ds_read_b64_tr_b16 a[34:35], v241 offset:41728\n"
"v_mfma_f32_16x16x32_bf16 v[88:91], a[64:67], a[168:171], v[88:91]\n"
"v_mfma_f32_16x16x32_bf16 v[96:99], a[68:71], a[168:171], v[96:99]\n"
"s_add_u32 m0, s0, 22528\n"
"v_mfma_f32_16x16x32_bf16 v[100:103], a[68:71], a[172:175], v[100:103]\n"
"s_mov_b64 exec, s[96:97]\n"
"buffer_load_dwordx4 v240, s[40:43], s73 offen offset:0 lds\n"
"s_mov_b64 exec, -1\n"
"s_add_u32 s73, s73, 73728\n"
"s_waitcnt lgkmcnt(0)\n"
"v_mfma_f32_16x16x32_bf16 v[32:35], a[0:3], a[208:211], v[32:35]\n"
"ds_read_b64_tr_b16 a[36:37], v241 offset:46080\n"
"ds_read_b64_tr_b16 a[38:39], v241 offset:50688\n"
"v_mfma_f32_16x16x32_bf16 v[36:39], a[0:3], a[212:215], v[36:39]\n"
"ds_read_b64_tr_b16 a[40:41], v242 offset:46080\n"
"v_mfma_f32_16x16x32_bf16 v[44:47], a[4:7], a[212:215], v[44:47]\n"
"ds_read_b64_tr_b16 a[42:43], v242 offset:50688\n"
"v_mfma_f32_16x16x32_bf16 v[40:43], a[4:7], a[208:211], v[40:43]\n"
"buffer_load_dwordx4 a[160:163], v1, s[44:47], s72 offen offset:0 sc1\n"
"v_mfma_f32_16x16x32_bf16 v[48:51], a[8:11], a[208:211], v[48:51]\n"
"ds_read_b64_tr_b16 a[44:45], v241 offset:46144\n"
"ds_read_b64_tr_b16 a[46:47], v241 offset:50752\n"
"v_mfma_f32_16x16x32_bf16 v[52:55], a[8:11], a[212:215], v[52:55]\n"
"ds_read_b64_tr_b16 a[48:49], v242 offset:46144\n"
"ds_read_b64_tr_b16 a[50:51], v242 offset:50752\n"
"v_mfma_f32_16x16x32_bf16 v[60:63], a[12:15], a[212:215], v[60:63]\n"
"ds_read_b64_tr_b16 a[52:53], v241 offset:46208\n"
"v_mfma_f32_16x16x32_bf16 v[56:59], a[12:15], a[208:211], v[56:59]\n"
"buffer_load_dwordx4 a[164:167], v1, s[44:47], s72 offen offset:1024 sc1\n"
"v_mfma_f32_16x16x32_bf16 v[64:67], a[16:19], a[208:211], v[64:67]\n"
"s_add_u32 s72, s72, 2048\n"
"ds_read_b64_tr_b16 a[54:55], v241 offset:50816\n"
"v_mfma_f32_16x16x32_bf16 v[68:71], a[16:19], a[212:215], v[68:71]\n"
"ds_read_b64_tr_b16 a[56:57], v242 offset:46208\n"
"ds_read_b64_tr_b16 a[58:59], v242 offset:50816\n"
"v_mfma_f32_16x16x32_bf16 v[76:79], a[20:23], a[212:215], v[76:79]\n"
"ds_read_b64_tr_b16 a[60:61], v241 offset:46272\n"
"ds_read_b64_tr_b16 a[62:63], v241 offset:50880\n"
"v_mfma_f32_16x16x32_bf16 v[72:75], a[20:23], a[208:211], v[72:75]\n"
"buffer_load_dwordx4 a[168:171], v1, s[44:47], s72 offen offset:0 sc1\n"
"v_mfma_f32_16x16x32_bf16 v[80:83], a[24:27], a[208:211], v[80:83]\n"
"ds_read_b64_tr_b16 a[64:65], v242 offset:46272\n"
"ds_read_b64_tr_b16 a[66:67], v242 offset:50880\n"
"v_mfma_f32_16x16x32_bf16 v[84:87], a[24:27], a[212:215], v[84:87]\n"
"ds_read_b64_tr_b16 a[68:69], v241 offset:46336\n"
"v_mfma_f32_16x16x32_bf16 v[92:95], a[28:31], a[212:215], v[92:95]\n"
"ds_read_b64_tr_b16 a[70:71], v241 offset:50944\n"
"v_mfma_f32_16x16x32_bf16 v[88:91], a[28:31], a[208:211], v[88:91]\n"
"buffer_load_dwordx4 a[172:175], v1, s[44:47], s72 offen offset:1024 sc1\n"
"v_mfma_f32_16x16x32_bf16 v[96:99], a[32:35], a[208:211], v[96:99]\n"
"s_add_u32 s72, s72, 2048\n"
"v_mfma_f32_16x16x32_bf16 v[100:103], a[32:35], a[212:215], v[100:103]\n"
"s_waitcnt lgkmcnt(0)\n"
"v_mfma_f32_16x16x32_bf16 v[32:35], a[36:39], a[216:219], v[32:35]\n"
"s_nop 0\n"
"s_barrier\n"
"v_mfma_f32_16x16x32_bf16 v[36:39], a[36:39], a[220:223], v[36:39]\n"
"s_add_u32 m0, s0, 36864\n"
"ds_read_b64_tr_b16 a[0:1], v20 offset:0\n"
"v_mfma_f32_16x16x32_bf16 v[44:47], a[40:43], a[220:223], v[44:47]\n"
"buffer_load_dwordx4 v28, s[40:43], s73 offen offset:0 lds\n"
"v_mfma_f32_16x16x32_bf16 v[40:43], a[40:43], a[216:219], v[40:43]\n"
"ds_read_b64_tr_b16 a[2:3], v20 offset:4608\n"
"ds_read_b64_tr_b16 a[4:5], v21 offset:0\n"
"v_mfma_f32_16x16x32_bf16 v[48:51], a[44:47], a[216:219], v[48:51]\n"
"ds_read_b64_tr_b16 a[6:7], v21 offset:4608\n"
"ds_read_b64_tr_b16 a[8:9], v20 offset:64\n"
"v_mfma_f32_16x16x32_bf16 v[52:55], a[44:47], a[220:223], v[52:55]\n"
"buffer_load_dwordx4 v29, s[40:43], s73 offen offset:1024 lds\n"
"v_mfma_f32_16x16x32_bf16 v[60:63], a[48:51], a[220:223], v[60:63]\n"
"ds_read_b64_tr_b16 a[10:11], v20 offset:4672\n"
"ds_read_b64_tr_b16 a[12:13], v21 offset:64\n"
"v_mfma_f32_16x16x32_bf16 v[56:59], a[48:51], a[216:219], v[56:59]\n"
"ds_read_b64_tr_b16 a[14:15], v21 offset:4672\n"
"ds_read_b64_tr_b16 a[16:17], v20 offset:128\n"
"v_mfma_f32_16x16x32_bf16 v[64:67], a[52:55], a[216:219], v[64:67]\n"
"ds_read_b64_tr_b16 a[18:19], v20 offset:4736\n"
"ds_read_b64_tr_b16 a[20:21], v21 offset:128\n"
"v_mfma_f32_16x16x32_bf16 v[68:71], a[52:55], a[220:223], v[68:71]\n"
"buffer_load_dwordx4 v30, s[40:43], s73 offen offset:2048 lds\n"
"v_mfma_f32_16x16x32_bf16 v[76:79], a[56:59], a[220:223], v[76:79]\n"
"ds_read_b64_tr_b16 a[22:23], v21 offset:4736\n"
"ds_read_b64_tr_b16 a[24:25], v20 offset:192\n"
"v_mfma_f32_16x16x32_bf16 v[72:75], a[56:59], a[216:219], v[72:75]\n"
"ds_read_b64_tr_b16 a[26:27], v20 offset:4800\n"
"ds_read_b64_tr_b16 a[28:29], v21 offset:192\n"
"v_mfma_f32_16x16x32_bf16 v[80:83], a[60:63], a[216:219], v[80:83]\n"
"ds_read_b64_tr_b16 a[30:31], v21 offset:4800\n"
"v_mfma_f32_16x16x32_bf16 v[84:87], a[60:63], a[220:223], v[84:87]\n"
"buffer_load_dwordx4 v31, s[40:43], s73 offen offset:3072 lds\n"
"v_mfma_f32_16x16x32_bf16 v[92:95], a[64:67], a[220:223], v[92:95]\n"
"ds_read_b64_tr_b16 a[32:33], v20 offset:256\n"
"ds_read_b64_tr_b16 a[34:35], v20 offset:4864\n"
"v_mfma_f32_16x16x32_bf16 v[88:91], a[64:67], a[216:219], v[88:91]\n"
"v_mfma_f32_16x16x32_bf16 v[96:99], a[68:71], a[216:219], v[96:99]\n"
"s_add_u32 m0, s0, 40960\n"
"v_mfma_f32_16x16x32_bf16 v[100:103], a[68:71], a[220:223], v[100:103]\n"
"s_mov_b64 exec, s[96:97]\n"
"buffer_load_dwordx4 v240, s[40:43], s73 offen offset:0 lds\n"
"s_mov_b64 exec, -1\n"
"s_add_u32 s73, s73, 73728\n"
"s_waitcnt vmcnt(26) lgkmcnt(0)\n"
"v_mfma_f32_16x16x32_bf16 v[32:35], a[0:3], a[224:227], v[32:35]\n"
"ds_read_b64_tr_b16 a[36:37], v20 offset:9216\n"
"ds_read_b64_tr_b16 a[38:39], v20 offset:13824\n"
"v_mfma_f32_16x16x32_bf16 v[36:39], a[0:3], a[228:231], v[36:39]\n"
"ds_read_b64_tr_b16 a[40:41], v21 offset:9216\n"
"v_mfma_f32_16x16x32_bf16 v[44:47], a[4:7], a[228:231], v[44:47]\n"
"ds_read_b64_tr_b16 a[42:43], v21 offset:13824\n"
"v_mfma_f32_16x16x32_bf16 v[40:43], a[4:7], a[224:227], v[40:43]\n"
"buffer_load_dwordx4 a[208:211], v1, s[44:47], s72 offen offset:0 sc1\n"
"v_mfma_f32_16x16x32_bf16 v[48:51], a[8:11], a[224:227], v[48:51]\n"
"ds_read_b64_tr_b16 a[44:45], v20 offset:9280\n"
"ds_read_b64_tr_b16 a[46:47], v20 offset:13888\n"
"v_mfma_f32_16x16x32_bf16 v[52:55], a[8:11], a[228:231], v[52:55]\n"
"ds_read_b64_tr_b16 a[48:49], v21 offset:9280\n"
"ds_read_b64_tr_b16 a[50:51], v21 offset:13888\n"
"v_mfma_f32_16x16x32_bf16 v[60:63], a[12:15], a[228:231], v[60:63]\n"
"ds_read_b64_tr_b16 a[52:53], v20 offset:9344\n"
"v_mfma_f32_16x16x32_bf16 v[56:59], a[12:15], a[224:227], v[56:59]\n"
"buffer_load_dwordx4 a[212:215], v1, s[44:47], s72 offen offset:1024 sc1\n"
"v_mfma_f32_16x16x32_bf16 v[64:67], a[16:19], a[224:227], v[64:67]\n"
"s_add_u32 s72, s72, 2048\n"
"ds_read_b64_tr_b16 a[54:55], v20 offset:13952\n"
"v_mfma_f32_16x16x32_bf16 v[68:71], a[16:19], a[228:231], v[68:71]\n"
"ds_read_b64_tr_b16 a[56:57], v21 offset:9344\n"
"ds_read_b64_tr_b16 a[58:59], v21 offset:13952\n"
"v_mfma_f32_16x16x32_bf16 v[76:79], a[20:23], a[228:231], v[76:79]\n"
"ds_read_b64_tr_b16 a[60:61], v20 offset:9408\n"
"ds_read_b64_tr_b16 a[62:63], v20 offset:14016\n"
"v_mfma_f32_16x16x32_bf16 v[72:75], a[20:23], a[224:227], v[72:75]\n"
"buffer_load_dwordx4 a[216:219], v1, s[44:47], s72 offen offset:0 sc1\n"
"v_mfma_f32_16x16x32_bf16 v[80:83], a[24:27], a[224:227], v[80:83]\n"
"ds_read_b64_tr_b16 a[64:65], v21 offset:9408\n"
"ds_read_b64_tr_b16 a[66:67], v21 offset:14016\n"
"v_mfma_f32_16x16x32_bf16 v[84:87], a[24:27], a[228:231], v[84:87]\n"
"ds_read_b64_tr_b16 a[68:69], v20 offset:9472\n"
"v_mfma_f32_16x16x32_bf16 v[92:95], a[28:31], a[228:231], v[92:95]\n"
"ds_read_b64_tr_b16 a[70:71], v20 offset:14080\n"
"v_mfma_f32_16x16x32_bf16 v[88:91], a[28:31], a[224:227], v[88:91]\n"
"buffer_load_dwordx4 a[220:223], v1, s[44:47], s72 offen offset:1024 sc1\n"
"v_mfma_f32_16x16x32_bf16 v[96:99], a[32:35], a[224:227], v[96:99]\n"
"s_add_u32 s72, s72, 2048\n"
"v_mfma_f32_16x16x32_bf16 v[100:103], a[32:35], a[228:231], v[100:103]\n"
"buffer_load_dwordx2 v[248:249], v251, s[36:39], s91 offen sc1\n"
"s_waitcnt vmcnt(29) lgkmcnt(0)\n"
"v_mfma_f32_16x16x32_bf16 v[32:35], a[36:39], a[232:235], v[32:35]\n"
"s_waitcnt vmcnt(24)\n"
"s_barrier\n"
"v_mfma_f32_16x16x32_bf16 v[36:39], a[36:39], a[236:239], v[36:39]\n"
"s_add_u32 m0, s1, 0\n"
"ds_read_b64_tr_b16 a[0:1], v241 offset:0\n"
"v_mfma_f32_16x16x32_bf16 v[44:47], a[40:43], a[236:239], v[44:47]\n"
"buffer_load_dwordx4 v28, s[40:43], s73 offen offset:0 lds\n"
"v_mfma_f32_16x16x32_bf16 v[40:43], a[40:43], a[232:235], v[40:43]\n"
"ds_read_b64_tr_b16 a[2:3], v241 offset:4608\n"
"ds_read_b64_tr_b16 a[4:5], v242 offset:0\n"
"v_mfma_f32_16x16x32_bf16 v[48:51], a[44:47], a[232:235], v[48:51]\n"
"ds_read_b64_tr_b16 a[6:7], v242 offset:4608\n"
"ds_read_b64_tr_b16 a[8:9], v241 offset:64\n"
"v_mfma_f32_16x16x32_bf16 v[52:55], a[44:47], a[236:239], v[52:55]\n"
"buffer_load_dwordx4 v29, s[40:43], s73 offen offset:1024 lds\n"
"v_mfma_f32_16x16x32_bf16 v[60:63], a[48:51], a[236:239], v[60:63]\n"
"ds_read_b64_tr_b16 a[10:11], v241 offset:4672\n"
"ds_read_b64_tr_b16 a[12:13], v242 offset:64\n"
"v_mfma_f32_16x16x32_bf16 v[56:59], a[48:51], a[232:235], v[56:59]\n"
"ds_read_b64_tr_b16 a[14:15], v242 offset:4672\n"
"ds_read_b64_tr_b16 a[16:17], v241 offset:128\n"
"v_mfma_f32_16x16x32_bf16 v[64:67], a[52:55], a[232:235], v[64:67]\n"
"ds_read_b64_tr_b16 a[18:19], v241 offset:4736\n"
"ds_read_b64_tr_b16 a[20:21], v242 offset:128\n"
"v_mfma_f32_16x16x32_bf16 v[68:71], a[52:55], a[236:239], v[68:71]\n"
"buffer_load_dwordx4 v30, s[40:43], s73 offen offset:2048 lds\n"
"v_mfma_f32_16x16x32_bf16 v[76:79], a[56:59], a[236:239], v[76:79]\n"
"ds_read_b64_tr_b16 a[22:23], v242 offset:4736\n"
"ds_read_b64_tr_b16 a[24:25], v241 offset:192\n"
"v_mfma_f32_16x16x32_bf16 v[72:75], a[56:59], a[232:235], v[72:75]\n"
"ds_read_b64_tr_b16 a[26:27], v241 offset:4800\n"
"ds_read_b64_tr_b16 a[28:29], v242 offset:192\n"
"v_mfma_f32_16x16x32_bf16 v[80:83], a[60:63], a[232:235], v[80:83]\n"
"ds_read_b64_tr_b16 a[30:31], v242 offset:4800\n"
"v_mfma_f32_16x16x32_bf16 v[84:87], a[60:63], a[236:239], v[84:87]\n"
"buffer_load_dwordx4 v31, s[40:43], s73 offen offset:3072 lds\n"
"v_mfma_f32_16x16x32_bf16 v[92:95], a[64:67], a[236:239], v[92:95]\n"
"ds_read_b64_tr_b16 a[32:33], v241 offset:256\n"
"ds_read_b64_tr_b16 a[34:35], v241 offset:4864\n"
"v_mfma_f32_16x16x32_bf16 v[88:91], a[64:67], a[232:235], v[88:91]\n"
"v_mfma_f32_16x16x32_bf16 v[96:99], a[68:71], a[232:235], v[96:99]\n"
"s_add_u32 m0, s1, 4096\n"
"v_mfma_f32_16x16x32_bf16 v[100:103], a[68:71], a[236:239], v[100:103]\n"
"s_mov_b64 exec, s[96:97]\n"
"buffer_load_dwordx4 v240, s[40:43], s73 offen offset:0 lds\n"
"s_mov_b64 exec, -1\n"
"s_add_u32 s73, s73, 73728\n"
"s_waitcnt vmcnt(27) lgkmcnt(0)\n"
"v_mfma_f32_16x16x32_bf16 v[32:35], a[0:3], a[144:147], v[32:35]\n"
"ds_read_b64_tr_b16 a[36:37], v241 offset:9216\n"
"ds_read_b64_tr_b16 a[38:39], v241 offset:13824\n"
"v_mfma_f32_16x16x32_bf16 v[36:39], a[0:3], a[148:151], v[36:39]\n"
"ds_read_b64_tr_b16 a[40:41], v242 offset:9216\n"
"v_mfma_f32_16x16x32_bf16 v[44:47], a[4:7], a[148:151], v[44:47]\n"
"ds_read_b64_tr_b16 a[42:43], v242 offset:13824\n"
"v_mfma_f32_16x16x32_bf16 v[40:43], a[4:7], a[144:147], v[40:43]\n"
"buffer_load_dwordx4 a[224:227], v1, s[44:47], s72 offen offset:0 sc1\n"
"v_mfma_f32_16x16x32_bf16 v[48:51], a[8:11], a[144:147], v[48:51]\n"
"ds_read_b64_tr_b16 a[44:45], v241 offset:9280\n"
"ds_read_b64_tr_b16 a[46:47], v241 offset:13888\n"
"v_mfma_f32_16x16x32_bf16 v[52:55], a[8:11], a[148:151], v[52:55]\n"
"ds_read_b64_tr_b16 a[48:49], v242 offset:9280\n"
"ds_read_b64_tr_b16 a[50:51], v242 offset:13888\n"
"v_mfma_f32_16x16x32_bf16 v[60:63], a[12:15], a[148:151], v[60:63]\n"
"ds_read_b64_tr_b16 a[52:53], v241 offset:9344\n"
"v_mfma_f32_16x16x32_bf16 v[56:59], a[12:15], a[144:147], v[56:59]\n"
"buffer_load_dwordx4 a[228:231], v1, s[44:47], s72 offen offset:1024 sc1\n"
"v_mfma_f32_16x16x32_bf16 v[64:67], a[16:19], a[144:147], v[64:67]\n"
"s_add_u32 s72, s72, 2048\n"
"ds_read_b64_tr_b16 a[54:55], v241 offset:13952\n"
"v_mfma_f32_16x16x32_bf16 v[68:71], a[16:19], a[148:151], v[68:71]\n"
"ds_read_b64_tr_b16 a[56:57], v242 offset:9344\n"
"ds_read_b64_tr_b16 a[58:59], v242 offset:13952\n"
"v_mfma_f32_16x16x32_bf16 v[76:79], a[20:23], a[148:151], v[76:79]\n"
"ds_read_b64_tr_b16 a[60:61], v241 offset:9408\n"
"ds_read_b64_tr_b16 a[62:63], v241 offset:14016\n"
"v_mfma_f32_16x16x32_bf16 v[72:75], a[20:23], a[144:147], v[72:75]\n"
"buffer_load_dwordx4 a[232:235], v1, s[44:47], s72 offen offset:0 sc1\n"
"v_mfma_f32_16x16x32_bf16 v[80:83], a[24:27], a[144:147], v[80:83]\n"
"ds_read_b64_tr_b16 a[64:65], v242 offset:9408\n"
"ds_read_b64_tr_b16 a[66:67], v242 offset:14016\n"
"v_mfma_f32_16x16x32_bf16 v[84:87], a[24:27], a[148:151], v[84:87]\n"
"ds_read_b64_tr_b16 a[68:69], v241 offset:9472\n"
"v_mfma_f32_16x16x32_bf16 v[92:95], a[28:31], a[148:151], v[92:95]\n"
"ds_read_b64_tr_b16 a[70:71], v241 offset:14080\n"
"v_mfma_f32_16x16x32_bf16 v[88:91], a[28:31], a[144:147], v[88:91]\n"
"buffer_load_dwordx4 a[236:239], v1, s[44:47], s72 offen offset:1024 sc1\n"
"v_mfma_f32_16x16x32_bf16 v[96:99], a[32:35], a[144:147], v[96:99]\n"
"s_add_u32 s72, s72, 2048\n"
"v_mfma_f32_16x16x32_bf16 v[100:103], a[32:35], a[148:151], v[100:103]\n"
"s_waitcnt vmcnt(29) lgkmcnt(0)\n"
"v_mfma_f32_16x16x32_bf16 v[32:35], a[36:39], a[152:155], v[32:35]\n"
"s_waitcnt vmcnt(23)\n"
"s_barrier\n"
"v_mfma_f32_16x16x32_bf16 v[36:39], a[36:39], a[156:159], v[36:39]\n"
"s_bfe_u64 s[60:61], s[58:59], 0x50014\n"
"s_mul_i32 s60, s60, 147456\n"
"s_add_u32 s73, s56, s60\n"
"s_add_u32 m0, s0, 0\n"
"v_mfma_f32_16x16x32_bf16 v[44:47], a[40:43], a[156:159], v[44:47]\n"
"buffer_load_dwordx4 v28, s[40:43], s73 offen offset:0 lds\n"
"v_mfma_f32_16x16x32_bf16 v[40:43], a[40:43], a[152:155], v[40:43]\n"
"ds_read_b64_tr_b16 a[0:1], v241 offset:18432\n"
"ds_read_b64_tr_b16 a[2:3], v241 offset:23040\n"
"v_mfma_f32_16x16x32_bf16 v[48:51], a[44:47], a[152:155], v[48:51]\n"
"ds_read_b64_tr_b16 a[4:5], v242 offset:18432\n"
"ds_read_b64_tr_b16 a[6:7], v242 offset:23040\n"
"v_mfma_f32_16x16x32_bf16 v[52:55], a[44:47], a[156:159], v[52:55]\n"
"buffer_load_dwordx4 v29, s[40:43], s73 offen offset:1024 lds\n"
"v_mfma_f32_16x16x32_bf16 v[60:63], a[48:51], a[156:159], v[60:63]\n"
"ds_read_b64_tr_b16 a[8:9], v241 offset:18496\n"
"ds_read_b64_tr_b16 a[10:11], v241 offset:23104\n"
"v_mfma_f32_16x16x32_bf16 v[56:59], a[48:51], a[152:155], v[56:59]\n"
"ds_read_b64_tr_b16 a[12:13], v242 offset:18496\n"
"ds_read_b64_tr_b16 a[14:15], v242 offset:23104\n"
"v_mfma_f32_16x16x32_bf16 v[64:67], a[52:55], a[152:155], v[64:67]\n"
"ds_read_b64_tr_b16 a[16:17], v241 offset:18560\n"
"ds_read_b64_tr_b16 a[18:19], v241 offset:23168\n"
"v_mfma_f32_16x16x32_bf16 v[68:71], a[52:55], a[156:159], v[68:71]\n"
"buffer_load_dwordx4 v30, s[40:43], s73 offen offset:2048 lds\n"
"v_mfma_f32_16x16x32_bf16 v[76:79], a[56:59], a[156:159], v[76:79]\n"
"ds_read_b64_tr_b16 a[20:21], v242 offset:18560\n"
"ds_read_b64_tr_b16 a[22:23], v242 offset:23168\n"
"v_mfma_f32_16x16x32_bf16 v[72:75], a[56:59], a[152:155], v[72:75]\n"
"ds_read_b64_tr_b16 a[24:25], v241 offset:18624\n"
"ds_read_b64_tr_b16 a[26:27], v241 offset:23232\n"
"v_mfma_f32_16x16x32_bf16 v[80:83], a[60:63], a[152:155], v[80:83]\n"
"ds_read_b64_tr_b16 a[28:29], v242 offset:18624\n"
"ds_read_b64_tr_b16 a[30:31], v242 offset:23232\n"
"v_mfma_f32_16x16x32_bf16 v[84:87], a[60:63], a[156:159], v[84:87]\n"
"buffer_load_dwordx4 v31, s[40:43], s73 offen offset:3072 lds\n"
"v_mfma_f32_16x16x32_bf16 v[92:95], a[64:67], a[156:159], v[92:95]\n"
"ds_read_b64_tr_b16 a[32:33], v241 offset:18688\n"
"ds_read_b64_tr_b16 a[34:35], v241 offset:23296\n"
"v_mfma_f32_16x16x32_bf16 v[88:91], a[64:67], a[152:155], v[88:91]\n"
"v_mfma_f32_16x16x32_bf16 v[96:99], a[68:71], a[152:155], v[96:99]\n"
"s_add_u32 m0, s0, 4096\n"
"v_mfma_f32_16x16x32_bf16 v[100:103], a[68:71], a[156:159], v[100:103]\n"
"s_mov_b64 exec, s[96:97]\n"
"buffer_load_dwordx4 v240, s[40:43], s73 offen offset:0 lds\n"
"s_mov_b64 exec, -1\n"
"s_add_u32 s73, s73, 73728\n"
"s_waitcnt vmcnt(14)\n"
"v_cmp_eq_u32 vcc, s34, v248\n"
"s_nop 0\n"
"s_andn2_b64 s[88:89], s[98:99], vcc\n"
"v_cmp_eq_u32 vcc, s34, v249\n"
"s_nop 0\n"
"s_andn2_b64 vcc, s[8:9], vcc\n"
"s_or_b64 s[88:89], s[88:89], vcc\n"
"s_cbranch_scc0 L_plA_ok\n"
"s_mov_b32 s55, 0x30000\n"
"L_plA_re:\n"
"s_sleep 1\n"
"buffer_load_dwordx2 v[248:249], v251, s[36:39], s91 offen sc1\n"
"s_waitcnt vmcnt(0)\n"
"v_cmp_eq_u32 vcc, s34, v248\n"
"s_nop 0\n"
"s_andn2_b64 s[88:89], s[98:99], vcc\n"
"v_cmp_eq_u32 vcc, s34, v249\n"
"s_nop 0\n"
"s_andn2_b64 vcc, s[8:9], vcc\n"
"s_or_b64 s[88:89], s[88:89], vcc\n"
"s_cbranch_scc0 L_plA_ok\n"
"s_sub_u32 s55, s55, 1\n"
"s_cmp_lg_u32 s55, 0\n"
"s_cbranch_scc1 L_plA_re\n"
"L_plA_ok:\n"
"s_waitcnt lgkmcnt(0)\n"
"v_mfma_f32_16x16x32_bf16 v[32:35], a[0:3], a[160:163], v[32:35]\n"
"ds_read_b64_tr_b16 a[36:37], v241 offset:27648\n"
"ds_read_b64_tr_b16 a[38:39], v241 offset:32256\n"
"v_mfma_f32_16x16x32_bf16 v[36:39], a[0:3], a[164:167], v[36:39]\n"
"ds_read_b64_tr_b16 a[40:41], v242 offset:27648\n"
"v_mfma_f32_16x16x32_bf16 v[44:47], a[4:7], a[164:167], v[44:47]\n"
"s_bfe_u64 s[62:63], s[58:59], 0x50014\n"
"s_mul_i32 s62, s62, 8192\n"
"s_add_u32 s72, s57, s62\n"
"v_mfma_f32_16x16x32_bf16 v[40:43], a[4:7], a[160:163], v[40:43]\n"
"buffer_load_dwordx4 a[144:147], v1, s[44:47], s72 offen offset:0 sc1\n"
"v_mfma_f32_16x16x32_bf16 v[48:51], a[8:11], a[160:163], v[48:51]\n"
"ds_read_b64_tr_b16 a[42:43], v242 offset:32256\n"
"ds_read_b64_tr_b16 a[44:45], v241 offset:27712\n"
"v_mfma_f32_16x16x32_bf16 v[52:55], a[8:11], a[164:167], v[52:55]\n"
"ds_read_b64_tr_b16 a[46:47], v241 offset:32320\n"
"ds_read_b64_tr_b16 a[48:49], v242 offset:27712\n"
"v_mfma_f32_16x16x32_bf16 v[60:63], a[12:15], a[164:167], v[60:63]\n"
"ds_read_b64_tr_b16 a[50:51], v242 offset:32320\n"
"ds_read_b64_tr_b16 a[52:53], v241 offset:27776\n"
"v_mfma_f32_16x16x32_bf16 v[56:59], a[12:15], a[160:163], v[56:59]\n"
"buffer_load_dwordx4 a[148:151], v1, s[44:47], s72 offen offset:1024 sc1\n"
"v_mfma_f32_16x16x32_bf16 v[64:67], a[16:19], a[160:163], v[64:67]\n"
"s_add_u32 s72, s72, 2048\n"
"ds_read_b64_tr_b16 a[54:55], v241 offset:32384\n"
"v_mfma_f32_16x16x32_bf16 v[68:71], a[16:19], a[164:167], v[68:71]\n"
"ds_read_b64_tr_b16 a[56:57], v242 offset:27776\n"
"ds_read_b64_tr_b16 a[58:59], v242 offset:32384\n"
"v_mfma_f32_16x16x32_bf16 v[76:79], a[20:23], a[164:167], v[76:79]\n"
"ds_read_b64_tr_b16 a[60:61], v241 offset:27840\n"
"ds_read_b64_tr_b16 a[62:63], v241 offset:32448\n"
"v_mfma_f32_16x16x32_bf16 v[72:75], a[20:23], a[160:163], v[72:75]\n"
"buffer_load_dwordx4 a[152:155], v1, s[44:47], s72 offen offset:0 sc1\n"
"v_mfma_f32_16x16x32_bf16 v[80:83], a[24:27], a[160:163], v[80:83]\n"
"ds_read_b64_tr_b16 a[64:65], v242 offset:27840\n"
"ds_read_b64_tr_b16 a[66:67], v242 offset:32448\n"
"v_mfma_f32_16x16x32_bf16 v[84:87], a[24:27], a[164:167], v[84:87]\n"
"ds_read_b64_tr_b16 a[68:69], v241 offset:27904\n"
"v_mfma_f32_16x16x32_bf16 v[92:95], a[28:31], a[164:167], v[92:95]\n"
"ds_read_b64_tr_b16 a[70:71], v241 offset:32512\n"
"v_mfma_f32_16x16x32_bf16 v[88:91], a[28:31], a[160:163], v[88:91]\n"
"buffer_load_dwordx4 a[156:159], v1, s[44:47], s72 offen offset:1024 sc1\n"
"v_mfma_f32_16x16x32_bf16 v[96:99], a[32:35], a[160:163], v[96:99]\n"
"s_add_u32 s72, s72, 2048\n"
"v_mfma_f32_16x16x32_bf16 v[100:103], a[32:35], a[164:167], v[100:103]\n"
"s_waitcnt lgkmcnt(0)\n"
"v_mfma_f32_16x16x32_bf16 v[32:35], a[36:39], a[168:171], v[32:35]\n"
"s_nop 0\n"
"s_barrier\n"
"v_mfma_f32_16x16x32_bf16 v[36:39], a[36:39], a[172:175], v[36:39]\n"
"s_add_u32 m0, s0, 18432\n"
"ds_read_b64_tr_b16 a[0:1], v241 offset:36864\n"
"v_mfma_f32_16x16x32_bf16 v[44:47], a[40:43], a[172:175], v[44:47]\n"
"buffer_load_dwordx4 v28, s[40:43], s73 offen offset:0 lds\n"
"v_mfma_f32_16x16x32_bf16 v[40:43], a[40:43], a[168:171], v[40:43]\n"
"ds_read_b64_tr_b16 a[2:3], v241 offset:41472\n"
"ds_read_b64_tr_b16 a[4:5], v242 offset:36864\n"
"v_mfma_f32_16x16x32_bf16 v[48:51], a[44:47], a[168:171], v[48:51]\n"
"ds_read_b64_tr_b16 a[6:7], v242 offset:41472\n"
"ds_read_b64_tr_b16 a[8:9], v241 offset:36928\n"
"v_mfma_f32_16x16x32_bf16 v[52:55], a[44:47], a[172:175], v[52:55]\n"
"buffer_load_dwordx4 v29, s[40:43], s73 offen offset:1024 lds\n"
"v_mfma_f32_16x16x32_bf16 v[60:63], a[48:51], a[172:175], v[60:63]\n"
"ds_read_b64_tr_b16 a[10:11], v241 offset:41536\n"
"ds_read_b64_tr_b16 a[12:13], v242 offset:36928\n"
"v_mfma_f32_16x16x32_bf16 v[56:59], a[48:51], a[168:171], v[56:59]\n"
"ds_read_b64_tr_b16 a[14:15], v242 offset:41536\n"
"ds_read_b64_tr_b16 a[16:17], v241 offset:36992\n"
"v_mfma_f32_16x16x32_bf16 v[64:67], a[52:55], a[168:171], v[64:67]\n"
"ds_read_b64_tr_b16 a[18:19], v241 offset:41600\n"
"ds_read_b64_tr_b16 a[20:21], v242 offset:36992\n"
"v_mfma_f32_16x16x32_bf16 v[68:71], a[52:55], a[172:175], v[68:71]\n"
"buffer_load_dwordx4 v30, s[40:43], s73 offen offset:2048 lds\n"
"v_mfma_f32_16x16x32_bf16 v[76:79], a[56:59], a[172:175], v[76:79]\n"
"ds_read_b64_tr_b16 a[22:23], v242 offset:41600\n"
"ds_read_b64_tr_b16 a[24:25], v241 offset:37056\n"
"v_mfma_f32_16x16x32_bf16 v[72:75], a[56:59], a[168:171], v[72:75]\n"
"ds_read_b64_tr_b16 a[26:27], v241 offset:41664\n"
"ds_read_b64_tr_b16 a[28:29], v242 offset:37056\n"
"v_mfma_f32_16x16x32_bf16 v[80:83], a[60:63], a[168:171], v[80:83]\n"
"ds_read_b64_tr_b16 a[30:31], v242 offset:41664\n"
"v_mfma_f32_16x16x32_bf16 v[84:87], a[60:63], a[172:175], v[84:87]\n"
"buffer_load_dwordx4 v31, s[40:43], s73 offen offset:3072 lds\n"
"v_mfma_f32_16x16x32_bf16 v[92:95], a[64:67], a[172:175], v[92:95]\n"
"ds_read_b64_tr_b16 a[32:33], v241 offset:37120\n"
"ds_read_b64_tr_b16 a[34:35], v241 offset:41728\n"
"v_mfma_f32_16x16x32_bf16 v[88:91], a[64:67], a[168:171], v[88:91]\n"
"v_mfma_f32_16x16x32_bf16 v[96:99], a[68:71], a[168:171], v[96:99]\n"
"s_add_u32 m0, s0, 22528\n"
"v_mfma_f32_16x16x32_bf16 v[100:103], a[68:71], a[172:175], v[100:103]\n"
"s_mov_b64 exec, s[96:97]\n"
"buffer_load_dwordx4 v240, s[40:43], s73 offen offset:0 lds\n"
"s_mov_b64 exec, -1\n"
"s_add_u32 s73, s73, 73728\n"
"s_waitcnt lgkmcnt(0)\n"
"v_mfma_f32_16x16x32_bf16 v[32:35], a[0:3], a[208:211], v[32:35]\n"
"ds_read_b64_tr_b16 a[36:37], v241 offset:46080\n"
"ds_read_b64_tr_b16 a[38:39], v241 offset:50688\n"
"v_mfma_f32_16x16x32_bf16 v[36:39], a[0:3], a[212:215], v[36:39]\n"
"ds_read_b64_tr_b16 a[40:41], v242 offset:46080\n"
"v_mfma_f32_16x16x32_bf16 v[44:47], a[4:7], a[212:215], v[44:47]\n"
"ds_read_b64_tr_b16 a[42:43], v242 offset:50688\n"
"v_mfma_f32_16x16x32_bf16 v[40:43], a[4:7], a[208:211], v[40:43]\n"
"buffer_load_dwordx4 a[160:163], v1, s[44:47], s72 offen offset:0 sc1\n"
"v_mfma_f32_16x16x32_bf16 v[48:51], a[8:11], a[208:211], v[48:51]\n"
"ds_read_b64_tr_b16 a[44:45], v241 offset:46144\n"
"ds_read_b64_tr_b16 a[46:47], v241 offset:50752\n"
"v_mfma_f32_16x16x32_bf16 v[52:55], a[8:11], a[212:215], v[52:55]\n"
"ds_read_b64_tr_b16 a[48:49], v242 offset:46144\n"
"ds_read_b64_tr_b16 a[50:51], v242 offset:50752\n"
"v_mfma_f32_16x16x32_bf16 v[60:63], a[12:15], a[212:215], v[60:63]\n"
"ds_read_b64_tr_b16 a[52:53], v241 offset:46208\n"
"v_mfma_f32_16x16x32_bf16 v[56:59], a[12:15], a[208:211], v[56:59]\n"
"buffer_load_dwordx4 a[164:167], v1, s[44:47], s72 offen offset:1024 sc1\n"
"v_mfma_f32_16x16x32_bf16 v[64:67], a[16:19], a[208:211], v[64:67]\n"
"s_add_u32 s72, s72, 2048\n"
"ds_read_b64_tr_b16 a[54:55], v241 offset:50816\n"
"v_mfma_f32_16x16x32_bf16 v[68:71], a[16:19], a[212:215], v[68:71]\n"
"ds_read_b64_tr_b16 a[56:57], v242 offset:46208\n"
"ds_read_b64_tr_b16 a[58:59], v242 offset:50816\n"
"v_mfma_f32_16x16x32_bf16 v[76:79], a[20:23], a[212:215], v[76:79]\n"
"ds_read_b64_tr_b16 a[60:61], v241 offset:46272\n"
"ds_read_b64_tr_b16 a[62:63], v241 offset:50880\n"
"v_mfma_f32_16x16x32_bf16 v[72:75], a[20:23], a[208:211], v[72:75]\n"
"buffer_load_dwordx4 a[168:171], v1, s[44:47], s72 offen offset:0 sc1\n"
"v_mfma_f32_16x16x32_bf16 v[80:83], a[24:27], a[208:211], v[80:83]\n"
"ds_read_b64_tr_b16 a[64:65], v242 offset:46272\n"
"ds_read_b64_tr_b16 a[66:67], v242 offset:50880\n"
"v_mfma_f32_16x16x32_bf16 v[84:87], a[24:27], a[212:215], v[84:87]\n"
"ds_read_b64_tr_b16 a[68:69], v241 offset:46336\n"
"v_mfma_f32_16x16x32_bf16 v[92:95], a[28:31], a[212:215], v[92:95]\n"
"ds_read_b64_tr_b16 a[70:71], v241 offset:50944\n"
"v_mfma_f32_16x16x32_bf16 v[88:91], a[28:31], a[208:211], v[88:91]\n"
"buffer_load_dwordx4 a[172:175], v1, s[44:47], s72 offen offset:1024 sc1\n"
"v_mfma_f32_16x16x32_bf16 v[96:99], a[32:35], a[208:211], v[96:99]\n"
"s_add_u32 s72, s72, 2048\n"
"v_mfma_f32_16x16x32_bf16 v[100:103], a[32:35], a[212:215], v[100:103]\n"
"s_waitcnt lgkmcnt(0)\n"
"v_mfma_f32_16x16x32_bf16 v[32:35], a[36:39], a[216:219], v[32:35]\n"
"s_waitcnt vmcnt(22)\n"
"s_barrier\n"
"v_mfma_f32_16x16x32_bf16 v[36:39], a[36:39], a[220:223], v[36:39]\n"
"s_add_u32 m0, s0, 36864\n"
"ds_read_b64_tr_b16 a[0:1], v20 offset:0\n"
"v_mfma_f32_16x16x32_bf16 v[44:47], a[40:43], a[220:223], v[44:47]\n"
"buffer_load_dwordx4 v28, s[40:43], s73 offen offset:0 lds\n"
"v_mfma_f32_16x16x32_bf16 v[40:43], a[40:43], a[216:219], v[40:43]\n"
"ds_read_b64_tr_b16 a[2:3], v20 offset:4608\n"
"ds_read_b64_tr_b16 a[4:5], v21 offset:0\n"
"v_mfma_f32_16x16x32_bf16 v[48:51], a[44:47], a[216:219], v[48:51]\n"
"ds_read_b64_tr_b16 a[6:7], v21 offset:4608\n"
"ds_read_b64_tr_b16 a[8:9], v20 offset:64\n"
"v_mfma_f32_16x16x32_bf16 v[52:55], a[44:47], a[220:223], v[52:55]\n"
"buffer_load_dwordx4 v29, s[40:43], s73 offen offset:1024 lds\n"
"v_mfma_f32_16x16x32_bf16 v[60:63], a[48:51], a[220:223], v[60:63]\n"
"ds_read_b64_tr_b16 a[10:11], v20 offset:4672\n"
"ds_read_b64_tr_b16 a[12:13], v21 offset:64\n"
"v_mfma_f32_16x16x32_bf16 v[56:59], a[48:51], a[216:219], v[56:59]\n"
"ds_read_b64_tr_b16 a[14:15], v21 offset:4672\n"
"ds_read_b64_tr_b16 a[16:17], v20 offset:128\n"
"v_mfma_f32_16x16x32_bf16 v[64:67], a[52:55], a[216:219], v[64:67]\n"
"ds_read_b64_tr_b16 a[18:19], v20 offset:4736\n"
"ds_read_b64_tr_b16 a[20:21], v21 offset:128\n"
"v_mfma_f32_16x16x32_bf16 v[68:71], a[52:55], a[220:223], v[68:71]\n"
"buffer_load_dwordx4 v30, s[40:43], s73 offen offset:2048 lds\n"
"v_mfma_f32_16x16x32_bf16 v[76:79], a[56:59], a[220:223], v[76:79]\n"
"ds_read_b64_tr_b16 a[22:23], v21 offset:4736\n"
"ds_read_b64_tr_b16 a[24:25], v20 offset:192\n"
"v_mfma_f32_16x16x32_bf16 v[72:75], a[56:59], a[216:219], v[72:75]\n"
"ds_read_b64_tr_b16 a[26:27], v20 offset:4800\n"
"ds_read_b64_tr_b16 a[28:29], v21 offset:192\n"
"v_mfma_f32_16x16x32_bf16 v[80:83], a[60:63], a[216:219], v[80:83]\n"
"ds_read_b64_tr_b16 a[30:31], v21 offset:4800\n"
"v_mfma_f32_16x16x32_bf16 v[84:87], a[60:63], a[220:223], v[84:87]\n"
"buffer_load_dwordx4 v31, s[40:43], s73 offen offset:3072 lds\n"
"v_mfma_f32_16x16x32_bf16 v[92:95], a[64:67], a[220:223], v[92:95]\n"
"ds_read_b64_tr_b16 a[32:33], v20 offset:256\n"
"ds_read_b64_tr_b16 a[34:35], v20 offset:4864\n"
"v_mfma_f32_16x16x32_bf16 v[88:91], a[64:67], a[216:219], v[88:91]\n"
"v_mfma_f32_16x16x32_bf16 v[96:99], a[68:71], a[216:219], v[96:99]\n"
"s_add_u32 m0, s0, 40960\n"
"v_mfma_f32_16x16x32_bf16 v[100:103], a[68:71], a[220:223], v[100:103]\n"
"s_mov_b64 exec, s[96:97]\n"
"buffer_load_dwordx4 v240, s[40:43], s73 offen offset:0 lds\n"
"s_mov_b64 exec, -1\n"
"s_add_u32 s73, s73, 73728\n"
"s_waitcnt vmcnt(25) lgkmcnt(0)\n"
"v_mfma_f32_16x16x32_bf16 v[32:35], a[0:3], a[224:227], v[32:35]\n"
"ds_read_b64_tr_b16 a[36:37], v20 offset:9216\n"
"ds_read_b64_tr_b16 a[38:39], v20 offset:13824\n"
"v_mfma_f32_16x16x32_bf16 v[36:39], a[0:3], a[228:231], v[36:39]\n"
"ds_read_b64_tr_b16 a[40:41], v21 offset:9216\n"
"v_mfma_f32_16x16x32_bf16 v[44:47], a[4:7], a[228:231], v[44:47]\n"
"ds_read_b64_tr_b16 a[42:43], v21 offset:13824\n"
"v_mfma_f32_16x16x32_bf16 v[40:43], a[4:7], a[224:227], v[40:43]\n"
"buffer_load_dwordx4 a[208:211], v1, s[44:47], s72 offen offset:0 sc1\n"
"v_mfma_f32_16x16x32_bf16 v[48:51], a[8:11], a[224:227], v[48:51]\n"
"ds_read_b64_tr_b16 a[44:45], v20 offset:9280\n"
"ds_read_b64_tr_b16 a[46:47], v20 offset:13888\n"
"v_mfma_f32_16x16x32_bf16 v[52:55], a[8:11], a[228:231], v[52:55]\n"
"ds_read_b64_tr_b16 a[48:49], v21 offset:9280\n"
"ds_read_b64_tr_b16 a[50:51], v21 offset:13888\n"
"v_mfma_f32_16x16x32_bf16 v[60:63], a[12:15], a[228:231], v[60:63]\n"
"ds_read_b64_tr_b16 a[52:53], v20 offset:9344\n"
"v_mfma_f32_16x16x32_bf16 v[56:59], a[12:15], a[224:227], v[56:59]\n"
"buffer_load_dwordx4 a[212:215], v1, s[44:47], s72 offen offset:1024 sc1\n"
"v_mfma_f32_16x16x32_bf16 v[64:67], a[16:19], a[224:227], v[64:67]\n"
"s_add_u32 s72, s72, 2048\n"
"ds_read_b64_tr_b16 a[54:55], v20 offset:13952\n"
"v_mfma_f32_16x16x32_bf16 v[68:71], a[16:19], a[228:231], v[68:71]\n"
"ds_read_b64_tr_b16 a[56:57], v21 offset:9344\n"
"ds_read_b64_tr_b16 a[58:59], v21 offset:13952\n"
"v_mfma_f32_16x16x32_bf16 v[76:79], a[20:23], a[228:231], v[76:79]\n"
"ds_read_b64_tr_b16 a[60:61], v20 offset:9408\n"
"ds_read_b64_tr_b16 a[62:63], v20 offset:14016\n"
"v_mfma_f32_16x16x32_bf16 v[72:75], a[20:23], a[224:227], v[72:75]\n"
"buffer_load_dwordx4 a[216:219], v1, s[44:47], s72 offen offset:0 sc1\n"
"v_mfma_f32_16x16x32_bf16 v[80:83], a[24:27], a[224:227], v[80:83]\n"
"ds_read_b64_tr_b16 a[64:65], v21 offset:9408\n"
"ds_read_b64_tr_b16 a[66:67], v21 offset:14016\n"
"v_mfma_f32_16x16x32_bf16 v[84:87], a[24:27], a[228:231], v[84:87]\n"
"ds_read_b64_tr_b16 a[68:69], v20 offset:9472\n"
"v_mfma_f32_16x16x32_bf16 v[92:95], a[28:31], a[228:231], v[92:95]\n"
"ds_read_b64_tr_b16 a[70:71], v20 offset:14080\n"
"v_mfma_f32_16x16x32_bf16 v[88:91], a[28:31], a[224:227], v[88:91]\n"
"buffer_load_dwordx4 a[220:223], v1, s[44:47], s72 offen offset:1024 sc1\n"
"v_mfma_f32_16x16x32_bf16 v[96:99], a[32:35], a[224:227], v[96:99]\n"
"s_add_u32 s72, s72, 2048\n"
"v_mfma_f32_16x16x32_bf16 v[100:103], a[32:35], a[228:231], v[100:103]\n"
"s_waitcnt vmcnt(27) lgkmcnt(0)\n"
"v_mfma_f32_16x16x32_bf16 v[32:35], a[36:39], a[232:235], v[32:35]\n"
"s_waitcnt vmcnt(22)\n"
"s_barrier\n"
"v_mfma_f32_16x16x32_bf16 v[36:39], a[36:39], a[236:239], v[36:39]\n"
"s_add_u32 m0, s1, 0\n"
"ds_read_b64_tr_b16 a[0:1], v241 offset:0\n"
"v_mfma_f32_16x16x32_bf16 v[44:47], a[40:43], a[236:239], v[44:47]\n"
"buffer_load_dwordx4 v28, s[40:43], s73 offen offset:0 lds\n"
"v_mfma_f32_16x16x32_bf16 v[40:43], a[40:43], a[232:235], v[40:43]\n"
"ds_read_b64_tr_b16 a[2:3], v241 offset:4608\n"
"ds_read_b64_tr_b16 a[4:5], v242 offset:0\n"
"v_mfma_f32_16x16x32_bf16 v[48:51], a[44:47], a[232:235], v[48:51]\n"
"ds_read_b64_tr_b16 a[6:7], v242 offset:4608\n"
"ds_read_b64_tr_b16 a[8:9], v241 offset:64\n"
"v_mfma_f32_16x16x32_bf16 v[52:55], a[44:47], a[236:239], v[52:55]\n"
"buffer_load_dwordx4 v29, s[40:43], s73 offen offset:1024 lds\n"
"v_mfma_f32_16x16x32_bf16 v[60:63], a[48:51], a[236:239], v[60:63]\n"
"ds_read_b64_tr_b16 a[10:11], v241 offset:4672\n"
"ds_read_b64_tr_b16 a[12:13], v242 offset:64\n"
"v_mfma_f32_16x16x32_bf16 v[56:59], a[48:51], a[232:235], v[56:59]\n"
"ds_read_b64_tr_b16 a[14:15], v242 offset:4672\n"
"ds_read_b64_tr_b16 a[16:17], v241 offset:128\n"
"v_mfma_f32_16x16x32_bf16 v[64:67], a[52:55], a[232:235], v[64:67]\n"
"ds_read_b64_tr_b16 a[18:19], v241 offset:4736\n"
"ds_read_b64_tr_b16 a[20:21], v242 offset:128\n"
"v_mfma_f32_16x16x32_bf16 v[68:71], a[52:55], a[236:239], v[68:71]\n"
"buffer_load_dwordx4 v30, s[40:43], s73 offen offset:2048 lds\n"
"v_mfma_f32_16x16x32_bf16 v[76:79], a[56:59], a[236:239], v[76:79]\n"
"ds_read_b64_tr_b16 a[22:23], v242 offset:4736\n"
"ds_read_b64_tr_b16 a[24:25], v241 offset:192\n"
"v_mfma_f32_16x16x32_bf16 v[72:75], a[56:59], a[232:235], v[72:75]\n"
"ds_read_b64_tr_b16 a[26:27], v241 offset:4800\n"
"ds_read_b64_tr_b16 a[28:29], v242 offset:192\n"
"v_mfma_f32_16x16x32_bf16 v[80:83], a[60:63], a[232:235], v[80:83]\n"
"ds_read_b64_tr_b16 a[30:31], v242 offset:4800\n"
"v_mfma_f32_16x16x32_bf16 v[84:87], a[60:63], a[236:239], v[84:87]\n"
"buffer_load_dwordx4 v31, s[40:43], s73 offen offset:3072 lds\n"
"v_mfma_f32_16x16x32_bf16 v[92:95], a[64:67], a[236:239], v[92:95]\n"
"ds_read_b64_tr_b16 a[32:33], v241 offset:256\n"
"ds_read_b64_tr_b16 a[34:35], v241 offset:4864\n"
"v_mfma_f32_16x16x32_bf16 v[88:91], a[64:67], a[232:235], v[88:91]\n"
"v_mfma_f32_16x16x32_bf16 v[96:99], a[68:71], a[232:235], v[96:99]\n"
"s_add_u32 m0, s1, 4096\n"
"v_mfma_f32_16x16x32_bf16 v[100:103], a[68:71], a[236:239], v[100:103]\n"
"s_mov_b64 exec, s[96:97]\n"
"buffer_load_dwordx4 v240, s[40:43], s73 offen offset:0 lds\n"
"s_mov_b64 exec, -1\n"
"s_add_u32 s73, s73, 73728\n"
"s_waitcnt vmcnt(25) lgkmcnt(0)\n"
"v_mfma_f32_16x16x32_bf16 v[32:35], a[0:3], a[144:147], v[32:35]\n"
"ds_read_b64_tr_b16 a[36:37], v241 offset:9216\n"
"ds_read_b64_tr_b16 a[38:39], v241 offset:13824\n"
"v_mfma_f32_16x16x32_bf16 v[36:39], a[0:3], a[148:151], v[36:39]\n"
"ds_read_b64_tr_b16 a[40:41], v242 offset:9216\n"
"v_mfma_f32_16x16x32_bf16 v[44:47], a[4:7], a[148:151], v[44:47]\n"
"ds_read_b64_tr_b16 a[42:43], v242 offset:13824\n"
"v_mfma_f32_16x16x32_bf16 v[40:43], a[4:7], a[144:147], v[40:43]\n"
"buffer_load_dwordx4 a[224:227], v1, s[44:47], s72 offen offset:0 sc1\n"
"v_mfma_f32_16x16x32_bf16 v[48:51], a[8:11], a[144:147], v[48:51]\n"
"ds_read_b64_tr_b16 a[44:45], v241 offset:9280\n"
"ds_read_b64_tr_b16 a[46:47], v241 offset:13888\n"
"v_mfma_f32_16x16x32_bf16 v[52:55], a[8:11], a[148:151], v[52:55]\n"
"ds_read_b64_tr_b16 a[48:49], v242 offset:9280\n"
"ds_read_b64_tr_b16 a[50:51], v242 offset:13888\n"
"v_mfma_f32_16x16x32_bf16 v[60:63], a[12:15], a[148:151], v[60:63]\n"
"ds_read_b64_tr_b16 a[52:53], v241 offset:9344\n"
"v_mfma_f32_16x16x32_bf16 v[56:59], a[12:15], a[144:147], v[56:59]\n"
"buffer_load_dwordx4 a[228:231], v1, s[44:47], s72 offen offset:1024 sc1\n"
"v_mfma_f32_16x16x32_bf16 v[64:67], a[16:19], a[144:147], v[64:67]\n"
"s_add_u32 s72, s72, 2048\n"
"ds_read_b64_tr_b16 a[54:55], v241 offset:13952\n"
"v_mfma_f32_16x16x32_bf16 v[68:71], a[16:19], a[148:151], v[68:71]\n"
"ds_read_b64_tr_b16 a[56:57], v242 offset:9344\n"
"ds_read_b64_tr_b16 a[58:59], v242 offset:13952\n"
"v_mfma_f32_16x16x32_bf16 v[76:79], a[20:23], a[148:151], v[76:79]\n"
"ds_read_b64_tr_b16 a[60:61], v241 offset:9408\n"
"ds_read_b64_tr_b16 a[62:63], v241 offset:14016\n"
"v_mfma_f32_16x16x32_bf16 v[72:75], a[20:23], a[144:147], v[72:75]\n"
"buffer_load_dwordx4 a[232:235], v1, s[44:47], s72 offen offset:0 sc1\n"
"v_mfma_f32_16x16x32_bf16 v[80:83], a[24:27], a[144:147], v[80:83]\n"
"ds_read_b64_tr_b16 a[64:65], v242 offset:9408\n"
"ds_read_b64_tr_b16 a[66:67], v242 offset:14016\n"
"v_mfma_f32_16x16x32_bf16 v[84:87], a[24:27], a[148:151], v[84:87]\n"
"ds_read_b64_tr_b16 a[68:69], v241 offset:9472\n"
"v_mfma_f32_16x16x32_bf16 v[92:95], a[28:31], a[148:151], v[92:95]\n"
"ds_read_b64_tr_b16 a[70:71], v241 offset:14080\n"
"v_mfma_f32_16x16x32_bf16 v[88:91], a[28:31], a[144:147], v[88:91]\n"
"buffer_load_dwordx4 a[236:239], v1, s[44:47], s72 offen offset:1024 sc1\n"
"v_mfma_f32_16x16x32_bf16 v[96:99], a[32:35], a[144:147], v[96:99]\n"
"s_add_u32 s72, s72, 2048\n"
"v_mfma_f32_16x16x32_bf16 v[100:103], a[32:35], a[148:151], v[100:103]\n"
"s_waitcnt vmcnt(27) lgkmcnt(0)\n"
"v_mfma_f32_16x16x32_bf16 v[32:35], a[36:39], a[152:155], v[32:35]\n"
"s_waitcnt vmcnt(22)\n"
"s_barrier\n"
"v_mfma_f32_16x16x32_bf16 v[36:39], a[36:39], a[156:159], v[36:39]\n"
"s_bfe_u64 s[60:61], s[58:59], 0x5001e\n"
"s_mul_i32 s60, s60, 147456\n"
"s_add_u32 s73, s56, s60\n"
"s_add_u32 m0, s0, 0\n"
"v_mfma_f32_16x16x32_bf16 v[44:47], a[40:43], a[156:159], v[44:47]\n"
"buffer_load_dwordx4 v28, s[40:43], s73 offen offset:0 lds\n"
"v_mfma_f32_16x16x32_bf16 v[40:43], a[40:43], a[152:155], v[40:43]\n"
"ds_read_b64_tr_b16 a[0:1], v241 offset:18432\n"
"ds_read_b64_tr_b16 a[2:3], v241 offset:23040\n"
"v_mfma_f32_16x16x32_bf16 v[48:51], a[44:47], a[152:155], v[48:51]\n"
"ds_read_b64_tr_b16 a[4:5], v242 offset:18432\n"
"ds_read_b64_tr_b16 a[6:7], v242 offset:23040\n"
"v_mfma_f32_16x16x32_bf16 v[52:55], a[44:47], a[156:159], v[52:55]\n"
"buffer_load_dwordx4 v29, s[40:43], s73 offen offset:1024 lds\n"
"v_mfma_f32_16x16x32_bf16 v[60:63], a[48:51], a[156:159], v[60:63]\n"
"ds_read_b64_tr_b16 a[8:9], v241 offset:18496\n"
"ds_read_b64_tr_b16 a[10:11], v241 offset:23104\n"
"v_mfma_f32_16x16x32_bf16 v[56:59], a[48:51], a[152:155], v[56:59]\n"
"ds_read_b64_tr_b16 a[12:13], v242 offset:18496\n"
"ds_read_b64_tr_b16 a[14:15], v242 offset:23104\n"
"v_mfma_f32_16x16x32_bf16 v[64:67], a[52:55], a[152:155], v[64:67]\n"
"ds_read_b64_tr_b16 a[16:17], v241 offset:18560\n"
"ds_read_b64_tr_b16 a[18:19], v241 offset:23168\n"
"v_mfma_f32_16x16x32_bf16 v[68:71], a[52:55], a[156:159], v[68:71]\n"
"buffer_load_dwordx4 v30, s[40:43], s73 offen offset:2048 lds\n"
"v_mfma_f32_16x16x32_bf16 v[76:79], a[56:59], a[156:159], v[76:79]\n"
"ds_read_b64_tr_b16 a[20:21], v242 offset:18560\n"
"ds_read_b64_tr_b16 a[22:23], v242 offset:23168\n"
"v_mfma_f32_16x16x32_bf16 v[72:75], a[56:59], a[152:155], v[72:75]\n"
"ds_read_b64_tr_b16 a[24:25], v241 offset:18624\n"
"ds_read_b64_tr_b16 a[26:27], v241 offset:23232\n"
"v_mfma_f32_16x16x32_bf16 v[80:83], a[60:63], a[152:155], v[80:83]\n"
"ds_read_b64_tr_b16 a[28:29], v242 offset:18624\n"
"ds_read_b64_tr_b16 a[30:31], v242 offset:23232\n"
"v_mfma_f32_16x16x32_bf16 v[84:87], a[60:63], a[156:159], v[84:87]\n"
"buffer_load_dwordx4 v31, s[40:43], s73 offen offset:3072 lds\n"
"v_mfma_f32_16x16x32_bf16 v[92:95], a[64:67], a[156:159], v[92:95]\n"
"ds_read_b64_tr_b16 a[32:33], v241 offset:18688\n"
"ds_read_b64_tr_b16 a[34:35], v241 offset:23296\n"
"v_mfma_f32_16x16x32_bf16 v[88:91], a[64:67], a[152:155], v[88:91]\n"
"v_mfma_f32_16x16x32_bf16 v[96:99], a[68:71], a[152:155], v[96:99]\n"
"s_add_u32 m0, s0, 4096\n"
"v_mfma_f32_16x16x32_bf16 v[100:103], a[68:71], a[156:159], v[100:103]\n"
"s_mov_b64 exec, s[96:97]\n"
"buffer_load_dwordx4 v240, s[40:43], s73 offen offset:0 lds\n"
"s_mov_b64 exec, -1\n"
"s_add_u32 s73, s73, 73728\n"
"s_waitcnt vmcnt(25) lgkmcnt(0)\n"
"v_mfma_f32_16x16x32_bf16 v[32:35], a[0:3], a[160:163], v[32:35]\n"
"ds_read_b64_tr_b16 a[36:37], v241 offset:27648\n"
"ds_read_b64_tr_b16 a[38:39], v241 offset:32256\n"
"v_mfma_f32_16x16x32_bf16 v[36:39], a[0:3], a[164:167], v[36:39]\n"
"ds_read_b64_tr_b16 a[40:41], v242 offset:27648\n"
"v_mfma_f32_16x16x32_bf16 v[44:47], a[4:7], a[164:167], v[44:47]\n"
"s_bfe_u64 s[62:63], s[58:59], 0x5001e\n"
"s_mul_i32 s62, s62, 8192\n"
"s_add_u32 s72, s57, s62\n"
"v_mfma_f32_16x16x32_bf16 v[40:43], a[4:7], a[160:163], v[40:43]\n"
"buffer_load_dwordx4 a[144:147], v1, s[44:47], s72 offen offset:0 sc1\n"
"v_mfma_f32_16x16x32_bf16 v[48:51], a[8:11], a[160:163], v[48:51]\n"
"ds_read_b64_tr_b16 a[42:43], v242 offset:32256\n"
"ds_read_b64_tr_b16 a[44:45], v241 offset:27712\n"
"v_mfma_f32_16x16x32_bf16 v[52:55], a[8:11], a[164:167], v[52:55]\n"
"ds_read_b64_tr_b16 a[46:47], v241 offset:32320\n"
"ds_read_b64_tr_b16 a[48:49], v242 offset:27712\n"
"v_mfma_f32_16x16x32_bf16 v[60:63], a[12:15], a[164:167], v[60:63]\n"
"ds_read_b64_tr_b16 a[50:51], v242 offset:32320\n"
"ds_read_b64_tr_b16 a[52:53], v241 offset:27776\n"
"v_mfma_f32_16x16x32_bf16 v[56:59], a[12:15], a[160:163], v[56:59]\n"
"buffer_load_dwordx4 a[148:151], v1, s[44:47], s72 offen offset:1024 sc1\n"
"v_mfma_f32_16x16x32_bf16 v[64:67], a[16:19], a[160:163], v[64:67]\n"
"s_add_u32 s72, s72, 2048\n"
"ds_read_b64_tr_b16 a[54:55], v241 offset:32384\n"
"v_mfma_f32_16x16x32_bf16 v[68:71], a[16:19], a[164:167], v[68:71]\n"
"ds_read_b64_tr_b16 a[56:57], v242 offset:27776\n"
"ds_read_b64_tr_b16 a[58:59], v242 offset:32384\n"
"v_mfma_f32_16x16x32_bf16 v[76:79], a[20:23], a[164:167], v[76:79]\n"
"ds_read_b64_tr_b16 a[60:61], v241 offset:27840\n"
"ds_read_b64_tr_b16 a[62:63], v241 offset:32448\n"
"v_mfma_f32_16x16x32_bf16 v[72:75], a[20:23], a[160:163], v[72:75]\n"
"buffer_load_dwordx4 a[152:155], v1, s[44:47], s72 offen offset:0 sc1\n"
"v_mfma_f32_16x16x32_bf16 v[80:83], a[24:27], a[160:163], v[80:83]\n"
"ds_read_b64_tr_b16 a[64:65], v242 offset:27840\n"
"ds_read_b64_tr_b16 a[66:67], v242 offset:32448\n"
"v_mfma_f32_16x16x32_bf16 v[84:87], a[24:27], a[164:167], v[84:87]\n"
"ds_read_b64_tr_b16 a[68:69], v241 offset:27904\n"
"v_mfma_f32_16x16x32_bf16 v[92:95], a[28:31], a[164:167], v[92:95]\n"
"ds_read_b64_tr_b16 a[70:71], v241 offset:32512\n"
"v_mfma_f32_16x16x32_bf16 v[88:91], a[28:31], a[160:163], v[88:91]\n"
"buffer_load_dwordx4 a[156:159], v1, s[44:47], s72 offen offset:1024 sc1\n"
"v_mfma_f32_16x16x32_bf16 v[96:99], a[32:35], a[160:163], v[96:99]\n"
"s_add_u32 s72, s72, 2048\n"
"v_mfma_f32_16x16x32_bf16 v[100:103], a[32:35], a[164:167], v[100:103]\n"
"s_waitcnt vmcnt(27) lgkmcnt(0)\n"
"v_mfma_f32_16x16x32_bf16 v[32:35], a[36:39], a[168:171], v[32:35]\n"
"s_waitcnt vmcnt(22)\n"
"s_barrier\n"
"v_mfma_f32_16x16x32_bf16 v[36:39], a[36:39], a[172:175], v[36:39]\n"
"s_add_u32 m0, s0, 18432\n"
"s_lshr_b32 s77, s70, 1\n"
"v_mfma_f32_16x16x32_bf16 v[44:47], a[40:43], a[172:175], v[44:47]\n"
"buffer_load_dwordx4 v28, s[40:43], s73 offen offset:0 lds\n"
"v_mfma_f32_16x16x32_bf16 v[40:43], a[40:43], a[168:171], v[40:43]\n"
"ds_read_b64_tr_b16 a[0:1], v241 offset:36864\n"
"s_lshl2_add_u32 s77, s77, s71\n"
"v_mfma_f32_16x16x32_bf16 v[48:51], a[44:47], a[168:171], v[48:51]\n"
"ds_read_b64_tr_b16 a[2:3], v241 offset:41472\n"
"s_and_b32 s78, s70, 1\n"
"v_mfma_f32_16x16x32_bf16 v[52:55], a[44:47], a[172:175], v[52:55]\n"
"buffer_load_dwordx4 v29, s[40:43], s73 offen offset:1024 lds\n"
"v_mfma_f32_16x16x32_bf16 v[60:63], a[48:51], a[172:175], v[60:63]\n"
"ds_read_b64_tr_b16 a[4:5], v242 offset:36864\n"
"s_lshl1_add_u32 s77, s77, s78\n"
"v_mfma_f32_16x16x32_bf16 v[56:59], a[48:51], a[168:171], v[56:59]\n"
"ds_read_b64_tr_b16 a[6:7], v242 offset:41472\n"
"s_lshr_b32 s78, s11, 1\n"
"v_mfma_f32_16x16x32_bf16 v[64:67], a[52:55], a[168:171], v[64:67]\n"
"ds_read_b64_tr_b16 a[8:9], v241 offset:36928\n"
"ds_read_b64_tr_b16 a[10:11], v241 offset:41536\n"
"v_mfma_f32_16x16x32_bf16 v[68:71], a[52:55], a[172:175], v[68:71]\n"
"buffer_load_dwordx4 v30, s[40:43], s73 offen offset:2048 lds\n"
"v_mfma_f32_16x16x32_bf16 v[76:79], a[56:59], a[172:175], v[76:79]\n"
"s_lshl1_add_u32 s77, s77, s78\n"
"ds_read_b64_tr_b16 a[12:13], v242 offset:36928\n"
"v_mfma_f32_16x16x32_bf16 v[72:75], a[56:59], a[168:171], v[72:75]\n"
"s_lshl_b32 s83, s77, 4\n"
"ds_read_b64_tr_b16 a[14:15], v242 offset:41536\n"
"v_mfma_f32_16x16x32_bf16 v[80:83], a[60:63], a[168:171], v[80:83]\n"
"s_add_u32 s83, s83, 0x2ae2000\n"
"ds_read_b64_tr_b16 a[16:17], v241 offset:36992\n"
"v_mfma_f32_16x16x32_bf16 v[84:87], a[60:63], a[172:175], v[84:87]\n"
"buffer_load_dwordx4 v31, s[40:43], s73 offen offset:3072 lds\n"
"v_mfma_f32_16x16x32_bf16 v[92:95], a[64:67], a[172:175], v[92:95]\n"
"ds_read_b64_tr_b16 a[18:19], v241 offset:41600\n"
"s_mov_b64 s[98:99], 15\n"
"ds_read_b64_tr_b16 a[20:21], v242 offset:36992\n"
"ds_read_b64_tr_b16 a[22:23], v242 offset:41600\n"
"ds_read_b64_tr_b16 a[24:25], v241 offset:37056\n"
"ds_read_b64_tr_b16 a[26:27], v241 offset:41664\n"
"ds_read_b64_tr_b16 a[28:29], v242 offset:37056\n"
"ds_read_b64_tr_b16 a[30:31], v242 offset:41664\n"
"ds_read_b64_tr_b16 a[32:33], v241 offset:37120\n"
"ds_read_b64_tr_b16 a[34:35], v241 offset:41728\n"
"v_mfma_f32_16x16x32_bf16 v[88:91], a[64:67], a[168:171], v[88:91]\n"
"s_mov_b64 s[8:9], 15\n"
"s_lshl2_add_u32 s78, s70, s71\n"
"v_mfma_f32_16x16x32_bf16 v[96:99], a[68:71], a[168:171], v[96:99]\n"
"s_add_u32 m0, s0, 22528\n"
"s_lshl2_add_u32 s78, s78, s11\n"
"s_lshl_b32 s78, s78, 4\n"
"s_add_u32 s78, s78, 0x2ae3ff0\n"
"v_add_u32 v6, s83, v251\n"
"v_add_u32 v7, s78, v251\n"
"v_mfma_f32_16x16x32_bf16 v[100:103], a[68:71], a[172:175], v[100:103]\n"
"s_mov_b64 exec, s[96:97]\n"
"buffer_load_dwordx4 v240, s[40:43], s73 offen offset:0 lds\n"
"s_mov_b64 exec, -1\n"
"s_add_u32 s73, s73, 73728\n"
"v_cmp_gt_u32 vcc, 2, v0\n"
"s_nop 1\n"
"v_cndmask_b32 v6, v7, v6, vcc\n"
"s_mov_b32 s83, 0\n"
"s_waitcnt vmcnt(25) lgkmcnt(0)\n"
"v_mfma_f32_16x16x32_bf16 v[32:35], a[0:3], a[208:211], v[32:35]\n"
"ds_read_b64_tr_b16 a[36:37], v241 offset:46080\n"
"ds_read_b64_tr_b16 a[38:39], v241 offset:50688\n"
"v_mfma_f32_16x16x32_bf16 v[36:39], a[0:3], a[212:215], v[36:39]\n"
"buffer_load_dwordx2 v[248:249], v6, s[36:39], s83 offen sc1\n"
"v_mfma_f32_16x16x32_bf16 v[44:47], a[4:7], a[212:215], v[44:47]\n"
"ds_read_b64_tr_b16 a[40:41], v242 offset:46080\n"
"ds_read_b64_tr_b16 a[42:43], v242 offset:50688\n"
"v_mfma_f32_16x16x32_bf16 v[40:43], a[4:7], a[208:211], v[40:43]\n"
"buffer_load_dwordx4 a[160:163], v1, s[44:47], s72 offen offset:0 sc1\n"
"v_mfma_f32_16x16x32_bf16 v[48:51], a[8:11], a[208:211], v[48:51]\n"
"s_mul_i32 s77, s77, 18432\n"
"ds_read_b64_tr_b16 a[44:45], v241 offset:46144\n"
"v_mfma_f32_16x16x32_bf16 v[52:55], a[8:11], a[212:215], v[52:55]\n"
"ds_read_b64_tr_b16 a[46:47], v241 offset:50752\n"
"s_and_b32 s78, s11, 1\n"
"v_mfma_f32_16x16x32_bf16 v[60:63], a[12:15], a[212:215], v[60:63]\n"
"ds_read_b64_tr_b16 a[48:49], v242 offset:46144\n"
"s_and_b32 s84, s69, 1\n"
"v_mfma_f32_16x16x32_bf16 v[56:59], a[12:15], a[208:211], v[56:59]\n"
"buffer_load_dwordx4 a[164:167], v1, s[44:47], s72 offen offset:1024 sc1\n"
"v_mfma_f32_16x16x32_bf16 v[64:67], a[16:19], a[208:211], v[64:67]\n"
"s_add_u32 s72, s72, 2048\n"
"ds_read_b64_tr_b16 a[50:51], v242 offset:50752\n"
"v_mfma_f32_16x16x32_bf16 v[68:71], a[16:19], a[212:215], v[68:71]\n"
"ds_read_b64_tr_b16 a[52:53], v241 offset:46208\n"
"s_lshl1_add_u32 s78, s78, s84\n"
"v_mfma_f32_16x16x32_bf16 v[76:79], a[20:23], a[212:215], v[76:79]\n"
"ds_read_b64_tr_b16 a[54:55], v241 offset:50816\n"
"s_mul_i32 s78, s78, 4608\n"
"v_mfma_f32_16x16x32_bf16 v[72:75], a[20:23], a[208:211], v[72:75]\n"
"buffer_load_dwordx4 a[168:171], v1, s[44:47], s72 offen offset:0 sc1\n"
"v_mfma_f32_16x16x32_bf16 v[80:83], a[24:27], a[208:211], v[80:83]\n"
"ds_read_b64_tr_b16 a[56:57], v242 offset:46208\n"
"ds_read_b64_tr_b16 a[58:59], v242 offset:50816\n"
"v_mfma_f32_16x16x32_bf16 v[84:87], a[24:27], a[212:215], v[84:87]\n"
"s_add_u32 s77, s77, s78\n"
"ds_read_b64_tr_b16 a[60:61], v241 offset:46272\n"
"v_mfma_f32_16x16x32_bf16 v[92:95], a[28:31], a[212:215], v[92:95]\n"
"s_lshr_b32 s78, s69, 1\n"
"ds_read_b64_tr_b16 a[62:63], v241 offset:50880\n"
"ds_read_b64_tr_b16 a[64:65], v242 offset:46272\n"
"ds_read_b64_tr_b16 a[66:67], v242 offset:50880\n"
"ds_read_b64_tr_b16 a[68:69], v241 offset:46336\n"
"ds_read_b64_tr_b16 a[70:71], v241 offset:50944\n"
"v_mfma_f32_16x16x32_bf16 v[88:91], a[28:31], a[208:211], v[88:91]\n"
"buffer_load_dwordx4 a[172:175], v1, s[44:47], s72 offen offset:1024 sc1\n"
"v_mfma_f32_16x16x32_bf16 v[96:99], a[32:35], a[208:211], v[96:99]\n"
"s_add_u32 s72, s72, 2048\n"
"s_mul_i32 s78, s78, 2560\n"
"v_mfma_f32_16x16x32_bf16 v[100:103], a[32:35], a[212:215], v[100:103]\n"
"s_add_u32 s77, s77, s78\n"
"s_add_u32 s82, s77, 0x19d8000\n"
"s_lshl2_add_u32 s77, s70, s11\n"
"s_lshl1_add_u32 s77, s77, s84\n"
"s_waitcnt vmcnt(28) lgkmcnt(0)\n"
"v_mfma_f32_16x16x32_bf16 v[32:35], a[36:39], a[216:219], v[32:35]\n"
"s_waitcnt vmcnt(23)\n"
"s_barrier\n"
"v_mfma_f32_16x16x32_bf16 v[36:39], a[36:39], a[220:223], v[36:39]\n"
"s_add_u32 m0, s0, 36864\n"
"s_lshl_b32 s74, s77, 5\n"
"v_mfma_f32_16x16x32_bf16 v[44:47], a[40:43], a[220:223], v[44:47]\n"
"buffer_load_dwordx4 v28, s[40:43], s73 offen offset:0 lds\n"
"v_mfma_f32_16x16x32_bf16 v[40:43], a[40:43], a[216:219], v[40:43]\n"
"ds_read_b64_tr_b16 a[0:1], v20 offset:0\n"
"ds_read_b64_tr_b16 a[2:3], v20 offset:4608\n"
"v_mfma_f32_16x16x32_bf16 v[48:51], a[44:47], a[216:219], v[48:51]\n"
"s_mul_i32 s84, s77, 18432\n"
"ds_read_b64_tr_b16 a[4:5], v21 offset:0\n"
"v_mfma_f32_16x16x32_bf16 v[52:55], a[44:47], a[220:223], v[52:55]\n"
"buffer_load_dwordx4 v29, s[40:43], s73 offen offset:1024 lds\n"
"v_mfma_f32_16x16x32_bf16 v[60:63], a[48:51], a[220:223], v[60:63]\n"
"ds_read_b64_tr_b16 a[6:7], v21 offset:4608\n"
"s_mul_i32 s77, s71, 288\n"
"v_mfma_f32_16x16x32_bf16 v[56:59], a[48:51], a[216:219], v[56:59]\n"
"ds_read_b64_tr_b16 a[8:9], v20 offset:64\n"
"ds_read_b64_tr_b16 a[10:11], v20 offset:4672\n"
"v_mfma_f32_16x16x32_bf16 v[64:67], a[52:55], a[216:219], v[64:67]\n"
"s_add_u32 s84, s84, s77\n"
"ds_read_b64_tr_b16 a[12:13], v21 offset:64\n"
"v_mfma_f32_16x16x32_bf16 v[68:71], a[52:55], a[220:223], v[68:71]\n"
"buffer_load_dwordx4 v30, s[40:43], s73 offen offset:2048 lds\n"
"v_mfma_f32_16x16x32_bf16 v[76:79], a[56:59], a[220:223], v[76:79]\n"
"ds_read_b64_tr_b16 a[14:15], v21 offset:4672\n"
"s_lshr_b32 s77, s69, 1\n"
"v_mfma_f32_16x16x32_bf16 v[72:75], a[56:59], a[216:219], v[72:75]\n"
"ds_read_b64_tr_b16 a[16:17], v20 offset:128\n"
"ds_read_b64_tr_b16 a[18:19], v20 offset:4736\n"
"v_mfma_f32_16x16x32_bf16 v[80:83], a[60:63], a[216:219], v[80:83]\n"
"ds_read_b64_tr_b16 a[20:21], v21 offset:128\n"
"s_mul_i32 s77, s77, 160\n"
"v_mfma_f32_16x16x32_bf16 v[84:87], a[60:63], a[220:223], v[84:87]\n"
"buffer_load_dwordx4 v31, s[40:43], s73 offen offset:3072 lds\n"
"v_mfma_f32_16x16x32_bf16 v[92:95], a[64:67], a[220:223], v[92:95]\n"
"ds_read_b64_tr_b16 a[22:23], v21 offset:4736\n"
"ds_read_b64_tr_b16 a[24:25], v20 offset:192\n"
"ds_read_b64_tr_b16 a[26:27], v20 offset:4800\n"
"ds_read_b64_tr_b16 a[28:29], v21 offset:192\n"
"ds_read_b64_tr_b16 a[30:31], v21 offset:4800\n"
"ds_read_b64_tr_b16 a[32:33], v20 offset:256\n"
"ds_read_b64_tr_b16 a[34:35], v20 offset:4864\n"
"v_mfma_f32_16x16x32_bf16 v[88:91], a[64:67], a[216:219], v[88:91]\n"
"s_add_u32 s84, s84, s77\n"
"v_mul_u32_u24 v16, 1152, v3\n"
"v_mfma_f32_16x16x32_bf16 v[96:99], a[68:71], a[216:219], v[96:99]\n"
"s_add_u32 m0, s0, 40960\n"
"v_lshl_add_u32 v16, v2, 3, v16\n"
"v_lshlrev_b32 v17, 1, v3\n"
"v_mfma_f32_16x16x32_bf16 v[100:103], a[68:71], a[220:223], v[100:103]\n"
"s_mov_b64 exec, s[96:97]\n"
"buffer_load_dwordx4 v240, s[40:43], s73 offen offset:0 lds\n"
"s_mov_b64 exec, -1\n"
"s_add_u32 s73, s73, 73728\n"
"s_waitcnt vmcnt(26) lgkmcnt(0)\n"
"v_mfma_f32_16x16x32_bf16 v[32:35], a[0:3], a[224:227], v[32:35]\n"
"ds_read_b64_tr_b16 a[36:37], v20 offset:9216\n"
"ds_read_b64_tr_b16 a[38:39], v20 offset:13824\n"
"v_mfma_f32_16x16x32_bf16 v[36:39], a[0:3], a[228:231], v[36:39]\n"
"s_cmp_lt_u32 s69, 2\n"
"s_cselect_b32 s87, s82, 0x80000000\n"
"v_mfma_f32_16x16x32_bf16 v[44:47], a[4:7], a[228:231], v[44:47]\n"
"ds_read_b64_tr_b16 a[40:41], v21 offset:9216\n"
"s_mov_b32 s48, s28\n"
"v_mfma_f32_16x16x32_bf16 v[40:43], a[4:7], a[224:227], v[40:43]\n"
"buffer_load_dwordx4 a[208:211], v1, s[44:47], s72 offen offset:0 sc1\n"
"v_mfma_f32_16x16x32_bf16 v[48:51], a[8:11], a[224:227], v[48:51]\n"
"ds_read_b64_tr_b16 a[42:43], v21 offset:13824\n"
"ds_read_b64_tr_b16 a[44:45], v20 offset:9280\n"
"v_mfma_f32_16x16x32_bf16 v[52:55], a[8:11], a[228:231], v[52:55]\n"
"s_and_b32 s49, s29, 0xffff\n"
"ds_read_b64_tr_b16 a[46:47], v20 offset:13888\n"
"v_mfma_f32_16x16x32_bf16 v[60:63], a[12:15], a[228:231], v[60:63]\n"
"ds_read_b64_tr_b16 a[48:49], v21 offset:9280\n"
"ds_read_b64_tr_b16 a[50:51], v21 offset:13888\n"
"v_mfma_f32_16x16x32_bf16 v[56:59], a[12:15], a[224:227], v[56:59]\n"
"buffer_load_dwordx4 a[212:215], v1, s[44:47], s72 offen offset:1024 sc1\n"
"v_mfma_f32_16x16x32_bf16 v[64:67], a[16:19], a[224:227], v[64:67]\n"
"s_add_u32 s72, s72, 2048\n"
"s_mov_b32 s50, 0x1000\n"
"v_mfma_f32_16x16x32_bf16 v[68:71], a[16:19], a[228:231], v[68:71]\n"
"ds_read_b64_tr_b16 a[52:53], v20 offset:9344\n"
"ds_read_b64_tr_b16 a[54:55], v20 offset:13952\n"
"v_mfma_f32_16x16x32_bf16 v[76:79], a[20:23], a[228:231], v[76:79]\n"
"s_mov_b32 s51, 0x20000\n"
"ds_read_b64_tr_b16 a[56:57], v21 offset:9344\n"
"v_mfma_f32_16x16x32_bf16 v[72:75], a[20:23], a[224:227], v[72:75]\n"
"buffer_load_dwordx4 a[216:219], v1, s[44:47], s72 offen offset:0 sc1\n"
"v_mfma_f32_16x16x32_bf16 v[80:83], a[24:27], a[224:227], v[80:83]\n"
"ds_read_b64_tr_b16 a[58:59], v21 offset:13952\n"
"ds_read_b64_tr_b16 a[60:61], v20 offset:9408\n"
"v_mfma_f32_16x16x32_bf16 v[84:87], a[24:27], a[228:231], v[84:87]\n"
"s_mov_b32 s92, s12\n"
"ds_read_b64_tr_b16 a[62:63], v20 offset:14016\n"
"v_mfma_f32_16x16x32_bf16 v[92:95], a[28:31], a[228:231], v[92:95]\n"
"ds_read_b64_tr_b16 a[64:65], v21 offset:9408\n"
"s_and_b32 s93, s13, 0xffff\n"
"ds_read_b64_tr_b16 a[66:67], v21 offset:14016\n"
"ds_read_b64_tr_b16 a[68:69], v20 offset:9472\n"
"ds_read_b64_tr_b16 a[70:71], v20 offset:14080\n"
"v_mfma_f32_16x16x32_bf16 v[88:91], a[28:31], a[224:227], v[88:91]\n"
"buffer_load_dwordx4 a[220:223], v1, s[44:47], s72 offen offset:1024 sc1\n"
"v_mfma_f32_16x16x32_bf16 v[96:99], a[32:35], a[224:227], v[96:99]\n"
"s_add_u32 s72, s72, 2048\n"
"s_mov_b32 s94, 0x240000\n"
"v_mfma_f32_16x16x32_bf16 v[100:103], a[32:35], a[228:231], v[100:103]\n"
"s_mov_b32 s95, 0x20000\n"
"s_waitcnt vmcnt(28) lgkmcnt(0)\n"
"v_mfma_f32_16x16x32_bf16 v[32:35], a[36:39], a[232:235], v[32:35]\n"
"s_waitcnt vmcnt(23)\n"
"s_barrier\n"
"v_mfma_f32_16x16x32_bf16 v[36:39], a[36:39], a[236:239], v[36:39]\n"
"s_add_u32 m0, s1, 0\n"
"ds_read_b64_tr_b16 a[0:1], v241 offset:0\n"
"v_mfma_f32_16x16x32_bf16 v[44:47], a[40:43], a[236:239], v[44:47]\n"
"buffer_load_dwordx4 v28, s[40:43], s73 offen offset:0 lds\n"
"v_mfma_f32_16x16x32_bf16 v[40:43], a[40:43], a[232:235], v[40:43]\n"
"ds_read_b64_tr_b16 a[2:3], v241 offset:4608\n"
"ds_read_b64_tr_b16 a[4:5], v242 offset:0\n"
"v_mfma_f32_16x16x32_bf16 v[48:51], a[44:47], a[232:235], v[48:51]\n"
"ds_read_b64_tr_b16 a[6:7], v242 offset:4608\n"
"ds_read_b64_tr_b16 a[8:9], v241 offset:64\n"
"v_mfma_f32_16x16x32_bf16 v[52:55], a[44:47], a[236:239], v[52:55]\n"
"buffer_load_dwordx4 v29, s[40:43], s73 offen offset:1024 lds\n"
"v_mfma_f32_16x16x32_bf16 v[60:63], a[48:51], a[236:239], v[60:63]\n"
"ds_read_b64_tr_b16 a[10:11], v241 offset:4672\n"
"ds_read_b64_tr_b16 a[12:13], v242 offset:64\n"
"v_mfma_f32_16x16x32_bf16 v[56:59], a[48:51], a[232:235], v[56:59]\n"
"ds_read_b64_tr_b16 a[14:15], v242 offset:4672\n"
"ds_read_b64_tr_b16 a[16:17], v241 offset:128\n"
"v_mfma_f32_16x16x32_bf16 v[64:67], a[52:55], a[232:235], v[64:67]\n"
"ds_read_b64_tr_b16 a[18:19], v241 offset:4736\n"
"ds_read_b64_tr_b16 a[20:21], v242 offset:128\n"
"v_mfma_f32_16x16x32_bf16 v[68:71], a[52:55], a[236:239], v[68:71]\n"
"buffer_load_dwordx4 v30, s[40:43], s73 offen offset:2048 lds\n"
"v_mfma_f32_16x16x32_bf16 v[76:79], a[56:59], a[236:239], v[76:79]\n"
"ds_read_b64_tr_b16 a[22:23], v242 offset:4736\n"
"ds_read_b64_tr_b16 a[24:25], v241 offset:192\n"
"v_mfma_f32_16x16x32_bf16 v[72:75], a[56:59], a[232:235], v[72:75]\n"
"ds_read_b64_tr_b16 a[26:27], v241 offset:4800\n"
"ds_read_b64_tr_b16 a[28:29], v242 offset:192\n"
"v_mfma_f32_16x16x32_bf16 v[80:83], a[60:63], a[232:235], v[80:83]\n"
"ds_read_b64_tr_b16 a[30:31], v242 offset:4800\n"
"v_mfma_f32_16x16x32_bf16 v[84:87], a[60:63], a[236:239], v[84:87]\n"
"buffer_load_dwordx4 v31, s[40:43], s73 offen offset:3072 lds\n"
"v_mfma_f32_16x16x32_bf16 v[92:95], a[64:67], a[236:239], v[92:95]\n"
"ds_read_b64_tr_b16 a[32:33], v241 offset:256\n"
"ds_read_b64_tr_b16 a[34:35], v241 offset:4864\n"
"v_mfma_f32_16x16x32_bf16 v[88:91], a[64:67], a[232:235], v[88:91]\n"
"v_mfma_f32_16x16x32_bf16 v[96:99], a[68:71], a[232:235], v[96:99]\n"
"s_add_u32 m0, s1, 4096\n"
"v_mfma_f32_16x16x32_bf16 v[100:103], a[68:71], a[236:239], v[100:103]\n"
"s_mov_b64 exec, s[96:97]\n"
"buffer_load_dwordx4 v240, s[40:43], s73 offen offset:0 lds\n"
"s_mov_b64 exec, -1\n"
"s_add_u32 s73, s73, 73728\n"
"L_g3z:\n"
"s_waitcnt vmcnt(26) lgkmcnt(0)\n"
"v_mfma_f32_16x16x32_bf16 v[32:35], a[0:3], a[144:147], v[32:35]\n"
"ds_read_b64_tr_b16 a[36:37], v241 offset:9216\n"
"ds_read_b64_tr_b16 a[38:39], v241 offset:13824\n"
"v_mfma_f32_16x16x32_bf16 v[36:39], a[0:3], a[148:151], v[36:39]\n"
"ds_read_b64_tr_b16 a[40:41], v242 offset:9216\n"
"v_mfma_f32_16x16x32_bf16 v[44:47], a[4:7], a[148:151], v[44:47]\n"
"ds_read_b64_tr_b16 a[42:43], v242 offset:13824\n"
"v_mfma_f32_16x16x32_bf16 v[40:43], a[4:7], a[144:147], v[40:43]\n"
"buffer_load_dwordx4 a[224:227], v1, s[44:47], s72 offen offset:0 sc1\n"
"v_mfma_f32_16x16x32_bf16 v[48:51], a[8:11], a[144:147], v[48:51]\n"
"ds_read_b64_tr_b16 a[44:45], v241 offset:9280\n"
"ds_read_b64_tr_b16 a[46:47], v241 offset:13888\n"
"v_mfma_f32_16x16x32_bf16 v[52:55], a[8:11], a[148:151], v[52:55]\n"
"ds_read_b64_tr_b16 a[48:49], v242 offset:9280\n"
"ds_read_b64_tr_b16 a[50:51], v242 offset:13888\n"
"v_mfma_f32_16x16x32_bf16 v[60:63], a[12:15], a[148:151], v[60:63]\n"
"ds_read_b64_tr_b16 a[52:53], v241 offset:9344\n"
"v_mfma_f32_16x16x32_bf16 v[56:59], a[12:15], a[144:147], v[56:59]\n"
"buffer_load_dwordx4 a[228:231], v1, s[44:47], s72 offen offset:1024 sc1\n"
"v_mfma_f32_16x16x32_bf16 v[64:67], a[16:19], a[144:147], v[64:67]\n"
"s_add_u32 s72, s72, 2048\n"
"ds_read_b64_tr_b16 a[54:55], v241 offset:13952\n"
"v_mfma_f32_16x16x32_bf16 v[68:71], a[16:19], a[148:151], v[68:71]\n"
"ds_read_b64_tr_b16 a[56:57], v242 offset:9344\n"
"ds_read_b64_tr_b16 a[58:59], v242 offset:13952\n"
"v_mfma_f32_16x16x32_bf16 v[76:79], a[20:23], a[148:151], v[76:79]\n"
"ds_read_b64_tr_b16 a[60:61], v241 offset:9408\n"
"ds_read_b64_tr_b16 a[62:63], v241 offset:14016\n"
"v_mfma_f32_16x16x32_bf16 v[72:75], a[20:23], a[144:147], v[72:75]\n"
"buffer_load_dwordx4 a[232:235], v1, s[44:47], s72 offen offset:0 sc1\n"
"v_mfma_f32_16x16x32_bf16 v[80:83], a[24:27], a[144:147], v[80:83]\n"
"ds_read_b64_tr_b16 a[64:65], v242 offset:9408\n"
"ds_read_b64_tr_b16 a[66:67], v242 offset:14016\n"
"v_mfma_f32_16x16x32_bf16 v[84:87], a[24:27], a[148:151], v[84:87]\n"
"ds_read_b64_tr_b16 a[68:69], v241 offset:9472\n"
"v_mfma_f32_16x16x32_bf16 v[92:95], a[28:31], a[148:151], v[92:95]\n"
"ds_read_b64_tr_b16 a[70:71], v241 offset:14080\n"
"v_mfma_f32_16x16x32_bf16 v[88:91], a[28:31], a[144:147], v[88:91]\n"
"buffer_load_dwordx4 a[236:239], v1, s[44:47], s72 offen offset:1024 sc1\n"
"v_mfma_f32_16x16x32_bf16 v[96:99], a[32:35], a[144:147], v[96:99]\n"
"s_add_u32 s72, s72, 2048\n"
"v_mfma_f32_16x16x32_bf16 v[100:103], a[32:35], a[148:151], v[100:103]\n"
"s_waitcnt vmcnt(22)\n"
"v_cmp_eq_u32 vcc, s34, v248\n"
"s_nop 0\n"
"s_andn2_b64 s[88:89], s[98:99], vcc\n"
"v_cmp_eq_u32 vcc, s34, v249\n"
"s_nop 0\n"
"s_andn2_b64 vcc, s[8:9], vcc\n"
"s_or_b64 s[88:89], s[88:89], vcc\n"
"s_cbranch_scc0 L_py_ok\n"
"s_mov_b32 s55, 0x30000\n"
"L_py_re:\n"
"s_sleep 1\n"
"buffer_load_dwordx2 v[248:249], v6, s[36:39], s83 offen sc1\n"
"s_waitcnt vmcnt(0)\n"
"v_cmp_eq_u32 vcc, s34, v248\n"
"s_nop 0\n"
"s_andn2_b64 s[88:89], s[98:99], vcc\n"
"v_cmp_eq_u32 vcc, s34, v249\n"
"s_nop 0\n"
"s_andn2_b64 vcc, s[8:9], vcc\n"
"s_or_b64 s[88:89], s[88:89], vcc\n"
"s_cbranch_scc0 L_py_ok\n"
"s_sub_u32 s55, s55, 1\n"
"s_cmp_lg_u32 s55, 0\n"
"s_cbranch_scc1 L_py_re\n"
"L_py_ok:\n"
"s_waitcnt lgkmcnt(0)\n"
"v_mfma_f32_16x16x32_bf16 v[32:35], a[36:39], a[152:155], v[32:35]\n"
"s_nop 0\n"
"s_barrier\n"
"v_mfma_f32_16x16x32_bf16 v[36:39], a[36:39], a[156:159], v[36:39]\n"
"ds_read_b64_tr_b16 a[0:1], v241 offset:18432\n"
"ds_read_b64_tr_b16 a[2:3], v241 offset:23040\n"
"v_mfma_f32_16x16x32_bf16 v[44:47], a[40:43], a[156:159], v[44:47]\n"
"buffer_load_ushort v134, v17, s[48:51], s74 offen\n"
"v_mfma_f32_16x16x32_bf16 v[40:43], a[40:43], a[152:155], v[40:43]\n"
"ds_read_b64_tr_b16 a[4:5], v242 offset:18432\n"
"ds_read_b64_tr_b16 a[6:7], v242 offset:23040\n"
"v_mfma_f32_16x16x32_bf16 v[48:51], a[44:47], a[152:155], v[48:51]\n"
"buffer_load_dwordx4 v[136:139], v1, s[36:39], s82 offen sc1\n"
"v_mfma_f32_16x16x32_bf16 v[52:55], a[44:47], a[156:159], v[52:55]\n"
"ds_read_b64_tr_b16 a[8:9], v241 offset:18496\n"
"ds_read_b64_tr_b16 a[10:11], v241 offset:23104\n"
"v_mfma_f32_16x16x32_bf16 v[60:63], a[48:51], a[156:159], v[60:63]\n"
"buffer_load_dwordx4 v[140:143], v1, s[36:39], s82 offen offset:1024 sc1\n"
"v_mfma_f32_16x16x32_bf16 v[56:59], a[48:51], a[152:155], v[56:59]\n"
"ds_read_b64_tr_b16 a[12:13], v242 offset:18496\n"
"ds_read_b64_tr_b16 a[14:15], v242 offset:23104\n"
"v_mfma_f32_16x16x32_bf16 v[64:67], a[52:55], a[152:155], v[64:67]\n"
"ds_read_b64_tr_b16 a[16:17], v241 offset:18560\n"
"ds_read_b64_tr_b16 a[18:19], v241 offset:23168\n"
"v_mfma_f32_16x16x32_bf16 v[68:71], a[52:55], a[156:159], v[68:71]\n"
"buffer_load_dwordx2 v[144:145], v251, s[36:39], s87 offen offset:2048 sc1\n"
"v_mfma_f32_16x16x32_bf16 v[76:79], a[56:59], a[156:159], v[76:79]\n"
"ds_read_b64_tr_b16 a[20:21], v242 offset:18560\n"
"s_add_u32 s82, s82, 2359296\n"
"v_mfma_f32_16x16x32_bf16 v[72:75], a[56:59], a[152:155], v[72:75]\n"
"ds_read_b64_tr_b16 a[22:23], v242 offset:23168\n"
"ds_read_b64_tr_b16 a[24:25], v241 offset:18624\n"
"v_mfma_f32_16x16x32_bf16 v[80:83], a[60:63], a[152:155], v[80:83]\n"
"ds_read_b64_tr_b16 a[26:27], v241 offset:23232\n"
"s_add_u32 s87, s87, 2359296\n"
"v_mfma_f32_16x16x32_bf16 v[84:87], a[60:63], a[156:159], v[84:87]\n"
"ds_read_b64_tr_b16 a[28:29], v242 offset:18624\n"
"ds_read_b64_tr_b16 a[30:31], v242 offset:23232\n"
"v_mfma_f32_16x16x32_bf16 v[92:95], a[64:67], a[156:159], v[92:95]\n"
"ds_read_b64_tr_b16 a[32:33], v241 offset:18688\n"
"ds_read_b64_tr_b16 a[34:35], v241 offset:23296\n"
"v_mfma_f32_16x16x32_bf16 v[88:91], a[64:67], a[152:155], v[88:91]\n"
"buffer_load_dwordx4 v[146:149], v1, s[36:39], s82 offen sc1\n"
"v_mfma_f32_16x16x32_bf16 v[96:99], a[68:71], a[152:155], v[96:99]\n"
"buffer_load_dwordx4 v[150:153], v1, s[36:39], s82 offen offset:1024 sc1\n"
"v_mfma_f32_16x16x32_bf16 v[100:103], a[68:71], a[156:159], v[100:103]\n"
"s_waitcnt vmcnt(26) lgkmcnt(0)\n"
"v_mfma_f32_16x16x32_bf16 v[32:35], a[0:3], a[160:163], v[32:35]\n"
"ds_read_b64_tr_b16 a[36:37], v241 offset:27648\n"
"ds_read_b64_tr_b16 a[38:39], v241 offset:32256\n"
"v_mfma_f32_16x16x32_bf16 v[36:39], a[0:3], a[164:167], v[36:39]\n"
"buffer_load_dwordx2 v[154:155], v251, s[36:39], s87 offen offset:2048 sc1\n"
"v_mfma_f32_16x16x32_bf16 v[44:47], a[4:7], a[164:167], v[44:47]\n"
"ds_read_b64_tr_b16 a[40:41], v242 offset:27648\n"
"s_add_u32 s82, s82, 2359296\n"
"v_mfma_f32_16x16x32_bf16 v[40:43], a[4:7], a[160:163], v[40:43]\n"
"ds_read_b64_tr_b16 a[42:43], v242 offset:32256\n"
"ds_read_b64_tr_b16 a[44:45], v241 offset:27712\n"
"v_mfma_f32_16x16x32_bf16 v[48:51], a[8:11], a[160:163], v[48:51]\n"
"ds_read_b64_tr_b16 a[46:47], v241 offset:32320\n"
"s_add_u32 s87, s87, 2359296\n"
"v_mfma_f32_16x16x32_bf16 v[52:55], a[8:11], a[164:167], v[52:55]\n"
"ds_read_b64_tr_b16 a[48:49], v242 offset:27712\n"
"ds_read_b64_tr_b16 a[50:51], v242 offset:32320\n"
"v_mfma_f32_16x16x32_bf16 v[60:63], a[12:15], a[164:167], v[60:63]\n"
"ds_read_b64_tr_b16 a[52:53], v241 offset:27776\n"
"v_mfma_f32_16x16x32_bf16 v[56:59], a[12:15], a[160:163], v[56:59]\n"
"buffer_load_dwordx4 v[156:159], v1, s[36:39], s82 offen sc1\n"
"v_mfma_f32_16x16x32_bf16 v[64:67], a[16:19], a[160:163], v[64:67]\n"
"ds_read_b64_tr_b16 a[54:55], v241 offset:32384\n"
"ds_read_b64_tr_b16 a[56:57], v242 offset:27776\n"
"v_mfma_f32_16x16x32_bf16 v[68:71], a[16:19], a[164:167], v[68:71]\n"
"buffer_load_dwordx4 v[160:163], v1, s[36:39], s82 offen offset:1024 sc1\n"
"v_mfma_f32_16x16x32_bf16 v[76:79], a[20:23], a[164:167], v[76:79]\n"
"ds_read_b64_tr_b16 a[58:59], v242 offset:32384\n"
"ds_read_b64_tr_b16 a[60:61], v241 offset:27840\n"
"v_mfma_f32_16x16x32_bf16 v[72:75], a[20:23], a[160:163], v[72:75]\n"
"ds_read_b64_tr_b16 a[62:63], v241 offset:32448\n"
"ds_read_b64_tr_b16 a[64:65], v242 offset:27840\n"
"v_mfma_f32_16x16x32_bf16 v[80:83], a[24:27], a[160:163], v[80:83]\n"
"buffer_load_dwordx2 v[164:165], v251, s[36:39], s87 offen offset:2048 sc1\n"
"v_mfma_f32_16x16x32_bf16 v[84:87], a[24:27], a[164:167], v[84:87]\n"
"ds_read_b64_tr_b16 a[66:67], v242 offset:32448\n"
"ds_read_b64_tr_b16 a[68:69], v241 offset:27904\n"
"v_mfma_f32_16x16x32_bf16 v[92:95], a[28:31], a[164:167], v[92:95]\n"
"s_add_u32 s82, s82, 2359296\n"
"ds_read_b64_tr_b16 a[70:71], v241 offset:32512\n"
"v_mfma_f32_16x16x32_bf16 v[88:91], a[28:31], a[160:163], v[88:91]\n"
"s_add_u32 s87, s87, 2359296\n"
"v_mfma_f32_16x16x32_bf16 v[96:99], a[32:35], a[160:163], v[96:99]\n"
"v_mfma_f32_16x16x32_bf16 v[100:103], a[32:35], a[164:167], v[100:103]\n"
"s_waitcnt vmcnt(28) lgkmcnt(0)\n"
"v_mfma_f32_16x16x32_bf16 v[32:35], a[36:39], a[168:171], v[32:35]\n"
"s_waitcnt vmcnt(23)\n"
"s_barrier\n"
"v_mfma_f32_16x16x32_bf16 v[36:39], a[36:39], a[172:175], v[36:39]\n"
"ds_read_b64_tr_b16 a[0:1], v241 offset:36864\n"
"ds_read_b64_tr_b16 a[2:3], v241 offset:41472\n"
"v_mfma_f32_16x16x32_bf16 v[44:47], a[40:43], a[172:175], v[44:47]\n"
"buffer_load_dwordx4 v[166:169], v1, s[36:39], s82 offen sc1\n"
"v_mfma_f32_16x16x32_bf16 v[40:43], a[40:43], a[168:171], v[40:43]\n"
"ds_read_b64_tr_b16 a[4:5], v242 offset:36864\n"
"ds_read_b64_tr_b16 a[6:7], v242 offset:41472\n"
"v_mfma_f32_16x16x32_bf16 v[48:51], a[44:47], a[168:171], v[48:51]\n"
"buffer_load_dwordx4 v[170:173], v1, s[36:39], s82 offen offset:1024 sc1\n"
"v_mfma_f32_16x16x32_bf16 v[52:55], a[44:47], a[172:175], v[52:55]\n"
"ds_read_b64_tr_b16 a[8:9], v241 offset:36928\n"
"ds_read_b64_tr_b16 a[10:11], v241 offset:41536\n"
"v_mfma_f32_16x16x32_bf16 v[60:63], a[48:51], a[172:175], v[60:63]\n"
"buffer_load_dwordx2 v[174:175], v251, s[36:39], s87 offen offset:2048 sc1\n"
"v_mfma_f32_16x16x32_bf16 v[56:59], a[48:51], a[168:171], v[56:59]\n"
"ds_read_b64_tr_b16 a[12:13], v242 offset:36928\n"
"ds_read_b64_tr_b16 a[14:15], v242 offset:41536\n"
"v_mfma_f32_16x16x32_bf16 v[64:67], a[52:55], a[168:171], v[64:67]\n"
"ds_read_b64_tr_b16 a[16:17], v241 offset:36992\n"
"ds_read_b64_tr_b16 a[18:19], v241 offset:41600\n"
"v_mfma_f32_16x16x32_bf16 v[68:71], a[52:55], a[172:175], v[68:71]\n"
"buffer_load_dwordx2 v[124:125], v16, s[92:95], s84 offen offset:0\n"
"v_mfma_f32_16x16x32_bf16 v[76:79], a[56:59], a[172:175], v[76:79]\n"
"ds_read_b64_tr_b16 a[20:21], v242 offset:36992\n"
"ds_read_b64_tr_b16 a[22:23], v242 offset:41600\n"
"v_mfma_f32_16x16x32_bf16 v[72:75], a[56:59], a[168:171], v[72:75]\n"
"buffer_load_dwordx2 v[126:127], v16, s[92:95], s84 offen offset:32\n"
"v_mfma_f32_16x16x32_bf16 v[80:83], a[60:63], a[168:171], v[80:83]\n"
"ds_read_b64_tr_b16 a[24:25], v241 offset:37056\n"
"ds_read_b64_tr_b16 a[26:27], v241 offset:41664\n"
"v_mfma_f32_16x16x32_bf16 v[84:87], a[60:63], a[172:175], v[84:87]\n"
"buffer_load_dwordx2 v[128:129], v16, s[92:95], s84 offen offset:64\n"
"v_mfma_f32_16x16x32_bf16 v[92:95], a[64:67], a[172:175], v[92:95]\n"
"ds_read_b64_tr_b16 a[28:29], v242 offset:37056\n"
"ds_read_b64_tr_b16 a[30:31], v242 offset:41664\n"
"ds_read_b64_tr_b16 a[32:33], v241 offset:37120\n"
"ds_read_b64_tr_b16 a[34:35], v241 offset:41728\n"
"v_mfma_f32_16x16x32_bf16 v[88:91], a[64:67], a[168:171], v[88:91]\n"
"buffer_load_dwordx2 v[130:131], v16, s[92:95], s84 offen offset:96\n"
"v_mfma_f32_16x16x32_bf16 v[96:99], a[68:71], a[168:171], v[96:99]\n"
"buffer_load_dwordx2 v[132:133], v16, s[92:95], s84 offen offset:128\n"
"v_mfma_f32_16x16x32_bf16 v[100:103], a[68:71], a[172:175], v[100:103]\n"
"s_waitcnt vmcnt(29) lgkmcnt(0)\n"
"v_mfma_f32_16x16x32_bf16 v[32:35], a[0:3], a[208:211], v[32:35]\n"
"ds_read_b64_tr_b16 a[36:37], v241 offset:46080\n"
"ds_read_b64_tr_b16 a[38:39], v241 offset:50688\n"
"v_mfma_f32_16x16x32_bf16 v[36:39], a[0:3], a[212:215], v[36:39]\n"
"ds_read_b64_tr_b16 a[40:41], v242 offset:46080\n"
"v_mfma_f32_16x16x32_bf16 v[44:47], a[4:7], a[212:215], v[44:47]\n"
"ds_read_b64_tr_b16 a[42:43], v242 offset:50688\n"
"v_mfma_f32_16x16x32_bf16 v[40:43], a[4:7], a[208:211], v[40:43]\n"
"ds_read_b64_tr_b16 a[44:45], v241 offset:46144\n"
"v_mfma_f32_16x16x32_bf16 v[48:51], a[8:11], a[208:211], v[48:51]\n"
"ds_read_b64_tr_b16 a[46:47], v241 offset:50752\n"
"v_mfma_f32_16x16x32_bf16 v[52:55], a[8:11], a[212:215], v[52:55]\n"
"ds_read_b64_tr_b16 a[48:49], v242 offset:46144\n"
"ds_read_b64_tr_b16 a[50:51], v242 offset:50752\n"
"v_mfma_f32_16x16x32_bf16 v[60:63], a[12:15], a[212:215], v[60:63]\n"
"ds_read_b64_tr_b16 a[52:53], v241 offset:46208\n"
"v_mfma_f32_16x16x32_bf16 v[56:59], a[12:15], a[208:211], v[56:59]\n"
"ds_read_b64_tr_b16 a[54:55], v241 offset:50816\n"
"v_mfma_f32_16x16x32_bf16 v[64:67], a[16:19], a[208:211], v[64:67]\n"
"ds_read_b64_tr_b16 a[56:57], v242 offset:46208\n"
"v_mfma_f32_16x16x32_bf16 v[68:71], a[16:19], a[212:215], v[68:71]\n"
"ds_read_b64_tr_b16 a[58:59], v242 offset:50816\n"
"v_mfma_f32_16x16x32_bf16 v[76:79], a[20:23], a[212:215], v[76:79]\n"
"ds_read_b64_tr_b16 a[60:61], v241 offset:46272\n"
"ds_read_b64_tr_b16 a[62:63], v241 offset:50880\n"
"v_mfma_f32_16x16x32_bf16 v[72:75], a[20:23], a[208:211], v[72:75]\n"
"ds_read_b64_tr_b16 a[64:65], v242 offset:46272\n"
"v_mfma_f32_16x16x32_bf16 v[80:83], a[24:27], a[208:211], v[80:83]\n"
"ds_read_b64_tr_b16 a[66:67], v242 offset:50880\n"
"v_mfma_f32_16x16x32_bf16 v[84:87], a[24:27], a[212:215], v[84:87]\n"
"ds_read_b64_tr_b16 a[68:69], v241 offset:46336\n"
"v_mfma_f32_16x16x32_bf16 v[92:95], a[28:31], a[212:215], v[92:95]\n"
"ds_read_b64_tr_b16 a[70:71], v241 offset:50944\n"
"v_mfma_f32_16x16x32_bf16 v[88:91], a[28:31], a[208:211], v[88:91]\n"
"v_mfma_f32_16x16x32_bf16 v[96:99], a[32:35], a[208:211], v[96:99]\n"
"v_mfma_f32_16x16x32_bf16 v[100:103], a[32:35], a[212:215], v[100:103]\n"
"s_waitcnt vmcnt(27) lgkmcnt(0)\n"
"v_mfma_f32_16x16x32_bf16 v[32:35], a[36:39], a[216:219], v[32:35]\n"
"s_waitcnt vmcnt(22)\n"
"s_barrier\n"
"v_mfma_f32_16x16x32_bf16 v[36:39], a[36:39], a[220:223], v[36:39]\n"
"ds_read_b64_tr_b16 a[0:1], v20 offset:0\n"
"ds_read_b64_tr_b16 a[2:3], v20 offset:4608\n"
"v_mfma_f32_16x16x32_bf16 v[44:47], a[40:43], a[220:223], v[44:47]\n"
"ds_read_b64_tr_b16 a[4:5], v21 offset:0\n"
"v_mfma_f32_16x16x32_bf16 v[40:43], a[40:43], a[216:219], v[40:43]\n"
"ds_read_b64_tr_b16 a[6:7], v21 offset:4608\n"
"v_mfma_f32_16x16x32_bf16 v[48:51], a[44:47], a[216:219], v[48:51]\n"
"ds_read_b64_tr_b16 a[8:9], v20 offset:64\n"
"ds_read_b64_tr_b16 a[10:11], v20 offset:4672\n"
"v_mfma_f32_16x16x32_bf16 v[52:55], a[44:47], a[220:223], v[52:55]\n"
"ds_read_b64_tr_b16 a[12:13], v21 offset:64\n"
"v_mfma_f32_16x16x32_bf16 v[60:63], a[48:51], a[220:223], v[60:63]\n"
"ds_read_b64_tr_b16 a[14:15], v21 offset:4672\n"
"v_mfma_f32_16x16x32_bf16 v[56:59], a[48:51], a[216:219], v[56:59]\n"
"ds_read_b64_tr_b16 a[16:17], v20 offset:128\n"
"v_mfma_f32_16x16x32_bf16 v[64:67], a[52:55], a[216:219], v[64:67]\n"
"ds_read_b64_tr_b16 a[18:19], v20 offset:4736\n"
"ds_read_b64_tr_b16 a[20:21], v21 offset:128\n"
"v_mfma_f32_16x16x32_bf16 v[68:71], a[52:55], a[220:223], v[68:71]\n"
"ds_read_b64_tr_b16 a[22:23], v21 offset:4736\n"
"v_mfma_f32_16x16x32_bf16 v[76:79], a[56:59], a[220:223], v[76:79]\n"
"ds_read_b64_tr_b16 a[24:25], v20 offset:192\n"
"v_mfma_f32_16x16x32_bf16 v[72:75], a[56:59], a[216:219], v[72:75]\n"
"ds_read_b64_tr_b16 a[26:27], v20 offset:4800\n"
"ds_read_b64_tr_b16 a[28:29], v21 offset:192\n"
"v_mfma_f32_16x16x32_bf16 v[80:83], a[60:63], a[216:219], v[80:83]\n"
"ds_read_b64_tr_b16 a[30:31], v21 offset:4800\n"
"v_mfma_f32_16x16x32_bf16 v[84:87], a[60:63], a[220:223], v[84:87]\n"
"ds_read_b64_tr_b16 a[32:33], v20 offset:256\n"
"v_mfma_f32_16x16x32_bf16 v[92:95], a[64:67], a[220:223], v[92:95]\n"
"ds_read_b64_tr_b16 a[34:35], v20 offset:4864\n"
"v_mfma_f32_16x16x32_bf16 v[88:91], a[64:67], a[216:219], v[88:91]\n"
"v_mfma_f32_16x16x32_bf16 v[96:99], a[68:71], a[216:219], v[96:99]\n"
"v_mfma_f32_16x16x32_bf16 v[100:103], a[68:71], a[220:223], v[100:103]\n"
"s_cmp_eq_u32 s69, 1\n"
"s_cbranch_scc1 L_fin1\n"
"s_cmp_eq_u32 s69, 2\n"
"s_cbranch_scc1 L_fin2\n"
"s_cmp_eq_u32 s69, 3\n"
"s_cbranch_scc1 L_fin3\n"
"L_fin0:\n"
"s_waitcnt vmcnt(18) lgkmcnt(0)\n"
"v_mfma_f32_16x16x32_bf16 v[36:39], a[0:3], a[228:231], v[36:39]\n"
"ds_read_b64_tr_b16 a[36:37], v20 offset:9216\n"
"ds_read_b64_tr_b16 a[38:39], v20 offset:13824\n"
"v_mfma_f32_16x16x32_bf16 v[44:47], a[4:7], a[228:231], v[44:47]\n"
"ds_read_b64_tr_b16 a[40:41], v21 offset:9216\n"
"ds_read_b64_tr_b16 a[42:43], v21 offset:13824\n"
"v_mfma_f32_16x16x32_bf16 v[52:55], a[8:11], a[228:231], v[52:55]\n"
"ds_read_b64_tr_b16 a[44:45], v20 offset:9280\n"
"ds_read_b64_tr_b16 a[46:47], v20 offset:13888\n"
"v_mfma_f32_16x16x32_bf16 v[60:63], a[12:15], a[228:231], v[60:63]\n"
"ds_read_b64_tr_b16 a[48:49], v21 offset:9280\n"
"ds_read_b64_tr_b16 a[50:51], v21 offset:13888\n"
"v_mfma_f32_16x16x32_bf16 v[68:71], a[16:19], a[228:231], v[68:71]\n"
"ds_read_b64_tr_b16 a[52:53], v20 offset:9344\n"
"ds_read_b64_tr_b16 a[54:55], v20 offset:13952\n"
"v_mfma_f32_16x16x32_bf16 v[72:75], a[20:23], a[224:227], v[72:75]\n"
"ds_read_b64_tr_b16 a[56:57], v21 offset:9344\n"
"ds_read_b64_tr_b16 a[58:59], v21 offset:13952\n"
"v_mfma_f32_16x16x32_bf16 v[80:83], a[24:27], a[224:227], v[80:83]\n"
"ds_read_b64_tr_b16 a[60:61], v20 offset:9408\n"
"ds_read_b64_tr_b16 a[62:63], v20 offset:14016\n"
"v_mfma_f32_16x16x32_bf16 v[88:91], a[28:31], a[224:227], v[88:91]\n"
"ds_read_b64_tr_b16 a[64:65], v21 offset:9408\n"
"ds_read_b64_tr_b16 a[66:67], v21 offset:14016\n"
"v_mfma_f32_16x16x32_bf16 v[96:99], a[32:35], a[224:227], v[96:99]\n"
"ds_read_b64_tr_b16 a[68:69], v20 offset:9472\n"
"ds_read_b64_tr_b16 a[70:71], v20 offset:14080\n"
"v_mfma_f32_16x16x32_bf16 v[76:79], a[20:23], a[228:231], v[76:79]\n"
"v_mfma_f32_16x16x32_bf16 v[84:87], a[24:27], a[228:231], v[84:87]\n"
"v_mfma_f32_16x16x32_bf16 v[92:95], a[28:31], a[228:231], v[92:95]\n"
"v_mfma_f32_16x16x32_bf16 v[100:103], a[32:35], a[228:231], v[100:103]\n"
"s_waitcnt lgkmcnt(0)\n"
"v_mfma_f32_16x16x32_bf16 v[36:39], a[36:39], a[236:239], v[36:39]\n"
"v_mfma_f32_16x16x32_bf16 v[44:47], a[40:43], a[236:239], v[44:47]\n"
"v_mfma_f32_16x16x32_bf16 v[52:55], a[44:47], a[236:239], v[52:55]\n"
"v_mfma_f32_16x16x32_bf16 v[60:63], a[48:51], a[236:239], v[60:63]\n"
"v_mfma_f32_16x16x32_bf16 v[68:71], a[52:55], a[236:239], v[68:71]\n"
"v_mfma_f32_16x16x32_bf16 v[72:75], a[56:59], a[232:235], v[72:75]\n"
"v_mfma_f32_16x16x32_bf16 v[80:83], a[60:63], a[232:235], v[80:83]\n"
"v_mfma_f32_16x16x32_bf16 v[88:91], a[64:67], a[232:235], v[88:91]\n"
"s_nop 0\n"
"v_cvt_pk_bf16_f32 v196, v36, v37\n"
"v_cvt_pk_bf16_f32 v197, v38, v39\n"
"v_mfma_f32_16x16x32_bf16 v[96:99], a[68:71], a[232:235], v[96:99]\n"
"v_cvt_pk_bf16_f32 v198, v44, v45\n"
"v_mfma_f32_16x16x32_bf16 v[76:79], a[56:59], a[236:239], v[76:79]\n"
"v_cvt_pk_bf16_f32 v199, v46, v47\n"
"v_mfma_f32_16x16x32_bf16 v[84:87], a[60:63], a[236:239], v[84:87]\n"
"v_cvt_pk_bf16_f32 v200, v52, v53\n"
"v_mfma_f32_16x16x32_bf16 v[92:95], a[64:67], a[236:239], v[92:95]\n"
"v_cvt_pk_bf16_f32 v201, v54, v55\n"
"v_mfma_f32_16x16x32_bf16 v[100:103], a[68:71], a[236:239], v[100:103]\n"
"v_cvt_pk_bf16_f32 v202, v60, v61\n"
"v_mfma_f32_16x16x32_bf16 v[32:35], a[0:3], a[224:227], v[32:35]\n"
"v_cvt_pk_bf16_f32 v203, v62, v63\n"
"v_mfma_f32_16x16x32_bf16 v[40:43], a[4:7], a[224:227], v[40:43]\n"
"v_cvt_pk_bf16_f32 v204, v68, v69\n"
"v_mfma_f32_16x16x32_bf16 v[48:51], a[8:11], a[224:227], v[48:51]\n"
"v_cvt_pk_bf16_f32 v208, v72, v73\n"
"v_cvt_pk_bf16_f32 v205, v70, v71\n"
"v_mfma_f32_16x16x32_bf16 v[56:59], a[12:15], a[224:227], v[56:59]\n"
"v_cvt_pk_bf16_f32 v209, v74, v75\n"
"v_mfma_f32_16x16x32_bf16 v[64:67], a[16:19], a[224:227], v[64:67]\n"
"s_add_u32 s77, s3, 32768\n"
"buffer_store_dwordx4 v[196:199], v1, s[36:39], s77 offen offset:0 sc1\n"
"v_mfma_f32_16x16x32_bf16 v[32:35], a[36:39], a[232:235], v[32:35]\n"
"v_cvt_pk_bf16_f32 v210, v80, v81\n"
"buffer_store_dwordx4 v[200:203], v1, s[36:39], s77 offen offset:1024 sc1\n"
"buffer_store_dwordx2 v[204:205], v251, s[36:39], s77 offen offset:2048 sc1\n"
"v_mfma_f32_16x16x32_bf16 v[40:43], a[40:43], a[232:235], v[40:43]\n"
"v_cvt_pk_bf16_f32 v211, v82, v83\n"
"v_cvt_pk_bf16_f32 v212, v88, v89\n"
"v_mfma_f32_16x16x32_bf16 v[48:51], a[44:47], a[232:235], v[48:51]\n"
"v_cvt_pk_bf16_f32 v213, v90, v91\n"
"v_mfma_f32_16x16x32_bf16 v[56:59], a[48:51], a[232:235], v[56:59]\n"
"v_cvt_pk_bf16_f32 v214, v96, v97\n"
"v_mfma_f32_16x16x32_bf16 v[64:67], a[52:55], a[232:235], v[64:67]\n"
"v_cvt_pk_bf16_f32 v215, v98, v99\n"
"s_add_u32 s77, s3, 68096\n"
"buffer_store_dwordx4 v[208:211], v1, s[36:39], s77 offen offset:0 sc1\n"
"buffer_store_dwordx4 v[212:215], v1, s[36:39], s77 offen offset:1024 sc1\n"
"v_cvt_pk_bf16_f32 v216, v76, v77\n"
"v_cvt_pk_bf16_f32 v217, v78, v79\n"
"v_cvt_pk_bf16_f32 v218, v84, v85\n"
"v_cvt_pk_bf16_f32 v219, v86, v87\n"
"v_cvt_pk_bf16_f32 v220, v92, v93\n"
"v_cvt_pk_bf16_f32 v221, v94, v95\n"
"v_cvt_pk_bf16_f32 v222, v100, v101\n"
"v_cvt_pk_bf16_f32 v223, v102, v103\n"
"s_add_u32 s77, s3, 103424\n"
"buffer_store_dwordx4 v[216:219], v1, s[36:39], s77 offen offset:0 sc1\n"
"s_waitcnt vmcnt(6)\n"
"s_mov_b32 s85, 0x3f80\n"
"s_mov_b32 s86, 0x3f800000\n"
"v_lshlrev_b32 v134, 16, v134\n"
"s_mov_b32 s80, 0xbfb8aa3b\n"
"v_mov_b32 v104, v134\n"
"v_mov_b32 v105, v134\n"
"v_mov_b32 v106, v134\n"
"v_mov_b32 v107, v134\n"
"v_mov_b32 v108, v134\n"
"v_mov_b32 v109, v134\n"
"v_mov_b32 v110, v134\n"
"v_mov_b32 v111, v134\n"
"v_mov_b32 v112, v134\n"
"v_mov_b32 v113, v134\n"
"v_mov_b32 v114, v134\n"
"v_mov_b32 v115, v134\n"
"v_mov_b32 v116, v134\n"
"buffer_store_dwordx4 v[220:223], v1, s[36:39], s77 offen offset:1024 sc1\n"
"v_mov_b32 v117, v134\n"
"v_mov_b32 v118, v134\n"
"v_mov_b32 v119, v134\n"
"v_mov_b32 v120, v134\n"
"v_mov_b32 v121, v134\n"
"v_mov_b32 v122, v134\n"
"v_mov_b32 v123, v134\n"
"v_dot2c_f32_bf16 v104, s85, v136\n"
"v_dot2c_f32_bf16 v106, s85, v137\n"
"v_dot2c_f32_bf16 v108, s85, v138\n"
"v_dot2c_f32_bf16 v110, s85, v139\n"
"v_dot2c_f32_bf16 v112, s85, v140\n"
"v_dot2c_f32_bf16 v114, s85, v141\n"
"v_dot2c_f32_bf16 v116, s85, v142\n"
"v_dot2c_f32_bf16 v118, s85, v143\n"
"v_dot2c_f32_bf16 v120, s85, v144\n"
"v_dot2c_f32_bf16 v122, s85, v145\n"
"v_dot2c_f32_bf16 v105, s86, v136\n"
"v_dot2c_f32_bf16 v107, s86, v137\n"
"v_dot2c_f32_bf16 v109, s86, v138\n"
"v_dot2c_f32_bf16 v111, s86, v139\n"
"v_dot2c_f32_bf16 v113, s86, v140\n"
"v_dot2c_f32_bf16 v115, s86, v141\n"
"v_dot2c_f32_bf16 v117, s86, v142\n"
"v_dot2c_f32_bf16 v119, s86, v143\n"
"v_dot2c_f32_bf16 v121, s86, v144\n"
"v_dot2c_f32_bf16 v123, s86, v145\n"
"v_dot2c_f32_bf16 v104, s85, v146\n"
"v_dot2c_f32_bf16 v106, s85, v147\n"
"v_dot2c_f32_bf16 v108, s85, v148\n"
"v_dot2c_f32_bf16 v110, s85, v149\n"
"v_dot2c_f32_bf16 v112, s85, v150\n"
"v_dot2c_f32_bf16 v114, s85, v151\n"
"v_dot2c_f32_bf16 v116, s85, v152\n"
"v_dot2c_f32_bf16 v118, s85, v153\n"
"v_dot2c_f32_bf16 v120, s85, v154\n"
"v_dot2c_f32_bf16 v122, s85, v155\n"
"v_dot2c_f32_bf16 v105, s86, v146\n"
"v_dot2c_f32_bf16 v107, s86, v147\n"
"v_dot2c_f32_bf16 v109, s86, v148\n"
"v_dot2c_f32_bf16 v111, s86, v149\n"
"v_dot2c_f32_bf16 v113, s86, v150\n"
"v_dot2c_f32_bf16 v115, s86, v151\n"
"v_dot2c_f32_bf16 v117, s86, v152\n"
"v_dot2c_f32_bf16 v119, s86, v153\n"
"v_dot2c_f32_bf16 v121, s86, v154\n"
"v_dot2c_f32_bf16 v123, s86, v155\n"
"v_dot2c_f32_bf16 v104, s85, v156\n"
"v_dot2c_f32_bf16 v106, s85, v157\n"
"v_dot2c_f32_bf16 v108, s85, v158\n"
"v_dot2c_f32_bf16 v110, s85, v159\n"
"v_dot2c_f32_bf16 v112, s85, v160\n"
"v_dot2c_f32_bf16 v114, s85, v161\n"
"v_dot2c_f32_bf16 v116, s85, v162\n"
"v_dot2c_f32_bf16 v118, s85, v163\n"
"v_dot2c_f32_bf16 v120, s85, v164\n"
"v_dot2c_f32_bf16 v122, s85, v165\n"
"v_dot2c_f32_bf16 v105, s86, v156\n"
"v_dot2c_f32_bf16 v107, s86, v157\n"
"v_dot2c_f32_bf16 v109, s86, v158\n"
"v_dot2c_f32_bf16 v111, s86, v159\n"
"v_dot2c_f32_bf16 v113, s86, v160\n"
"v_dot2c_f32_bf16 v115, s86, v161\n"
"v_dot2c_f32_bf16 v117, s86, v162\n"
"v_dot2c_f32_bf16 v119, s86, v163\n"
"v_dot2c_f32_bf16 v121, s86, v164\n"
"v_dot2c_f32_bf16 v123, s86, v165\n"
"v_dot2c_f32_bf16 v104, s85, v166\n"
"v_dot2c_f32_bf16 v106, s85, v167\n"
"v_dot2c_f32_bf16 v108, s85, v168\n"
"v_dot2c_f32_bf16 v110, s85, v169\n"
"v_dot2c_f32_bf16 v112, s85, v170\n"
"v_dot2c_f32_bf16 v114, s85, v171\n"
"v_dot2c_f32_bf16 v116, s85, v172\n"
"v_dot2c_f32_bf16 v118, s85, v173\n"
"v_dot2c_f32_bf16 v120, s85, v174\n"
"v_dot2c_f32_bf16 v122, s85, v175\n"
"v_dot2c_f32_bf16 v105, s86, v166\n"
"v_dot2c_f32_bf16 v107, s86, v167\n"
"v_dot2c_f32_bf16 v109, s86, v168\n"
"v_dot2c_f32_bf16 v111, s86, v169\n"
"v_dot2c_f32_bf16 v113, s86, v170\n"
"v_dot2c_f32_bf16 v115, s86, v171\n"
"v_dot2c_f32_bf16 v117, s86, v172\n"
"v_dot2c_f32_bf16 v119, s86, v173\n"
"v_dot2c_f32_bf16 v121, s86, v174\n"
"v_dot2c_f32_bf16 v123, s86, v175\n"
"v_mul_f32 v176, s80, v104\n"
"v_mul_f32 v177, s80, v105\n"
"v_mul_f32 v178, s80, v106\n"
"v_mul_f32 v179, s80, v107\n"
"v_mul_f32 v180, s80, v108\n"
"v_mul_f32 v181, s80, v109\n"
"v_mul_f32 v182, s80, v110\n"
"v_mul_f32 v183, s80, v111\n"
"v_mul_f32 v184, s80, v112\n"
"v_mul_f32 v185, s80, v113\n"
"v_mul_f32 v186, s80, v114\n"
"v_mul_f32 v187, s80, v115\n"
"v_mul_f32 v188, s80, v116\n"
"v_mul_f32 v189, s80, v117\n"
"v_mul_f32 v190, s80, v118\n"
"v_mul_f32 v191, s80, v119\n"
"v_mul_f32 v192, s80, v120\n"
"v_mul_f32 v193, s80, v121\n"
"v_mul_f32 v194, s80, v122\n"
"v_mul_f32 v195, s80, v123\n"
"s_add_u32 s77, s3, 0\n"
"buffer_load_dwordx4 v[36:39], v1, s[36:39], s77 offen offset:0 sc1\n"
"v_exp_f32 v176, v176\n"
"v_exp_f32 v177, v177\n"
"v_exp_f32 v178, v178\n"
"v_exp_f32 v179, v179\n"
"v_exp_f32 v180, v180\n"
"buffer_load_dwordx4 v[44:47], v1, s[36:39], s77 offen offset:1024 sc1\n"
"v_exp_f32 v181, v181\n"
"v_exp_f32 v182, v182\n"
"v_exp_f32 v183, v183\n"
"v_exp_f32 v184, v184\n"
"v_exp_f32 v185, v185\n"
"v_exp_f32 v186, v186\n"
"buffer_load_dwordx2 v[52:53], v251, s[36:39], s77 offen offset:2048 sc1\n"
"v_exp_f32 v187, v187\n"
"v_exp_f32 v188, v188\n"
"v_exp_f32 v189, v189\n"
"v_exp_f32 v190, v190\n"
"v_exp_f32 v191, v191\n"
"v_exp_f32 v192, v192\n"
"s_add_u32 s77, s3, 2560\n"
"buffer_load_dwordx4 v[60:63], v1, s[36:39], s77 offen offset:0 sc1\n"
"v_exp_f32 v193, v193\n"
"v_exp_f32 v194, v194\n"
"v_exp_f32 v195, v195\n"
"v_add_f32 v176, 1.0, v176\n"
"v_add_f32 v177, 1.0, v177\n"
"v_add_f32 v178, 1.0, v178\n"
"buffer_load_dwordx4 v[68:71], v1, s[36:39], s77 offen offset:1024 sc1\n"
"v_add_f32 v179, 1.0, v179\n"
"v_add_f32 v180, 1.0, v180\n"
"v_add_f32 v181, 1.0, v181\n"
"v_add_f32 v182, 1.0, v182\n"
"v_add_f32 v183, 1.0, v183\n"
"buffer_load_dwordx2 v[76:77], v251, s[36:39], s77 offen offset:2048 sc1\n"
"v_add_f32 v184, 1.0, v184\n"
"v_add_f32 v185, 1.0, v185\n"
"v_add_f32 v186, 1.0, v186\n"
"v_add_f32 v187, 1.0, v187\n"
"v_add_f32 v188, 1.0, v188\n"
"v_add_f32 v189, 1.0, v189\n"
"s_add_u32 s77, s3, 5120\n"
"buffer_load_dwordx4 v[84:87], v1, s[36:39], s77 offen offset:0 sc1\n"
"v_add_f32 v190, 1.0, v190\n"
"v_add_f32 v191, 1.0, v191\n"
"v_add_f32 v192, 1.0, v192\n"
"v_add_f32 v193, 1.0, v193\n"
"v_add_f32 v194, 1.0, v194\n"
"v_add_f32 v195, 1.0, v195\n"
"buffer_load_dwordx4 v[92:95], v1, s[36:39], s77 offen offset:1024 sc1\n"
"v_rcp_f32 v176, v176\n"
"v_rcp_f32 v177, v177\n"
"v_rcp_f32 v178, v178\n"
"v_rcp_f32 v179, v179\n"
"v_rcp_f32 v180, v180\n"
"v_rcp_f32 v181, v181\n"
"buffer_load_dwordx2 v[100:101], v251, s[36:39], s77 offen offset:2048 sc1\n"
"v_rcp_f32 v182, v182\n"
"v_rcp_f32 v183, v183\n"
"v_rcp_f32 v184, v184\n"
"v_rcp_f32 v185, v185\n"
"v_rcp_f32 v186, v186\n"
"v_rcp_f32 v187, v187\n"
"v_rcp_f32 v188, v188\n"
"v_rcp_f32 v189, v189\n"
"v_rcp_f32 v190, v190\n"
"v_rcp_f32 v191, v191\n"
"v_rcp_f32 v192, v192\n"
"v_rcp_f32 v193, v193\n"
"v_rcp_f32 v194, v194\n"
"v_rcp_f32 v195, v195\n"
"v_mul_f32 v104, v104, v176\n"
"v_mul_f32 v105, v105, v177\n"
"v_mul_f32 v106, v106, v178\n"
"v_mul_f32 v107, v107, v179\n"
"v_mul_f32 v108, v108, v180\n"
"v_mul_f32 v109, v109, v181\n"
"v_mul_f32 v110, v110, v182\n"
"v_mul_f32 v111, v111, v183\n"
"v_mul_f32 v112, v112, v184\n"
"v_mul_f32 v113, v113, v185\n"
"v_mul_f32 v114, v114, v186\n"
"v_mul_f32 v115, v115, v187\n"
"v_mul_f32 v116, v116, v188\n"
"v_mul_f32 v117, v117, v189\n"
"v_mul_f32 v118, v118, v190\n"
"v_mul_f32 v119, v119, v191\n"
"v_mul_f32 v120, v120, v192\n"
"v_mul_f32 v121, v121, v193\n"
"v_mul_f32 v122, v122, v194\n"
"v_mul_f32 v123, v123, v195\n"
"v_lshlrev_b32 v136, 16, v124\n"
"v_and_b32 v137, 0xffff0000, v124\n"
"v_lshlrev_b32 v138, 16, v125\n"
"v_and_b32 v139, 0xffff0000, v125\n"
"v_lshlrev_b32 v140, 16, v126\n"
"v_and_b32 v141, 0xffff0000, v126\n"
"v_lshlrev_b32 v142, 16, v127\n"
"v_and_b32 v143, 0xffff0000, v127\n"
"v_lshlrev_b32 v144, 16, v128\n"
"v_and_b32 v145, 0xffff0000, v128\n"
"v_lshlrev_b32 v146, 16, v129\n"
"v_and_b32 v147, 0xffff0000, v129\n"
"v_lshlrev_b32 v148, 16, v130\n"
"v_and_b32 v149, 0xffff0000, v130\n"
"v_lshlrev_b32 v150, 16, v131\n"
"v_and_b32 v151, 0xffff0000, v131\n"
"v_lshlrev_b32 v152, 16, v132\n"
"v_and_b32 v153, 0xffff0000, v132\n"
"v_lshlrev_b32 v154, 16, v133\n"
"v_and_b32 v155, 0xffff0000, v133\n"
"s_mov_b32 s48, s30\n"
"s_and_b32 s49, s31, 0xffff\n"
"s_mov_b32 s50, 0x240000\n"
"s_mov_b32 s51, 0x20000\n"
"s_mov_b32 s55, 0x30000\n"
"s_waitcnt vmcnt(0)\n"
"v_max3_u32 v6, v36, v37, v38\n"
"v_max3_u32 v7, v69, v70, v71\n"
"v_max3_u32 v6, v6, v39, v44\n"
"v_max3_u32 v7, v7, v76, v77\n"
"v_max3_u32 v6, v6, v45, v46\n"
"v_max3_u32 v7, v7, v84, v85\n"
"v_max3_u32 v6, v6, v47, v52\n"
"v_max3_u32 v7, v7, v86, v87\n"
"v_max3_u32 v6, v6, v53, v60\n"
"v_max3_u32 v7, v7, v92, v93\n"
"v_max3_u32 v6, v6, v61, v62\n"
"v_max3_u32 v7, v7, v94, v95\n"
"v_max3_u32 v6, v6, v63, v68\n"
"v_max3_u32 v7, v7, v100, v101\n"
"v_max_u32 v6, v6, v7\n"
"v_cmp_eq_u32 vcc, -1, v6\n"
"s_nop 1\n"
"s_cbranch_vccz L_finc0\n"
"L_finr0:\n"
"s_add_u32 s77, s3, 0\n"
"buffer_load_dwordx4 v[36:39], v1, s[36:39], s77 offen offset:0 sc1\n"
"buffer_load_dwordx4 v[44:47], v1, s[36:39], s77 offen offset:1024 sc1\n"
"buffer_load_dwordx2 v[52:53], v251, s[36:39], s77 offen offset:2048 sc1\n"
"s_add_u32 s77, s3, 2560\n"
"buffer_load_dwordx4 v[60:63], v1, s[36:39], s77 offen offset:0 sc1\n"
"buffer_load_dwordx4 v[68:71], v1, s[36:39], s77 offen offset:1024 sc1\n"
"buffer_load_dwordx2 v[76:77], v251, s[36:39], s77 offen offset:2048 sc1\n"
"s_add_u32 s77, s3, 5120\n"
"buffer_load_dwordx4 v[84:87], v1, s[36:39], s77 offen offset:0 sc1\n"
"buffer_load_dwordx4 v[92:95], v1, s[36:39], s77 offen offset:1024 sc1\n"
"buffer_load_dwordx2 v[100:101], v251, s[36:39], s77 offen offset:2048 sc1\n"
"s_waitcnt vmcnt(0)\n"
"v_max3_u32 v6, v36, v37, v38\n"
"v_max3_u32 v7, v69, v70, v71\n"
"v_max3_u32 v6, v6, v39, v44\n"
"v_max3_u32 v7, v7, v76, v77\n"
"v_max3_u32 v6, v6, v45, v46\n"
"v_max3_u32 v7, v7, v84, v85\n"
"v_max3_u32 v6, v6, v47, v52\n"
"v_max3_u32 v7, v7, v86, v87\n"
"v_max3_u32 v6, v6, v53, v60\n"
"v_max3_u32 v7, v7, v92, v93\n"
"v_max3_u32 v6, v6, v61, v62\n"
"v_max3_u32 v7, v7, v94, v95\n"
"v_max3_u32 v6, v6, v63, v68\n"
"v_max3_u32 v7, v7, v100, v101\n"
"v_max_u32 v6, v6, v7\n"
"v_cmp_eq_u32 vcc, -1, v6\n"
"s_nop 1\n"
"s_cbranch_vccz L_finc0\n"
"s_sub_u32 s55, s55, 1\n"
"s_cmp_lg_u32 s55, 0\n"
"s_cbranch_scc1 L_finr0\n"
"L_finc0:\n"
"v_dot2c_f32_bf16 v32, s85, v36\n"
"v_dot2c_f32_bf16 v34, s85, v37\n"
"v_dot2c_f32_bf16 v40, s85, v38\n"
"v_dot2c_f32_bf16 v42, s85, v39\n"
"v_dot2c_f32_bf16 v48, s85, v44\n"
"v_dot2c_f32_bf16 v50, s85, v45\n"
"v_dot2c_f32_bf16 v56, s85, v46\n"
"v_dot2c_f32_bf16 v58, s85, v47\n"
"v_dot2c_f32_bf16 v64, s85, v52\n"
"v_dot2c_f32_bf16 v66, s85, v53\n"
"v_dot2c_f32_bf16 v33, s86, v36\n"
"v_dot2c_f32_bf16 v35, s86, v37\n"
"v_dot2c_f32_bf16 v41, s86, v38\n"
"v_dot2c_f32_bf16 v43, s86, v39\n"
"v_dot2c_f32_bf16 v49, s86, v44\n"
"v_dot2c_f32_bf16 v51, s86, v45\n"
"v_dot2c_f32_bf16 v57, s86, v46\n"
"v_dot2c_f32_bf16 v59, s86, v47\n"
"v_dot2c_f32_bf16 v65, s86, v52\n"
"v_dot2c_f32_bf16 v67, s86, v53\n"
"v_dot2c_f32_bf16 v32, s85, v60\n"
"v_dot2c_f32_bf16 v34, s85, v61\n"
"v_dot2c_f32_bf16 v40, s85, v62\n"
"v_dot2c_f32_bf16 v42, s85, v63\n"
"v_dot2c_f32_bf16 v48, s85, v68\n"
"v_dot2c_f32_bf16 v50, s85, v69\n"
"v_dot2c_f32_bf16 v56, s85, v70\n"
"v_dot2c_f32_bf16 v58, s85, v71\n"
"v_dot2c_f32_bf16 v64, s85, v76\n"
"v_dot2c_f32_bf16 v66, s85, v77\n"
"v_dot2c_f32_bf16 v33, s86, v60\n"
"v_dot2c_f32_bf16 v35, s86, v61\n"
"v_dot2c_f32_bf16 v41, s86, v62\n"
"v_dot2c_f32_bf16 v43, s86, v63\n"
"v_dot2c_f32_bf16 v49, s86, v68\n"
"v_dot2c_f32_bf16 v51, s86, v69\n"
"v_dot2c_f32_bf16 v57, s86, v70\n"
"v_dot2c_f32_bf16 v59, s86, v71\n"
"v_dot2c_f32_bf16 v65, s86, v76\n"
"v_dot2c_f32_bf16 v67, s86, v77\n"
"v_dot2c_f32_bf16 v32, s85, v84\n"
"v_dot2c_f32_bf16 v34, s85, v85\n"
"v_dot2c_f32_bf16 v40, s85, v86\n"
"v_dot2c_f32_bf16 v42, s85, v87\n"
"v_dot2c_f32_bf16 v48, s85, v92\n"
"v_dot2c_f32_bf16 v50, s85, v93\n"
"v_dot2c_f32_bf16 v56, s85, v94\n"
"v_dot2c_f32_bf16 v58, s85, v95\n"
"v_dot2c_f32_bf16 v64, s85, v100\n"
"v_dot2c_f32_bf16 v66, s85, v101\n"
"v_dot2c_f32_bf16 v33, s86, v84\n"
"v_dot2c_f32_bf16 v35, s86, v85\n"
"v_dot2c_f32_bf16 v41, s86, v86\n"
"v_dot2c_f32_bf16 v43, s86, v87\n"
"v_dot2c_f32_bf16 v49, s86, v92\n"
"v_dot2c_f32_bf16 v51, s86, v93\n"
"v_dot2c_f32_bf16 v57, s86, v94\n"
"v_dot2c_f32_bf16 v59, s86, v95\n"
"v_dot2c_f32_bf16 v65, s86, v100\n"
"v_dot2c_f32_bf16 v67, s86, v101\n"
"v_fmac_f32 v136, v32, v104\n"
"v_fmac_f32 v137, v33, v105\n"
"v_fmac_f32 v138, v34, v106\n"
"v_fmac_f32 v139, v35, v107\n"
"v_fmac_f32 v140, v40, v108\n"
"v_fmac_f32 v141, v41, v109\n"
"v_fmac_f32 v142, v42, v110\n"
"v_fmac_f32 v143, v43, v111\n"
"v_fmac_f32 v144, v48, v112\n"
"v_fmac_f32 v145, v49, v113\n"
"v_fmac_f32 v146, v50, v114\n"
"v_fmac_f32 v147, v51, v115\n"
"v_fmac_f32 v148, v56, v116\n"
"v_fmac_f32 v149, v57, v117\n"
"v_fmac_f32 v150, v58, v118\n"
"v_fmac_f32 v151, v59, v119\n"
"v_fmac_f32 v152, v64, v120\n"
"v_fmac_f32 v153, v65, v121\n"
"v_fmac_f32 v154, v66, v122\n"
"v_fmac_f32 v155, v67, v123\n"
"v_cvt_pk_bf16_f32 v176, v136, v137\n"
"v_cvt_pk_bf16_f32 v177, v138, v139\n"
"buffer_store_dwordx2 v[176:177], v16, s[48:51], s84 offen offset:0\n"
"v_cvt_pk_bf16_f32 v178, v140, v141\n"
"v_cvt_pk_bf16_f32 v179, v142, v143\n"
"buffer_store_dwordx2 v[178:179], v16, s[48:51], s84 offen offset:32\n"
"v_cvt_pk_bf16_f32 v180, v144, v145\n"
"v_cvt_pk_bf16_f32 v181, v146, v147\n"
"buffer_store_dwordx2 v[180:181], v16, s[48:51], s84 offen offset:64\n"
"v_cvt_pk_bf16_f32 v182, v148, v149\n"
"v_cvt_pk_bf16_f32 v183, v150, v151\n"
"buffer_store_dwordx2 v[182:183], v16, s[48:51], s84 offen offset:96\n"
"v_cvt_pk_bf16_f32 v184, v152, v153\n"
"v_cvt_pk_bf16_f32 v185, v154, v155\n"
"buffer_store_dwordx2 v[184:185], v16, s[48:51], s84 offen offset:128\n"
"s_branch L_end\n"
"L_fin1:\n"
"s_waitcnt vmcnt(18) lgkmcnt(0)\n"
"v_mfma_f32_16x16x32_bf16 v[32:35], a[0:3], a[224:227], v[32:35]\n"
"ds_read_b64_tr_b16 a[36:37], v20 offset:9216\n"
"ds_read_b64_tr_b16 a[38:39], v20 offset:13824\n"
"v_mfma_f32_16x16x32_bf16 v[40:43], a[4:7], a[224:227], v[40:43]\n"
"ds_read_b64_tr_b16 a[40:41], v21 offset:9216\n"
"ds_read_b64_tr_b16 a[42:43], v21 offset:13824\n"
"v_mfma_f32_16x16x32_bf16 v[48:51], a[8:11], a[224:227], v[48:51]\n"
"ds_read_b64_tr_b16 a[44:45], v20 offset:9280\n"
"ds_read_b64_tr_b16 a[46:47], v20 offset:13888\n"
"v_mfma_f32_16x16x32_bf16 v[56:59], a[12:15], a[224:227], v[56:59]\n"
"ds_read_b64_tr_b16 a[48:49], v21 offset:9280\n"
"ds_read_b64_tr_b16 a[50:51], v21 offset:13888\n"
"v_mfma_f32_16x16x32_bf16 v[64:67], a[16:19], a[224:227], v[64:67]\n"
"ds_read_b64_tr_b16 a[52:53], v20 offset:9344\n"
"ds_read_b64_tr_b16 a[54:55], v20 offset:13952\n"
"v_mfma_f32_16x16x32_bf16 v[76:79], a[20:23], a[228:231], v[76:79]\n"
"ds_read_b64_tr_b16 a[56:57], v21 offset:9344\n"
"ds_read_b64_tr_b16 a[58:59], v21 offset:13952\n"
"v_mfma_f32_16x16x32_bf16 v[84:87], a[24:27], a[228:231], v[84:87]\n"
"ds_read_b64_tr_b16 a[60:61], v20 offset:9408\n"
"ds_read_b64_tr_b16 a[62:63], v20 offset:14016\n"
"v_mfma_f32_16x16x32_bf16 v[92:95], a[28:31], a[228:231], v[92:95]\n"
"ds_read_b64_tr_b16 a[64:65], v21 offset:9408\n"
"ds_read_b64_tr_b16 a[66:67], v21 offset:14016\n"
"v_mfma_f32_16x16x32_bf16 v[100:103], a[32:35], a[228:231], v[100:103]\n"
"ds_read_b64_tr_b16 a[68:69], v20 offset:9472\n"
"ds_read_b64_tr_b16 a[70:71], v20 offset:14080\n"
"v_mfma_f32_16x16x32_bf16 v[72:75], a[20:23], a[224:227], v[72:75]\n"
"v_mfma_f32_16x16x32_bf16 v[80:83], a[24:27], a[224:227], v[80:83]\n"
"v_mfma_f32_16x16x32_bf16 v[88:91], a[28:31], a[224:227], v[88:91]\n"
"v_mfma_f32_16x16x32_bf16 v[96:99], a[32:35], a[224:227], v[96:99]\n"
"s_waitcnt lgkmcnt(0)\n"
"v_mfma_f32_16x16x32_bf16 v[32:35], a[36:39], a[232:235], v[32:35]\n"
"v_mfma_f32_16x16x32_bf16 v[40:43], a[40:43], a[232:235], v[40:43]\n"
"v_mfma_f32_16x16x32_bf16 v[48:51], a[44:47], a[232:235], v[48:51]\n"
"v_mfma_f32_16x16x32_bf16 v[56:59], a[48:51], a[232:235], v[56:59]\n"
"v_mfma_f32_16x16x32_bf16 v[64:67], a[52:55], a[232:235], v[64:67]\n"
"v_mfma_f32_16x16x32_bf16 v[76:79], a[56:59], a[236:239], v[76:79]\n"
"v_mfma_f32_16x16x32_bf16 v[84:87], a[60:63], a[236:239], v[84:87]\n"
"v_mfma_f32_16x16x32_bf16 v[92:95], a[64:67], a[236:239], v[92:95]\n"
"s_nop 0\n"
"v_cvt_pk_bf16_f32 v196, v32, v33\n"
"v_cvt_pk_bf16_f32 v197, v34, v35\n"
"v_mfma_f32_16x16x32_bf16 v[100:103], a[68:71], a[236:239], v[100:103]\n"
"v_cvt_pk_bf16_f32 v198, v40, v41\n"
"v_mfma_f32_16x16x32_bf16 v[72:75], a[56:59], a[232:235], v[72:75]\n"
"v_cvt_pk_bf16_f32 v199, v42, v43\n"
"v_mfma_f32_16x16x32_bf16 v[80:83], a[60:63], a[232:235], v[80:83]\n"
"v_cvt_pk_bf16_f32 v200, v48, v49\n"
"v_mfma_f32_16x16x32_bf16 v[88:91], a[64:67], a[232:235], v[88:91]\n"
"v_cvt_pk_bf16_f32 v201, v50, v51\n"
"v_mfma_f32_16x16x32_bf16 v[96:99], a[68:71], a[232:235], v[96:99]\n"
"v_cvt_pk_bf16_f32 v202, v56, v57\n"
"v_mfma_f32_16x16x32_bf16 v[36:39], a[0:3], a[228:231], v[36:39]\n"
"v_cvt_pk_bf16_f32 v203, v58, v59\n"
"v_mfma_f32_16x16x32_bf16 v[44:47], a[4:7], a[228:231], v[44:47]\n"
"v_cvt_pk_bf16_f32 v204, v64, v65\n"
"v_mfma_f32_16x16x32_bf16 v[52:55], a[8:11], a[228:231], v[52:55]\n"
"v_cvt_pk_bf16_f32 v208, v76, v77\n"
"v_cvt_pk_bf16_f32 v205, v66, v67\n"
"v_mfma_f32_16x16x32_bf16 v[60:63], a[12:15], a[228:231], v[60:63]\n"
"v_cvt_pk_bf16_f32 v209, v78, v79\n"
"v_mfma_f32_16x16x32_bf16 v[68:71], a[16:19], a[228:231], v[68:71]\n"
"s_add_u32 s77, s3, 0\n"
"buffer_store_dwordx4 v[196:199], v1, s[36:39], s77 offen offset:0 sc1\n"
"v_mfma_f32_16x16x32_bf16 v[36:39], a[36:39], a[236:239], v[36:39]\n"
"v_cvt_pk_bf16_f32 v210, v84, v85\n"
"buffer_store_dwordx4 v[200:203], v1, s[36:39], s77 offen offset:1024 sc1\n"
"buffer_store_dwordx2 v[204:205], v251, s[36:39], s77 offen offset:2048 sc1\n"
"v_mfma_f32_16x16x32_bf16 v[44:47], a[40:43], a[236:239], v[44:47]\n"
"v_cvt_pk_bf16_f32 v211, v86, v87\n"
"v_cvt_pk_bf16_f32 v212, v92, v93\n"
"v_mfma_f32_16x16x32_bf16 v[52:55], a[44:47], a[236:239], v[52:55]\n"
"v_cvt_pk_bf16_f32 v213, v94, v95\n"
"v_mfma_f32_16x16x32_bf16 v[60:63], a[48:51], a[236:239], v[60:63]\n"
"v_cvt_pk_bf16_f32 v214, v100, v101\n"
"v_mfma_f32_16x16x32_bf16 v[68:71], a[52:55], a[236:239], v[68:71]\n"
"v_cvt_pk_bf16_f32 v215, v102, v103\n"
"s_add_u32 s77, s3, 100864\n"
"buffer_store_dwordx4 v[208:211], v1, s[36:39], s77 offen offset:0 sc1\n"
"buffer_store_dwordx4 v[212:215], v1, s[36:39], s77 offen offset:1024 sc1\n"
"v_cvt_pk_bf16_f32 v216, v72, v73\n"
"v_cvt_pk_bf16_f32 v217, v74, v75\n"
"v_cvt_pk_bf16_f32 v218, v80, v81\n"
"v_cvt_pk_bf16_f32 v219, v82, v83\n"
"v_cvt_pk_bf16_f32 v220, v88, v89\n"
"v_cvt_pk_bf16_f32 v221, v90, v91\n"
"v_cvt_pk_bf16_f32 v222, v96, v97\n"
"v_cvt_pk_bf16_f32 v223, v98, v99\n"
"s_add_u32 s77, s3, 70656\n"
"buffer_store_dwordx4 v[216:219], v1, s[36:39], s77 offen offset:0 sc1\n"
"s_waitcnt vmcnt(6)\n"
"s_mov_b32 s85, 0x3f80\n"
"s_mov_b32 s86, 0x3f800000\n"
"v_lshlrev_b32 v134, 16, v134\n"
"s_mov_b32 s80, 0xbfb8aa3b\n"
"v_mov_b32 v104, v134\n"
"v_mov_b32 v105, v134\n"
"v_mov_b32 v106, v134\n"
"v_mov_b32 v107, v134\n"
"v_mov_b32 v108, v134\n"
"v_mov_b32 v109, v134\n"
"v_mov_b32 v110, v134\n"
"v_mov_b32 v111, v134\n"
"v_mov_b32 v112, v134\n"
"v_mov_b32 v113, v134\n"
"v_mov_b32 v114, v134\n"
"v_mov_b32 v115, v134\n"
"v_mov_b32 v116, v134\n"
"buffer_store_dwordx4 v[220:223], v1, s[36:39], s77 offen offset:1024 sc1\n"
"v_mov_b32 v117, v134\n"
"v_mov_b32 v118, v134\n"
"v_mov_b32 v119, v134\n"
"v_mov_b32 v120, v134\n"
"v_mov_b32 v121, v134\n"
"v_mov_b32 v122, v134\n"
"v_mov_b32 v123, v134\n"
"v_dot2c_f32_bf16 v104, s85, v136\n"
"v_dot2c_f32_bf16 v106, s85, v137\n"
"v_dot2c_f32_bf16 v108, s85, v138\n"
"v_dot2c_f32_bf16 v110, s85, v139\n"
"v_dot2c_f32_bf16 v112, s85, v140\n"
"v_dot2c_f32_bf16 v114, s85, v141\n"
"v_dot2c_f32_bf16 v116, s85, v142\n"
"v_dot2c_f32_bf16 v118, s85, v143\n"
"v_dot2c_f32_bf16 v120, s85, v144\n"
"v_dot2c_f32_bf16 v122, s85, v145\n"
"v_dot2c_f32_bf16 v105, s86, v136\n"
"v_dot2c_f32_bf16 v107, s86, v137\n"
"v_dot2c_f32_bf16 v109, s86, v138\n"
"v_dot2c_f32_bf16 v111, s86, v139\n"
"v_dot2c_f32_bf16 v113, s86, v140\n"
"v_dot2c_f32_bf16 v115, s86, v141\n"
"v_dot2c_f32_bf16 v117, s86, v142\n"
"v_dot2c_f32_bf16 v119, s86, v143\n"
"v_dot2c_f32_bf16 v121, s86, v144\n"
"v_dot2c_f32_bf16 v123, s86, v145\n"
"v_dot2c_f32_bf16 v104, s85, v146\n"
"v_dot2c_f32_bf16 v106, s85, v147\n"
"v_dot2c_f32_bf16 v108, s85, v148\n"
"v_dot2c_f32_bf16 v110, s85, v149\n"
"v_dot2c_f32_bf16 v112, s85, v150\n"
"v_dot2c_f32_bf16 v114, s85, v151\n"
"v_dot2c_f32_bf16 v116, s85, v152\n"
"v_dot2c_f32_bf16 v118, s85, v153\n"
"v_dot2c_f32_bf16 v120, s85, v154\n"
"v_dot2c_f32_bf16 v122, s85, v155\n"
"v_dot2c_f32_bf16 v105, s86, v146\n"
"v_dot2c_f32_bf16 v107, s86, v147\n"
"v_dot2c_f32_bf16 v109, s86, v148\n"
"v_dot2c_f32_bf16 v111, s86, v149\n"
"v_dot2c_f32_bf16 v113, s86, v150\n"
"v_dot2c_f32_bf16 v115, s86, v151\n"
"v_dot2c_f32_bf16 v117, s86, v152\n"
"v_dot2c_f32_bf16 v119, s86, v153\n"
"v_dot2c_f32_bf16 v121, s86, v154\n"
"v_dot2c_f32_bf16 v123, s86, v155\n"
"v_dot2c_f32_bf16 v104, s85, v156\n"
"v_dot2c_f32_bf16 v106, s85, v157\n"
"v_dot2c_f32_bf16 v108, s85, v158\n"
"v_dot2c_f32_bf16 v110, s85, v159\n"
"v_dot2c_f32_bf16 v112, s85, v160\n"
"v_dot2c_f32_bf16 v114, s85, v161\n"
"v_dot2c_f32_bf16 v116, s85, v162\n"
"v_dot2c_f32_bf16 v118, s85, v163\n"
"v_dot2c_f32_bf16 v120, s85, v164\n"
"v_dot2c_f32_bf16 v122, s85, v165\n"
"v_dot2c_f32_bf16 v105, s86, v156\n"
"v_dot2c_f32_bf16 v107, s86, v157\n"
"v_dot2c_f32_bf16 v109, s86, v158\n"
"v_dot2c_f32_bf16 v111, s86, v159\n"
"v_dot2c_f32_bf16 v113, s86, v160\n"
"v_dot2c_f32_bf16 v115, s86, v161\n"
"v_dot2c_f32_bf16 v117, s86, v162\n"
"v_dot2c_f32_bf16 v119, s86, v163\n"
"v_dot2c_f32_bf16 v121, s86, v164\n"
"v_dot2c_f32_bf16 v123, s86, v165\n"
"v_dot2c_f32_bf16 v104, s85, v166\n"
"v_dot2c_f32_bf16 v106, s85, v167\n"
"v_dot2c_f32_bf16 v108, s85, v168\n"
"v_dot2c_f32_bf16 v110, s85, v169\n"
"v_dot2c_f32_bf16 v112, s85, v170\n"
"v_dot2c_f32_bf16 v114, s85, v171\n"
"v_dot2c_f32_bf16 v116, s85, v172\n"
"v_dot2c_f32_bf16 v118, s85, v173\n"
"v_dot2c_f32_bf16 v120, s85, v174\n"
"v_dot2c_f32_bf16 v122, s85, v175\n"
"v_dot2c_f32_bf16 v105, s86, v166\n"
"v_dot2c_f32_bf16 v107, s86, v167\n"
"v_dot2c_f32_bf16 v109, s86, v168\n"
"v_dot2c_f32_bf16 v111, s86, v169\n"
"v_dot2c_f32_bf16 v113, s86, v170\n"
"v_dot2c_f32_bf16 v115, s86, v171\n"
"v_dot2c_f32_bf16 v117, s86, v172\n"
"v_dot2c_f32_bf16 v119, s86, v173\n"
"v_dot2c_f32_bf16 v121, s86, v174\n"
"v_dot2c_f32_bf16 v123, s86, v175\n"
"v_mul_f32 v176, s80, v104\n"
"v_mul_f32 v177, s80, v105\n"
"v_mul_f32 v178, s80, v106\n"
"v_mul_f32 v179, s80, v107\n"
"v_mul_f32 v180, s80, v108\n"
"v_mul_f32 v181, s80, v109\n"
"v_mul_f32 v182, s80, v110\n"
"v_mul_f32 v183, s80, v111\n"
"v_mul_f32 v184, s80, v112\n"
"v_mul_f32 v185, s80, v113\n"
"v_mul_f32 v186, s80, v114\n"
"v_mul_f32 v187, s80, v115\n"
"v_mul_f32 v188, s80, v116\n"
"v_mul_f32 v189, s80, v117\n"
"v_mul_f32 v190, s80, v118\n"
"v_mul_f32 v191, s80, v119\n"
"v_mul_f32 v192, s80, v120\n"
"v_mul_f32 v193, s80, v121\n"
"v_mul_f32 v194, s80, v122\n"
"v_mul_f32 v195, s80, v123\n"
"s_add_u32 s77, s3, 32768\n"
"buffer_load_dwordx4 v[32:35], v1, s[36:39], s77 offen offset:0 sc1\n"
"v_exp_f32 v176, v176\n"
"v_exp_f32 v177, v177\n"
"v_exp_f32 v178, v178\n"
"v_exp_f32 v179, v179\n"
"v_exp_f32 v180, v180\n"
"buffer_load_dwordx4 v[40:43], v1, s[36:39], s77 offen offset:1024 sc1\n"
"v_exp_f32 v181, v181\n"
"v_exp_f32 v182, v182\n"
"v_exp_f32 v183, v183\n"
"v_exp_f32 v184, v184\n"
"v_exp_f32 v185, v185\n"
"v_exp_f32 v186, v186\n"
"buffer_load_dwordx2 v[48:49], v251, s[36:39], s77 offen offset:2048 sc1\n"
"v_exp_f32 v187, v187\n"
"v_exp_f32 v188, v188\n"
"v_exp_f32 v189, v189\n"
"v_exp_f32 v190, v190\n"
"v_exp_f32 v191, v191\n"
"v_exp_f32 v192, v192\n"
"s_add_u32 s77, s3, 35328\n"
"buffer_load_dwordx4 v[56:59], v1, s[36:39], s77 offen offset:0 sc1\n"
"v_exp_f32 v193, v193\n"
"v_exp_f32 v194, v194\n"
"v_exp_f32 v195, v195\n"
"v_add_f32 v176, 1.0, v176\n"
"v_add_f32 v177, 1.0, v177\n"
"v_add_f32 v178, 1.0, v178\n"
"buffer_load_dwordx4 v[64:67], v1, s[36:39], s77 offen offset:1024 sc1\n"
"v_add_f32 v179, 1.0, v179\n"
"v_add_f32 v180, 1.0, v180\n"
"v_add_f32 v181, 1.0, v181\n"
"v_add_f32 v182, 1.0, v182\n"
"v_add_f32 v183, 1.0, v183\n"
"buffer_load_dwordx2 v[72:73], v251, s[36:39], s77 offen offset:2048 sc1\n"
"v_add_f32 v184, 1.0, v184\n"
"v_add_f32 v185, 1.0, v185\n"
"v_add_f32 v186, 1.0, v186\n"
"v_add_f32 v187, 1.0, v187\n"
"v_add_f32 v188, 1.0, v188\n"
"v_add_f32 v189, 1.0, v189\n"
"s_add_u32 s77, s3, 37888\n"
"buffer_load_dwordx4 v[80:83], v1, s[36:39], s77 offen offset:0 sc1\n"
"v_add_f32 v190, 1.0, v190\n"
"v_add_f32 v191, 1.0, v191\n"
"v_add_f32 v192, 1.0, v192\n"
"v_add_f32 v193, 1.0, v193\n"
"v_add_f32 v194, 1.0, v194\n"
"v_add_f32 v195, 1.0, v195\n"
"buffer_load_dwordx4 v[88:91], v1, s[36:39], s77 offen offset:1024 sc1\n"
"v_rcp_f32 v176, v176\n"
"v_rcp_f32 v177, v177\n"
"v_rcp_f32 v178, v178\n"
"v_rcp_f32 v179, v179\n"
"v_rcp_f32 v180, v180\n"
"v_rcp_f32 v181, v181\n"
"buffer_load_dwordx2 v[96:97], v251, s[36:39], s77 offen offset:2048 sc1\n"
"v_rcp_f32 v182, v182\n"
"v_rcp_f32 v183, v183\n"
"v_rcp_f32 v184, v184\n"
"v_rcp_f32 v185, v185\n"
"v_rcp_f32 v186, v186\n"
"v_rcp_f32 v187, v187\n"
"v_rcp_f32 v188, v188\n"
"v_rcp_f32 v189, v189\n"
"v_rcp_f32 v190, v190\n"
"v_rcp_f32 v191, v191\n"
"v_rcp_f32 v192, v192\n"
"v_rcp_f32 v193, v193\n"
"v_rcp_f32 v194, v194\n"
"v_rcp_f32 v195, v195\n"
"v_mul_f32 v104, v104, v176\n"
"v_mul_f32 v105, v105, v177\n"
"v_mul_f32 v106, v106, v178\n"
"v_mul_f32 v107, v107, v179\n"
"v_mul_f32 v108, v108, v180\n"
"v_mul_f32 v109, v109, v181\n"
"v_mul_f32 v110, v110, v182\n"
"v_mul_f32 v111, v111, v183\n"
"v_mul_f32 v112, v112, v184\n"
"v_mul_f32 v113, v113, v185\n"
"v_mul_f32 v114, v114, v186\n"
"v_mul_f32 v115, v115, v187\n"
"v_mul_f32 v116, v116, v188\n"
"v_mul_f32 v117, v117, v189\n"
"v_mul_f32 v118, v118, v190\n"
"v_mul_f32 v119, v119, v191\n"
"v_mul_f32 v120, v120, v192\n"
"v_mul_f32 v121, v121, v193\n"
"v_mul_f32 v122, v122, v194\n"
"v_mul_f32 v123, v123, v195\n"
"v_lshlrev_b32 v136, 16, v124\n"
"v_and_b32 v137, 0xffff0000, v124\n"
"v_lshlrev_b32 v138, 16, v125\n"
"v_and_b32 v139, 0xffff0000, v125\n"
"v_lshlrev_b32 v140, 16, v126\n"
"v_and_b32 v141, 0xffff0000, v126\n"
"v_lshlrev_b32 v142, 16, v127\n"
"v_and_b32 v143, 0xffff0000, v127\n"
"v_lshlrev_b32 v144, 16, v128\n"
"v_and_b32 v145, 0xffff0000, v128\n"
"v_lshlrev_b32 v146, 16, v129\n"
"v_and_b32 v147, 0xffff0000, v129\n"
"v_lshlrev_b32 v148, 16, v130\n"
"v_and_b32 v149, 0xffff0000, v130\n"
"v_lshlrev_b32 v150, 16, v131\n"
"v_and_b32 v151, 0xffff0000, v131\n"
"v_lshlrev_b32 v152, 16, v132\n"
"v_and_b32 v153, 0xffff0000, v132\n"
"v_lshlrev_b32 v154, 16, v133\n"
"v_and_b32 v155, 0xffff0000, v133\n"
"s_mov_b32 s48, s30\n"
"s_and_b32 s49, s31, 0xffff\n"
"s_mov_b32 s50, 0x240000\n"
"s_mov_b32 s51, 0x20000\n"
"s_mov_b32 s55, 0x30000\n"
"s_waitcnt vmcnt(0)\n"
"v_max3_u32 v6, v32, v33, v34\n"
"v_max3_u32 v7, v65, v66, v67\n"
"v_max3_u32 v6, v6, v35, v40\n"
"v_max3_u32 v7, v7, v72, v73\n"
"v_max3_u32 v6, v6, v41, v42\n"
"v_max3_u32 v7, v7, v80, v81\n"
"v_max3_u32 v6, v6, v43, v48\n"
"v_max3_u32 v7, v7, v82, v83\n"
"v_max3_u32 v6, v6, v49, v56\n"
"v_max3_u32 v7, v7, v88, v89\n"
"v_max3_u32 v6, v6, v57, v58\n"
"v_max3_u32 v7, v7, v90, v91\n"
"v_max3_u32 v6, v6, v59, v64\n"
"v_max3_u32 v7, v7, v96, v97\n"
"v_max_u32 v6, v6, v7\n"
"v_cmp_eq_u32 vcc, -1, v6\n"
"s_nop 1\n"
"s_cbranch_vccz L_finc1\n"
"L_finr1:\n"
"s_add_u32 s77, s3, 32768\n"
"buffer_load_dwordx4 v[32:35], v1, s[36:39], s77 offen offset:0 sc1\n"
"buffer_load_dwordx4 v[40:43], v1, s[36:39], s77 offen offset:1024 sc1\n"
"buffer_load_dwordx2 v[48:49], v251, s[36:39], s77 offen offset:2048 sc1\n"
"s_add_u32 s77, s3, 35328\n"
"buffer_load_dwordx4 v[56:59], v1, s[36:39], s77 offen offset:0 sc1\n"
"buffer_load_dwordx4 v[64:67], v1, s[36:39], s77 offen offset:1024 sc1\n"
"buffer_load_dwordx2 v[72:73], v251, s[36:39], s77 offen offset:2048 sc1\n"
"s_add_u32 s77, s3, 37888\n"
"buffer_load_dwordx4 v[80:83], v1, s[36:39], s77 offen offset:0 sc1\n"
"buffer_load_dwordx4 v[88:91], v1, s[36:39], s77 offen offset:1024 sc1\n"
"buffer_load_dwordx2 v[96:97], v251, s[36:39], s77 offen offset:2048 sc1\n"
"s_waitcnt vmcnt(0)\n"
"v_max3_u32 v6, v32, v33, v34\n"
"v_max3_u32 v7, v65, v66, v67\n"
"v_max3_u32 v6, v6, v35, v40\n"
"v_max3_u32 v7, v7, v72, v73\n"
"v_max3_u32 v6, v6, v41, v42\n"
"v_max3_u32 v7, v7, v80, v81\n"
"v_max3_u32 v6, v6, v43, v48\n"
"v_max3_u32 v7, v7, v82, v83\n"
"v_max3_u32 v6, v6, v49, v56\n"
"v_max3_u32 v7, v7, v88, v89\n"
"v_max3_u32 v6, v6, v57, v58\n"
"v_max3_u32 v7, v7, v90, v91\n"
"v_max3_u32 v6, v6, v59, v64\n"
"v_max3_u32 v7, v7, v96, v97\n"
"v_max_u32 v6, v6, v7\n"
"v_cmp_eq_u32 vcc, -1, v6\n"
"s_nop 1\n"
"s_cbranch_vccz L_finc1\n"
"s_sub_u32 s55, s55, 1\n"
"s_cmp_lg_u32 s55, 0\n"
"s_cbranch_scc1 L_finr1\n"
"L_finc1:\n"
"v_dot2c_f32_bf16 v36, s85, v32\n"
"v_dot2c_f32_bf16 v38, s85, v33\n"
"v_dot2c_f32_bf16 v44, s85, v34\n"
"v_dot2c_f32_bf16 v46, s85, v35\n"
"v_dot2c_f32_bf16 v52, s85, v40\n"
"v_dot2c_f32_bf16 v54, s85, v41\n"
"v_dot2c_f32_bf16 v60, s85, v42\n"
"v_dot2c_f32_bf16 v62, s85, v43\n"
"v_dot2c_f32_bf16 v68, s85, v48\n"
"v_dot2c_f32_bf16 v70, s85, v49\n"
"v_dot2c_f32_bf16 v37, s86, v32\n"
"v_dot2c_f32_bf16 v39, s86, v33\n"
"v_dot2c_f32_bf16 v45, s86, v34\n"
"v_dot2c_f32_bf16 v47, s86, v35\n"
"v_dot2c_f32_bf16 v53, s86, v40\n"
"v_dot2c_f32_bf16 v55, s86, v41\n"
"v_dot2c_f32_bf16 v61, s86, v42\n"
"v_dot2c_f32_bf16 v63, s86, v43\n"
"v_dot2c_f32_bf16 v69, s86, v48\n"
"v_dot2c_f32_bf16 v71, s86, v49\n"
"v_dot2c_f32_bf16 v36, s85, v56\n"
"v_dot2c_f32_bf16 v38, s85, v57\n"
"v_dot2c_f32_bf16 v44, s85, v58\n"
"v_dot2c_f32_bf16 v46, s85, v59\n"
"v_dot2c_f32_bf16 v52, s85, v64\n"
"v_dot2c_f32_bf16 v54, s85, v65\n"
"v_dot2c_f32_bf16 v60, s85, v66\n"
"v_dot2c_f32_bf16 v62, s85, v67\n"
"v_dot2c_f32_bf16 v68, s85, v72\n"
"v_dot2c_f32_bf16 v70, s85, v73\n"
"v_dot2c_f32_bf16 v37, s86, v56\n"
"v_dot2c_f32_bf16 v39, s86, v57\n"
"v_dot2c_f32_bf16 v45, s86, v58\n"
"v_dot2c_f32_bf16 v47, s86, v59\n"
"v_dot2c_f32_bf16 v53, s86, v64\n"
"v_dot2c_f32_bf16 v55, s86, v65\n"
"v_dot2c_f32_bf16 v61, s86, v66\n"
"v_dot2c_f32_bf16 v63, s86, v67\n"
"v_dot2c_f32_bf16 v69, s86, v72\n"
"v_dot2c_f32_bf16 v71, s86, v73\n"
"v_dot2c_f32_bf16 v36, s85, v80\n"
"v_dot2c_f32_bf16 v38, s85, v81\n"
"v_dot2c_f32_bf16 v44, s85, v82\n"
"v_dot2c_f32_bf16 v46, s85, v83\n"
"v_dot2c_f32_bf16 v52, s85, v88\n"
"v_dot2c_f32_bf16 v54, s85, v89\n"
"v_dot2c_f32_bf16 v60, s85, v90\n"
"v_dot2c_f32_bf16 v62, s85, v91\n"
"v_dot2c_f32_bf16 v68, s85, v96\n"
"v_dot2c_f32_bf16 v70, s85, v97\n"
"v_dot2c_f32_bf16 v37, s86, v80\n"
"v_dot2c_f32_bf16 v39, s86, v81\n"
"v_dot2c_f32_bf16 v45, s86, v82\n"
"v_dot2c_f32_bf16 v47, s86, v83\n"
"v_dot2c_f32_bf16 v53, s86, v88\n"
"v_dot2c_f32_bf16 v55, s86, v89\n"
"v_dot2c_f32_bf16 v61, s86, v90\n"
"v_dot2c_f32_bf16 v63, s86, v91\n"
"v_dot2c_f32_bf16 v69, s86, v96\n"
"v_dot2c_f32_bf16 v71, s86, v97\n"
"v_fmac_f32 v136, v36, v104\n"
"v_fmac_f32 v137, v37, v105\n"
"v_fmac_f32 v138, v38, v106\n"
"v_fmac_f32 v139, v39, v107\n"
"v_fmac_f32 v140, v44, v108\n"
"v_fmac_f32 v141, v45, v109\n"
"v_fmac_f32 v142, v46, v110\n"
"v_fmac_f32 v143, v47, v111\n"
"v_fmac_f32 v144, v52, v112\n"
"v_fmac_f32 v145, v53, v113\n"
"v_fmac_f32 v146, v54, v114\n"
"v_fmac_f32 v147, v55, v115\n"
"v_fmac_f32 v148, v60, v116\n"
"v_fmac_f32 v149, v61, v117\n"
"v_fmac_f32 v150, v62, v118\n"
"v_fmac_f32 v151, v63, v119\n"
"v_fmac_f32 v152, v68, v120\n"
"v_fmac_f32 v153, v69, v121\n"
"v_fmac_f32 v154, v70, v122\n"
"v_fmac_f32 v155, v71, v123\n"
"v_cvt_pk_bf16_f32 v176, v136, v137\n"
"v_cvt_pk_bf16_f32 v177, v138, v139\n"
"buffer_store_dwordx2 v[176:177], v16, s[48:51], s84 offen offset:0\n"
"v_cvt_pk_bf16_f32 v178, v140, v141\n"
"v_cvt_pk_bf16_f32 v179, v142, v143\n"
"buffer_store_dwordx2 v[178:179], v16, s[48:51], s84 offen offset:32\n"
"v_cvt_pk_bf16_f32 v180, v144, v145\n"
"v_cvt_pk_bf16_f32 v181, v146, v147\n"
"buffer_store_dwordx2 v[180:181], v16, s[48:51], s84 offen offset:64\n"
"v_cvt_pk_bf16_f32 v182, v148, v149\n"
"v_cvt_pk_bf16_f32 v183, v150, v151\n"
"buffer_store_dwordx2 v[182:183], v16, s[48:51], s84 offen offset:96\n"
"v_cvt_pk_bf16_f32 v184, v152, v153\n"
"v_cvt_pk_bf16_f32 v185, v154, v155\n"
"buffer_store_dwordx2 v[184:185], v16, s[48:51], s84 offen offset:128\n"
"s_branch L_end\n"
"L_fin2:\n"
"s_waitcnt vmcnt(18) lgkmcnt(0)\n"
"v_mfma_f32_16x16x32_bf16 v[76:79], a[20:23], a[228:231], v[76:79]\n"
"ds_read_b64_tr_b16 a[36:37], v20 offset:9216\n"
"ds_read_b64_tr_b16 a[38:39], v20 offset:13824\n"
"v_mfma_f32_16x16x32_bf16 v[84:87], a[24:27], a[228:231], v[84:87]\n"
"ds_read_b64_tr_b16 a[40:41], v21 offset:9216\n"
"ds_read_b64_tr_b16 a[42:43], v21 offset:13824\n"
"v_mfma_f32_16x16x32_bf16 v[92:95], a[28:31], a[228:231], v[92:95]\n"
"ds_read_b64_tr_b16 a[44:45], v20 offset:9280\n"
"ds_read_b64_tr_b16 a[46:47], v20 offset:13888\n"
"v_mfma_f32_16x16x32_bf16 v[100:103], a[32:35], a[228:231], v[100:103]\n"
"ds_read_b64_tr_b16 a[48:49], v21 offset:9280\n"
"ds_read_b64_tr_b16 a[50:51], v21 offset:13888\n"
"v_mfma_f32_16x16x32_bf16 v[32:35], a[0:3], a[224:227], v[32:35]\n"
"ds_read_b64_tr_b16 a[52:53], v20 offset:9344\n"
"ds_read_b64_tr_b16 a[54:55], v20 offset:13952\n"
"v_mfma_f32_16x16x32_bf16 v[40:43], a[4:7], a[224:227], v[40:43]\n"
"ds_read_b64_tr_b16 a[56:57], v21 offset:9344\n"
"ds_read_b64_tr_b16 a[58:59], v21 offset:13952\n"
"v_mfma_f32_16x16x32_bf16 v[48:51], a[8:11], a[224:227], v[48:51]\n"
"ds_read_b64_tr_b16 a[60:61], v20 offset:9408\n"
"ds_read_b64_tr_b16 a[62:63], v20 offset:14016\n"
"v_mfma_f32_16x16x32_bf16 v[56:59], a[12:15], a[224:227], v[56:59]\n"
"ds_read_b64_tr_b16 a[64:65], v21 offset:9408\n"
"ds_read_b64_tr_b16 a[66:67], v21 offset:14016\n"
"v_mfma_f32_16x16x32_bf16 v[64:67], a[16:19], a[224:227], v[64:67]\n"
"ds_read_b64_tr_b16 a[68:69], v20 offset:9472\n"
"ds_read_b64_tr_b16 a[70:71], v20 offset:14080\n"
"v_mfma_f32_16x16x32_bf16 v[36:39], a[0:3], a[228:231], v[36:39]\n"
"v_mfma_f32_16x16x32_bf16 v[44:47], a[4:7], a[228:231], v[44:47]\n"
"v_mfma_f32_16x16x32_bf16 v[52:55], a[8:11], a[228:231], v[52:55]\n"
"v_mfma_f32_16x16x32_bf16 v[60:63], a[12:15], a[228:231], v[60:63]\n"
"v_mfma_f32_16x16x32_bf16 v[68:71], a[16:19], a[228:231], v[68:71]\n"
"s_waitcnt lgkmcnt(0)\n"
"v_mfma_f32_16x16x32_bf16 v[76:79], a[56:59], a[236:239], v[76:79]\n"
"v_mfma_f32_16x16x32_bf16 v[84:87], a[60:63], a[236:239], v[84:87]\n"
"v_mfma_f32_16x16x32_bf16 v[92:95], a[64:67], a[236:239], v[92:95]\n"
"v_mfma_f32_16x16x32_bf16 v[100:103], a[68:71], a[236:239], v[100:103]\n"
"v_mfma_f32_16x16x32_bf16 v[32:35], a[36:39], a[232:235], v[32:35]\n"
"v_mfma_f32_16x16x32_bf16 v[40:43], a[40:43], a[232:235], v[40:43]\n"
"v_mfma_f32_16x16x32_bf16 v[48:51], a[44:47], a[232:235], v[48:51]\n"
"s_nop 1\n"
"v_cvt_pk_bf16_f32 v196, v76, v77\n"
"v_mfma_f32_16x16x32_bf16 v[56:59], a[48:51], a[232:235], v[56:59]\n"
"v_cvt_pk_bf16_f32 v197, v78, v79\n"
"v_mfma_f32_16x16x32_bf16 v[64:67], a[52:55], a[232:235], v[64:67]\n"
"v_cvt_pk_bf16_f32 v198, v84, v85\n"
"v_mfma_f32_16x16x32_bf16 v[36:39], a[36:39], a[236:239], v[36:39]\n"
"v_cvt_pk_bf16_f32 v199, v86, v87\n"
"v_mfma_f32_16x16x32_bf16 v[44:47], a[40:43], a[236:239], v[44:47]\n"
"v_cvt_pk_bf16_f32 v200, v92, v93\n"
"v_mfma_f32_16x16x32_bf16 v[52:55], a[44:47], a[236:239], v[52:55]\n"
"v_mfma_f32_16x16x32_bf16 v[60:63], a[48:51], a[236:239], v[60:63]\n"
"v_cvt_pk_bf16_f32 v201, v94, v95\n"
"v_mfma_f32_16x16x32_bf16 v[68:71], a[52:55], a[236:239], v[68:71]\n"
"v_cvt_pk_bf16_f32 v202, v100, v101\n"
"v_mfma_f32_16x16x32_bf16 v[72:75], a[20:23], a[224:227], v[72:75]\n"
"v_cvt_pk_bf16_f32 v204, v32, v33\n"
"v_cvt_pk_bf16_f32 v203, v102, v103\n"
"v_mfma_f32_16x16x32_bf16 v[80:83], a[24:27], a[224:227], v[80:83]\n"
"v_cvt_pk_bf16_f32 v205, v34, v35\n"
"v_cvt_pk_bf16_f32 v206, v40, v41\n"
"v_mfma_f32_16x16x32_bf16 v[88:91], a[28:31], a[224:227], v[88:91]\n"
"s_add_u32 s77, s3, 98304\n"
"buffer_store_dwordx4 v[196:199], v1, s[36:39], s77 offen offset:0 sc1\n"
"v_mfma_f32_16x16x32_bf16 v[96:99], a[32:35], a[224:227], v[96:99]\n"
"v_cvt_pk_bf16_f32 v207, v42, v43\n"
"buffer_store_dwordx4 v[200:203], v1, s[36:39], s77 offen offset:1024 sc1\n"
"v_mfma_f32_16x16x32_bf16 v[72:75], a[56:59], a[232:235], v[72:75]\n"
"v_cvt_pk_bf16_f32 v208, v48, v49\n"
"v_cvt_pk_bf16_f32 v209, v50, v51\n"
"v_mfma_f32_16x16x32_bf16 v[80:83], a[60:63], a[232:235], v[80:83]\n"
"v_cvt_pk_bf16_f32 v210, v56, v57\n"
"v_cvt_pk_bf16_f32 v211, v58, v59\n"
"v_mfma_f32_16x16x32_bf16 v[88:91], a[64:67], a[232:235], v[88:91]\n"
"v_cvt_pk_bf16_f32 v212, v64, v65\n"
"v_mfma_f32_16x16x32_bf16 v[96:99], a[68:71], a[232:235], v[96:99]\n"
"v_cvt_pk_bf16_f32 v213, v66, v67\n"
"s_add_u32 s77, s3, 2560\n"
"buffer_store_dwordx4 v[204:207], v1, s[36:39], s77 offen offset:0 sc1\n"
"buffer_store_dwordx4 v[208:211], v1, s[36:39], s77 offen offset:1024 sc1\n"
"v_cvt_pk_bf16_f32 v216, v36, v37\n"
"v_cvt_pk_bf16_f32 v217, v38, v39\n"
"v_cvt_pk_bf16_f32 v218, v44, v45\n"
"v_cvt_pk_bf16_f32 v219, v46, v47\n"
"v_cvt_pk_bf16_f32 v220, v52, v53\n"
"buffer_store_dwordx2 v[212:213], v251, s[36:39], s77 offen offset:2048 sc1\n"
"v_cvt_pk_bf16_f32 v221, v54, v55\n"
"v_cvt_pk_bf16_f32 v222, v60, v61\n"
"v_cvt_pk_bf16_f32 v223, v62, v63\n"
"v_cvt_pk_bf16_f32 v224, v68, v69\n"
"v_cvt_pk_bf16_f32 v225, v70, v71\n"
"s_add_u32 s77, s3, 37888\n"
"buffer_store_dwordx4 v[216:219], v1, s[36:39], s77 offen offset:0 sc1\n"
"s_waitcnt vmcnt(6)\n"
"s_mov_b32 s85, 0x3f80\n"
"s_mov_b32 s86, 0x3f800000\n"
"v_lshlrev_b32 v134, 16, v134\n"
"s_mov_b32 s80, 0xbfb8aa3b\n"
"v_mov_b32 v104, v134\n"
"v_mov_b32 v105, v134\n"
"v_mov_b32 v106, v134\n"
"v_mov_b32 v107, v134\n"
"v_mov_b32 v108, v134\n"
"buffer_store_dwordx4 v[220:223], v1, s[36:39], s77 offen offset:1024 sc1\n"
"v_mov_b32 v109, v134\n"
"v_mov_b32 v110, v134\n"
"v_mov_b32 v111, v134\n"
"v_mov_b32 v112, v134\n"
"v_mov_b32 v113, v134\n"
"v_mov_b32 v114, v134\n"
"v_mov_b32 v115, v134\n"
"v_mov_b32 v116, v134\n"
"v_mov_b32 v117, v134\n"
"v_mov_b32 v118, v134\n"
"buffer_store_dwordx2 v[224:225], v251, s[36:39], s77 offen offset:2048 sc1\n"
"v_mov_b32 v119, v134\n"
"v_dot2c_f32_bf16 v104, s85, v136\n"
"v_dot2c_f32_bf16 v106, s85, v137\n"
"v_dot2c_f32_bf16 v108, s85, v138\n"
"v_dot2c_f32_bf16 v110, s85, v139\n"
"v_dot2c_f32_bf16 v112, s85, v140\n"
"v_dot2c_f32_bf16 v114, s85, v141\n"
"v_dot2c_f32_bf16 v116, s85, v142\n"
"v_dot2c_f32_bf16 v118, s85, v143\n"
"v_dot2c_f32_bf16 v105, s86, v136\n"
"v_dot2c_f32_bf16 v107, s86, v137\n"
"v_dot2c_f32_bf16 v109, s86, v138\n"
"v_dot2c_f32_bf16 v111, s86, v139\n"
"v_dot2c_f32_bf16 v113, s86, v140\n"
"v_dot2c_f32_bf16 v115, s86, v141\n"
"v_dot2c_f32_bf16 v117, s86, v142\n"
"v_dot2c_f32_bf16 v119, s86, v143\n"
"v_dot2c_f32_bf16 v104, s85, v146\n"
"v_dot2c_f32_bf16 v106, s85, v147\n"
"v_dot2c_f32_bf16 v108, s85, v148\n"
"v_dot2c_f32_bf16 v110, s85, v149\n"
"v_dot2c_f32_bf16 v112, s85, v150\n"
"v_dot2c_f32_bf16 v114, s85, v151\n"
"v_dot2c_f32_bf16 v116, s85, v152\n"
"v_dot2c_f32_bf16 v118, s85, v153\n"
"v_dot2c_f32_bf16 v105, s86, v146\n"
"v_dot2c_f32_bf16 v107, s86, v147\n"
"v_dot2c_f32_bf16 v109, s86, v148\n"
"v_dot2c_f32_bf16 v111, s86, v149\n"
"v_dot2c_f32_bf16 v113, s86, v150\n"
"v_dot2c_f32_bf16 v115, s86, v151\n"
"v_dot2c_f32_bf16 v117, s86, v152\n"
"v_dot2c_f32_bf16 v119, s86, v153\n"
"v_dot2c_f32_bf16 v104, s85, v156\n"
"v_dot2c_f32_bf16 v106, s85, v157\n"
"v_dot2c_f32_bf16 v108, s85, v158\n"
"v_dot2c_f32_bf16 v110, s85, v159\n"
"v_dot2c_f32_bf16 v112, s85, v160\n"
"v_dot2c_f32_bf16 v114, s85, v161\n"
"v_dot2c_f32_bf16 v116, s85, v162\n"
"v_dot2c_f32_bf16 v118, s85, v163\n"
"v_dot2c_f32_bf16 v105, s86, v156\n"
"v_dot2c_f32_bf16 v107, s86, v157\n"
"v_dot2c_f32_bf16 v109, s86, v158\n"
"v_dot2c_f32_bf16 v111, s86, v159\n"
"v_dot2c_f32_bf16 v113, s86, v160\n"
"v_dot2c_f32_bf16 v115, s86, v161\n"
"v_dot2c_f32_bf16 v117, s86, v162\n"
"v_dot2c_f32_bf16 v119, s86, v163\n"
"v_dot2c_f32_bf16 v104, s85, v166\n"
"v_dot2c_f32_bf16 v106, s85, v167\n"
"v_dot2c_f32_bf16 v108, s85, v168\n"
"v_dot2c_f32_bf16 v110, s85, v169\n"
"v_dot2c_f32_bf16 v112, s85, v170\n"
"v_dot2c_f32_bf16 v114, s85, v171\n"
"v_dot2c_f32_bf16 v116, s85, v172\n"
"v_dot2c_f32_bf16 v118, s85, v173\n"
"v_dot2c_f32_bf16 v105, s86, v166\n"
"v_dot2c_f32_bf16 v107, s86, v167\n"
"v_dot2c_f32_bf16 v109, s86, v168\n"
"v_dot2c_f32_bf16 v111, s86, v169\n"
"v_dot2c_f32_bf16 v113, s86, v170\n"
"v_dot2c_f32_bf16 v115, s86, v171\n"
"v_dot2c_f32_bf16 v117, s86, v172\n"
"v_dot2c_f32_bf16 v119, s86, v173\n"
"v_mul_f32 v176, s80, v104\n"
"v_mul_f32 v177, s80, v105\n"
"v_mul_f32 v178, s80, v106\n"
"v_mul_f32 v179, s80, v107\n"
"v_mul_f32 v180, s80, v108\n"
"v_mul_f32 v181, s80, v109\n"
"v_mul_f32 v182, s80, v110\n"
"v_mul_f32 v183, s80, v111\n"
"v_mul_f32 v184, s80, v112\n"
"v_mul_f32 v185, s80, v113\n"
"v_mul_f32 v186, s80, v114\n"
"v_mul_f32 v187, s80, v115\n"
"v_mul_f32 v188, s80, v116\n"
"v_mul_f32 v189, s80, v117\n"
"v_mul_f32 v190, s80, v118\n"
"v_mul_f32 v191, s80, v119\n"
"s_add_u32 s77, s3, 65536\n"
"buffer_load_dwordx4 v[36:39], v1, s[36:39], s77 offen offset:0 sc1\n"
"v_exp_f32 v176, v176\n"
"v_exp_f32 v177, v177\n"
"v_exp_f32 v178, v178\n"
"v_exp_f32 v179, v179\n"
"v_exp_f32 v180, v180\n"
"v_exp_f32 v181, v181\n"
"v_exp_f32 v182, v182\n"
"buffer_load_dwordx4 v[44:47], v1, s[36:39], s77 offen offset:1024 sc1\n"
"v_exp_f32 v183, v183\n"
"v_exp_f32 v184, v184\n"
"v_exp_f32 v185, v185\n"
"v_exp_f32 v186, v186\n"
"v_exp_f32 v187, v187\n"
"v_exp_f32 v188, v188\n"
"v_exp_f32 v189, v189\n"
"s_add_u32 s77, s3, 68096\n"
"buffer_load_dwordx4 v[60:63], v1, s[36:39], s77 offen offset:0 sc1\n"
"v_exp_f32 v190, v190\n"
"v_exp_f32 v191, v191\n"
"v_add_f32 v176, 1.0, v176\n"
"v_add_f32 v177, 1.0, v177\n"
"v_add_f32 v178, 1.0, v178\n"
"v_add_f32 v179, 1.0, v179\n"
"v_add_f32 v180, 1.0, v180\n"
"buffer_load_dwordx4 v[68:71], v1, s[36:39], s77 offen offset:1024 sc1\n"
"v_add_f32 v181, 1.0, v181\n"
"v_add_f32 v182, 1.0, v182\n"
"v_add_f32 v183, 1.0, v183\n"
"v_add_f32 v184, 1.0, v184\n"
"v_add_f32 v185, 1.0, v185\n"
"v_add_f32 v186, 1.0, v186\n"
"v_add_f32 v187, 1.0, v187\n"
"s_add_u32 s77, s3, 70656\n"
"buffer_load_dwordx4 v[84:87], v1, s[36:39], s77 offen offset:0 sc1\n"
"v_add_f32 v188, 1.0, v188\n"
"v_add_f32 v189, 1.0, v189\n"
"v_add_f32 v190, 1.0, v190\n"
"v_add_f32 v191, 1.0, v191\n"
"v_rcp_f32 v176, v176\n"
"v_rcp_f32 v177, v177\n"
"v_rcp_f32 v178, v178\n"
"buffer_load_dwordx4 v[92:95], v1, s[36:39], s77 offen offset:1024 sc1\n"
"v_rcp_f32 v179, v179\n"
"v_rcp_f32 v180, v180\n"
"v_rcp_f32 v181, v181\n"
"v_rcp_f32 v182, v182\n"
"v_rcp_f32 v183, v183\n"
"v_rcp_f32 v184, v184\n"
"v_rcp_f32 v185, v185\n"
"v_rcp_f32 v186, v186\n"
"v_rcp_f32 v187, v187\n"
"v_rcp_f32 v188, v188\n"
"v_rcp_f32 v189, v189\n"
"v_rcp_f32 v190, v190\n"
"v_rcp_f32 v191, v191\n"
"v_mul_f32 v104, v104, v176\n"
"v_mul_f32 v105, v105, v177\n"
"v_mul_f32 v106, v106, v178\n"
"v_mul_f32 v107, v107, v179\n"
"v_mul_f32 v108, v108, v180\n"
"v_mul_f32 v109, v109, v181\n"
"v_mul_f32 v110, v110, v182\n"
"v_mul_f32 v111, v111, v183\n"
"v_mul_f32 v112, v112, v184\n"
"v_mul_f32 v113, v113, v185\n"
"v_mul_f32 v114, v114, v186\n"
"v_mul_f32 v115, v115, v187\n"
"v_mul_f32 v116, v116, v188\n"
"v_mul_f32 v117, v117, v189\n"
"v_mul_f32 v118, v118, v190\n"
"v_mul_f32 v119, v119, v191\n"
"v_lshlrev_b32 v136, 16, v124\n"
"v_and_b32 v137, 0xffff0000, v124\n"
"v_lshlrev_b32 v138, 16, v125\n"
"v_and_b32 v139, 0xffff0000, v125\n"
"v_lshlrev_b32 v140, 16, v126\n"
"v_and_b32 v141, 0xffff0000, v126\n"
"v_lshlrev_b32 v142, 16, v127\n"
"v_and_b32 v143, 0xffff0000, v127\n"
"v_lshlrev_b32 v144, 16, v128\n"
"v_and_b32 v145, 0xffff0000, v128\n"
"v_lshlrev_b32 v146, 16, v129\n"
"v_and_b32 v147, 0xffff0000, v129\n"
"v_lshlrev_b32 v148, 16, v130\n"
"v_and_b32 v149, 0xffff0000, v130\n"
"v_lshlrev_b32 v150, 16, v131\n"
"v_and_b32 v151, 0xffff0000, v131\n"
"s_mov_b32 s48, s30\n"
"s_and_b32 s49, s31, 0xffff\n"
"s_mov_b32 s50, 0x240000\n"
"s_mov_b32 s51, 0x20000\n"
"s_mov_b32 s55, 0x30000\n"
"s_waitcnt vmcnt(0)\n"
"v_max_u32 v6, v36, v37\n"
"v_max_u32 v7, v68, v69\n"
"v_max3_u32 v6, v6, v38, v39\n"
"v_max3_u32 v7, v7, v70, v71\n"
"v_max3_u32 v6, v6, v44, v45\n"
"v_max3_u32 v7, v7, v84, v85\n"
"v_max3_u32 v6, v6, v46, v47\n"
"v_max3_u32 v7, v7, v86, v87\n"
"v_max3_u32 v6, v6, v60, v61\n"
"v_max3_u32 v7, v7, v92, v93\n"
"v_max3_u32 v6, v6, v62, v63\n"
"v_max3_u32 v7, v7, v94, v95\n"
"v_max_u32 v6, v6, v7\n"
"v_cmp_eq_u32 vcc, -1, v6\n"
"s_nop 1\n"
"s_cbranch_vccz L_finc2\n"
"L_finr2:\n"
"s_add_u32 s77, s3, 65536\n"
"buffer_load_dwordx4 v[36:39], v1, s[36:39], s77 offen offset:0 sc1\n"
"buffer_load_dwordx4 v[44:47], v1, s[36:39], s77 offen offset:1024 sc1\n"
"s_add_u32 s77, s3, 68096\n"
"buffer_load_dwordx4 v[60:63], v1, s[36:39], s77 offen offset:0 sc1\n"
"buffer_load_dwordx4 v[68:71], v1, s[36:39], s77 offen offset:1024 sc1\n"
"s_add_u32 s77, s3, 70656\n"
"buffer_load_dwordx4 v[84:87], v1, s[36:39], s77 offen offset:0 sc1\n"
"buffer_load_dwordx4 v[92:95], v1, s[36:39], s77 offen offset:1024 sc1\n"
"s_waitcnt vmcnt(0)\n"
"v_max_u32 v6, v36, v37\n"
"v_max_u32 v7, v68, v69\n"
"v_max3_u32 v6, v6, v38, v39\n"
"v_max3_u32 v7, v7, v70, v71\n"
"v_max3_u32 v6, v6, v44, v45\n"
"v_max3_u32 v7, v7, v84, v85\n"
"v_max3_u32 v6, v6, v46, v47\n"
"v_max3_u32 v7, v7, v86, v87\n"
"v_max3_u32 v6, v6, v60, v61\n"
"v_max3_u32 v7, v7, v92, v93\n"
"v_max3_u32 v6, v6, v62, v63\n"
"v_max3_u32 v7, v7, v94, v95\n"
"v_max_u32 v6, v6, v7\n"
"v_cmp_eq_u32 vcc, -1, v6\n"
"s_nop 1\n"
"s_cbranch_vccz L_finc2\n"
"s_sub_u32 s55, s55, 1\n"
"s_cmp_lg_u32 s55, 0\n"
"s_cbranch_scc1 L_finr2\n"
"L_finc2:\n"
"v_dot2c_f32_bf16 v72, s85, v36\n"
"v_dot2c_f32_bf16 v74, s85, v37\n"
"v_dot2c_f32_bf16 v80, s85, v38\n"
"v_dot2c_f32_bf16 v82, s85, v39\n"
"v_dot2c_f32_bf16 v88, s85, v44\n"
"v_dot2c_f32_bf16 v90, s85, v45\n"
"v_dot2c_f32_bf16 v96, s85, v46\n"
"v_dot2c_f32_bf16 v98, s85, v47\n"
"v_dot2c_f32_bf16 v73, s86, v36\n"
"v_dot2c_f32_bf16 v75, s86, v37\n"
"v_dot2c_f32_bf16 v81, s86, v38\n"
"v_dot2c_f32_bf16 v83, s86, v39\n"
"v_dot2c_f32_bf16 v89, s86, v44\n"
"v_dot2c_f32_bf16 v91, s86, v45\n"
"v_dot2c_f32_bf16 v97, s86, v46\n"
"v_dot2c_f32_bf16 v99, s86, v47\n"
"v_dot2c_f32_bf16 v72, s85, v60\n"
"v_dot2c_f32_bf16 v74, s85, v61\n"
"v_dot2c_f32_bf16 v80, s85, v62\n"
"v_dot2c_f32_bf16 v82, s85, v63\n"
"v_dot2c_f32_bf16 v88, s85, v68\n"
"v_dot2c_f32_bf16 v90, s85, v69\n"
"v_dot2c_f32_bf16 v96, s85, v70\n"
"v_dot2c_f32_bf16 v98, s85, v71\n"
"v_dot2c_f32_bf16 v73, s86, v60\n"
"v_dot2c_f32_bf16 v75, s86, v61\n"
"v_dot2c_f32_bf16 v81, s86, v62\n"
"v_dot2c_f32_bf16 v83, s86, v63\n"
"v_dot2c_f32_bf16 v89, s86, v68\n"
"v_dot2c_f32_bf16 v91, s86, v69\n"
"v_dot2c_f32_bf16 v97, s86, v70\n"
"v_dot2c_f32_bf16 v99, s86, v71\n"
"v_dot2c_f32_bf16 v72, s85, v84\n"
"v_dot2c_f32_bf16 v74, s85, v85\n"
"v_dot2c_f32_bf16 v80, s85, v86\n"
"v_dot2c_f32_bf16 v82, s85, v87\n"
"v_dot2c_f32_bf16 v88, s85, v92\n"
"v_dot2c_f32_bf16 v90, s85, v93\n"
"v_dot2c_f32_bf16 v96, s85, v94\n"
"v_dot2c_f32_bf16 v98, s85, v95\n"
"v_dot2c_f32_bf16 v73, s86, v84\n"
"v_dot2c_f32_bf16 v75, s86, v85\n"
"v_dot2c_f32_bf16 v81, s86, v86\n"
"v_dot2c_f32_bf16 v83, s86, v87\n"
"v_dot2c_f32_bf16 v89, s86, v92\n"
"v_dot2c_f32_bf16 v91, s86, v93\n"
"v_dot2c_f32_bf16 v97, s86, v94\n"
"v_dot2c_f32_bf16 v99, s86, v95\n"
"v_fmac_f32 v136, v72, v104\n"
"v_fmac_f32 v137, v73, v105\n"
"v_fmac_f32 v138, v74, v106\n"
"v_fmac_f32 v139, v75, v107\n"
"v_fmac_f32 v140, v80, v108\n"
"v_fmac_f32 v141, v81, v109\n"
"v_fmac_f32 v142, v82, v110\n"
"v_fmac_f32 v143, v83, v111\n"
"v_fmac_f32 v144, v88, v112\n"
"v_fmac_f32 v145, v89, v113\n"
"v_fmac_f32 v146, v90, v114\n"
"v_fmac_f32 v147, v91, v115\n"
"v_fmac_f32 v148, v96, v116\n"
"v_fmac_f32 v149, v97, v117\n"
"v_fmac_f32 v150, v98, v118\n"
"v_fmac_f32 v151, v99, v119\n"
"v_cvt_pk_bf16_f32 v176, v136, v137\n"
"v_cvt_pk_bf16_f32 v177, v138, v139\n"
"buffer_store_dwordx2 v[176:177], v16, s[48:51], s84 offen offset:0\n"
"v_cvt_pk_bf16_f32 v178, v140, v141\n"
"v_cvt_pk_bf16_f32 v179, v142, v143\n"
"buffer_store_dwordx2 v[178:179], v16, s[48:51], s84 offen offset:32\n"
"v_cvt_pk_bf16_f32 v180, v144, v145\n"
"v_cvt_pk_bf16_f32 v181, v146, v147\n"
"buffer_store_dwordx2 v[180:181], v16, s[48:51], s84 offen offset:64\n"
"v_cvt_pk_bf16_f32 v182, v148, v149\n"
"v_cvt_pk_bf16_f32 v183, v150, v151\n"
"buffer_store_dwordx2 v[182:183], v16, s[48:51], s84 offen offset:96\n"
"s_branch L_end\n"
"L_fin3:\n"
"s_waitcnt vmcnt(18) lgkmcnt(0)\n"
"v_mfma_f32_16x16x32_bf16 v[72:75], a[20:23], a[224:227], v[72:75]\n"
"ds_read_b64_tr_b16 a[36:37], v20 offset:9216\n"
"ds_read_b64_tr_b16 a[38:39], v20 offset:13824\n"
"v_mfma_f32_16x16x32_bf16 v[80:83], a[24:27], a[224:227], v[80:83]\n"
"ds_read_b64_tr_b16 a[40:41], v21 offset:9216\n"
"ds_read_b64_tr_b16 a[42:43], v21 offset:13824\n"
"v_mfma_f32_16x16x32_bf16 v[88:91], a[28:31], a[224:227], v[88:91]\n"
"ds_read_b64_tr_b16 a[44:45], v20 offset:9280\n"
"ds_read_b64_tr_b16 a[46:47], v20 offset:13888\n"
"v_mfma_f32_16x16x32_bf16 v[96:99], a[32:35], a[224:227], v[96:99]\n"
"ds_read_b64_tr_b16 a[48:49], v21 offset:9280\n"
"ds_read_b64_tr_b16 a[50:51], v21 offset:13888\n"
"v_mfma_f32_16x16x32_bf16 v[36:39], a[0:3], a[228:231], v[36:39]\n"
"ds_read_b64_tr_b16 a[52:53], v20 offset:9344\n"
"ds_read_b64_tr_b16 a[54:55], v20 offset:13952\n"
"v_mfma_f32_16x16x32_bf16 v[44:47], a[4:7], a[228:231], v[44:47]\n"
"ds_read_b64_tr_b16 a[56:57], v21 offset:9344\n"
"ds_read_b64_tr_b16 a[58:59], v21 offset:13952\n"
"v_mfma_f32_16x16x32_bf16 v[52:55], a[8:11], a[228:231], v[52:55]\n"
"ds_read_b64_tr_b16 a[60:61], v20 offset:9408\n"
"ds_read_b64_tr_b16 a[62:63], v20 offset:14016\n"
"v_mfma_f32_16x16x32_bf16 v[60:63], a[12:15], a[228:231], v[60:63]\n"
"ds_read_b64_tr_b16 a[64:65], v21 offset:9408\n"
"ds_read_b64_tr_b16 a[66:67], v21 offset:14016\n"
"v_mfma_f32_16x16x32_bf16 v[68:71], a[16:19], a[228:231], v[68:71]\n"
"ds_read_b64_tr_b16 a[68:69], v20 offset:9472\n"
"ds_read_b64_tr_b16 a[70:71], v20 offset:14080\n"
"v_mfma_f32_16x16x32_bf16 v[32:35], a[0:3], a[224:227], v[32:35]\n"
"v_mfma_f32_16x16x32_bf16 v[40:43], a[4:7], a[224:227], v[40:43]\n"
"v_mfma_f32_16x16x32_bf16 v[48:51], a[8:11], a[224:227], v[48:51]\n"
"v_mfma_f32_16x16x32_bf16 v[56:59], a[12:15], a[224:227], v[56:59]\n"
"v_mfma_f32_16x16x32_bf16 v[64:67], a[16:19], a[224:227], v[64:67]\n"
"s_waitcnt lgkmcnt(0)\n"
"v_mfma_f32_16x16x32_bf16 v[72:75], a[56:59], a[232:235], v[72:75]\n"
"v_mfma_f32_16x16x32_bf16 v[80:83], a[60:63], a[232:235], v[80:83]\n"
"v_mfma_f32_16x16x32_bf16 v[88:91], a[64:67], a[232:235], v[88:91]\n"
"v_mfma_f32_16x16x32_bf16 v[96:99], a[68:71], a[232:235], v[96:99]\n"
"v_mfma_f32_16x16x32_bf16 v[36:39], a[36:39], a[236:239], v[36:39]\n"
"v_mfma_f32_16x16x32_bf16 v[44:47], a[40:43], a[236:239], v[44:47]\n"
"v_mfma_f32_16x16x32_bf16 v[52:55], a[44:47], a[236:239], v[52:55]\n"
"s_nop 1\n"
"v_cvt_pk_bf16_f32 v196, v72, v73\n"
"v_mfma_f32_16x16x32_bf16 v[60:63], a[48:51], a[236:239], v[60:63]\n"
"v_cvt_pk_bf16_f32 v197, v74, v75\n"
"v_mfma_f32_16x16x32_bf16 v[68:71], a[52:55], a[236:239], v[68:71]\n"
"v_cvt_pk_bf16_f32 v198, v80, v81\n"
"v_mfma_f32_16x16x32_bf16 v[32:35], a[36:39], a[232:235], v[32:35]\n"
"v_cvt_pk_bf16_f32 v199, v82, v83\n"
"v_mfma_f32_16x16x32_bf16 v[40:43], a[40:43], a[232:235], v[40:43]\n"
"v_cvt_pk_bf16_f32 v200, v88, v89\n"
"v_mfma_f32_16x16x32_bf16 v[48:51], a[44:47], a[232:235], v[48:51]\n"
"v_mfma_f32_16x16x32_bf16 v[56:59], a[48:51], a[232:235], v[56:59]\n"
"v_cvt_pk_bf16_f32 v201, v90, v91\n"
"v_mfma_f32_16x16x32_bf16 v[64:67], a[52:55], a[232:235], v[64:67]\n"
"v_cvt_pk_bf16_f32 v202, v96, v97\n"
"v_mfma_f32_16x16x32_bf16 v[76:79], a[20:23], a[228:231], v[76:79]\n"
"v_cvt_pk_bf16_f32 v204, v36, v37\n"
"v_cvt_pk_bf16_f32 v203, v98, v99\n"
"v_mfma_f32_16x16x32_bf16 v[84:87], a[24:27], a[228:231], v[84:87]\n"
"v_cvt_pk_bf16_f32 v205, v38, v39\n"
"v_cvt_pk_bf16_f32 v206, v44, v45\n"
"v_mfma_f32_16x16x32_bf16 v[92:95], a[28:31], a[228:231], v[92:95]\n"
"s_add_u32 s77, s3, 65536\n"
"buffer_store_dwordx4 v[196:199], v1, s[36:39], s77 offen offset:0 sc1\n"
"v_mfma_f32_16x16x32_bf16 v[100:103], a[32:35], a[228:231], v[100:103]\n"
"v_cvt_pk_bf16_f32 v207, v46, v47\n"
"buffer_store_dwordx4 v[200:203], v1, s[36:39], s77 offen offset:1024 sc1\n"
"v_mfma_f32_16x16x32_bf16 v[76:79], a[56:59], a[236:239], v[76:79]\n"
"v_cvt_pk_bf16_f32 v208, v52, v53\n"
"v_cvt_pk_bf16_f32 v209, v54, v55\n"
"v_mfma_f32_16x16x32_bf16 v[84:87], a[60:63], a[236:239], v[84:87]\n"
"v_cvt_pk_bf16_f32 v210, v60, v61\n"
"v_cvt_pk_bf16_f32 v211, v62, v63\n"
"v_mfma_f32_16x16x32_bf16 v[92:95], a[64:67], a[236:239], v[92:95]\n"
"v_cvt_pk_bf16_f32 v212, v68, v69\n"
"v_mfma_f32_16x16x32_bf16 v[100:103], a[68:71], a[236:239], v[100:103]\n"
"v_cvt_pk_bf16_f32 v213, v70, v71\n"
"s_add_u32 s77, s3, 35328\n"
"buffer_store_dwordx4 v[204:207], v1, s[36:39], s77 offen offset:0 sc1\n"
"buffer_store_dwordx4 v[208:211], v1, s[36:39], s77 offen offset:1024 sc1\n"
"v_cvt_pk_bf16_f32 v216, v32, v33\n"
"v_cvt_pk_bf16_f32 v217, v34, v35\n"
"v_cvt_pk_bf16_f32 v218, v40, v41\n"
"v_cvt_pk_bf16_f32 v219, v42, v43\n"
"v_cvt_pk_bf16_f32 v220, v48, v49\n"
"buffer_store_dwordx2 v[212:213], v251, s[36:39], s77 offen offset:2048 sc1\n"
"v_cvt_pk_bf16_f32 v221, v50, v51\n"
"v_cvt_pk_bf16_f32 v222, v56, v57\n"
"v_cvt_pk_bf16_f32 v223, v58, v59\n"
"v_cvt_pk_bf16_f32 v224, v64, v65\n"
"v_cvt_pk_bf16_f32 v225, v66, v67\n"
"s_add_u32 s77, s3, 5120\n"
"buffer_store_dwordx4 v[216:219], v1, s[36:39], s77 offen offset:0 sc1\n"
"s_waitcnt vmcnt(6)\n"
"s_mov_b32 s85, 0x3f80\n"
"s_mov_b32 s86, 0x3f800000\n"
"v_lshlrev_b32 v134, 16, v134\n"
"s_mov_b32 s80, 0xbfb8aa3b\n"
"v_mov_b32 v104, v134\n"
"v_mov_b32 v105, v134\n"
"v_mov_b32 v106, v134\n"
"v_mov_b32 v107, v134\n"
"v_mov_b32 v108, v134\n"
"buffer_store_dwordx4 v[220:223], v1, s[36:39], s77 offen offset:1024 sc1\n"
"v_mov_b32 v109, v134\n"
"v_mov_b32 v110, v134\n"
"v_mov_b32 v111, v134\n"
"v_mov_b32 v112, v134\n"
"v_mov_b32 v113, v134\n"
"v_mov_b32 v114, v134\n"
"v_mov_b32 v115, v134\n"
"v_mov_b32 v116, v134\n"
"v_mov_b32 v117, v134\n"
"v_mov_b32 v118, v134\n"
"buffer_store_dwordx2 v[224:225], v251, s[36:39], s77 offen offset:2048 sc1\n"
"v_mov_b32 v119, v134\n"
"v_dot2c_f32_bf16 v104, s85, v136\n"
"v_dot2c_f32_bf16 v106, s85, v137\n"
"v_dot2c_f32_bf16 v108, s85, v138\n"
"v_dot2c_f32_bf16 v110, s85, v139\n"
"v_dot2c_f32_bf16 v112, s85, v140\n"
"v_dot2c_f32_bf16 v114, s85, v141\n"
"v_dot2c_f32_bf16 v116, s85, v142\n"
"v_dot2c_f32_bf16 v118, s85, v143\n"
"v_dot2c_f32_bf16 v105, s86, v136\n"
"v_dot2c_f32_bf16 v107, s86, v137\n"
"v_dot2c_f32_bf16 v109, s86, v138\n"
"v_dot2c_f32_bf16 v111, s86, v139\n"
"v_dot2c_f32_bf16 v113, s86, v140\n"
"v_dot2c_f32_bf16 v115, s86, v141\n"
"v_dot2c_f32_bf16 v117, s86, v142\n"
"v_dot2c_f32_bf16 v119, s86, v143\n"
"v_dot2c_f32_bf16 v104, s85, v146\n"
"v_dot2c_f32_bf16 v106, s85, v147\n"
"v_dot2c_f32_bf16 v108, s85, v148\n"
"v_dot2c_f32_bf16 v110, s85, v149\n"
"v_dot2c_f32_bf16 v112, s85, v150\n"
"v_dot2c_f32_bf16 v114, s85, v151\n"
"v_dot2c_f32_bf16 v116, s85, v152\n"
"v_dot2c_f32_bf16 v118, s85, v153\n"
"v_dot2c_f32_bf16 v105, s86, v146\n"
"v_dot2c_f32_bf16 v107, s86, v147\n"
"v_dot2c_f32_bf16 v109, s86, v148\n"
"v_dot2c_f32_bf16 v111, s86, v149\n"
"v_dot2c_f32_bf16 v113, s86, v150\n"
"v_dot2c_f32_bf16 v115, s86, v151\n"
"v_dot2c_f32_bf16 v117, s86, v152\n"
"v_dot2c_f32_bf16 v119, s86, v153\n"
"v_dot2c_f32_bf16 v104, s85, v156\n"
"v_dot2c_f32_bf16 v106, s85, v157\n"
"v_dot2c_f32_bf16 v108, s85, v158\n"
"v_dot2c_f32_bf16 v110, s85, v159\n"
"v_dot2c_f32_bf16 v112, s85, v160\n"
"v_dot2c_f32_bf16 v114, s85, v161\n"
"v_dot2c_f32_bf16 v116, s85, v162\n"
"v_dot2c_f32_bf16 v118, s85, v163\n"
"v_dot2c_f32_bf16 v105, s86, v156\n"
"v_dot2c_f32_bf16 v107, s86, v157\n"
"v_dot2c_f32_bf16 v109, s86, v158\n"
"v_dot2c_f32_bf16 v111, s86, v159\n"
"v_dot2c_f32_bf16 v113, s86, v160\n"
"v_dot2c_f32_bf16 v115, s86, v161\n"
"v_dot2c_f32_bf16 v117, s86, v162\n"
"v_dot2c_f32_bf16 v119, s86, v163\n"
"v_dot2c_f32_bf16 v104, s85, v166\n"
"v_dot2c_f32_bf16 v106, s85, v167\n"
"v_dot2c_f32_bf16 v108, s85, v168\n"
"v_dot2c_f32_bf16 v110, s85, v169\n"
"v_dot2c_f32_bf16 v112, s85, v170\n"
"v_dot2c_f32_bf16 v114, s85, v171\n"
"v_dot2c_f32_bf16 v116, s85, v172\n"
"v_dot2c_f32_bf16 v118, s85, v173\n"
"v_dot2c_f32_bf16 v105, s86, v166\n"
"v_dot2c_f32_bf16 v107, s86, v167\n"
"v_dot2c_f32_bf16 v109, s86, v168\n"
"v_dot2c_f32_bf16 v111, s86, v169\n"
"v_dot2c_f32_bf16 v113, s86, v170\n"
"v_dot2c_f32_bf16 v115, s86, v171\n"
"v_dot2c_f32_bf16 v117, s86, v172\n"
"v_dot2c_f32_bf16 v119, s86, v173\n"
"v_mul_f32 v176, s80, v104\n"
"v_mul_f32 v177, s80, v105\n"
"v_mul_f32 v178, s80, v106\n"
"v_mul_f32 v179, s80, v107\n"
"v_mul_f32 v180, s80, v108\n"
"v_mul_f32 v181, s80, v109\n"
"v_mul_f32 v182, s80, v110\n"
"v_mul_f32 v183, s80, v111\n"
"v_mul_f32 v184, s80, v112\n"
"v_mul_f32 v185, s80, v113\n"
"v_mul_f32 v186, s80, v114\n"
"v_mul_f32 v187, s80, v115\n"
"v_mul_f32 v188, s80, v116\n"
"v_mul_f32 v189, s80, v117\n"
"v_mul_f32 v190, s80, v118\n"
"v_mul_f32 v191, s80, v119\n"
"s_add_u32 s77, s3, 98304\n"
"buffer_load_dwordx4 v[32:35], v1, s[36:39], s77 offen offset:0 sc1\n"
"v_exp_f32 v176, v176\n"
"v_exp_f32 v177, v177\n"
"v_exp_f32 v178, v178\n"
"v_exp_f32 v179, v179\n"
"v_exp_f32 v180, v180\n"
"v_exp_f32 v181, v181\n"
"v_exp_f32 v182, v182\n"
"buffer_load_dwordx4 v[40:43], v1, s[36:39], s77 offen offset:1024 sc1\n"
"v_exp_f32 v183, v183\n"
"v_exp_f32 v184, v184\n"
"v_exp_f32 v185, v185\n"
"v_exp_f32 v186, v186\n"
"v_exp_f32 v187, v187\n"
"v_exp_f32 v188, v188\n"
"v_exp_f32 v189, v189\n"
"s_add_u32 s77, s3, 100864\n"
"buffer_load_dwordx4 v[56:59], v1, s[36:39], s77 offen offset:0 sc1\n"
"v_exp_f32 v190, v190\n"
"v_exp_f32 v191, v191\n"
"v_add_f32 v176, 1.0, v176\n"
"v_add_f32 v177, 1.0, v177\n"
"v_add_f32 v178, 1.0, v178\n"
"v_add_f32 v179, 1.0, v179\n"
"v_add_f32 v180, 1.0, v180\n"
"buffer_load_dwordx4 v[64:67], v1, s[36:39], s77 offen offset:1024 sc1\n"
"v_add_f32 v181, 1.0, v181\n"
"v_add_f32 v182, 1.0, v182\n"
"v_add_f32 v183, 1.0, v183\n"
"v_add_f32 v184, 1.0, v184\n"
"v_add_f32 v185, 1.0, v185\n"
"v_add_f32 v186, 1.0, v186\n"
"v_add_f32 v187, 1.0, v187\n"
"s_add_u32 s77, s3, 103424\n"
"buffer_load_dwordx4 v[80:83], v1, s[36:39], s77 offen offset:0 sc1\n"
"v_add_f32 v188, 1.0, v188\n"
"v_add_f32 v189, 1.0, v189\n"
"v_add_f32 v190, 1.0, v190\n"
"v_add_f32 v191, 1.0, v191\n"
"v_rcp_f32 v176, v176\n"
"v_rcp_f32 v177, v177\n"
"v_rcp_f32 v178, v178\n"
"buffer_load_dwordx4 v[88:91], v1, s[36:39], s77 offen offset:1024 sc1\n"
"v_rcp_f32 v179, v179\n"
"v_rcp_f32 v180, v180\n"
"v_rcp_f32 v181, v181\n"
"v_rcp_f32 v182, v182\n"
"v_rcp_f32 v183, v183\n"
"v_rcp_f32 v184, v184\n"
"v_rcp_f32 v185, v185\n"
"v_rcp_f32 v186, v186\n"
"v_rcp_f32 v187, v187\n"
"v_rcp_f32 v188, v188\n"
"v_rcp_f32 v189, v189\n"
"v_rcp_f32 v190, v190\n"
"v_rcp_f32 v191, v191\n"
"v_mul_f32 v104, v104, v176\n"
"v_mul_f32 v105, v105, v177\n"
"v_mul_f32 v106, v106, v178\n"
"v_mul_f32 v107, v107, v179\n"
"v_mul_f32 v108, v108, v180\n"
"v_mul_f32 v109, v109, v181\n"
"v_mul_f32 v110, v110, v182\n"
"v_mul_f32 v111, v111, v183\n"
"v_mul_f32 v112, v112, v184\n"
"v_mul_f32 v113, v113, v185\n"
"v_mul_f32 v114, v114, v186\n"
"v_mul_f32 v115, v115, v187\n"
"v_mul_f32 v116, v116, v188\n"
"v_mul_f32 v117, v117, v189\n"
"v_mul_f32 v118, v118, v190\n"
"v_mul_f32 v119, v119, v191\n"
"v_lshlrev_b32 v136, 16, v124\n"
"v_and_b32 v137, 0xffff0000, v124\n"
"v_lshlrev_b32 v138, 16, v125\n"
"v_and_b32 v139, 0xffff0000, v125\n"
"v_lshlrev_b32 v140, 16, v126\n"
"v_and_b32 v141, 0xffff0000, v126\n"
"v_lshlrev_b32 v142, 16, v127\n"
"v_and_b32 v143, 0xffff0000, v127\n"
"v_lshlrev_b32 v144, 16, v128\n"
"v_and_b32 v145, 0xffff0000, v128\n"
"v_lshlrev_b32 v146, 16, v129\n"
"v_and_b32 v147, 0xffff0000, v129\n"
"v_lshlrev_b32 v148, 16, v130\n"
"v_and_b32 v149, 0xffff0000, v130\n"
"v_lshlrev_b32 v150, 16, v131\n"
"v_and_b32 v151, 0xffff0000, v131\n"
"s_mov_b32 s48, s30\n"
"s_and_b32 s49, s31, 0xffff\n"
"s_mov_b32 s50, 0x240000\n"
"s_mov_b32 s51, 0x20000\n"
"s_mov_b32 s55, 0x30000\n"
"s_waitcnt vmcnt(0)\n"
"v_max_u32 v6, v32, v33\n"
"v_max_u32 v7, v64, v65\n"
"v_max3_u32 v6, v6, v34, v35\n"
"v_max3_u32 v7, v7, v66, v67\n"
"v_max3_u32 v6, v6, v40, v41\n"
"v_max3_u32 v7, v7, v80, v81\n"
"v_max3_u32 v6, v6, v42, v43\n"
"v_max3_u32 v7, v7, v82, v83\n"
"v_max3_u32 v6, v6, v56, v57\n"
"v_max3_u32 v7, v7, v88, v89\n"
"v_max3_u32 v6, v6, v58, v59\n"
"v_max3_u32 v7, v7, v90, v91\n"
"v_max_u32 v6, v6, v7\n"
"v_cmp_eq_u32 vcc, -1, v6\n"
"s_nop 1\n"
"s_cbranch_vccz L_finc3\n"
"L_finr3:\n"
"s_add_u32 s77, s3, 98304\n"
"buffer_load_dwordx4 v[32:35], v1, s[36:39], s77 offen offset:0 sc1\n"
"buffer_load_dwordx4 v[40:43], v1, s[36:39], s77 offen offset:1024 sc1\n"
"s_add_u32 s77, s3, 100864\n"
"buffer_load_dwordx4 v[56:59], v1, s[36:39], s77 offen offset:0 sc1\n"
"buffer_load_dwordx4 v[64:67], v1, s[36:39], s77 offen offset:1024 sc1\n"
"s_add_u32 s77, s3, 103424\n"
"buffer_load_dwordx4 v[80:83], v1, s[36:39], s77 offen offset:0 sc1\n"
"buffer_load_dwordx4 v[88:91], v1, s[36:39], s77 offen offset:1024 sc1\n"
"s_waitcnt vmcnt(0)\n"
"v_max_u32 v6, v32, v33\n"
"v_max_u32 v7, v64, v65\n"
"v_max3_u32 v6, v6, v34, v35\n"
"v_max3_u32 v7, v7, v66, v67\n"
"v_max3_u32 v6, v6, v40, v41\n"
"v_max3_u32 v7, v7, v80, v81\n"
"v_max3_u32 v6, v6, v42, v43\n"
"v_max3_u32 v7, v7, v82, v83\n"
"v_max3_u32 v6, v6, v56, v57\n"
"v_max3_u32 v7, v7, v88, v89\n"
"v_max3_u32 v6, v6, v58, v59\n"
"v_max3_u32 v7, v7, v90, v91\n"
"v_max_u32 v6, v6, v7\n"
"v_cmp_eq_u32 vcc, -1, v6\n"
"s_nop 1\n"
"s_cbranch_vccz L_finc3\n"
"s_sub_u32 s55, s55, 1\n"
"s_cmp_lg_u32 s55, 0\n"
"s_cbranch_scc1 L_finr3\n"
"L_finc3:\n"
"v_dot2c_f32_bf16 v76, s85, v32\n"
"v_dot2c_f32_bf16 v78, s85, v33\n"
"v_dot2c_f32_bf16 v84, s85, v34\n"
"v_dot2c_f32_bf16 v86, s85, v35\n"
"v_dot2c_f32_bf16 v92, s85, v40\n"
"v_dot2c_f32_bf16 v94, s85, v41\n"
"v_dot2c_f32_bf16 v100, s85, v42\n"
"v_dot2c_f32_bf16 v102, s85, v43\n"
"v_dot2c_f32_bf16 v77, s86, v32\n"
"v_dot2c_f32_bf16 v79, s86, v33\n"
"v_dot2c_f32_bf16 v85, s86, v34\n"
"v_dot2c_f32_bf16 v87, s86, v35\n"
"v_dot2c_f32_bf16 v93, s86, v40\n"
"v_dot2c_f32_bf16 v95, s86, v41\n"
"v_dot2c_f32_bf16 v101, s86, v42\n"
"v_dot2c_f32_bf16 v103, s86, v43\n"
"v_dot2c_f32_bf16 v76, s85, v56\n"
"v_dot2c_f32_bf16 v78, s85, v57\n"
"v_dot2c_f32_bf16 v84, s85, v58\n"
"v_dot2c_f32_bf16 v86, s85, v59\n"
"v_dot2c_f32_bf16 v92, s85, v64\n"
"v_dot2c_f32_bf16 v94, s85, v65\n"
"v_dot2c_f32_bf16 v100, s85, v66\n"
"v_dot2c_f32_bf16 v102, s85, v67\n"
"v_dot2c_f32_bf16 v77, s86, v56\n"
"v_dot2c_f32_bf16 v79, s86, v57\n"
"v_dot2c_f32_bf16 v85, s86, v58\n"
"v_dot2c_f32_bf16 v87, s86, v59\n"
"v_dot2c_f32_bf16 v93, s86, v64\n"
"v_dot2c_f32_bf16 v95, s86, v65\n"
"v_dot2c_f32_bf16 v101, s86, v66\n"
"v_dot2c_f32_bf16 v103, s86, v67\n"
"v_dot2c_f32_bf16 v76, s85, v80\n"
"v_dot2c_f32_bf16 v78, s85, v81\n"
"v_dot2c_f32_bf16 v84, s85, v82\n"
"v_dot2c_f32_bf16 v86, s85, v83\n"
"v_dot2c_f32_bf16 v92, s85, v88\n"
"v_dot2c_f32_bf16 v94, s85, v89\n"
"v_dot2c_f32_bf16 v100, s85, v90\n"
"v_dot2c_f32_bf16 v102, s85, v91\n"
"v_dot2c_f32_bf16 v77, s86, v80\n"
"v_dot2c_f32_bf16 v79, s86, v81\n"
"v_dot2c_f32_bf16 v85, s86, v82\n"
"v_dot2c_f32_bf16 v87, s86, v83\n"
"v_dot2c_f32_bf16 v93, s86, v88\n"
"v_dot2c_f32_bf16 v95, s86, v89\n"
"v_dot2c_f32_bf16 v101, s86, v90\n"
"v_dot2c_f32_bf16 v103, s86, v91\n"
"v_fmac_f32 v136, v76, v104\n"
"v_fmac_f32 v137, v77, v105\n"
"v_fmac_f32 v138, v78, v106\n"
"v_fmac_f32 v139, v79, v107\n"
"v_fmac_f32 v140, v84, v108\n"
"v_fmac_f32 v141, v85, v109\n"
"v_fmac_f32 v142, v86, v110\n"
"v_fmac_f32 v143, v87, v111\n"
"v_fmac_f32 v144, v92, v112\n"
"v_fmac_f32 v145, v93, v113\n"
"v_fmac_f32 v146, v94, v114\n"
"v_fmac_f32 v147, v95, v115\n"
"v_fmac_f32 v148, v100, v116\n"
"v_fmac_f32 v149, v101, v117\n"
"v_fmac_f32 v150, v102, v118\n"
"v_fmac_f32 v151, v103, v119\n"
"v_cvt_pk_bf16_f32 v176, v136, v137\n"
"v_cvt_pk_bf16_f32 v177, v138, v139\n"
"buffer_store_dwordx2 v[176:177], v16, s[48:51], s84 offen offset:0\n"
"v_cvt_pk_bf16_f32 v178, v140, v141\n"
"v_cvt_pk_bf16_f32 v179, v142, v143\n"
"buffer_store_dwordx2 v[178:179], v16, s[48:51], s84 offen offset:32\n"
"v_cvt_pk_bf16_f32 v180, v144, v145\n"
"v_cvt_pk_bf16_f32 v181, v146, v147\n"
"buffer_store_dwordx2 v[180:181], v16, s[48:51], s84 offen offset:64\n"
"v_cvt_pk_bf16_f32 v182, v148, v149\n"
"v_cvt_pk_bf16_f32 v183, v150, v151\n"
"buffer_store_dwordx2 v[182:183], v16, s[48:51], s84 offen offset:96\n"
"s_branch L_end\n"
"L_slow:\n"
"s_nop 7\n"
"s_waitcnt vmcnt(0) lgkmcnt(0)\n"
"s_mov_b32 s9, m0\n"
"s_mov_b32 s35, 0\n"
"s_mov_b32 s99, s6\n"
"s_add_u32 s92, s14, s99\n"
"s_addc_u32 s93, s15, 0\n"
"s_and_b32 s93, s93, 0xffff\n"
"s_mov_b32 s94, 0x20000\n"
"s_mov_b32 s95, 0x20000\n"
"v_lshlrev_b32 v6, 8, v3\n"
"v_lshl_add_u32 v6, v2, 4, v6\n"
"s_mov_b32 s98, 0\n"
"v_mov_b32 v160, 0xff800000\n"
"v_mov_b32 v161, 0xff800000\n"
"v_mov_b32 v162, 0xff800000\n"
"v_mov_b32 v163, 0xff800000\n"
"L_slow1:\n"
"buffer_load_dwordx4 v[128:131], v6, s[92:95], s98 offen offset:0\n"
"buffer_load_dwordx4 v[132:135], v6, s[92:95], s98 offen offset:64\n"
"buffer_load_dwordx4 v[136:139], v6, s[92:95], s98 offen offset:128\n"
"buffer_load_dwordx4 v[140:143], v6, s[92:95], s98 offen offset:192\n"
"s_add_u32 s98, s98, 4096\n"
"s_waitcnt vmcnt(0)\n"
"v_mfma_f32_16x16x32_bf16 v[96:99], v[128:131], v[32:35], 0\n"
"v_mfma_f32_16x16x32_bf16 v[100:103], v[128:131], v[48:51], 0\n"
"v_mfma_f32_16x16x32_bf16 v[104:107], v[128:131], v[64:67], 0\n"
"v_mfma_f32_16x16x32_bf16 v[108:111], v[128:131], v[80:83], 0\n"
"v_mfma_f32_16x16x32_bf16 v[96:99], v[132:135], v[36:39], v[96:99]\n"
"v_mfma_f32_16x16x32_bf16 v[100:103], v[132:135], v[52:55], v[100:103]\n"
"v_mfma_f32_16x16x32_bf16 v[104:107], v[132:135], v[68:71], v[104:107]\n"
"v_mfma_f32_16x16x32_bf16 v[108:111], v[132:135], v[84:87], v[108:111]\n"
"v_mfma_f32_16x16x32_bf16 v[96:99], v[136:139], v[40:43], v[96:99]\n"
"v_mfma_f32_16x16x32_bf16 v[100:103], v[136:139], v[56:59], v[100:103]\n"
"v_mfma_f32_16x16x32_bf16 v[104:107], v[136:139], v[72:75], v[104:107]\n"
"v_mfma_f32_16x16x32_bf16 v[108:111], v[136:139], v[88:91], v[108:111]\n"
"v_mfma_f32_16x16x32_bf16 v[96:99], v[140:143], v[44:47], v[96:99]\n"
"v_mfma_f32_16x16x32_bf16 v[100:103], v[140:143], v[60:63], v[100:103]\n"
"v_mfma_f32_16x16x32_bf16 v[104:107], v[140:143], v[76:79], v[104:107]\n"
"v_mfma_f32_16x16x32_bf16 v[108:111], v[140:143], v[92:95], v[108:111]\n"
"s_nop 7\n"
"buffer_load_dwordx4 v[128:131], v6, s[92:95], s98 offen offset:0\n"
"buffer_load_dwordx4 v[132:135], v6, s[92:95], s98 offen offset:64\n"
"buffer_load_dwordx4 v[136:139], v6, s[92:95], s98 offen offset:128\n"
"buffer_load_dwordx4 v[140:143], v6, s[92:95], s98 offen offset:192\n"
"s_add_u32 s98, s98, 4096\n"
"s_waitcnt vmcnt(0)\n"
"v_mfma_f32_16x16x32_bf16 v[112:115], v[128:131], v[32:35], 0\n"
"v_mfma_f32_16x16x32_bf16 v[116:119], v[128:131], v[48:51], 0\n"
"v_mfma_f32_16x16x32_bf16 v[120:123], v[128:131], v[64:67], 0\n"
"v_mfma_f32_16x16x32_bf16 v[124:127], v[128:131], v[80:83], 0\n"
"v_mfma_f32_16x16x32_bf16 v[112:115], v[132:135], v[36:39], v[112:115]\n"
"v_mfma_f32_16x16x32_bf16 v[116:119], v[132:135], v[52:55], v[116:119]\n"
"v_mfma_f32_16x16x32_bf16 v[120:123], v[132:135], v[68:71], v[120:123]\n"
"v_mfma_f32_16x16x32_bf16 v[124:127], v[132:135], v[84:87], v[124:127]\n"
"v_mfma_f32_16x16x32_bf16 v[112:115], v[136:139], v[40:43], v[112:115]\n"
"v_mfma_f32_16x16x32_bf16 v[116:119], v[136:139], v[56:59], v[116:119]\n"
"v_mfma_f32_16x16x32_bf16 v[120:123], v[136:139], v[72:75], v[120:123]\n"
"v_mfma_f32_16x16x32_bf16 v[124:127], v[136:139], v[88:91], v[124:127]\n"
"v_mfma_f32_16x16x32_bf16 v[112:115], v[140:143], v[44:47], v[112:115]\n"
"v_mfma_f32_16x16x32_bf16 v[116:119], v[140:143], v[60:63], v[116:119]\n"
"v_mfma_f32_16x16x32_bf16 v[120:123], v[140:143], v[76:79], v[120:123]\n"
"v_mfma_f32_16x16x32_bf16 v[124:127], v[140:143], v[92:95], v[124:127]\n"
"s_nop 7\n"
"v_max3_f32 v160, v160, v96, v97\n"
"v_max3_f32 v161, v161, v100, v101\n"
"v_max3_f32 v162, v162, v104, v105\n"
"v_max3_f32 v163, v163, v108, v109\n"
"v_max3_f32 v160, v160, v98, v99\n"
"v_max3_f32 v161, v161, v102, v103\n"
"v_max3_f32 v162, v162, v106, v107\n"
"v_max3_f32 v163, v163, v110, v111\n"
"v_max3_f32 v160, v160, v112, v113\n"
"v_max3_f32 v161, v161, v116, v117\n"
"v_max3_f32 v162, v162, v120, v121\n"
"v_max3_f32 v163, v163, v124, v125\n"
"v_max3_f32 v160, v160, v114, v115\n"
"v_max3_f32 v161, v161, v118, v119\n"
"v_max3_f32 v162, v162, v122, v123\n"
"v_max3_f32 v163, v163, v126, v127\n"
"s_cmp_lt_u32 s98, 131072\n"
"s_cbranch_scc1 L_slow1\n"
"v_mov_b32 v164, v160\n"
"v_mov_b32 v165, v161\n"
"v_mov_b32 v166, v162\n"
"v_mov_b32 v167, v163\n"
"v_permlane32_swap_b32 v164, v160\n"
"v_permlane32_swap_b32 v165, v161\n"
"v_permlane32_swap_b32 v166, v162\n"
"v_permlane32_swap_b32 v167, v163\n"
"v_max_f32 v160, v160, v164\n"
"v_max_f32 v161, v161, v165\n"
"v_max_f32 v162, v162, v166\n"
"v_max_f32 v163, v163, v167\n"
"v_mov_b32 v164, v160\n"
"v_mov_b32 v165, v161\n"
"v_mov_b32 v166, v162\n"
"v_mov_b32 v167, v163\n"
"v_permlane16_swap_b32 v164, v160\n"
"v_permlane16_swap_b32 v165, v161\n"
"v_permlane16_swap_b32 v166, v162\n"
"v_permlane16_swap_b32 v167, v163\n"
"v_max_f32 v160, v160, v164\n"
"v_max_f32 v161, v161, v165\n"
"v_max_f32 v162, v162, v166\n"
"v_max_f32 v163, v163, v167\n"
"v_sub_f32 v192, 0x80000000, v160\n"
"v_sub_f32 v196, 0x80000000, v161\n"
"v_sub_f32 v200, 0x80000000, v162\n"
"v_sub_f32 v204, 0x80000000, v163\n"
"v_mov_b32 v193, v192\n"
"v_mov_b32 v197, v196\n"
"v_mov_b32 v201, v200\n"
"v_mov_b32 v205, v204\n"
"v_mov_b32 v194, v192\n"
"v_mov_b32 v198, v196\n"
"v_mov_b32 v202, v200\n"
"v_mov_b32 v206, v204\n"
"v_mov_b32 v195, v192\n"
"v_mov_b32 v199, v196\n"
"v_mov_b32 v203, v200\n"
"v_mov_b32 v207, v204\n"
"v_accvgpr_write_b32 a0, 0\n"
"v_accvgpr_write_b32 a1, 0\n"
"v_accvgpr_write_b32 a2, 0\n"
"v_accvgpr_write_b32 a3, 0\n"
"v_accvgpr_write_b32 a4, 0\n"
"v_accvgpr_write_b32 a5, 0\n"
"v_accvgpr_write_b32 a6, 0\n"
"v_accvgpr_write_b32 a7, 0\n"
"v_accvgpr_write_b32 a8, 0\n"
"v_accvgpr_write_b32 a9, 0\n"
"v_accvgpr_write_b32 a10, 0\n"
"v_accvgpr_write_b32 a11, 0\n"
"v_accvgpr_write_b32 a12, 0\n"
"v_accvgpr_write_b32 a13, 0\n"
"v_accvgpr_write_b32 a14, 0\n"
"v_accvgpr_write_b32 a15, 0\n"
"v_accvgpr_write_b32 a16, 0\n"
"v_accvgpr_write_b32 a17, 0\n"
"v_accvgpr_write_b32 a18, 0\n"
"v_accvgpr_write_b32 a19, 0\n"
"v_accvgpr_write_b32 a20, 0\n"
"v_accvgpr_write_b32 a21, 0\n"
"v_accvgpr_write_b32 a22, 0\n"
"v_accvgpr_write_b32 a23, 0\n"
"v_accvgpr_write_b32 a24, 0\n"
"v_accvgpr_write_b32 a25, 0\n"
"v_accvgpr_write_b32 a26, 0\n"
"v_accvgpr_write_b32 a27, 0\n"
"v_accvgpr_write_b32 a28, 0\n"
"v_accvgpr_write_b32 a29, 0\n"
"v_accvgpr_write_b32 a30, 0\n"
"v_accvgpr_write_b32 a31, 0\n"
"v_accvgpr_write_b32 a32, 0\n"
"v_accvgpr_write_b32 a33, 0\n"
"v_accvgpr_write_b32 a34, 0\n"
"v_accvgpr_write_b32 a35, 0\n"
"v_accvgpr_write_b32 a36, 0\n"
"v_accvgpr_write_b32 a37, 0\n"
"v_accvgpr_write_b32 a38, 0\n"
"v_accvgpr_write_b32 a39, 0\n"
"v_accvgpr_write_b32 a40, 0\n"
"v_accvgpr_write_b32 a41, 0\n"
"v_accvgpr_write_b32 a42, 0\n"
"v_accvgpr_write_b32 a43, 0\n"
"v_accvgpr_write_b32 a44, 0\n"
"v_accvgpr_write_b32 a45, 0\n"
"v_accvgpr_write_b32 a46, 0\n"
"v_accvgpr_write_b32 a47, 0\n"
"v_accvgpr_write_b32 a48, 0\n"
"v_accvgpr_write_b32 a49, 0\n"
"v_accvgpr_write_b32 a50, 0\n"
"v_accvgpr_write_b32 a51, 0\n"
"v_accvgpr_write_b32 a52, 0\n"
"v_accvgpr_write_b32 a53, 0\n"
"v_accvgpr_write_b32 a54, 0\n"
"v_accvgpr_write_b32 a55, 0\n"
"v_accvgpr_write_b32 a56, 0\n"
"v_accvgpr_write_b32 a57, 0\n"
"v_accvgpr_write_b32 a58, 0\n"
"v_accvgpr_write_b32 a59, 0\n"
"v_accvgpr_write_b32 a60, 0\n"
"v_accvgpr_write_b32 a61, 0\n"
"v_accvgpr_write_b32 a62, 0\n"
"v_accvgpr_write_b32 a63, 0\n"
"v_accvgpr_write_b32 a64, 0\n"
"v_accvgpr_write_b32 a65, 0\n"
"v_accvgpr_write_b32 a66, 0\n"
"v_accvgpr_write_b32 a67, 0\n"
"v_accvgpr_write_b32 a68, 0\n"
"v_accvgpr_write_b32 a69, 0\n"
"v_accvgpr_write_b32 a70, 0\n"
"v_accvgpr_write_b32 a71, 0\n"
"v_accvgpr_write_b32 a72, 0\n"
"v_accvgpr_write_b32 a73, 0\n"
"v_accvgpr_write_b32 a74, 0\n"
"v_accvgpr_write_b32 a75, 0\n"
"v_accvgpr_write_b32 a76, 0\n"
"v_accvgpr_write_b32 a77, 0\n"
"v_accvgpr_write_b32 a78, 0\n"
"v_accvgpr_write_b32 a79, 0\n"
"v_accvgpr_write_b32 a80, 0\n"
"v_accvgpr_write_b32 a81, 0\n"
"v_accvgpr_write_b32 a82, 0\n"
"v_accvgpr_write_b32 a83, 0\n"
"v_accvgpr_write_b32 a84, 0\n"
"v_accvgpr_write_b32 a85, 0\n"
"v_accvgpr_write_b32 a86, 0\n"
"v_accvgpr_write_b32 a87, 0\n"
"v_accvgpr_write_b32 a88, 0\n"
"v_accvgpr_write_b32 a89, 0\n"
"v_accvgpr_write_b32 a90, 0\n"
"v_accvgpr_write_b32 a91, 0\n"
"v_accvgpr_write_b32 a92, 0\n"
"v_accvgpr_write_b32 a93, 0\n"
"v_accvgpr_write_b32 a94, 0\n"
"v_accvgpr_write_b32 a95, 0\n"
"v_accvgpr_write_b32 a96, 0\n"
"v_accvgpr_write_b32 a97, 0\n"
"v_accvgpr_write_b32 a98, 0\n"
"v_accvgpr_write_b32 a99, 0\n"
"v_accvgpr_write_b32 a100, 0\n"
"v_accvgpr_write_b32 a101, 0\n"
"v_accvgpr_write_b32 a102, 0\n"
"v_accvgpr_write_b32 a103, 0\n"
"v_accvgpr_write_b32 a104, 0\n"
"v_accvgpr_write_b32 a105, 0\n"
"v_accvgpr_write_b32 a106, 0\n"
"v_accvgpr_write_b32 a107, 0\n"
"v_accvgpr_write_b32 a108, 0\n"
"v_accvgpr_write_b32 a109, 0\n"
"v_accvgpr_write_b32 a110, 0\n"
"v_accvgpr_write_b32 a111, 0\n"
"v_accvgpr_write_b32 a112, 0\n"
"v_accvgpr_write_b32 a113, 0\n"
"v_accvgpr_write_b32 a114, 0\n"
"v_accvgpr_write_b32 a115, 0\n"
"v_accvgpr_write_b32 a116, 0\n"
"v_accvgpr_write_b32 a117, 0\n"
"v_accvgpr_write_b32 a118, 0\n"
"v_accvgpr_write_b32 a119, 0\n"
"v_accvgpr_write_b32 a120, 0\n"
"v_accvgpr_write_b32 a121, 0\n"
"v_accvgpr_write_b32 a122, 0\n"
"v_accvgpr_write_b32 a123, 0\n"
"v_accvgpr_write_b32 a124, 0\n"
"v_accvgpr_write_b32 a125, 0\n"
"v_accvgpr_write_b32 a126, 0\n"
"v_accvgpr_write_b32 a127, 0\n"
"v_accvgpr_write_b32 a128, 0\n"
"v_accvgpr_write_b32 a129, 0\n"
"v_accvgpr_write_b32 a130, 0\n"
"v_accvgpr_write_b32 a131, 0\n"
"v_accvgpr_write_b32 a132, 0\n"
"v_accvgpr_write_b32 a133, 0\n"
"v_accvgpr_write_b32 a134, 0\n"
"v_accvgpr_write_b32 a135, 0\n"
"v_accvgpr_write_b32 a136, 0\n"
"v_accvgpr_write_b32 a137, 0\n"
"v_accvgpr_write_b32 a138, 0\n"
"v_accvgpr_write_b32 a139, 0\n"
"v_accvgpr_write_b32 a140, 0\n"
"v_accvgpr_write_b32 a141, 0\n"
"v_accvgpr_write_b32 a142, 0\n"
"v_accvgpr_write_b32 a143, 0\n"
"v_lshrrev_b32 v7, 2, v0\n"
"v_lshlrev_b32 v7, 8, v7\n"
"v_lshl_add_u32 v7, v5, 4, v7\n"
"v_lshl_add_u32 v160, v2, 2, v4\n"
"v_lshlrev_b32 v160, 6, v160\n"
"v_lshl_add_u32 v160, v5, 3, v160\n"
"s_mul_i32 s8, s11, 2560\n"
"s_mul_i32 s98, s54, 120832\n"
"s_add_u32 s8, s8, s98\n"
"s_add_u32 s8, s8, 32768\n"
"v_add_u32 v160, s8, v160\n"
"v_add_u32 v161, 3072, v7\n"
"s_mov_b32 s98, 0\n"
"s_add_u32 s80, s16, s99\n"
"s_addc_u32 s81, s17, 0\n"
"s_add_u32 s88, s14, s99\n"
"s_addc_u32 s89, s15, 0\n"
"s_and_b32 s81, s81, 0xffff\n"
"s_and_b32 s89, s89, 0xffff\n"
"s_mov_b32 m0, s8\n"
"L_slow2:\n"
"s_mov_b64 s[92:93], s[88:89]\n"
"s_mov_b32 s99, s98\n"
"buffer_load_dwordx4 v[128:131], v6, s[92:95], s98 offen offset:0\n"
"buffer_load_dwordx4 v[132:135], v6, s[92:95], s98 offen offset:64\n"
"buffer_load_dwordx4 v[136:139], v6, s[92:95], s98 offen offset:128\n"
"buffer_load_dwordx4 v[140:143], v6, s[92:95], s98 offen offset:192\n"
"s_add_u32 s98, s98, 4096\n"
"s_waitcnt vmcnt(0)\n"
"v_mfma_f32_16x16x32_bf16 v[96:99], v[128:131], v[32:35], v[192:195]\n"
"v_mfma_f32_16x16x32_bf16 v[100:103], v[128:131], v[48:51], v[196:199]\n"
"v_mfma_f32_16x16x32_bf16 v[104:107], v[128:131], v[64:67], v[200:203]\n"
"v_mfma_f32_16x16x32_bf16 v[108:111], v[128:131], v[80:83], v[204:207]\n"
"v_mfma_f32_16x16x32_bf16 v[96:99], v[132:135], v[36:39], v[96:99]\n"
"v_mfma_f32_16x16x32_bf16 v[100:103], v[132:135], v[52:55], v[100:103]\n"
"v_mfma_f32_16x16x32_bf16 v[104:107], v[132:135], v[68:71], v[104:107]\n"
"v_mfma_f32_16x16x32_bf16 v[108:111], v[132:135], v[84:87], v[108:111]\n"
"v_mfma_f32_16x16x32_bf16 v[96:99], v[136:139], v[40:43], v[96:99]\n"
"v_mfma_f32_16x16x32_bf16 v[100:103], v[136:139], v[56:59], v[100:103]\n"
"v_mfma_f32_16x16x32_bf16 v[104:107], v[136:139], v[72:75], v[104:107]\n"
"v_mfma_f32_16x16x32_bf16 v[108:111], v[136:139], v[88:91], v[108:111]\n"
"v_mfma_f32_16x16x32_bf16 v[96:99], v[140:143], v[44:47], v[96:99]\n"
"v_mfma_f32_16x16x32_bf16 v[100:103], v[140:143], v[60:63], v[100:103]\n"
"v_mfma_f32_16x16x32_bf16 v[104:107], v[140:143], v[76:79], v[104:107]\n"
"v_mfma_f32_16x16x32_bf16 v[108:111], v[140:143], v[92:95], v[108:111]\n"
"s_nop 7\n"
"buffer_load_dwordx4 v[128:131], v6, s[92:95], s98 offen offset:0\n"
"buffer_load_dwordx4 v[132:135], v6, s[92:95], s98 offen offset:64\n"
"buffer_load_dwordx4 v[136:139], v6, s[92:95], s98 offen offset:128\n"
"buffer_load_dwordx4 v[140:143], v6, s[92:95], s98 offen offset:192\n"
"s_add_u32 s98, s98, 4096\n"
"s_waitcnt vmcnt(0)\n"
"v_mfma_f32_16x16x32_bf16 v[112:115], v[128:131], v[32:35], v[192:195]\n"
"v_mfma_f32_16x16x32_bf16 v[116:119], v[128:131], v[48:51], v[196:199]\n"
"v_mfma_f32_16x16x32_bf16 v[120:123], v[128:131], v[64:67], v[200:203]\n"
"v_mfma_f32_16x16x32_bf16 v[124:127], v[128:131], v[80:83], v[204:207]\n"
"v_mfma_f32_16x16x32_bf16 v[112:115], v[132:135], v[36:39], v[112:115]\n"
"v_mfma_f32_16x16x32_bf16 v[116:119], v[132:135], v[52:55], v[116:119]\n"
"v_mfma_f32_16x16x32_bf16 v[120:123], v[132:135], v[68:71], v[120:123]\n"
"v_mfma_f32_16x16x32_bf16 v[124:127], v[132:135], v[84:87], v[124:127]\n"
"v_mfma_f32_16x16x32_bf16 v[112:115], v[136:139], v[40:43], v[112:115]\n"
"v_mfma_f32_16x16x32_bf16 v[116:119], v[136:139], v[56:59], v[116:119]\n"
"v_mfma_f32_16x16x32_bf16 v[120:123], v[136:139], v[72:75], v[120:123]\n"
"v_mfma_f32_16x16x32_bf16 v[124:127], v[136:139], v[88:91], v[124:127]\n"
"v_mfma_f32_16x16x32_bf16 v[112:115], v[140:143], v[44:47], v[112:115]\n"
"v_mfma_f32_16x16x32_bf16 v[116:119], v[140:143], v[60:63], v[116:119]\n"
"v_mfma_f32_16x16x32_bf16 v[120:123], v[140:143], v[76:79], v[120:123]\n"
"v_mfma_f32_16x16x32_bf16 v[124:127], v[140:143], v[92:95], v[124:127]\n"
"s_nop 7\n"
"v_exp_f32 v96, v96\n"
"v_exp_f32 v97, v97\n"
"v_exp_f32 v98, v98\n"
"v_exp_f32 v99, v99\n"
"v_exp_f32 v100, v100\n"
"v_exp_f32 v101, v101\n"
"v_exp_f32 v102, v102\n"
"v_exp_f32 v103, v103\n"
"v_exp_f32 v104, v104\n"
"v_exp_f32 v105, v105\n"
"v_exp_f32 v106, v106\n"
"v_exp_f32 v107, v107\n"
"v_exp_f32 v108, v108\n"
"v_exp_f32 v109, v109\n"
"v_exp_f32 v110, v110\n"
"v_exp_f32 v111, v111\n"
"v_exp_f32 v112, v112\n"
"v_exp_f32 v113, v113\n"
"v_exp_f32 v114, v114\n"
"v_exp_f32 v115, v115\n"
"v_exp_f32 v116, v116\n"
"v_exp_f32 v117, v117\n"
"v_exp_f32 v118, v118\n"
"v_exp_f32 v119, v119\n"
"v_exp_f32 v120, v120\n"
"v_exp_f32 v121, v121\n"
"v_exp_f32 v122, v122\n"
"v_exp_f32 v123, v123\n"
"v_exp_f32 v124, v124\n"
"v_exp_f32 v125, v125\n"
"v_exp_f32 v126, v126\n"
"v_exp_f32 v127, v127\n"
"v_cvt_pk_bf16_f32 v176, v96, v97\n"
"v_cvt_pk_bf16_f32 v177, v98, v99\n"
"v_cvt_pk_bf16_f32 v180, v100, v101\n"
"v_cvt_pk_bf16_f32 v181, v102, v103\n"
"v_cvt_pk_bf16_f32 v184, v104, v105\n"
"v_cvt_pk_bf16_f32 v185, v106, v107\n"
"v_cvt_pk_bf16_f32 v188, v108, v109\n"
"v_cvt_pk_bf16_f32 v189, v110, v111\n"
"v_cvt_pk_bf16_f32 v178, v112, v113\n"
"v_cvt_pk_bf16_f32 v179, v114, v115\n"
"v_cvt_pk_bf16_f32 v182, v116, v117\n"
"v_cvt_pk_bf16_f32 v183, v118, v119\n"
"v_cvt_pk_bf16_f32 v186, v120, v121\n"
"v_cvt_pk_bf16_f32 v187, v122, v123\n"
"v_cvt_pk_bf16_f32 v190, v124, v125\n"
"v_cvt_pk_bf16_f32 v191, v126, v127\n"
"s_cmp_eq_u32 s98, 131072\n"
"s_cbranch_scc1 L_slow3\n"
"s_mov_b64 s[92:93], s[80:81]\n"
"v_mfma_f32_16x16x32_bf16 a[128:131], v[208:211], v[176:179], a[128:131]\n"
"v_mfma_f32_16x16x32_bf16 a[132:135], v[208:211], v[180:183], a[132:135]\n"
"v_mfma_f32_16x16x32_bf16 a[136:139], v[208:211], v[184:187], a[136:139]\n"
"v_mfma_f32_16x16x32_bf16 a[140:143], v[208:211], v[188:191], a[140:143]\n"
"buffer_load_dwordx4 v7, s[92:95], s99 offen lds\n"
"buffer_load_dwordx4 v161, s[92:95], s99 offen offset:1024 lds\n"
"s_add_u32 s99, s99, 64\n"
"s_waitcnt vmcnt(0)\n"
"ds_read_b64_tr_b16 v[144:145], v160 offset:0\n"
"ds_read_b64_tr_b16 v[146:147], v160 offset:1024\n"
"ds_read_b64_tr_b16 v[148:149], v160 offset:32\n"
"ds_read_b64_tr_b16 v[150:151], v160 offset:1056\n"
"s_waitcnt lgkmcnt(0)\n"
"v_mfma_f32_16x16x32_bf16 a[0:3], v[144:147], v[176:179], a[0:3]\n"
"v_mfma_f32_16x16x32_bf16 a[4:7], v[144:147], v[180:183], a[4:7]\n"
"v_mfma_f32_16x16x32_bf16 a[8:11], v[144:147], v[184:187], a[8:11]\n"
"v_mfma_f32_16x16x32_bf16 a[12:15], v[144:147], v[188:191], a[12:15]\n"
"v_mfma_f32_16x16x32_bf16 a[16:19], v[148:151], v[176:179], a[16:19]\n"
"v_mfma_f32_16x16x32_bf16 a[20:23], v[148:151], v[180:183], a[20:23]\n"
"v_mfma_f32_16x16x32_bf16 a[24:27], v[148:151], v[184:187], a[24:27]\n"
"v_mfma_f32_16x16x32_bf16 a[28:31], v[148:151], v[188:191], a[28:31]\n"
"buffer_load_dwordx4 v7, s[92:95], s99 offen lds\n"
"buffer_load_dwordx4 v161, s[92:95], s99 offen offset:1024 lds\n"
"s_add_u32 s99, s99, 64\n"
"s_waitcnt vmcnt(0)\n"
"ds_read_b64_tr_b16 v[144:145], v160 offset:0\n"
"ds_read_b64_tr_b16 v[146:147], v160 offset:1024\n"
"ds_read_b64_tr_b16 v[148:149], v160 offset:32\n"
"ds_read_b64_tr_b16 v[150:151], v160 offset:1056\n"
"s_waitcnt lgkmcnt(0)\n"
"v_mfma_f32_16x16x32_bf16 a[32:35], v[144:147], v[176:179], a[32:35]\n"
"v_mfma_f32_16x16x32_bf16 a[36:39], v[144:147], v[180:183], a[36:39]\n"
"v_mfma_f32_16x16x32_bf16 a[40:43], v[144:147], v[184:187], a[40:43]\n"
"v_mfma_f32_16x16x32_bf16 a[44:47], v[144:147], v[188:191], a[44:47]\n"
"v_mfma_f32_16x16x32_bf16 a[48:51], v[148:151], v[176:179], a[48:51]\n"
"v_mfma_f32_16x16x32_bf16 a[52:55], v[148:151], v[180:183], a[52:55]\n"
"v_mfma_f32_16x16x32_bf16 a[56:59], v[148:151], v[184:187], a[56:59]\n"
"v_mfma_f32_16x16x32_bf16 a[60:63], v[148:151], v[188:191], a[60:63]\n"
"buffer_load_dwordx4 v7, s[92:95], s99 offen lds\n"
"buffer_load_dwordx4 v161, s[92:95], s99 offen offset:1024 lds\n"
"s_add_u32 s99, s99, 64\n"
"s_waitcnt vmcnt(0)\n"
"ds_read_b64_tr_b16 v[144:145], v160 offset:0\n"
"ds_read_b64_tr_b16 v[146:147], v160 offset:1024\n"
"ds_read_b64_tr_b16 v[148:149], v160 offset:32\n"
"ds_read_b64_tr_b16 v[150:151], v160 offset:1056\n"
"s_waitcnt lgkmcnt(0)\n"
"v_mfma_f32_16x16x32_bf16 a[64:67], v[144:147], v[176:179], a[64:67]\n"
"v_mfma_f32_16x16x32_bf16 a[68:71], v[144:147], v[180:183], a[68:71]\n"
"v_mfma_f32_16x16x32_bf16 a[72:75], v[144:147], v[184:187], a[72:75]\n"
"v_mfma_f32_16x16x32_bf16 a[76:79], v[144:147], v[188:191], a[76:79]\n"
"v_mfma_f32_16x16x32_bf16 a[80:83], v[148:151], v[176:179], a[80:83]\n"
"v_mfma_f32_16x16x32_bf16 a[84:87], v[148:151], v[180:183], a[84:87]\n"
"v_mfma_f32_16x16x32_bf16 a[88:91], v[148:151], v[184:187], a[88:91]\n"
"v_mfma_f32_16x16x32_bf16 a[92:95], v[148:151], v[188:191], a[92:95]\n"
"buffer_load_dwordx4 v7, s[92:95], s99 offen lds\n"
"buffer_load_dwordx4 v161, s[92:95], s99 offen offset:1024 lds\n"
"s_add_u32 s99, s99, 64\n"
"s_waitcnt vmcnt(0)\n"
"ds_read_b64_tr_b16 v[144:145], v160 offset:0\n"
"ds_read_b64_tr_b16 v[146:147], v160 offset:1024\n"
"ds_read_b64_tr_b16 v[148:149], v160 offset:32\n"
"ds_read_b64_tr_b16 v[150:151], v160 offset:1056\n"
"s_waitcnt lgkmcnt(0)\n"
"v_mfma_f32_16x16x32_bf16 a[96:99], v[144:147], v[176:179], a[96:99]\n"
"v_mfma_f32_16x16x32_bf16 a[100:103], v[144:147], v[180:183], a[100:103]\n"
"v_mfma_f32_16x16x32_bf16 a[104:107], v[144:147], v[184:187], a[104:107]\n"
"v_mfma_f32_16x16x32_bf16 a[108:111], v[144:147], v[188:191], a[108:111]\n"
"v_mfma_f32_16x16x32_bf16 a[112:115], v[148:151], v[176:179], a[112:115]\n"
"v_mfma_f32_16x16x32_bf16 a[116:119], v[148:151], v[180:183], a[116:119]\n"
"v_mfma_f32_16x16x32_bf16 a[120:123], v[148:151], v[184:187], a[120:123]\n"
"v_mfma_f32_16x16x32_bf16 a[124:127], v[148:151], v[188:191], a[124:127]\n"
"s_branch L_slow2\n"
"L_slow3:\n"
"s_mov_b32 m0, s9\n"
"s_mov_b32 s94, 0\n"
"s_cmp_eq_u32 s54, 1\n"
"s_cbranch_scc1 L_pv15B\n"
"s_branch L_pv15A\n"
"L_end:\n"
"L_code1:\n"
  :: "s"(__builtin_amdgcn_kernarg_segment_ptr()), "s"(blockIdx.x), "s"(wid), "s"((unsigned)(size_t)smem)
  : "s5","s6","s7","s8","s9","s10","s11","s12","s13","s14","s15","s16","s17","s18","s19","s20","s21","s22","s23","s24","s25","s26","s27","s28","s29","s30","s31","s32","s33","s34","s35","s36","s37","s38","s39","s40","s41","s42","s43","s44","s45","s46","s47","s48","s49","s50","s51","s52","s53","s54","s55","s56","s57","s58","s59","s60","s61","s62","s63","s64","s65","s66","s67","s68","s69","s70","s71","s72","s73","s74","s75","s76","s77","s78","s79","s80","s81","s82","s83","s84","s85","s86","s87","s88","s89","s90","s91","s92","s93","s94","s95","s96","s97","s98","s99","v0","v1","v2","v3","v4","v5","v6","v7","v8","v9","v10","v11","v12","v13","v14","v15","v16","v17","v18","v19","v20","v21","v22","v23","v24","v25","v26","v27","v28","v29","v30","v31","v32","v33","v34","v35","v36","v37","v38","v39","v40","v41","v42","v43","v44","v45","v46","v47","v48","v49","v50","v51","v52","v53","v54","v55","v56","v57","v58","v59","v60","v61","v62","v63","v64","v65","v66","v67","v68","v69","v70","v71","v72","v73","v74","v75","v76","v77","v78","v79","v80","v81","v82","v83","v84","v85","v86","v87","v88","v89","v90","v91","v92","v93","v94","v95","v96","v97","v98","v99","v100","v101","v102","v103","v104","v105","v106","v107","v108","v109","v110","v111","v112","v113","v114","v115","v116","v117","v118","v119","v120","v121","v122","v123","v124","v125","v126","v127","v128","v129","v130","v131","v132","v133","v134","v135","v136","v137","v138","v139","v140","v141","v142","v143","v144","v145","v146","v147","v148","v149","v150","v151","v152","v153","v154","v155","v156","v157","v158","v159","v160","v161","v162","v163","v164","v165","v166","v167","v168","v169","v170","v171","v172","v173","v174","v175","v176","v177","v178","v179","v180","v181","v182","v183","v184","v185","v186","v187","v188","v189","v190","v191","v192","v193","v194","v195","v196","v197","v198","v199","v200","v201","v202","v203","v204","v205","v206","v207","v208","v209","v210","v211","v212","v213","v214","v215","v216","v217","v218","v219","v220","v221","v222","v223","v224","v225","v226","v227","v228","v229","v230","v231","v232","v233","v234","v235","v236","v237","v238","v239","v240","v241","v242","v243","v244","v245","v246","v247","v248","v249","v250","v251","v252","v253","v254","v255","a0","a1","a2","a3","a4","a5","a6","a7","a8","a9","a10","a11","a12","a13","a14","a15","a16","a17","a18","a19","a20","a21","a22","a23","a24","a25","a26","a27","a28","a29","a30","a31","a32","a33","a34","a35","a36","a37","a38","a39","a40","a41","a42","a43","a44","a45","a46","a47","a48","a49","a50","a51","a52","a53","a54","a55","a56","a57","a58","a59","a60","a61","a62","a63","a64","a65","a66","a67","a68","a69","a70","a71","a72","a73","a74","a75","a76","a77","a78","a79","a80","a81","a82","a83","a84","a85","a86","a87","a88","a89","a90","a91","a92","a93","a94","a95","a96","a97","a98","a99","a100","a101","a102","a103","a104","a105","a106","a107","a108","a109","a110","a111","a112","a113","a114","a115","a116","a117","a118","a119","a120","a121","a122","a123","a124","a125","a126","a127","a128","a129","a130","a131","a132","a133","a134","a135","a136","a137","a138","a139","a140","a141","a142","a143","a144","a145","a146","a147","a148","a149","a150","a151","a152","a153","a154","a155","a156","a157","a158","a159","a160","a161","a162","a163","a164","a165","a166","a167","a168","a169","a170","a171","a172","a173","a174","a175","a176","a177","a178","a179","a180","a181","a182","a183","a184","a185","a186","a187","a188","a189","a190","a191","a192","a193","a194","a195","a196","a197","a198","a199","a200","a201","a202","a203","a204","a205","a206","a207","a208","a209","a210","a211","a212","a213","a214","a215","a216","a217","a218","a219","a220","a221","a222","a223","a224","a225","a226","a227","a228","a229","a230","a231","a232","a233","a234","a235","a236","a237","a238","a239","a240","a241","a242","a243","a244","a245","a246","a247","a248","a249","a250","a251","a252","a253","a254","a255","vcc","memory");
}

extern "C" void kernel_launch(void* const* d_in, const int* in_sizes, int n_in, void* d_out, int out_size, void* d_ws, size_t ws_size, hipStream_t stream) {
  fused_block<<<256, 256, 0, stream>>>((const u16*)d_in[0], (const u16*)d_in[1], (const u16*)d_in[2], (const u16*)d_in[3], (const u16*)d_in[4], (const u16*)d_in[6],
                                       (const u16*)d_in[7], (const u16*)d_in[8], (const u16*)d_in[9], (u16*)d_out, (char*)d_ws);
}
```
